```python
import torch

def _softmax(x, axis=-1):
    return torch.softmax(x, dim=axis)
def _gelu(x):
    return torch.nn.functional.gelu(x, approximate="tanh")
def _silu(x):
    return torch.nn.functional.silu(x)
def _sigmoid(x):
    return torch.sigmoid(x)
def _layernorm(x, axis=-1, eps=1e-5):
    m = x.mean(dim=axis, keepdim=True)
    v = x.var(dim=axis, keepdim=True, unbiased=False)
    return (x - m) / torch.sqrt(v + eps)
def _scatter_add(tgt, idx, src, axis=0):
    return tgt.clone().index_add_(axis, idx, src)
def _segment_sum(src, seg, n):
    out = torch.zeros((n,) + src.shape[1:], dtype=src.dtype)
    return out.index_add_(0, seg, src)
def _pad(x, pw):
    flat = tuple(v for pair in reversed(pw) for v in pair)
    return torch.nn.functional.pad(x, flat)

kernel_name = 'concat_heads__compressed_kv_cla__gqa_block_cached__decode'

def setup_inputs():

    _w = (lambda a: (lambda t: t.to(torch.bfloat16) if t.is_floating_point() else t)(torch.as_tensor(a)))
    def _A_setup():
        B, H, Sq, Sk, D, M = 2, 32, 1, 4096, 128, 512
        Dc = 4 * D
        return {
            "x": _w((lambda *s: torch.randn(*s, dtype=torch.bfloat16) * 3.2)(B, Sq, M)),
            "c_kv": _w((lambda *s: torch.randn(*s, dtype=torch.bfloat16) * 0.1)(B, Sk, Dc)),
            "w_q": _w((lambda *s: torch.randn(*s, dtype=torch.bfloat16) * 6.4)(M, H, D) * (M ** -0.5)),
            "w_uk": _w((lambda *s: torch.randn(*s, dtype=torch.bfloat16) * 6.4)(Dc, H, D) * (Dc ** -0.5)),
            "w_uv": _w((lambda *s: torch.randn(*s, dtype=torch.bfloat16) * 0.1)(Dc, H, D) * (Dc ** -0.5)),
            "w_o": _w((lambda *s: torch.randn(*s, dtype=torch.bfloat16) * 0.1)(H, D, M) * ((H * D) ** -0.5)),
        }
    _A_in = _A_setup()

    def _B_setup():
        B, Sq, D = 2, 1, 512
        Hq, Hkv, Sk, Dh = 32, 32, 4096, 128
        assert Hq % Hkv == 0, (Hq, Hkv)
        return {
            "x": _w((lambda *s: torch.randn(*s, dtype=torch.bfloat16) * 1.6)(B, Sq, D)),
            "k_cache": _w((lambda *s: torch.randn(*s, dtype=torch.bfloat16) * 1.6)(B, Hkv, Sk, Dh)),
            "v_cache": _w((lambda *s: torch.randn(*s, dtype=torch.bfloat16) * 0.05)(B, Hkv, Sk, Dh)),
            "wq": _w((lambda *s: torch.randn(*s, dtype=torch.bfloat16) * 1.6)(D, Hq * Dh) * (D ** -0.5)),
            "wo": _w((lambda *s: torch.randn(*s, dtype=torch.bfloat16) * 0.05)(Hq * Dh, D) * ((Hq * Dh) ** -0.5)),
        }
    _B_in = _B_setup()

    return {
        "A_x": _A_in["x"],
        "A_c_kv": _A_in["c_kv"],
        "A_w_q": _A_in["w_q"],
        "A_w_uk": _A_in["w_uk"],
        "A_w_uv": _A_in["w_uv"],
        "A_w_o": _A_in["w_o"],
        "B_x": _B_in["x"],
        "B_k_cache": _B_in["k_cache"],
        "B_v_cache": _B_in["v_cache"],
        "B_wq": _B_in["wq"],
        "B_wo": _B_in["wo"],
    }

def reference(A_x, A_c_kv, A_w_q, A_w_uk, A_w_uv, A_w_o, B_x, B_k_cache, B_v_cache, B_wq, B_wo):
    def _A_ref(x, c_kv, w_q, w_uk, w_uv, w_o):
        B, Sq, M = x.shape[0], x.shape[1], x.shape[2]
        Sk = c_kv.shape[1]
        H, D = w_q.shape[1], w_q.shape[2]
        q = torch.einsum("bqm,mhd->bhqd", x, w_q)
        k = torch.einsum("bkc,chd->bhkd", c_kv, w_uk)
        v = torch.einsum("bkc,chd->bhkd", c_kv, w_uv)
        scores = torch.einsum("bhqd,bhkd->bhqk", q, k) * (D ** -0.5)
        off = Sk - Sq
        qi = torch.arange(Sq)[:, None]
        kj = torch.arange(Sk)[None, :]
        cmask = kj <= (qi + off)
        scores = torch.where(cmask[None, None], scores, -1e9)
        p = _softmax(scores, axis=-1)
        ctx = torch.einsum("bhqk,bhkd->bhqd", p, v)
        return torch.einsum("bhqd,hdm->bqm", ctx, w_o)

    def _B_ref(x, k_cache, v_cache, wq, wo):
        B, Sq, D = x.shape
        Hq, Dh = 32, 128
        Hkv = k_cache.shape[1]
        rep = Hq // Hkv
        q = (lambda x, *d: x.permute(*d))((x @ wq).reshape(B, Sq, Hq, Dh), 0, 2, 1, 3)
        k = (lambda x, n, axis: torch.repeat_interleave(x, n, dim=axis))(k_cache, rep, axis=1)
        v = (lambda x, n, axis: torch.repeat_interleave(x, n, dim=axis))(v_cache, rep, axis=1)
        scores = torch.einsum("bhqd,bhkd->bhqk", q, k) * (Dh ** -0.5)
        p = _softmax(scores, axis=-1)
        o = torch.einsum("bhqk,bhkd->bhqd", p, v)
        return (lambda x, *d: x.permute(*d))(o, 0, 2, 1, 3).reshape(B, Sq, Hq * Dh) @ wo

    a = _A_ref(A_x, A_c_kv, A_w_q, A_w_uk, A_w_uv, A_w_o)
    b = _B_ref(B_x, B_k_cache, B_v_cache, B_wq, B_wo)
    return torch.concatenate([a, b], axis=-1)
```

```cpp
#include <hip/hip_runtime.h>
#include <hip/hip_bf16.h>
#include <stdint.h>

#define DI __device__ __forceinline__
typedef __attribute__((ext_vector_type(4))) unsigned u32x4;
typedef __attribute__((ext_vector_type(2))) unsigned u32x2;
typedef __attribute__((ext_vector_type(4))) float f32x4;
typedef __attribute__((ext_vector_type(2))) float f32x2;
typedef __attribute__((ext_vector_type(16))) float f32x16;
typedef __attribute__((ext_vector_type(8))) __bf16 bf16x8_t;
typedef __attribute__((ext_vector_type(2))) __bf16 bf16x2_t;
typedef __attribute__((ext_vector_type(4))) short s16x4;
typedef __attribute__((ext_vector_type(8))) short s16x8;
typedef unsigned short u16;
typedef unsigned long long u64;

#define NOA 0
constexpr int NS0 = 14, NS1 = 16, NS2 = 15, NS3 = 19;
#define HOLD 1
#define NOAML 0
#define P1W 1
#define P1S 4
#define P2W 1
#define P2S 2
#define G2B 0
#define WB0 3
#define FLI 20
#define QIA 31
#define X2 0
#define SPM 27
#define QSL 1
#define PW 4
#define PWM 15
#define QPE 12
#define CSL 16
#define RA2V 0xC0
#define RA3V 0x02
constexpr int RA2 = RA2V, RA3 = RA3V;
#define WE 4
#define WL 5
constexpr u64 TL0 = 0xbeb96069644b1c0ull, TH0 = 0xfbcdf3ull, TL1 = 0x9217595523c9142ull, TH1 = 0xef8d30b28ull;
constexpr u64 TL2 = 0x9a279b5d338a183ull, TH2 = 0x3adb2baaull, TL3 = 0x7dc61440d308101ull, TH3 = 0x3ff79d71b699e3ull;
constexpr u64 LA2 = TH2 >> (6 * (NS2 - 11)), LA3 = TH3 >> (6 * (NS3 - 11));
constexpr u64 chunk_mask(u64 lo, u64 hi, int n) { u64 m = 0; for (int i = 0; i < n; ++i) m |= 1ull << ((i < 10 ? lo >> (6 * i) : hi >> (6 * (i - 10))) & 63); return m; }
static_assert((chunk_mask(TL0, TH0, NS0) | chunk_mask(TL1, TH1, NS1) | chunk_mask(TL2, TH2, NS2) | chunk_mask(TL3, TH3, NS3)) == ~0ull && (RA2 & RA3) == 0, "every chunk once");
static_assert(NS0 >= 12 && NS1 >= 12 && NS2 >= 13 && NS3 >= 13 && NS0 + NS1 + NS2 + NS3 == 64, "split sizes");
static_assert(FLI + WB0 + 2 <= QIA, "order");
constexpr int SK = 4096;
constexpr float C2 = 0.08838834764831845f * 1.4426950408889634f;
constexpr u64 GOLD = 0x9E3779B97F4A7C15ull;

constexpr unsigned WS_EP = 0;
constexpr unsigned WS_QX = 4096;
constexpr unsigned WS_QA = WS_QX + 2 * 32 * 256 * 8;
constexpr unsigned WS_AML = WS_QA + 2 * 32 * 512 * 2;
constexpr unsigned WS_APO = WS_AML + 32 * 2 * 64 * 8;
constexpr unsigned WS_CX = WS_APO + 32 * 2 * 64 * 256 * 2;
constexpr unsigned WS_GB = WS_CX + 32 * 2 * 2 * 128 * 8;
constexpr unsigned WS_GA = WS_GB + 2 * 128 * 512 * 8;
constexpr unsigned WS_END = WS_GA + 64 * 2 * 512 * 8;
#define STAMP(i) do {} while (0)
#define STAMP_DECL do {} while (0)
#define STAMP_FLUSH(grp) do {} while (0)

#define TR() do {} while (0)
#define TR_FLUSH() do {} while (0)
DI float bflo(unsigned u) { return __uint_as_float(u << 16); }
DI float bfhi(unsigned u) { return __uint_as_float(u & 0xffff0000u); }
DI unsigned pk_bf(float a, float b) { const f32x2 v = {a, b}; return __builtin_bit_cast(unsigned, __builtin_convertvector(v, bf16x2_t)); }
DI float dot2(unsigned k, unsigned q, float acc) {
  return __builtin_amdgcn_fdot2_f32_bf16(__builtin_bit_cast(bf16x2_t, k), __builtin_bit_cast(bf16x2_t, q), acc, false);
}
DI unsigned plo(unsigned w0, unsigned w1) { return __builtin_amdgcn_perm(w1, w0, 0x05040100u); }
DI unsigned phi(unsigned w0, unsigned w1) { return __builtin_amdgcn_perm(w1, w0, 0x07060302u); }
template <int CTRL> DI float dpp(float x) {
  return __builtin_bit_cast(float, __builtin_amdgcn_mov_dpp(__builtin_bit_cast(int, x), CTRL, 0xf, 0xf, true));
}
DI float row16_sum(float x) { x += dpp<0xB1>(x); x += dpp<0x4E>(x); x += dpp<0x141>(x); x += dpp<0x128>(x); return x; }
DI float row16_max(float x) { x = fmaxf(x, dpp<0xB1>(x)); x = fmaxf(x, dpp<0x4E>(x)); x = fmaxf(x, dpp<0x141>(x)); x = fmaxf(x, dpp<0x128>(x)); return x; }
DI float x16_sum(float x) { auto s = __builtin_amdgcn_permlane16_swap(__float_as_uint(x), __float_as_uint(x), false, false); return __uint_as_float(s[0]) + __uint_as_float(s[1]); }
DI float x16_max(float x) { auto s = __builtin_amdgcn_permlane16_swap(__float_as_uint(x), __float_as_uint(x), false, false); return fmaxf(__uint_as_float(s[0]), __uint_as_float(s[1])); }
DI float xhalf_max(float x) { auto t = __builtin_amdgcn_permlane32_swap(__float_as_uint(x), __float_as_uint(x), false, false); return fmaxf(__uint_as_float(t[0]), __uint_as_float(t[1])); }
DI float xhalf_sum(float x) { auto t = __builtin_amdgcn_permlane32_swap(__float_as_uint(x), __float_as_uint(x), false, false); return __uint_as_float(t[0]) + __uint_as_float(t[1]); }
DI float xrow_max(float x) { return xhalf_max(x16_max(x)); }
DI float xrow_sum(float x) { return xhalf_sum(x16_sum(x)); }
DI u32x4 ld16(const void* p) { return *reinterpret_cast<const u32x4*>(p); }
DI u32x4 ld16nt(const void* p) { return __builtin_nontemporal_load(reinterpret_cast<const u32x4*>(p)); }
DI float ex2(float x) { return __builtin_amdgcn_exp2f(x); }
#define SCHED() __builtin_amdgcn_sched_barrier(0)

typedef __attribute__((address_space(1))) u64 gu64;
typedef __amdgpu_buffer_rsrc_t rsrc_t;
DI u32x4 ld16c(rsrc_t rs, unsigned off) { return __builtin_amdgcn_raw_buffer_load_b128(rs, (int)off, 0, 16); }
DI u32x2 ld8c(rsrc_t rs, unsigned off) { return __builtin_amdgcn_raw_buffer_load_b64(rs, (int)off, 0, 16); }
DI void st16c(rsrc_t rs, unsigned off, u32x4 v) { __builtin_amdgcn_raw_buffer_store_b128(v, rs, (int)off, 0, 16); }
DI void st8c(rsrc_t rs, unsigned off, u32x2 v) { __builtin_amdgcn_raw_buffer_store_b64(v, rs, (int)off, 0, 16); }
typedef __attribute__((ext_vector_type(16))) unsigned u32x16;
typedef __attribute__((ext_vector_type(8))) u64 u64x8;
template <int O1> DI void sld2(const char* p, u32x2& a, u32x2& b) {
  asm volatile("s_load_dwordx2 %0, %2, 0x0 glc\n\ts_load_dwordx2 %1, %2, %3 glc\n\ts_waitcnt lgkmcnt(0)" : "=&s"(a), "=&s"(b) : "s"(p), "n"(O1) : "memory");
}
DI u32x16 sld64(const char* p) {
  u32x16 v;
  asm volatile("s_load_dwordx16 %0, %1, 0x0 glc\n\ts_waitcnt lgkmcnt(0)" : "=&s"(v) : "s"(p) : "memory");
  return v;
}
DI void sld128(const char* p, u32x16 (&v)[2]) {
  asm volatile("s_load_dwordx16 %0, %2, 0x0 glc\n\ts_load_dwordx16 %1, %2, 0x40 glc\n\ts_waitcnt lgkmcnt(0)" : "=&s"(v[0]), "=&s"(v[1]) : "s"(p) : "memory");
}
constexpr int SPIN_MAX = 1 << 20;
constexpr unsigned OOB = 0x7ffffff0u;
DI void rawbar() { __builtin_amdgcn_sched_barrier(0); __builtin_amdgcn_s_barrier(); __builtin_amdgcn_sched_barrier(0); }
struct Tag { unsigned hi, lo8; };
DI unsigned g24(float v, const Tag& t) { return t.lo8 | ((__float_as_uint(v) + 0x80u) >> 8); }
DI bool ok24(unsigned lo, unsigned hi, const Tag& t) { return (hi == t.hi) & ((lo & 0xff000000u) == t.lo8); }
DI float v24(unsigned lo) { return __uint_as_float(lo << 8); }
#define COMPILER_FENCE() asm volatile("" ::: "memory")
template <int O1, int SL> DI void shint(const char* p, unsigned tag) {
#pragma unroll 1
  for (int spin = 0; spin < SPIN_MAX; ++spin) {
    u32x2 a, b;
    sld2<O1>(p, a, b);
    if (((a[1] ^ tag) | (b[1] ^ tag)) == 0u) break;
    __builtin_amdgcn_s_sleep(SL);
  }
}

template <int N> struct IC { static constexpr int v = N; };
template <int I, int N, class F> DI void sfor(F&& f) { if constexpr (I < N) { f(IC<I>{}); sfor<I + 1, N>(f); } }
typedef __attribute__((address_space(3))) unsigned lu32;
DI void lds_wait(unsigned* cnt, unsigned target) {
#pragma unroll 1
  for (int spin = 0; spin < SPIN_MAX; ++spin) {
    if (__builtin_amdgcn_readfirstlane(__hip_atomic_load((lu32*)cnt, __ATOMIC_RELAXED, __HIP_MEMORY_SCOPE_WORKGROUP)) >= target) break;
    __builtin_amdgcn_s_sleep(1);
  }
  COMPILER_FENCE();
}
DI void lds_wait_slow(unsigned* cnt, unsigned target) {
#pragma unroll 1
  for (int spin = 0; spin < SPIN_MAX; ++spin) {
    if (__builtin_amdgcn_readfirstlane(__hip_atomic_load((lu32*)cnt, __ATOMIC_RELAXED, __HIP_MEMORY_SCOPE_WORKGROUP)) >= target) break;
    __builtin_amdgcn_s_sleep(3);
  }
  COMPILER_FENCE();
}
DI void lds_hold(unsigned* flag) {
#pragma unroll 1
  for (int spin = 0; spin < SPIN_MAX; ++spin) {
    if (__builtin_amdgcn_readfirstlane(__hip_atomic_load((lu32*)flag, __ATOMIC_RELAXED, __HIP_MEMORY_SCOPE_WORKGROUP)) == 0u) break;
    __builtin_amdgcn_s_sleep(2);
  }
}
DI unsigned lds_arrive_nw(unsigned addr) {
  unsigned old, one = 1u;
  asm volatile("s_mov_b64 exec, 1\n\tds_add_rtn_u32 %0, %1, %2\n\ts_mov_b64 exec, -1" : "=v"(old) : "v"(addr), "v"(one) : "memory");
  return old;
}
DI unsigned lds_done(unsigned old) { asm volatile("s_waitcnt lgkmcnt(0)" : "+v"(old) :: "memory"); return __builtin_amdgcn_readfirstlane(old); }
DI unsigned lds_arrive(unsigned* cnt, int) { return lds_done(lds_arrive_nw((unsigned)(size_t)(lu32*)cnt)); }
DI void lds_add(unsigned* cnt, unsigned v, int) {
  asm volatile("s_mov_b64 exec, 1\n\tds_add_u32 %0, %1\n\ts_mov_b64 exec, -1" :: "v"((unsigned)(size_t)(lu32*)cnt), "v"(v) : "memory");
}
DI void sbar4(unsigned* cnt, int lane) { if (lds_arrive(cnt, lane) < 3u) lds_wait(cnt, 4u); }
DI float rs32(float a, float b) { auto t = __builtin_amdgcn_permlane32_swap(__float_as_uint(a), __float_as_uint(b), false, false); return __uint_as_float(t[0]) + __uint_as_float(t[1]); }
DI float rs16(float a, float b) { auto t = __builtin_amdgcn_permlane16_swap(__float_as_uint(a), __float_as_uint(b), false, false); return __uint_as_float(t[0]) + __uint_as_float(t[1]); }
template <int N> DI unsigned rowbcast(unsigned x) { return (unsigned)__builtin_amdgcn_mov_dpp((int)x, 0x150 + N, 0xf, 0xf, true); }
DI void pin2(u32x4& a, u32x4& b) { asm volatile("" : "+v"(a), "+v"(b)); }
DI float rdlane(float x, int l) { return __builtin_bit_cast(float, __builtin_amdgcn_readlane(__builtin_bit_cast(int, x), l)); }
DI bool nz8(const u32x4& v) { return ((v[0] | v[1]) != 0u) & ((v[2] | v[3]) != 0u); }

DI u32x4 qsum(const u32x4& g) {
  const auto ta = __builtin_amdgcn_permlane32_swap(g[0], g[0], false, false), tc = __builtin_amdgcn_permlane32_swap(g[2], g[2], false, false);
  const unsigned a = pk_bf(bflo(ta[0]) + bflo(ta[1]), bfhi(ta[0]) + bfhi(ta[1])), c = pk_bf(bflo(tc[0]) + bflo(tc[1]), bfhi(tc[0]) + bfhi(tc[1]));
  const auto ra = __builtin_amdgcn_permlane16_swap(a, a, false, false), rc = __builtin_amdgcn_permlane16_swap(c, c, false, false);
  return u32x4{ra[0], rc[0], ra[1], rc[1]};
}
DI unsigned toff(unsigned row, unsigned ch) { return 256u * row + 16u * (ch ^ (((row & 3) << 2) | ((row >> 2) & 3))); }
constexpr int SMEM = 160 * 1024;
constexpr int L_SCR = 131072;
constexpr int L_MRG = 65536;
constexpr int L_CNT = L_SCR + 32 * 1008;

struct KV { u32x4 k[4], v[4]; };

__global__ __launch_bounds__(512, 2) void kmain(const u16* __restrict__ xA, const u16* __restrict__ ckv, const u16* __restrict__ w_q, const u16* __restrict__ w_uk,
                                                const u16* __restrict__ w_uv, const u16* __restrict__ w_o,
                                                const u16* __restrict__ xB, const u16* __restrict__ kc, const u16* __restrict__ vc,
                                                const u16* __restrict__ wq, const u16* __restrict__ wo,
                                                char* __restrict__ ws, u16* __restrict__ out) {
  __shared__ __attribute__((aligned(16))) char smem[SMEM];
  const int tid = threadIdx.x, blk = blockIdx.x, lane = tid & 63;
  const int wave = __builtin_amdgcn_readfirstlane(tid >> 6), w4 = wave & 3;
  const int cls = (blk >> 3) & 3;
  int idx = 8 * (blk & 7) + (blk >> 5);
  if (wave < 4) idx = (idx & ~7) | ((idx + (cls == 3 ? 2 : (cls == 2 ? 4 : 0))) & 7);
  const int h = idx >> 1, b = idx & 1;
  const rsrc_t rs = __builtin_amdgcn_make_buffer_rsrc(ws, 0, (int)WS_END, 0x00020000);
  const int r4 = lane >> 4, c16 = lane & 15;
  unsigned* cnt = reinterpret_cast<unsigned*>(smem + L_CNT);
  STAMP_DECL;
  STAMP(0);
  const u64 tstart = X2 & 1 ? wall_clock64() : 0;
  asm volatile("" :: "s"(xA), "s"(ckv), "s"(w_q), "s"(w_uk), "s"(w_uv), "s"(w_o), "s"(xB), "s"(kc), "s"(vc), "s"(wq), "s"(wo), "s"(ws), "s"(out));
  Tag tg;
  unsigned tlow;
  const u32x2 epw = __builtin_amdgcn_raw_buffer_load_b64(rs, (int)WS_EP, 0, 16);
  auto take_epoch = [&]() {
    const u64 t64 = (((u64)(unsigned)__builtin_amdgcn_readfirstlane(epw[1]) << 32) | (unsigned)__builtin_amdgcn_readfirstlane(epw[0])) + GOLD;
    tg.hi = (unsigned)(t64 >> 32); tlow = (unsigned)t64;
    asm volatile("s_waitcnt lgkmcnt(0)" : "+s"(tg.hi), "+s"(tlow) :: "memory");
    tg.lo8 = tlow & 0xff000000u;
  };
  if (wave >= 4) {
    if (tid - 256 < 16) cnt[tid - 256] = 0u;
    asm volatile("s_waitcnt lgkmcnt(0)" ::: "memory");
    rawbar();
    const int e2 = (RA2 >> (idx & 7)) & 1, e3 = (RA3 >> (idx & 7)) & 1;
    const int ns = cls == 0 ? NS0 : (cls == 1 ? NS1 + e2 + e3 : (cls == 2 ? NS2 - e2 : NS3 - e3));
    const size_t kvo = ((size_t)(b * 32 + h) * SK + w4 * 16) * 256;
    const char* kb = reinterpret_cast<const char*>(kc) + kvo;
    const char* vb = reinterpret_cast<const char*>(vc) + kvo;
    const unsigned lo = lane * 16u;
    u64 tlo = cls == 0 ? TL0 : (cls == 1 ? TL1 : (cls == 2 ? TL2 : TL3));
    u64 thi = cls == 0 ? TH0 : (cls == 1 ? TH1 | ((e2 ? LA2 : LA3) << (6 * (NS1 - 10))) : (cls == 2 ? TH2 : TH3));
    auto nexts = [&](unsigned) { const unsigned o = (((unsigned)tlo + (unsigned)idx) & 63u) << 14; tlo = (tlo >> 6) | ((thi & 63u) << 54); thi >>= 6; return o; };
    unsigned so = nexts(0u);
    KV A, B;
    u32x4 W[8] = {};
    u32x4 T[4][8];
    auto ldp = [&](KV& S, auto P, unsigned o) {
      constexpr int p = P.v;
      constexpr int o0 = (p & 1) * 2048, o1 = o0 + 1024;
      if constexpr (p < 2) { const char* q = kb + o; S.k[2 * p] = ld16nt(q + lo + o0); S.k[2 * p + 1] = ld16nt(q + lo + o1); }
      else { const char* q = vb + o; S.v[2 * p - 4] = ld16nt(q + lo + o0); S.v[2 * p - 3] = ld16nt(q + lo + o1); }
    };
    auto landed = [&](KV& S, auto P) {
      constexpr int p = P.v;
      if constexpr (p < 2) asm volatile("" : "+v"(S.k[2 * p]), "+v"(S.k[2 * p + 1])); else asm volatile("" : "+v"(S.v[2 * p - 4]), "+v"(S.v[2 * p - 3]));
    };
    KV X;
    const unsigned qo = WS_QX + ((32 + h) * 2 + b) * 1024 + lane * 16;
    u32x4 g0 = {0u, 0u, 0u, 0u};
    {
      const unsigned so1 = nexts(so), so2 = nexts(so1);
      auto pro = [&](auto PWN) {
        sfor<0, 12>([&](auto I) {
          constexpr int i = I.v, j = i - PWN.v;
          if constexpr (j >= 0) { if constexpr (j < 4) landed(X, IC<j & 3>{}); else if constexpr (j < 8) landed(B, IC<j & 3>{}); else landed(A, IC<j & 3>{}); }
          if constexpr (j >= 0 && (j & 1)) TR();
          if constexpr (i == QPE) g0 = ld16c(rs, qo);
          if constexpr (i < 4) ldp(X, IC<i & 3>{}, so); else if constexpr (i < 8) ldp(B, IC<i & 3>{}, so1); else ldp(A, IC<i & 3>{}, so2);
          SCHED();
        });
      };
      if constexpr (PWM == 15) pro(IC<PW>{}); else { if ((PWM >> cls) & 1) pro(IC<PW>{}); else pro(IC<4>{}); }
      so = so2;
    }
    take_epoch();
    const char* wp = reinterpret_cast<const char*>(wo) + ((size_t)h * 128 + r4 * 8) * 1024 + w4 * 256 + c16 * 16;
    const rsrc_t rwo_ = __builtin_amdgcn_make_buffer_rsrc(const_cast<u16*>(wo), 0, 4 << 20, 0x00020000);
    u32x4 qpk;
    u32x2 twk;
    {
      const u32x2 tw = ld8c(rs, lane == 0 ? WS_GB + ((b * 128 + h * 4 + cls) * 512 + 128 * w4) * 8 : OOB);
      if (QPE >= 12 || !__all((g0[1] == tg.hi) & (g0[3] == tg.hi))) {
#pragma unroll 1
        for (int spin = 0; spin < SPIN_MAX; ++spin) {
          COMPILER_FENCE();
          g0 = ld16c(rs, qo);
          if (__all((g0[1] == tg.hi) & (g0[3] == tg.hi))) break;
        }
        STAMP(9);
      }
      qpk = qsum(g0);
      twk = tw;
    }
    STAMP(7);
    const bool holds = HOLD && cls == 0;
    float m = -INFINITY, l = 0.f, acc[8];
#pragma unroll
    for (int k = 0; k < 8; ++k) acc[k] = 0.f;
    auto step = [&](KV& S, KV& N, auto REFILL, auto WR, auto DIN_, auto WIN_) {
      constexpr bool refill = REFILL.v;
      constexpr int wr = WR.v;
      constexpr int din = DIN_.v, win = WIN_.v, dout = win < 4 ? 4 - win : 0, nreq = refill ? din + 4 - dout : din;
      unsigned sn = 0u;
      if constexpr (refill) sn = nexts(so);
      auto upto = [](int p) constexpr { const int n = refill ? win - 4 + din + p + 1 : din; return n < 0 ? 0 : (n > nreq ? nreq : n); };
      auto req = [&](auto R) { if constexpr (R.v < din) ldp(S, IC<4 - din + R.v>{}, so); else ldp(N, IC<(R.v - din) & 3>{}, sn); };
      auto point = [&](auto P) {
        if constexpr (P.v == 0 && upto(-1) > 0) { if (holds) lds_hold(cnt + 8); sfor<0, upto(-1)>(req); SCHED(); }
        if constexpr (refill) {
          landed(S, P);
          if constexpr ((wr != 0 || QPE < 11) && (P.v & 1)) TR();
          if constexpr (upto(P.v) > upto(P.v - 1)) { if constexpr ((P.v & 1) == 0) { if (holds) lds_hold(cnt + 8); } sfor<upto(P.v - 1), upto(P.v)>(req); }
          if constexpr (wr >= 0 && wr < 8) W[(4 * wr + P.v) & 7] = ld16(wp + (((4 * wr + P.v) >> 3) * 32 + ((4 * wr + P.v) & 7)) * 1024);
          SCHED();
        }
      };
      point(IC<0>{});
      point(IC<1>{});
      float x[4];
#pragma unroll
      for (int t = 0; t < 4; ++t) {
        float d = 0.f;
#pragma unroll
        for (int k = 0; k < 4; ++k) d = dot2(S.k[t][k], qpk[k], d);
        x[t] = row16_sum(d);
      }
      const float mn = fmaxf(fmaxf(m, fmaxf(x[0], x[1])), fmaxf(x[2], x[3]));
      const float alpha = ex2((m - mn) * C2);
      m = mn;
      const float mc = mn * C2;
      unsigned p01 = pk_bf(ex2(fmaf(x[0], C2, -mc)), ex2(fmaf(x[1], C2, -mc))), p23 = pk_bf(ex2(fmaf(x[2], C2, -mc)), ex2(fmaf(x[3], C2, -mc)));
      l = dot2(p23, 0x3f803f80u, dot2(p01, 0x3f803f80u, l * alpha));
#pragma unroll
      for (int k = 0; k < 8; ++k) acc[k] *= alpha;
      asm volatile("" : "+v"(p01), "+v"(p23), "+v"(l));
      asm volatile("" : "+v"(acc[0]), "+v"(acc[1]), "+v"(acc[2]), "+v"(acc[3]), "+v"(acc[4]), "+v"(acc[5]), "+v"(acc[6]), "+v"(acc[7]));
      SCHED();
      point(IC<2>{});
#pragma unroll
      for (int k = 0; k < 4; ++k) { acc[2 * k] = dot2(plo(S.v[0][k], S.v[1][k]), p01, acc[2 * k]); acc[2 * k + 1] = dot2(phi(S.v[0][k], S.v[1][k]), p01, acc[2 * k + 1]); }
      asm volatile("" : "+v"(acc[0]), "+v"(acc[1]), "+v"(acc[2]), "+v"(acc[3]), "+v"(acc[4]), "+v"(acc[5]), "+v"(acc[6]), "+v"(acc[7]));
      SCHED();
      point(IC<3>{});
#pragma unroll
      for (int k = 0; k < 4; ++k) { acc[2 * k] = dot2(plo(S.v[2][k], S.v[3][k]), p23, acc[2 * k]); acc[2 * k + 1] = dot2(phi(S.v[2][k], S.v[3][k]), p23, acc[2 * k + 1]); }
      asm volatile("" : "+v"(acc[0]), "+v"(acc[1]), "+v"(acc[2]), "+v"(acc[3]), "+v"(acc[4]), "+v"(acc[5]), "+v"(acc[6]), "+v"(acc[7]));
      if constexpr (wr >= 1) {
        constexpr int t = (wr - 1) >> 1, j0 = ((wr - 1) & 1) * 2;
#pragma unroll
        for (int jj = 0; jj < 2; ++jj)
#pragma unroll
          for (int e = 0; e < 4; ++e) {
            const unsigned a_ = W[(4 * wr - 4 + 2 * jj) & 7][e], b_ = W[(4 * wr - 3 + 2 * jj) & 7][e];
            T[t][2 * e][j0 + jj] = plo(a_, b_); T[t][2 * e + 1][j0 + jj] = phi(a_, b_);
          }
#pragma unroll
        for (int e = 0; e < 8; e += 2) pin2(T[t][e], T[t][e + 1]);
      }
      if constexpr (refill) so = sn;
      SCHED();
    };
    constexpr int DE = WE < 4 ? 4 - WE : 0, DL = WL < 4 ? 4 - WL : 0;
    step(B, B, IC<0>{}, IC<-1>{}, IC<0>{}, IC<4>{});
    step(A, B, IC<1>{}, IC<0>{}, IC<0>{}, IC<WE>{});
    asm volatile("" :: "v"(twk));
    step(X, X, IC<0>{}, IC<-1>{}, IC<0>{}, IC<4>{});
    sfor<0, 4>([&](auto I) {
      step(B, A, IC<1>{}, IC<2 * I.v + 1>{}, IC<DE>{}, IC<WE>{});
      step(A, B, IC<1>{}, IC<2 * I.v + 2>{}, IC<DE>{}, IC<(I.v == 3 ? (WL < 4 ? WL : 4) : WE)>{});
    });
    unsigned yoff = WS_GB + ((b * 128 + h * 4 + cls) * 512 + 128 * w4 + 8 * c16 + 2 * r4) * 8;
    unsigned rwo = L_MRG + (w4 * 4 + r4) * 272 + c16 * 16, rro = L_MRG + c16 * 272 + r4 * 16, mwo = L_MRG + 16 * 272 + ((c16 & 1) * 16 + w4 * 4 + r4) * 4, mro = L_MRG + 16 * 272 + c16 * 4, m4o = L_MRG + 16 * 272 + r4 * 16;
    asm volatile("" : "+v"(yoff), "+v"(rwo), "+v"(rro), "+v"(mwo), "+v"(mro), "+v"(m4o));
    const int nit = (ns - 12) >> 1;
#pragma unroll 1
    for (int i = 0; i < nit; ++i) {
      step(B, A, IC<1>{}, IC<-1>{}, IC<DL>{}, IC<WL>{});
      step(A, B, IC<1>{}, IC<-1>{}, IC<DL>{}, IC<WL>{});
    }
    lds_wait(cnt + 15, 4u);
    if (cls == 0) lds_wait(cnt + 1, 4u);
    if (ns & 1) { step(B, A, IC<1>{}, IC<-1>{}, IC<DL>{}, IC<WL>{}); step(A, A, IC<0>{}, IC<-1>{}, IC<DL>{}, IC<9>{}); }
    else step(B, B, IC<0>{}, IC<-1>{}, IC<DL>{}, IC<9>{});
    STAMP(1);
    *reinterpret_cast<u32x4*>(smem + rwo) = u32x4{pk_bf(acc[0], acc[1]), pk_bf(acc[2], acc[3]), pk_bf(acc[4], acc[5]), pk_bf(acc[6], acc[7])};
    *reinterpret_cast<float*>(smem + mwo) = (c16 & 1) ? l : m;
    {
      u32x4 af[4];
      float mr, lr;
      f32x4 m4;
      unsigned got = lds_arrive_nw(L_CNT), need = 3u;
#pragma unroll 1
      for (int spin = 0; spin < SPIN_MAX; ++spin) {
#pragma unroll
        for (int t = 0; t < 4; ++t) af[t] = *reinterpret_cast<const u32x4*>(smem + rro + t * 64);
        mr = *reinterpret_cast<const float*>(smem + mro); lr = *reinterpret_cast<const float*>(smem + mro + 64);
        m4 = *reinterpret_cast<const f32x4*>(smem + m4o);
        SCHED();
        if (lds_done(got) >= need) break;
        need = 4u;
        __builtin_amdgcn_s_sleep(1);
        asm volatile("ds_read_b32 %0, %1" : "=v"(got) : "v"((unsigned)L_CNT) : "memory");
      }
      SCHED();
      f32x4 d[8];
#pragma unroll
      for (int e = 0; e < 8; ++e) {
        d[e] = f32x4{0.f, 0.f, 0.f, 0.f};
#pragma unroll
        for (int t = 0; t < 4; ++t) d[e] = __builtin_amdgcn_mfma_f32_16x16x32_bf16(__builtin_bit_cast(bf16x8_t, af[t]), __builtin_bit_cast(bf16x8_t, T[t][e]), d[e], 0, 0, 0);
      }
      const float M = row16_max(mr);
      const float L = row16_sum(ex2((mr - M) * C2) * lr);
      const float z = fmaf(M, C2, __builtin_amdgcn_logf(L));
      float wt[4], a[8];
#pragma unroll
      for (int i = 0; i < 4; ++i) wt[i] = ex2(fmaf(m4[i], C2, -z));
#pragma unroll
      for (int e = 0; e < 8; ++e) a[e] = fmaf(wt[3], d[e][3], fmaf(wt[2], d[e][2], fmaf(wt[1], d[e][1], wt[0] * d[e][0])));
      float v4[4];
#pragma unroll
      for (int k = 0; k < 4; ++k) v4[k] = rs32(a[k], a[k + 4]);
      const float y0 = rs16(v4[0], v4[2]), y1 = rs16(v4[1], v4[3]);
      st16c(rs, yoff, u32x4{pk_bf(y0, y1), tg.hi, __float_as_uint(z), tlow});
    }
    STAMP(2);
    STAMP_FLUSH(1);
    TR_FLUSH();
    return;
  }
  auto fin = [&](float y0, float y1, u16* dst) {
    y0 += dpp<0x128>(y0); y1 += dpp<0x128>(y1);
    const float y = xhalf_sum(rs16(y0, y1));
    float* fr = reinterpret_cast<float*>(smem + 32768);
    if (lane < 32 && (lane & 8) == 0) fr[w4 * 16 + 2 * (lane & 7) + r4] = y;
    COMPILER_FENCE();
    const unsigned got = lds_arrive_nw(L_CNT + 24);
    f32x2 t[4];
#pragma unroll
    for (int w = 0; w < 4; ++w) t[w] = *reinterpret_cast<const f32x2*>(fr + w * 16 + 2 * (lane & 7));
    SCHED();
    if (lds_done(got) == 3u) {
      const f32x2 r = (t[0] + t[1]) + (t[2] + t[3]);
      if (lane < 8) *reinterpret_cast<unsigned*>(dst + 2 * lane) = pk_bf(r[0], r[1]);
    }
  };
  u32x4 Wa[16], Wb[16], xv[4];
  const u16* pb; size_t sb, sb2;
  const unsigned qa_off = WS_QA + (((c16 >> 1) & 1) * 32 + h) * 1024 + (b * 256 + w4 * 64 + 16 * r4 + 8 * (c16 & 1)) * 2;
  {
    const u16* pa; size_t sa;
    if (cls) {
      const size_t o_ = ((size_t)(b * 256 + w4 * 64 + 16 * r4) * 32 + h) * 128 + 8 * c16;
      pa = (cls == 1 ? w_q : (cls == 2 ? w_uk : wq)) + o_; sa = 4096;
      if (cls == 2) { pb = w_uv + o_; sb = 4096; }
      else { pb = w_o + ((size_t)h * 128 + b * 64 + w4 * 2) * 512 + 8 * lane; sb = 512; }
      if (cls == 1 && c16 < 4) st16c(rs, qa_off, u32x4{0u, 0u, 0u, 0u});
    } else { pa = ckv + ((size_t)idx * 128 + w4) * 512 + 8 * lane; sa = sb = 2048; pb = pa + 64 * 512; }
    sb2 = cls == 1 ? 4096 : 2 * sb;
    const u16* xp = (cls == 1 ? xA : xB) + b * 256 + w4 * 64 + 16 * r4;
#pragma unroll
    for (int i = 0; i < 4; ++i) xv[i] = ld16(xp + (i >> 1) * 512 + 8 * (i & 1));
    SCHED();
#pragma unroll
    for (int i = 0; i < 16; ++i) Wa[i] = ld16nt(pa + i * sa);
    SCHED();
    rawbar();
    take_epoch();
    lds_add(cnt + 15, 1u, lane);
  }
  STAMP(13);
  float* red = reinterpret_cast<float*>(smem);
  if (cls & 1) {
    float acc[2][8];
#pragma unroll
    for (int bb = 0; bb < 2; ++bb)
#pragma unroll
      for (int k = 0; k < 8; ++k) acc[bb][k] = 0.f;
#pragma unroll
    for (int ip = 0; ip < 8; ++ip) {
      const unsigned xp0 = xv[ip >> 2][ip & 3], xp1 = xv[2 + (ip >> 2)][ip & 3];
#pragma unroll
      for (int k = 0; k < 4; ++k) {
        const unsigned lo = plo(Wa[2 * ip][k], Wa[2 * ip + 1][k]), hi = phi(Wa[2 * ip][k], Wa[2 * ip + 1][k]);
        acc[0][2 * k] = dot2(lo, xp0, acc[0][2 * k]); acc[0][2 * k + 1] = dot2(hi, xp0, acc[0][2 * k + 1]);
        acc[1][2 * k] = dot2(lo, xp1, acc[1][2 * k]); acc[1][2 * k + 1] = dot2(hi, xp1, acc[1][2 * k + 1]);
      }
    }
    {
      float v8[8], v4[4];
#pragma unroll
      for (int k = 0; k < 8; ++k) v8[k] = rs32(acc[0][k], acc[1][k]);
#pragma unroll
      for (int k = 0; k < 4; ++k) v4[k] = rs16(v8[k], v8[k + 4]);
      *reinterpret_cast<f32x4*>(red + (w4 * 2 + (lane >> 5)) * 128 + 8 * c16 + 4 * (r4 & 1)) = f32x4{v4[0], v4[1], v4[2], v4[3]};
    }
    COMPILER_FENCE();
    const unsigned got = lds_arrive_nw(L_CNT + 8);
    f32x4 t4[4];
#pragma unroll
    for (int w = 0; w < 4; ++w) t4[w] = *reinterpret_cast<const f32x4*>(red + (w * 2 + (lane >> 5)) * 128 + 8 * c16 + 4 * (r4 & 1));
    SCHED();
    if (lds_done(got) == 3u) {
      const f32x4 t = (t4[0] + t4[1]) + (t4[2] + t4[3]);
      st16c(rs, WS_QX + (((cls >> 1) * 32 + h) * 2 + (lane >> 5)) * 1024 + b * 512 + (lane & 31) * 16, u32x4{pk_bf(t[0], t[1]), tg.hi, pk_bf(t[2], t[3]), tg.hi});
      STAMP(15);
      SCHED();
      lds_add(cnt + 2, 1u, lane);
    } else lds_wait(cnt + 2, 5u);
    {
      const unsigned tw = __builtin_nontemporal_load(reinterpret_cast<const unsigned*>(out + (idx >> 5) * 1024 + (cls >> 1) * 512 + 16 * (idx & 31)));
      asm volatile("" :: "v"(tw));
    }
    STAMP(8);
  } else if (cls == 2 && NOA != 1) {
    u32x4 g0, g1;
    const unsigned qo = WS_QX + h * 2048 + lane * 16;
    if (SPM & 1) shint<512, 1>(ws + WS_QX + h * 2048, tg.hi);
    STAMP(5);
#pragma unroll 1
    for (int spin = 0; spin < SPIN_MAX; ++spin) {
      COMPILER_FENCE();
      g0 = ld16c(rs, qo); g1 = ld16c(rs, qo + 1024);
      if (__all((g0[1] == tg.hi) & (g0[3] == tg.hi) & (g1[1] == tg.hi) & (g1[3] == tg.hi))) break;
      __builtin_amdgcn_s_sleep(QSL);
    }
    const u32x4 q0 = qsum(g0), q1 = qsum(g1);
    STAMP(9);
    float v[8];
    {
      float u[16];
#pragma unroll
      for (int i = 0; i < 16; ++i) {
        float a = 0.f, c = 0.f;
#pragma unroll
        for (int k = 0; k < 4; ++k) { a = dot2(Wa[i][k], q0[k], a); c = dot2(Wa[i][k], q1[k], c); }
        u[i] = (c16 & 2 ? c : a) + dpp<0x4E>(c16 & 2 ? a : c);
        asm volatile("" : "+v"(u[i]));
      }
#pragma unroll
      for (int i = 0; i < 8; ++i) {
        float t = (c16 & 1 ? u[i + 8] : u[i]) + dpp<0xB1>(c16 & 1 ? u[i] : u[i + 8]);
        asm volatile("" : "+v"(t));
        t += dpp<0x124>(t);
        asm volatile("" : "+v"(t));
        t += dpp<0x128>(t);
        asm volatile("" : "+v"(t));
        v[i] = t;
      }
    }
    u32x4 o = {pk_bf(v[0], v[1]), pk_bf(v[2], v[3]), pk_bf(v[4], v[5]), pk_bf(v[6], v[7])};
    if ((o[0] | o[1]) == 0u) o[0] = 0x8000u;
    if ((o[2] | o[3]) == 0u) o[2] = 0x8000u;
    if (c16 < 4) st16c(rs, qa_off, o);
    SCHED();
    sbar4(cnt + 2, lane);
    STAMP(10);
  }
  asm volatile("" :: "v"(xv[0]), "v"(xv[1]), "v"(xv[2]), "v"(xv[3]));
  SCHED();
  if (cls == 3) {
    const int fb = idx >> 5, fc = 16 * (idx & 31), hd = 8 * w4 + (lane >> 3);
    const unsigned doff = WS_GB + ((fb * 128 + hd * 4) * 512 + fc + 2 * (lane & 7)) * 8;
    lds_wait(cnt + 0, 4u);
    STAMP(3);
    u32x4 d[4];
    bool ok = false;
#pragma unroll 1
    for (int spin = 0; spin < SPIN_MAX; ++spin) {
      COMPILER_FENCE();
      if (!ok) {
#pragma unroll
        for (int j = 0; j < 4; ++j) d[j] = ld16c(rs, doff + j * 4096);
      }
      unsigned eh = 0u;
#pragma unroll
      for (int j = 0; j < 4; ++j) eh |= (d[j][1] ^ tg.hi) | (d[j][3] ^ tlow);
      ok = eh == 0u;
      if (__all(ok)) break;
    }
    STAMP(14);
    const float zz[4] = {__uint_as_float(d[0][2]), __uint_as_float(d[1][2]), __uint_as_float(d[2][2]), __uint_as_float(d[3][2])};
    const float Z = fmaxf(fmaxf(zz[0], zz[1]), fmaxf(zz[2], zz[3]));
    float L = 0.f, y0 = 0.f, y1 = 0.f;
#pragma unroll
    for (int j = 0; j < 4; ++j) { const float wt = ex2(zz[j] - Z); L += wt; y0 = fmaf(wt, bflo(d[j][0]), y0); y1 = fmaf(wt, bfhi(d[j][0]), y1); }
    const float rl = __builtin_amdgcn_rcpf(L);
    fin(y0 * rl, y1 * rl, out + fb * 1024 + 512 + fc);
    STAMP(6);
    STAMP_FLUSH(0); TR_FLUSH();
    return;
  }
  if (cls != 0) {
#pragma unroll
    for (int i = 0; i < 16; ++i) {
      Wb[i] = ld16nt(pb + (i >> 1) * sb2 + (i & 1) * sb);
      if (i >= 2 && (i & 1)) asm volatile("" : "+v"(Wb[i - 2]));
      if (i == 5 || i == 9 || i == 13) TR();
      SCHED();
    }
  }
  auto quit = [&]() {
    lds_arrive(cnt + 1, lane);
    if (cls == 0 && idx == 0 && w4 == 0) {
      lds_wait(cnt + 0, 4u);
      bool ok = false;
#pragma unroll 1
      for (int spin = 0; spin < SPIN_MAX; ++spin) {
        COMPILER_FENCE();
        if (!ok) {
          unsigned eh = 0u;
#pragma unroll
          for (int j = 0; j < 4; ++j) { const u32x4 t = ld16c(rs, WS_GB + (lane * 4 + j) * 4096); eh |= (t[1] ^ tg.hi) | (t[3] ^ tlow); }
          ok = eh == 0u;
        }
        if (__all(ok)) break;
        __builtin_amdgcn_s_sleep(1);
      }
      if (lane == 0) __hip_atomic_store((gu64*)(ws + WS_EP), ((u64)tg.hi << 32) | tlow, __ATOMIC_RELAXED, __HIP_MEMORY_SCOPE_AGENT);
    }
    STAMP_FLUSH(0); TR_FLUSH();
  };
  if ((NOA >= 2 && NOA <= 4 && cls == 2) || (NOA >= 2 && cls == 1)) {
#pragma unroll
    for (int i = 0; i < 16; ++i) asm volatile("" :: "v"(Wa[i]), "v"(Wb[i]));
    quit();
    return;
  }
  if (cls == 0) {
    char* scr = smem + L_SCR;
    const int ab = idx >> 5, r = lane & 31, hh = lane >> 5;
    const unsigned fo = WS_QX + (w4 * 8 + (c16 >> 1)) * 2048 + (c16 & 1) * 512 + r4 * 8;
    u32x2 fl;
    u32x4 g[9];
    const unsigned o8 = WS_QA + (ab * 32 + w4 * 8) * 1024 + lane * 16, ol = WS_QA + (ab * 32 + r) * 1024 + 992 + 16 * hh;
#pragma unroll
    for (int i = 0; i < 32; ++i) {
      if (i + WB0 >= 16 && i + WB0 < 32) Wb[(i + WB0) & 15] = ld16nt(pb + ((i + WB0) & 15) * 2048);
      if (i == FLI) fl = ld8c(rs, fo);
      if (i == FLI + WB0 + 2) {
        if (!__all(fl[1] == tg.hi)) {
#pragma unroll 1
          for (int spin = 0; spin < SPIN_MAX; ++spin) {
            COMPILER_FENCE();
            if (__all(ld8c(rs, fo)[1] == tg.hi)) break;
          }
        }
        lds_add(cnt + 7, 1u, lane);
      }
      if (i == QIA) {
        lds_wait(cnt + 7, 4u);
        STAMP(12);
#pragma unroll
        for (int j = 0; j < 9; ++j) g[j] = ld16c(rs, j < 8 ? o8 + j * 1024 : ol);
      }
      SCHED();
      *reinterpret_cast<u32x4*>(smem + ((i >> 3) * 4 + r4) * 8192 + toff(4 * (i & 7) + w4, c16)) = i < 16 ? Wa[i & 15] : Wb[i & 15];
      if (i >= 16 && (i & 3) == 3) TR();
      SCHED();
    }
    STAMP(8);
    u32x4 qlast;
    {
      unsigned pend = 0u;
#pragma unroll
      for (int i = 0; i < 9; ++i) if (!__all(nz8(g[i]))) pend |= 1u << i;
      STAMP(5);
#pragma unroll 1
      for (int spin = 0; spin < SPIN_MAX && pend; ++spin) {
        COMPILER_FENCE();
        u32x4 t[9];
#pragma unroll
        for (int i = 0; i < 9; ++i) t[i] = ld16c(rs, (pend >> i & 1) ? (i < 8 ? o8 + i * 1024 : ol) : OOB);
#pragma unroll
        for (int i = 0; i < 9; ++i) {
          if (pend >> i & 1) g[i] = t[i];
          if (__all(nz8(t[i]))) pend &= ~(1u << i);
        }
      }
      STAMP(9);
      qlast = g[8];
#pragma unroll
      for (int i = 0; i < 8; ++i)
        if (lane < 62) *reinterpret_cast<u32x4*>(scr + (w4 * 8 + i) * 1008 + 16 * lane) = g[i];
    }
    sbar4(cnt + 3, lane);
    STAMP(10);
    if (NOA == 2) { asm volatile("" :: "v"(qlast)); quit(); return; }
    f32x16 sacc;
#pragma unroll
    for (int i = 0; i < 16; ++i) sacc[i] = 0.f;
#pragma unroll
    for (int jj = 0; jj < 32; ++jj) {
      const u32x4 af = *reinterpret_cast<const u32x4*>(smem + (w4 * 4 + (jj >> 3)) * 8192 + toff(r, 2 * (jj & 7) + hh));
      const u32x4 bf = jj < 31 ? *reinterpret_cast<const u32x4*>(scr + r * 1008 + 16 * (2 * (jj < 31 ? jj : 0) + hh)) : qlast;
      sacc = __builtin_amdgcn_mfma_f32_32x32x16_bf16(__builtin_bit_cast(bf16x8_t, af), __builtin_bit_cast(bf16x8_t, bf), sacc, 0, 0, 0);
    }
    float m = sacc[0];
#pragma unroll
    for (int i = 1; i < 16; ++i) m = fmaxf(m, sacc[i]);
    m = xhalf_max(m);
    if (hh == 0) *reinterpret_cast<float*>(scr + r * 1008 + 992 + 4 * w4) = m;
    sbar4(cnt + 4, lane);
    STAMP(11);
    {
      const f32x4 m4 = *reinterpret_cast<const f32x4*>(scr + r * 1008 + 992);
      m = fmaxf(fmaxf(m4[0], m4[1]), fmaxf(m4[2], m4[3]));
    }
    const float mc = m * C2;
    float p[16], ls = 0.f;
#pragma unroll
    for (int i = 0; i < 16; ++i) { p[i] = ex2(fmaf(sacc[i], C2, -mc)); ls += p[i]; }
    ls = xhalf_sum(ls);
    sbar4(cnt + 5, lane);
    u32x4* pf = reinterpret_cast<u32x4*>(scr);
    float* wl = reinterpret_cast<float*>(scr + 8192);
    if (hh == 0) wl[r * 4 + w4] = ls;
    pf[(2 * w4) * 64 + lane] = u32x4{pk_bf(p[0], p[1]), pk_bf(p[2], p[3]), pk_bf(p[4], p[5]), pk_bf(p[6], p[7])};
    pf[(2 * w4 + 1) * 64 + lane] = u32x4{pk_bf(p[8], p[9]), pk_bf(p[10], p[11]), pk_bf(p[12], p[13]), pk_bf(p[14], p[15])};
    sbar4(cnt + 6, lane);
    u32x4 pb8[8];
#pragma unroll
    for (int ks = 0; ks < 8; ++ks) pb8[ks] = pf[ks * 64 + lane];
    const f32x4 l4 = *reinterpret_cast<const f32x4*>(wl + r * 4);
    const float lsum = (l4[0] + l4[1]) + (l4[2] + l4[3]), rl = __builtin_amdgcn_rcpf(lsum);
    const int qq = (lane & 15) >> 2, pp = lane & 3, blk2 = (lane >> 4) & 1;
    char* ot = scr + 12288 + w4 * 4096;
#pragma unroll
    for (int c2 = 0; c2 < 2; ++c2) {
      f32x16 oacc[2];
#pragma unroll
      for (int ct = 0; ct < 2; ++ct)
#pragma unroll
        for (int i = 0; i < 16; ++i) oacc[ct][i] = 0.f;
#pragma unroll
      for (int ks = 0; ks < 8; ++ks)
#pragma unroll
        for (int ct = 0; ct < 2; ++ct) {
          typedef __attribute__((address_space(3))) s16x4* lp;
          const char* tile = smem + ((ks >> 1) * 4 + w4) * 8192;
          const int cc = 2 * c2 + ct;
          const s16x4 lo = __builtin_amdgcn_ds_read_tr16_b64_v4i16((lp)(tile + toff(16 * (ks & 1) + 4 * hh + qq, 4 * cc + 2 * blk2 + (pp >> 1)) + 8 * (pp & 1)));
          const s16x4 hi = __builtin_amdgcn_ds_read_tr16_b64_v4i16((lp)(tile + toff(16 * (ks & 1) + 8 + 4 * hh + qq, 4 * cc + 2 * blk2 + (pp >> 1)) + 8 * (pp & 1)));
          const s16x8 afr = __builtin_shufflevector(lo, hi, 0, 1, 2, 3, 4, 5, 6, 7);
          oacc[ct] = __builtin_amdgcn_mfma_f32_32x32x16_bf16(__builtin_bit_cast(bf16x8_t, afr), __builtin_bit_cast(bf16x8_t, pb8[ks]), oacc[ct], 0, 0, 0);
        }
#pragma unroll
      for (int ct = 0; ct < 2; ++ct)
#pragma unroll
        for (int g = 0; g < 4; ++g)
          *reinterpret_cast<u32x2*>(ot + r * 128 + 16 * ((4 * ct + g) ^ (r & 7)) + 8 * hh) = u32x2{pk_bf(oacc[ct][4 * g] * rl, oacc[ct][4 * g + 1] * rl), pk_bf(oacc[ct][4 * g + 2] * rl, oacc[ct][4 * g + 3] * rl)};
      if (c2 == 0 && NOA != 3) lds_add(cnt + 8, 1u, lane);
      __builtin_amdgcn_wave_barrier();
#pragma unroll
      for (int i = 0; i < 4; ++i) {
        const int hd = 8 * i + (lane >> 3), pc = lane & 7;
        const u32x4 vv = *reinterpret_cast<const u32x4*>(ot + hd * 128 + 16 * (pc ^ (hd & 7)));
        st16c(rs, NOA == 3 ? OOB : WS_APO + (((hd * 2 + (w4 >> 1)) * 64 + idx) * 256 + (w4 & 1) * 128 + 64 * c2 + 8 * pc) * 2, vv);
      }
      __builtin_amdgcn_wave_barrier();
    }
    lds_arrive(cnt + 1, lane);
    STAMP(1);
    asm volatile("s_waitcnt vmcnt(0)" ::: "memory");
    if (lds_arrive(cnt + 9 + (w4 >> 1), lane) == 1u && hh == 0)
      st8c(rs, NOAML ? OOB : WS_AML + ((r * 2 + (w4 >> 1)) * 64 + idx) * 8, u32x2{__float_as_uint(mc + __builtin_amdgcn_logf(lsum)), tg.hi});
    if (NOA != 3) lds_add(cnt + 8, ~0u, lane);
    STAMP(2);
    if (idx == 0 && w4 == 0) {
      lds_wait(cnt + 0, 4u);
      bool ok = false;
#pragma unroll 1
      for (int spin = 0; spin < SPIN_MAX; ++spin) {
        COMPILER_FENCE();
        if (!ok) {
          unsigned eh = 0u;
#pragma unroll
          for (int j = 0; j < 4; ++j) { const u32x4 t = ld16c(rs, WS_GB + (lane * 4 + j) * 4096); eh |= (t[1] ^ tg.hi) | (t[3] ^ tlow); }
          ok = eh == 0u;
        }
        if (__all(ok)) break;
        __builtin_amdgcn_s_sleep(1);
      }
      if (lane == 0) __hip_atomic_store((gu64*)(ws + WS_EP), ((u64)tg.hi << 32) | tlow, __ATOMIC_RELAXED, __HIP_MEMORY_SCOPE_AGENT);
    }
    STAMP_FLUSH(0); TR_FLUSH();
    return;
  }
#pragma unroll
  for (int ip = 0; ip < 8; ++ip) {
#pragma unroll
    for (int k = 0; k < 4; ++k) { const unsigned lo = plo(Wb[2 * ip][k], Wb[2 * ip + 1][k]), hi = phi(Wb[2 * ip][k], Wb[2 * ip + 1][k]); Wb[2 * ip][k] = lo; Wb[2 * ip + 1][k] = hi; }
    pin2(Wb[2 * ip], Wb[2 * ip + 1]);
  }
  if (cls == 2) {
    float* red2 = red + 4 * 256;
    float* wml = red + 3072;
    u32x16 f[2];
    if (X2 & 1) { while (wall_clock64() - tstart < 1720) __builtin_amdgcn_s_sleep(8); }
    if (P2W && !(X2 & 1)) {
      if (w4 == 0) {
#pragma unroll 1
        for (int spin = 0; spin < SPIN_MAX; ++spin) {
          const u32x16 t = sld64(ws + WS_AML + (h * 2 + b) * 512);
          bool any = false;
#pragma unroll
          for (int i = 0; i < 8; ++i) any |= t[2 * i + 1] == tg.hi;
          if (any) break;
          __builtin_amdgcn_s_sleep(8);
        }
        lds_add(cnt + 7, 1u, lane);
      } else lds_wait_slow(cnt + 7, 1u);
    }
#pragma unroll 1
    for (int spin = 0; spin < SPIN_MAX; ++spin) {
      sld128(ws + WS_AML + ((h * 2 + b) * 64 + w4 * 16) * 8, f);
      unsigned e = 0u, any = ~0u;
#pragma unroll
      for (int i = 0; i < 16; ++i) { const unsigned x = f[i >> 3][2 * (i & 7) + 1] ^ tg.hi; e |= x; any = min(any, x); }
      if (e == 0u || (X2 & 1)) break;
      if (P2W) __builtin_amdgcn_s_sleep(P2S); else if (any != 0u) __builtin_amdgcn_s_sleep(8);
    }
    if (G2B) sbar4(cnt + 13, lane);
    STAMP(3);
    u32x4 pp[8];
#pragma unroll
    for (int i = 0; i < 8; ++i) { if (X2 & 2) { pp[i] = Wb[i]; pin2(pp[i], pp[i]); } else pp[i] = ld16c(rs, WS_APO + ((h * 2 + b) * 64 + w4 * 16 + 2 * i) * 512 + lane * 16); }
    if (X2 & 4) {
#pragma unroll
      for (int i = 0; i < 8; ++i) asm volatile("" :: "v"(pp[i]), "v"(Wb[2 * i]), "v"(Wb[2 * i + 1]));
      return; }
    SCHED();
    float wt[8];
    {
      float M = -INFINITY, L = 0.f;
#pragma unroll
      for (int i = 0; i < 8; ++i) {
        unsigned z0 = f[i >> 2][4 * (i & 3)], z1 = f[i >> 2][4 * (i & 3) + 2];
        asm("" : "+s"(z0), "+s"(z1));
        wt[i] = __uint_as_float(lane < 32 ? z0 : z1); M = fmaxf(M, wt[i]);
      }
      M = xhalf_max(M);
#pragma unroll
      for (int i = 0; i < 8; ++i) { wt[i] = ex2(wt[i] - M); L += wt[i]; }
      L = xhalf_sum(L);
      if (lane == 0) *reinterpret_cast<f32x2*>(wml + 2 * w4) = f32x2{M, L};
    }
    SCHED();
    float a8[8];
#pragma unroll
    for (int k = 0; k < 8; ++k) a8[k] = 0.f;
#pragma unroll
    for (int i = 0; i < 8; ++i) {
#pragma unroll
      for (int k = 0; k < 4; ++k) { a8[2 * k] = fmaf(wt[i], bflo(pp[i][k]), a8[2 * k]); a8[2 * k + 1] = fmaf(wt[i], bfhi(pp[i][k]), a8[2 * k + 1]); }
      asm volatile("" : "+v"(a8[0]), "+v"(a8[1]), "+v"(a8[2]), "+v"(a8[3]), "+v"(a8[4]), "+v"(a8[5]), "+v"(a8[6]), "+v"(a8[7]));
    }
    STAMP(6);
    *reinterpret_cast<f32x4*>(red + w4 * 256 + 8 * (lane & 31) + 4 * (lane >> 5)) = f32x4{rs32(a8[0], a8[4]), rs32(a8[1], a8[5]), rs32(a8[2], a8[6]), rs32(a8[3], a8[7])};
    sbar4(cnt + 3, lane);
    STAMP(4);
    float acc[2][8];
#pragma unroll
    for (int bb = 0; bb < 2; ++bb) {
      u32x4 cv[2];
      const f32x4 ml = *reinterpret_cast<const f32x4*>(wml + 4 * bb);
      const float M = fmaxf(ml[0], ml[2]), e0 = ex2(ml[0] - M), e1 = ex2(ml[2] - M), rl = 1.f / fmaf(e0, ml[1], e1 * ml[3]), s0 = e0 * rl, s1 = e1 * rl;
#pragma unroll
      for (int e = 0; e < 2; ++e) {
        const float* src = red + bb * 512 + w4 * 64 + 16 * r4 + 8 * e;
        const f32x4 t0 = s0 * *reinterpret_cast<const f32x4*>(src) + s1 * *reinterpret_cast<const f32x4*>(src + 256), t1 = s0 * *reinterpret_cast<const f32x4*>(src + 4) + s1 * *reinterpret_cast<const f32x4*>(src + 260);
        cv[e] = u32x4{pk_bf(t0[0], t0[1]), pk_bf(t0[2], t0[3]), pk_bf(t1[0], t1[1]), pk_bf(t1[2], t1[3])};
      }
#pragma unroll
      for (int k = 0; k < 8; ++k) acc[bb][k] = 0.f;
#pragma unroll
      for (int ip = 0; ip < 8; ++ip)
#pragma unroll
        for (int k = 0; k < 4; ++k) { acc[bb][2 * k] = dot2(Wb[2 * ip][k], cv[ip >> 2][ip & 3], acc[bb][2 * k]); acc[bb][2 * k + 1] = dot2(Wb[2 * ip + 1][k], cv[ip >> 2][ip & 3], acc[bb][2 * k + 1]); }
    }
    {
      float v8[8], v4[4];
#pragma unroll
      for (int k = 0; k < 8; ++k) v8[k] = rs32(acc[0][k], acc[1][k]);
#pragma unroll
      for (int k = 0; k < 4; ++k) v4[k] = rs16(v8[k], v8[k + 4]);
      *reinterpret_cast<f32x4*>(red2 + (w4 * 2 + (lane >> 5)) * 128 + 8 * c16 + 4 * (r4 & 1)) = f32x4{v4[0], v4[1], v4[2], v4[3]};
    }
    {
      COMPILER_FENCE();
      const unsigned got = lds_arrive_nw(L_CNT + 16);
      f32x4 t4[4];
#pragma unroll
      for (int w = 0; w < 4; ++w) t4[w] = *reinterpret_cast<const f32x4*>(red2 + w * 256 + 4 * lane);
      SCHED();
      if (lds_done(got) == 3u) {
        const f32x4 t = (t4[0] + t4[1]) + (t4[2] + t4[3]);
        const unsigned co = WS_CX + ((h * 2 + b) * 256 + 64 * r4 + 32 * (c16 & 1) + 2 * (c16 >> 1)) * 8;
        st16c(rs, co, u32x4{__float_as_uint(t[0]), tg.hi, __float_as_uint(t[1]), tg.hi});
        st16c(rs, co + 128, u32x4{__float_as_uint(t[2]), tg.hi, __float_as_uint(t[3]), tg.hi});
      }
    }
    STAMP(12);
    STAMP_FLUSH(0); TR_FLUSH();
    return;
  }
  {
    unsigned op;
    {
      u32x4 c;
      const unsigned co = WS_CX + ((h * 2 + ((lane >> 3) & 1)) * 256 + (lane >> 5) * 128 + b * 64 + w4 * 16 + 2 * (lane & 7)) * 8;
      if (P1W) {
        if (w4 == 0) { shint<2048, P1S>(ws + WS_CX + (h * 512 + b * 64) * 8, tg.hi); lds_add(cnt + 7, 1u, lane); }
        else lds_wait_slow(cnt + 7, 1u);
      } else shint<2048, P1S>(ws + WS_CX + (h * 512 + b * 64 + w4 * 16) * 8, tg.hi);
      STAMP(3);
#pragma unroll 1
      for (int spin = 0; spin < SPIN_MAX; ++spin) {
        COMPILER_FENCE();
        c = ld16c(rs, co);
        if (__all((c[1] == tg.hi) & (c[3] == tg.hi))) break;
        __builtin_amdgcn_s_sleep((SPM & 8) ? 1 : CSL);
      }
      float o0 = __uint_as_float(c[0]), o1 = __uint_as_float(c[2]);
      o0 += dpp<0x128>(o0); o1 += dpp<0x128>(o1);
      op = pk_bf(o0, o1);
    }
    STAMP(14);
    float* red3 = red + 4096;
    float a[2][8];
#pragma unroll
    for (int bb = 0; bb < 2; ++bb)
#pragma unroll
      for (int k = 0; k < 8; ++k) a[bb][k] = 0.f;
#pragma unroll
    for (int ip = 0; ip < 8; ++ip) {
      const unsigned v0 = __builtin_amdgcn_readlane(op, ip), v1 = __builtin_amdgcn_readlane(op, 32 + ip);
#pragma unroll
      for (int k = 0; k < 4; ++k) {
        a[0][2 * k] = dot2(Wb[2 * ip][k], v0, a[0][2 * k]); a[0][2 * k + 1] = dot2(Wb[2 * ip + 1][k], v0, a[0][2 * k + 1]);
        a[1][2 * k] = dot2(Wb[2 * ip][k], v1, a[1][2 * k]); a[1][2 * k + 1] = dot2(Wb[2 * ip + 1][k], v1, a[1][2 * k + 1]);
      }
    }
#pragma unroll
    for (int bb = 0; bb < 2; ++bb) {
      *reinterpret_cast<f32x4*>(red3 + (w4 * 2 + bb) * 512 + 8 * lane) = f32x4{a[bb][0], a[bb][1], a[bb][2], a[bb][3]};
      *reinterpret_cast<f32x4*>(red3 + (w4 * 2 + bb) * 512 + 8 * lane + 4) = f32x4{a[bb][4], a[bb][5], a[bb][6], a[bb][7]};
    }
    sbar4(cnt + 5, lane);
#pragma unroll
    for (int e = 0; e < 2; ++e) {
      f32x2 t = {0.f, 0.f};
#pragma unroll
      for (int w = 0; w < 4; ++w) t += *reinterpret_cast<const f32x2*>(red3 + w * 1024 + e * 512 + 2 * tid);
      st16c(rs, WS_GA + (idx * 1024 + e * 512 + 2 * tid) * 8, u32x4{g24(t[0], tg), tg.hi, g24(t[1], tg), tg.hi});
    }
    STAMP(5);
  }
  {
    const int fb = idx >> 5, fc = 16 * (idx & 31);
    const unsigned doff = WS_GA + ((w4 * 16 + (lane >> 3)) * 1024 + fb * 512 + fc + 2 * (lane & 7)) * 8;
    if (SPM & 16) lds_wait(cnt + 0, 4u);
    u32x4 d0, d1;
    bool ok = false;
#pragma unroll 1
    for (int spin = 0; spin < SPIN_MAX; ++spin) {
      COMPILER_FENCE();
      if (!ok) { d0 = ld16c(rs, doff); d1 = ld16c(rs, doff + 8 * 8192); }
      const unsigned eh = (d0[1] ^ tg.hi) | (d0[3] ^ tg.hi) | (d1[1] ^ tg.hi) | (d1[3] ^ tg.hi), el = (d0[0] ^ tg.lo8) | (d0[2] ^ tg.lo8) | (d1[0] ^ tg.lo8) | (d1[2] ^ tg.lo8);
      ok = (eh | (el & 0xff000000u)) == 0u;
      if (__all(ok)) break;
      __builtin_amdgcn_s_sleep(1);
    }
    fin(v24(d0[0]) + v24(d1[0]), v24(d0[2]) + v24(d1[2]), out + fb * 1024 + fc);
  }
  STAMP(6);
  STAMP_FLUSH(0); TR_FLUSH();
}

extern "C" void kernel_launch(void* const* d_in, const int* in_sizes, int n_in, void* d_out, int out_size, void* d_ws, size_t ws_size, hipStream_t stream) {
  const u16* xA = (const u16*)d_in[0];
  const u16* ckv = (const u16*)d_in[1];
  const u16* w_q = (const u16*)d_in[2];
  const u16* w_uk = (const u16*)d_in[3];
  const u16* w_uv = (const u16*)d_in[4];
  const u16* w_o = (const u16*)d_in[5];
  const u16* xB = (const u16*)d_in[6];
  const u16* kc = (const u16*)d_in[7];
  const u16* vc = (const u16*)d_in[8];
  const u16* wq = (const u16*)d_in[9];
  const u16* wo = (const u16*)d_in[10];
  kmain<<<256, 512, 0, stream>>>(xA, ckv, w_q, w_uk, w_uv, w_o, xB, kc, vc, wq, wo, (char*)d_ws, (u16*)d_out);
}
```

```cpp
#include <hip/hip_runtime.h>
#include <hip/hip_bf16.h>
#include <stdint.h>

#define DI __device__ __forceinline__
typedef __attribute__((ext_vector_type(4))) unsigned u32x4;
typedef __attribute__((ext_vector_type(2))) unsigned u32x2;
typedef __attribute__((ext_vector_type(4))) float f32x4;
typedef __attribute__((ext_vector_type(2))) float f32x2;
typedef __attribute__((ext_vector_type(16))) float f32x16;
typedef __attribute__((ext_vector_type(8))) __bf16 bf16x8_t;
typedef __attribute__((ext_vector_type(2))) __bf16 bf16x2_t;
typedef __attribute__((ext_vector_type(4))) short s16x4;
typedef __attribute__((ext_vector_type(8))) short s16x8;
typedef unsigned short u16;
typedef unsigned long long u64;

#define NOA 0
constexpr int NS0 = 14, NS1 = 16, NS2 = 15, NS3 = 19;
#define HOLD 1
#define NOAML 0
#define P1W 1
#define P1S 4
#define P2W 1
#define P2S 2
#define G2B 0
#define WB0 3
#define FLI 20
#define QIA 31
#define X2 0
#define PHASE idx
#define EPP -1
#define XPP 0
#define OUTAUX 0
#define TOUCH 0
#define NOTW 1
#define AHL 1
#define VT 1
#define ZW 1
#define LIN 1
#define RSL 1
#define SPM 27
#define QSL 1
#define PW 4
#define PWM 15
#define QPE 12
#define CSL 16
#define RA2V 0xC0
#define RA3V 0x33
constexpr int RA2 = RA2V, RA3 = RA3V;
#define WE 4
#define WL 5
constexpr u64 TL0 = 0xbeb96069644b1c0ull, TH0 = 0xfbcdf3ull, TL1 = 0x9217595523c9142ull, TH1 = 0xef8d30b28ull;
constexpr u64 TL2 = 0x9a279b5d338a183ull, TH2 = 0x3adb2baaull, TL3 = 0x7dc61440d308101ull, TH3 = 0x3ff79d71b699e3ull;
constexpr u64 LA2 = TH2 >> (6 * (NS2 - 11)), LA3 = TH3 >> (6 * (NS3 - 11));
constexpr u64 chunk_mask(u64 lo, u64 hi, int n) { u64 m = 0; for (int i = 0; i < n; ++i) m |= 1ull << ((i < 10 ? lo >> (6 * i) : hi >> (6 * (i - 10))) & 63); return m; }
static_assert((chunk_mask(TL0, TH0, NS0) | chunk_mask(TL1, TH1, NS1) | chunk_mask(TL2, TH2, NS2) | chunk_mask(TL3, TH3, NS3)) == ~0ull && (RA2 & RA3) == 0, "every chunk once");
static_assert(NS0 >= 12 && NS1 >= 12 && NS2 >= 13 && NS3 >= 13 && NS0 + NS1 + NS2 + NS3 == 64, "split sizes");
static_assert(FLI + WB0 + 2 <= QIA, "order");
constexpr int NSMIN = 14, NSMAX = 19;
static_assert(NS0 >= NSMIN && NS1 >= NSMIN && NS2 - (RA2 != 0) >= NSMIN && NS3 - (RA3 != 0) >= NSMIN && NS0 <= NSMAX && NS1 + 1 <= NSMAX && NS2 <= NSMAX && NS3 <= NSMAX, "range");
constexpr int SK = 4096;
constexpr float C2 = 0.08838834764831845f * 1.4426950408889634f;
constexpr u64 GOLD = 0x9E3779B97F4A7C15ull;

constexpr unsigned WS_EP = 0;
constexpr unsigned WS_QX = 4096;
constexpr unsigned WS_QA = WS_QX + 2 * 32 * 256 * 8;
constexpr unsigned WS_AML = WS_QA + 2 * 32 * 512 * 2;
constexpr unsigned WS_CX = WS_AML + 32 * 2 * 64 * 8;
constexpr unsigned WS_GZ = WS_CX + 32 * 2 * 2 * 128 * 8;
constexpr unsigned WS_GB = WS_GZ + 256 * 128;
constexpr unsigned WS_GA = WS_GB + 2 * 128 * 256 * 8;
constexpr unsigned WS_APO = 2u << 20;
static_assert(WS_GA + 64 * 2 * 256 * 8 <= WS_APO, "layout");
constexpr unsigned WS_END = WS_APO + 32 * 2 * 64 * 256 * 2;
#define STAMP(i) do {} while (0)
#define STAMP_DECL do {} while (0)
#define STAMP_FLUSH(grp) do {} while (0)

#define TR() do {} while (0)
#define TR_FLUSH() do {} while (0)
DI float bflo(unsigned u) { return __uint_as_float(u << 16); }
DI float bfhi(unsigned u) { return __uint_as_float(u & 0xffff0000u); }
DI unsigned pk_bf(float a, float b) { const f32x2 v = {a, b}; return __builtin_bit_cast(unsigned, __builtin_convertvector(v, bf16x2_t)); }
DI float dot2(unsigned k, unsigned q, float acc) {
  return __builtin_amdgcn_fdot2_f32_bf16(__builtin_bit_cast(bf16x2_t, k), __builtin_bit_cast(bf16x2_t, q), acc, false);
}
DI unsigned plo(unsigned w0, unsigned w1) { return __builtin_amdgcn_perm(w1, w0, 0x05040100u); }
DI unsigned phi(unsigned w0, unsigned w1) { return __builtin_amdgcn_perm(w1, w0, 0x07060302u); }
template <int CTRL> DI float dpp(float x) {
  return __builtin_bit_cast(float, __builtin_amdgcn_mov_dpp(__builtin_bit_cast(int, x), CTRL, 0xf, 0xf, true));
}
DI float row16_sum(float x) { x += dpp<0xB1>(x); x += dpp<0x4E>(x); x += dpp<0x141>(x); x += dpp<0x128>(x); return x; }
DI float row16_max(float x) { x = fmaxf(x, dpp<0xB1>(x)); x = fmaxf(x, dpp<0x4E>(x)); x = fmaxf(x, dpp<0x141>(x)); x = fmaxf(x, dpp<0x128>(x)); return x; }
DI float x16_sum(float x) { auto s = __builtin_amdgcn_permlane16_swap(__float_as_uint(x), __float_as_uint(x), false, false); return __uint_as_float(s[0]) + __uint_as_float(s[1]); }
DI float x16_max(float x) { auto s = __builtin_amdgcn_permlane16_swap(__float_as_uint(x), __float_as_uint(x), false, false); return fmaxf(__uint_as_float(s[0]), __uint_as_float(s[1])); }
DI float xhalf_max(float x) { auto t = __builtin_amdgcn_permlane32_swap(__float_as_uint(x), __float_as_uint(x), false, false); return fmaxf(__uint_as_float(t[0]), __uint_as_float(t[1])); }
DI float xhalf_sum(float x) { auto t = __builtin_amdgcn_permlane32_swap(__float_as_uint(x), __float_as_uint(x), false, false); return __uint_as_float(t[0]) + __uint_as_float(t[1]); }
DI float xrow_max(float x) { return xhalf_max(x16_max(x)); }
DI float xrow_sum(float x) { return xhalf_sum(x16_sum(x)); }
DI u32x4 ld16(const void* p) { return *reinterpret_cast<const u32x4*>(p); }
DI u32x4 ld16nt(const void* p) { return __builtin_nontemporal_load(reinterpret_cast<const u32x4*>(p)); }
DI float ex2(float x) { return __builtin_amdgcn_exp2f(x); }
#define SCHED() __builtin_amdgcn_sched_barrier(0)

typedef __attribute__((address_space(1))) u64 gu64;
typedef __amdgpu_buffer_rsrc_t rsrc_t;
DI u32x4 ld16c(rsrc_t rs, unsigned off) { return __builtin_amdgcn_raw_buffer_load_b128(rs, (int)off, 0, 16); }
DI u32x2 ld8c(rsrc_t rs, unsigned off) { return __builtin_amdgcn_raw_buffer_load_b64(rs, (int)off, 0, 16); }
DI void st16c(rsrc_t rs, unsigned off, u32x4 v) { __builtin_amdgcn_raw_buffer_store_b128(v, rs, (int)off, 0, 16); }
DI void st8c(rsrc_t rs, unsigned off, u32x2 v) { __builtin_amdgcn_raw_buffer_store_b64(v, rs, (int)off, 0, 16); }
typedef __attribute__((ext_vector_type(16))) unsigned u32x16;
typedef __attribute__((ext_vector_type(8))) u64 u64x8;
template <int O1> DI void sld2(const char* p, u32x2& a, u32x2& b) {
  asm volatile("s_load_dwordx2 %0, %2, 0x0 glc\n\ts_load_dwordx2 %1, %2, %3 glc\n\ts_waitcnt lgkmcnt(0)" : "=&s"(a), "=&s"(b) : "s"(p), "n"(O1) : "memory");
}
DI u32x16 sld64(const char* p) {
  u32x16 v;
  asm volatile("s_load_dwordx16 %0, %1, 0x0 glc\n\ts_waitcnt lgkmcnt(0)" : "=&s"(v) : "s"(p) : "memory");
  return v;
}
DI void sld128(const char* p, u32x16 (&v)[2]) {
  asm volatile("s_load_dwordx16 %0, %2, 0x0 glc\n\ts_load_dwordx16 %1, %2, 0x40 glc\n\ts_waitcnt lgkmcnt(0)" : "=&s"(v[0]), "=&s"(v[1]) : "s"(p) : "memory");
}
constexpr int SPIN_MAX = 1 << 20;
constexpr unsigned OOB = 0x7ffffff0u;
DI void rawbar() { __builtin_amdgcn_sched_barrier(0); __builtin_amdgcn_s_barrier(); __builtin_amdgcn_sched_barrier(0); }
struct Tag { unsigned hi, lo8; };
DI unsigned g24(float v, const Tag& t) { return t.lo8 | ((__float_as_uint(v) + 0x80u) >> 8); }
DI bool ok24(unsigned lo, unsigned hi, const Tag& t) { return (hi == t.hi) & ((lo & 0xff000000u) == t.lo8); }
DI float v24(unsigned lo) { return __uint_as_float(lo << 8); }
#define COMPILER_FENCE() asm volatile("" ::: "memory")
template <int O1, int SL> DI void shint(const char* p, unsigned tag) {
#pragma unroll 1
  for (int spin = 0; spin < SPIN_MAX; ++spin) {
    u32x2 a, b;
    sld2<O1>(p, a, b);
    if (((a[1] ^ tag) | (b[1] ^ tag)) == 0u) break;
    __builtin_amdgcn_s_sleep(SL);
  }
}

template <int N> struct IC { static constexpr int v = N; };
template <int I, int N, class F> DI void sfor(F&& f) { if constexpr (I < N) { f(IC<I>{}); sfor<I + 1, N>(f); } }
typedef __attribute__((address_space(3))) unsigned lu32;
DI void lds_wait(unsigned* cnt, unsigned target) {
#pragma unroll 1
  for (int spin = 0; spin < SPIN_MAX; ++spin) {
    if (__builtin_amdgcn_readfirstlane(__hip_atomic_load((lu32*)cnt, __ATOMIC_RELAXED, __HIP_MEMORY_SCOPE_WORKGROUP)) >= target) break;
    __builtin_amdgcn_s_sleep(1);
  }
  COMPILER_FENCE();
}
DI void lds_wait_slow(unsigned* cnt, unsigned target) {
#pragma unroll 1
  for (int spin = 0; spin < SPIN_MAX; ++spin) {
    if (__builtin_amdgcn_readfirstlane(__hip_atomic_load((lu32*)cnt, __ATOMIC_RELAXED, __HIP_MEMORY_SCOPE_WORKGROUP)) >= target) break;
    __builtin_amdgcn_s_sleep(3);
  }
  COMPILER_FENCE();
}
DI void lds_hold(unsigned* flag) {
#pragma unroll 1
  for (int spin = 0; spin < SPIN_MAX; ++spin) {
    if (__builtin_amdgcn_readfirstlane(__hip_atomic_load((lu32*)flag, __ATOMIC_RELAXED, __HIP_MEMORY_SCOPE_WORKGROUP)) == 0u) break;
    __builtin_amdgcn_s_sleep(2);
  }
}
DI unsigned lds_arrive_nw(unsigned addr) {
  unsigned old, one = 1u;
  asm volatile("s_mov_b64 exec, 1\n\tds_add_rtn_u32 %0, %1, %2\n\ts_mov_b64 exec, -1" : "=v"(old) : "v"(addr), "v"(one) : "memory");
  return old;
}
DI unsigned lds_done(unsigned old) { asm volatile("s_waitcnt lgkmcnt(0)" : "+v"(old) :: "memory"); return __builtin_amdgcn_readfirstlane(old); }
DI unsigned lds_arrive(unsigned* cnt, int) { return lds_done(lds_arrive_nw((unsigned)(size_t)(lu32*)cnt)); }
DI void lds_add(unsigned* cnt, unsigned v, int) {
  asm volatile("s_mov_b64 exec, 1\n\tds_add_u32 %0, %1\n\ts_mov_b64 exec, -1" :: "v"((unsigned)(size_t)(lu32*)cnt), "v"(v) : "memory");
}
DI void sbar4(unsigned* cnt, int lane) { if (lds_arrive(cnt, lane) < 3u) lds_wait(cnt, 4u); }
DI float rs32(float a, float b) { auto t = __builtin_amdgcn_permlane32_swap(__float_as_uint(a), __float_as_uint(b), false, false); return __uint_as_float(t[0]) + __uint_as_float(t[1]); }
DI float rs16(float a, float b) { auto t = __builtin_amdgcn_permlane16_swap(__float_as_uint(a), __float_as_uint(b), false, false); return __uint_as_float(t[0]) + __uint_as_float(t[1]); }
template <int N> DI unsigned rowbcast(unsigned x) { return (unsigned)__builtin_amdgcn_mov_dpp((int)x, 0x150 + N, 0xf, 0xf, true); }
DI void pin2(u32x4& a, u32x4& b) { asm volatile("" : "+v"(a), "+v"(b)); }
DI float rdlane(float x, int l) { return __builtin_bit_cast(float, __builtin_amdgcn_readlane(__builtin_bit_cast(int, x), l)); }
DI bool nz8(const u32x4& v) { return ((v[0] | v[1]) != 0u) & ((v[2] | v[3]) != 0u); }

DI u32x4 qsum(const u32x4& g) {
  const auto ta = __builtin_amdgcn_permlane32_swap(g[0], g[0], false, false), tc = __builtin_amdgcn_permlane32_swap(g[2], g[2], false, false);
  const unsigned a = pk_bf(bflo(ta[0]) + bflo(ta[1]), bfhi(ta[0]) + bfhi(ta[1])), c = pk_bf(bflo(tc[0]) + bflo(tc[1]), bfhi(tc[0]) + bfhi(tc[1]));
  const auto ra = __builtin_amdgcn_permlane16_swap(a, a, false, false), rc = __builtin_amdgcn_permlane16_swap(c, c, false, false);
  return u32x4{ra[0], rc[0], ra[1], rc[1]};
}
DI unsigned toff(unsigned row, unsigned ch) { return 256u * row + 16u * (ch ^ (((row & 3) << 2) | ((row >> 2) & 3))); }
constexpr int SMEM = 160 * 1024;
constexpr int L_SCR = 131072;
constexpr int L_MRG = 65536;
constexpr int L_CNT = L_SCR + 32 * 1008;

struct KV { u32x4 r[8]; };

__global__ __launch_bounds__(512, 2) void kmain(const u16* __restrict__ xA, const u16* __restrict__ ckv, const u16* __restrict__ w_q, const u16* __restrict__ w_uk,
                                                const u16* __restrict__ w_uv, const u16* __restrict__ w_o,
                                                const u16* __restrict__ xB, const u16* __restrict__ kc, const u16* __restrict__ vc,
                                                const u16* __restrict__ wq, const u16* __restrict__ wo,
                                                char* __restrict__ ws, u16* __restrict__ out) {
  __shared__ __attribute__((aligned(16))) char smem[SMEM];
  const int tid = threadIdx.x, blk = blockIdx.x, lane = tid & 63;
  const int wave = __builtin_amdgcn_readfirstlane(tid >> 6), w4 = wave & 3;
  const int cls = (blk >> 3) & 3;
  int idx = 8 * (blk & 7) + (blk >> 5);
  if (wave < 4) idx = (idx & ~7) | ((idx + (cls == 3 ? 2 : (cls == 2 ? 4 : 0))) & 7);
  const int h = idx >> 1, b = idx & 1;
  const rsrc_t rs = __builtin_amdgcn_make_buffer_rsrc(ws, 0, (int)WS_END, 0x00020000);
  const int r4 = lane >> 4, c16 = lane & 15;
  unsigned* cnt = reinterpret_cast<unsigned*>(smem + L_CNT);
  STAMP_DECL;
  STAMP(0);
  const u64 tstart = X2 & 1 ? wall_clock64() : 0;
  asm volatile("" :: "s"(xA), "s"(ckv), "s"(w_q), "s"(w_uk), "s"(w_uv), "s"(w_o), "s"(xB), "s"(kc), "s"(vc), "s"(wq), "s"(wo), "s"(ws), "s"(out));
  Tag tg;
  unsigned tlow;
  u32x2 epw;
  auto take_epoch = [&]() {
    const u64 t64 = (((u64)(unsigned)__builtin_amdgcn_readfirstlane(epw[1]) << 32) | (unsigned)__builtin_amdgcn_readfirstlane(epw[0])) + GOLD;
    tg.hi = (unsigned)(t64 >> 32); tlow = (unsigned)t64;
    asm volatile("s_waitcnt lgkmcnt(0)" : "+s"(tg.hi), "+s"(tlow) :: "memory");
    tg.lo8 = tlow & 0xff000000u;
  };
  if (wave >= 4) {
    if (tid - 256 < 16) cnt[tid - 256] = 0u;
    asm volatile("s_waitcnt lgkmcnt(0)" ::: "memory");
    rawbar();
    const int e2 = (RA2 >> (idx & 7)) & 1, e3 = (RA3 >> (idx & 7)) & 1;
    const int ns = cls == 0 ? NS0 : (cls == 1 ? NS1 + e2 + e3 : (cls == 2 ? NS2 - e2 : NS3 - e3));
    const size_t kvo = ((size_t)(b * 32 + h) * SK + w4 * 16) * 256;
    const char* kb = reinterpret_cast<const char*>(kc) + kvo;
    const char* vb = reinterpret_cast<const char*>(vc) + kvo;
    const unsigned lo = lane * 16u;
    u64 tlo = cls == 0 ? TL0 : (cls == 1 ? TL1 : (cls == 2 ? TL2 : TL3));
    u64 thi = cls == 0 ? TH0 : (cls == 1 ? TH1 | ((e2 ? LA2 : LA3) << (6 * (NS1 - 10))) : (cls == 2 ? TH2 : TH3));
    const u64 tlo0 = tlo, thi0 = thi;
    int hn = 0;
    const char* hb = kb;
    const char* pc[2];
    auto next2 = [&]() {
#pragma unroll
      for (int i = 0; i < 2; ++i) {
        if (hn == ns) { tlo = tlo0; thi = thi0; hb = vb; }
        ++hn;
        pc[i] = hb + ((((unsigned)tlo + (unsigned)(PHASE)) & 63u) << 14);
        tlo = (tlo >> 6) | ((thi & 63u) << 54); thi >>= 6;
      }
    };
    KV A, B;
    u32x4 W[8];
    u32x4 T[4][8];
    auto ldp = [&](KV& S, auto P) {
      constexpr int p = P.v;
      const char* q = pc[p >> 1] + lo + (p & 1) * 2048;
      S.r[2 * p] = ld16nt(q); S.r[2 * p + 1] = ld16nt(q + 1024);
    };
    auto landed = [&](KV& S, auto P) { pin2(S.r[2 * P.v], S.r[2 * P.v + 1]); };
    KV X;
    const unsigned qo = WS_QX + ((32 + h) * 2 + b) * 1024 + lane * 16;
    u32x4 g0 = {0u, 0u, 0u, 0u};
    sfor<0, 12>([&](auto I) {
      constexpr int i = I.v, j = i - 4;
      if constexpr ((i & 3) == 0) next2();
      if constexpr (j >= 0) { if constexpr (j < 4) landed(X, IC<j & 3>{}); else landed(B, IC<j & 3>{}); }
      if constexpr (j >= 0 && (j & 1)) TR();
      if constexpr (i == QPE) g0 = ld16c(rs, qo);
      if constexpr (i < 4) ldp(X, IC<i & 3>{}); else if constexpr (i < 8) ldp(B, IC<i & 3>{}); else ldp(A, IC<i & 3>{});
      SCHED();
    });
    lds_wait(cnt + 15, 1u);
    {
      const u32x2 t = *reinterpret_cast<const u32x2*>(cnt + 16);
      tlow = __builtin_amdgcn_readfirstlane(t[0]); tg.hi = __builtin_amdgcn_readfirstlane(t[1]);
    }
    const char* wp = reinterpret_cast<const char*>(wo) + ((size_t)h * 128 + r4 * 8) * 1024 + w4 * 256 + c16 * 16;
    u32x4 qpk;
    {
      unsigned tv = 0u;
      if (VT && tid == 256) tv = __builtin_nontemporal_load(reinterpret_cast<const unsigned*>(vb + cls * 128));
      if (QPE >= 12 || !__all((g0[1] == tg.hi) & (g0[3] == tg.hi))) {
#pragma unroll 1
        for (int spin = 0; spin < SPIN_MAX; ++spin) {
          COMPILER_FENCE();
          g0 = ld16c(rs, qo);
          if (__all((g0[1] == tg.hi) & (g0[3] == tg.hi))) break;
        }
        STAMP(9);
      }
      qpk = qsum(g0);
      asm volatile("" :: "v"(tv));
    }
    STAMP(7);
    const bool holds = HOLD && cls == 0;
    float m = 0.f, l = 0.f, acc[8];
    unsigned sc[5];
#pragma unroll
    for (int k = 0; k < 8; ++k) acc[k] = 0.f;
#pragma unroll
    for (int k = 0; k < 5; ++k) sc[k] = 0xff800000u;
    int vn = 0;
    unsigned rwo;
    const unsigned zo = WS_GZ + (b * 32 + h) * 512;
    u32x2 zs[4];
    float wr_;
    auto zreq = [&]() {
      asm volatile("s_load_dwordx2 %0, %4, 0x0 glc\n\ts_load_dwordx2 %1, %4, 0x80 glc\n\ts_load_dwordx2 %2, %4, 0x100 glc\n\ts_load_dwordx2 %3, %4, 0x180 glc"
                   : "=&s"(zs[0]), "=&s"(zs[1]), "=&s"(zs[2]), "=&s"(zs[3]) : "s"(ws + zo) : "memory");
    };
    auto zget = [&]() {
#pragma unroll 1
      for (int spin = 0; spin < SPIN_MAX; ++spin) {
        asm volatile("s_waitcnt lgkmcnt(0)" : "+s"(zs[0]), "+s"(zs[1]), "+s"(zs[2]), "+s"(zs[3]) :: "memory");
        if (((zs[0][1] ^ tg.hi) | (zs[1][1] ^ tg.hi) | (zs[2][1] ^ tg.hi) | (zs[3][1] ^ tg.hi)) == 0u) break;
        __builtin_amdgcn_s_sleep(2);
        zreq();
      }
      asm volatile("s_waitcnt lgkmcnt(0)" : "+s"(zs[0]), "+s"(zs[1]), "+s"(zs[2]), "+s"(zs[3]) :: "memory");
      const float z0 = __uint_as_float(zs[0][0]), z1 = __uint_as_float(zs[1][0]), z2 = __uint_as_float(zs[2][0]), z3 = __uint_as_float(zs[3][0]);
      const float Z = fmaxf(fmaxf(z0, z1), fmaxf(z2, z3));
      wr_ = ex2(fmaf(m, C2, -Z)) * __builtin_amdgcn_rcpf((ex2(z0 - Z) + ex2(z1 - Z)) + (ex2(z2 - Z) + ex2(z3 - Z)));
      asm volatile("" : "+v"(wr_));
    };
    auto qk = [&](const u32x4* K, auto I) {
      float x[4];
#pragma unroll
      for (int t = 0; t < 4; ++t) {
        x[t] = 0.f;
#pragma unroll
        for (int k = 0; k < 4; ++k) x[t] = dot2(K[t][k], qpk[k], x[t]);
      }
      const float u0 = (c16 & 1 ? x[1] : x[0]) + dpp<0xB1>(c16 & 1 ? x[0] : x[1]), u1 = (c16 & 1 ? x[3] : x[2]) + dpp<0xB1>(c16 & 1 ? x[2] : x[3]);
      float v = (c16 & 2 ? u1 : u0) + dpp<0x4E>(c16 & 2 ? u0 : u1);
      v += dpp<0x124>(v); v += dpp<0x128>(v);
      sc[I.v >> 2] = __builtin_amdgcn_update_dpp(sc[I.v >> 2], __float_as_uint(v), 0xE4, 0xf, 1 << (I.v & 3), false);
      asm volatile("" : "+v"(sc[I.v >> 2]));
    };
    auto soft = [&]() {
      m = row16_max(fmaxf(fmaxf(fmaxf(__uint_as_float(sc[0]), __uint_as_float(sc[1])), fmaxf(__uint_as_float(sc[2]), __uint_as_float(sc[3]))), __uint_as_float(sc[4])));
      const float mc = m * C2;
      l = 0.f;
#pragma unroll
      for (int k = 0; k < 5; ++k) {
        const float p = ex2(fmaf(__uint_as_float(sc[k]), C2, -mc));
        sc[k] = pk_bf(p, dpp<0xB1>(p));
        l = dot2(sc[k], 0x3f003f00u, l);
      }
      l = row16_sum(l);
    };
    auto pv = [&](const u32x4* V) {
      const unsigned p01 = rowbcast<0>(sc[0]), p23 = rowbcast<2>(sc[0]);
#pragma unroll
      for (int k = 0; k < 4; ++k) { acc[2 * k] = dot2(plo(V[0][k], V[1][k]), p01, acc[2 * k]); acc[2 * k + 1] = dot2(phi(V[0][k], V[1][k]), p01, acc[2 * k + 1]); }
#pragma unroll
      for (int k = 0; k < 4; ++k) { acc[2 * k] = dot2(plo(V[2][k], V[3][k]), p23, acc[2 * k]); acc[2 * k + 1] = dot2(phi(V[2][k], V[3][k]), p23, acc[2 * k + 1]); }
      sc[0] = (unsigned)__builtin_amdgcn_mov_dpp((int)sc[0], 0x12C, 0xf, 0xf, true);
      if ((++vn & 3) == 0) { sc[0] = sc[1]; sc[1] = sc[2]; sc[2] = sc[3]; sc[3] = sc[4]; }
      asm volatile("" : "+v"(acc[0]), "+v"(acc[1]), "+v"(acc[2]), "+v"(acc[3]), "+v"(acc[4]), "+v"(acc[5]), "+v"(acc[6]), "+v"(acc[7]));
    };
    auto step = [&](KV& S, KV& N, auto SI, auto REFILL, auto WR) {
      constexpr bool refill = REFILL.v == 1;
      constexpr int wr = WR.v;
      if constexpr (refill) next2();
      constexpr int ah = wr < 0 ? AHL : 0;
      auto point = [&](auto P) {
        if constexpr (refill) {
          if constexpr (P.v == 0 && ah > 0) { if (holds) lds_hold(cnt + 8); sfor<0, ah>([&](auto Q) { ldp(N, Q); }); SCHED(); }
          landed(S, P);
          if constexpr ((wr != 0 || QPE < 11) && (P.v & 1)) TR();
          if constexpr (P.v + ah < 4) { if constexpr ((P.v & 1) == 0) { if (holds) lds_hold(cnt + 8); } ldp(N, IC<(P.v + ah) & 3>{}); }
          if constexpr (wr >= 0 && wr < 8) W[(4 * wr + P.v) & 7] = ld16(wp + (((4 * wr + P.v) >> 3) * 32 + ((4 * wr + P.v) & 7)) * 1024);
          SCHED();
        }
      };
      sfor<0, 2>([&](auto H) {
        constexpr int i = 2 * SI.v + H.v;
        if constexpr (REFILL.v == 2) { if constexpr (H.v == 0) zreq(); else if constexpr (!ZW) zget(); SCHED(); }
        point(IC<2 * H.v>{});
        point(IC<2 * H.v + 1>{});
        if constexpr (i < NSMIN) qk(S.r + 4 * H.v, IC<i>{});
        else if constexpr (i > NSMAX) pv(S.r + 4 * H.v);
        else {
          if (i < ns) { if constexpr (i < NSMAX) qk(S.r + 4 * H.v, IC<i>{}); }
          else { if (i == ns) soft(); pv(S.r + 4 * H.v); }
        }
        SCHED();
      });
      if constexpr (REFILL.v == 2) {
        if constexpr (ZW) zget();
        STAMP(1);
        *reinterpret_cast<u32x4*>(smem + rwo) = u32x4{pk_bf(acc[0] * wr_, acc[1] * wr_), pk_bf(acc[2] * wr_, acc[3] * wr_), pk_bf(acc[4] * wr_, acc[5] * wr_), pk_bf(acc[6] * wr_, acc[7] * wr_)};
        COMPILER_FENCE();
      }
      if constexpr (wr >= 1) {
        constexpr int t = (wr - 1) >> 1, j0 = ((wr - 1) & 1) * 2;
#pragma unroll
        for (int jj = 0; jj < 2; ++jj)
#pragma unroll
          for (int e = 0; e < 4; ++e) {
            const unsigned a_ = W[(4 * wr - 4 + 2 * jj) & 7][e], b_ = W[(4 * wr - 3 + 2 * jj) & 7][e];
            T[t][2 * e][j0 + jj] = plo(a_, b_); T[t][2 * e + 1][j0 + jj] = phi(a_, b_);
          }
#pragma unroll
        for (int e = 0; e < 8; e += 2) pin2(T[t][e], T[t][e + 1]);
      }
      SCHED();
    };
    step(B, B, IC<1>{}, IC<0>{}, IC<-1>{});
    step(A, B, IC<2>{}, IC<1>{}, IC<0>{});
    step(X, X, IC<0>{}, IC<0>{}, IC<-1>{});
    sfor<0, 4>([&](auto I) {
      step(B, A, IC<2 * I.v + 3>{}, IC<1>{}, IC<2 * I.v + 1>{});
      if constexpr (I.v == 3) {
        float* ml = reinterpret_cast<float*>(cnt + 32);
        ml[(c16 & 1) * 16 + w4 * 4 + r4] = (c16 & 1) ? l : m;
        COMPILER_FENCE();
        const unsigned got = lds_arrive_nw(L_CNT + 44);
        const float mr = ml[c16], lr = ml[16 + c16];
        SCHED();
        if (lds_done(got) == 3u) {
          const float M = row16_max(mr);
          const unsigned z = __float_as_uint(fmaf(M, C2, __builtin_amdgcn_logf(row16_sum(ex2((mr - M) * C2) * lr))));
          asm volatile("buffer_store_dwordx4 %0, %1, %2, 0 offen sc1" :: "v"(u32x4{z, tg.hi, z, tg.hi}), "v"(lane < 8 ? zo + cls * 128 + lane * 16 : OOB), "s"(rs) : "memory");
        }
        SCHED();
      }
      step(A, B, IC<2 * I.v + 4>{}, IC<1>{}, IC<2 * I.v + 2>{});
    });
    unsigned yoff = WS_GB + ((b * 128 + h * 4 + cls) * 256 + 64 * w4 + (LIN ? lane : 4 * c16 + r4)) * 8, rro = L_MRG + c16 * 272 + r4 * 16;
    rwo = L_MRG + (w4 * 4 + r4) * 272 + c16 * 16;
    asm volatile("" : "+v"(yoff), "+v"(rwo), "+v"(rro));
    const int nit = (ns - 12) >> 1;
#pragma unroll 1
    for (int i = 0; i < nit; ++i) {
      step(B, A, IC<11>{}, IC<1>{}, IC<-1>{});
      step(A, B, IC<11>{}, IC<1>{}, IC<-1>{});
    }
    if (cls == 0) lds_wait(cnt + 1, 4u);
    if (ns & 1) { step(B, A, IC<11>{}, IC<1>{}, IC<-1>{}); step(A, A, IC<11>{}, IC<2>{}, IC<-1>{}); }
    else step(B, B, IC<11>{}, IC<2>{}, IC<-1>{});
    {
      u32x4 af[4];
      unsigned got = lds_arrive_nw(L_CNT), need = 3u;
#pragma unroll 1
      for (int spin = 0; spin < SPIN_MAX; ++spin) {
#pragma unroll
        for (int t = 0; t < 4; ++t) af[t] = *reinterpret_cast<const u32x4*>(smem + rro + t * 64);
        SCHED();
        if (lds_done(got) >= need) break;
        need = 4u;
        if (RSL) __builtin_amdgcn_s_sleep(1);
        asm volatile("ds_read_b32 %0, %1" : "=v"(got) : "v"((unsigned)L_CNT) : "memory");
      }
      SCHED();
      f32x4 d[8];
      float a[8], v4[4];
      sfor<0, 5>([&](auto I) {
        constexpr int i = I.v;
        sfor<0, 8>([&](auto J) {
          constexpr int e = i + 4 * (J.v & 1), t = J.v >> 1;
          if constexpr (i < 4) { d[e] = __builtin_amdgcn_mfma_f32_16x16x32_bf16(__builtin_bit_cast(bf16x8_t, af[t]), __builtin_bit_cast(bf16x8_t, T[t][e]), t ? d[e] : f32x4{0.f, 0.f, 0.f, 0.f}, 0, 0, 0); SCHED(); }
          if constexpr (i >= 1 && (J.v == 4 || J.v == 5)) { const f32x2 u = f32x2{d[e - 1][0], d[e - 1][1]} + f32x2{d[e - 1][2], d[e - 1][3]}; a[e - 1] = u[0] + u[1]; asm volatile("" : "+v"(a[e - 1])); }
          if constexpr (i >= 1 && J.v == 6) v4[i - 1] = rs32(a[i - 1], a[i + 3]);
          if constexpr (i < 4) SCHED();
        });
      });
      st8c(rs, yoff, u32x2{pk_bf(rs16(v4[0], v4[2]), rs16(v4[1], v4[3])), tg.hi});
    }
    STAMP(2);
    STAMP_FLUSH(1);
    TR_FLUSH();
    return;
  }
  const rsrc_t ro = __builtin_amdgcn_make_buffer_rsrc(out, 0, 4096, 0x00020000);
  auto fin = [&](auto NL, unsigned base, unsigned col, bool bump) {
    constexpr int nl = NL.v;
    unsigned doff = base + (((idx >> 5) * 4 + w4) * 16 * nl + (lane >> 2)) * 2048 + (idx & 31) * 64 + (lane & 3) * 16;
    unsigned fwo = 32768 + (c16 < 4 ? (w4 * 16 + 4 * c16 + r4) * 4 : 256 + lane * 4);
    unsigned fro = 32768 + (lane & 7) * 8, foo = lane < 8 ? ((idx >> 5) * 1024 + col + (LIN && nl == 2 ? 16 * (idx & 24) + 64 * (idx & 1) + (idx & 6) + 8 * lane : 16 * (idx & 31) + 2 * lane)) * 2 : OOB;
    asm volatile("" : "+v"(doff), "+v"(fwo), "+v"(fro), "+v"(foo));
    lds_wait(cnt + 0, 4u);
    STAMP(4);
    u32x4 d[nl];
    unsigned eh = 1u;
#pragma unroll 1
    for (int spin = SPIN_MAX; spin > 0; --spin) {
      COMPILER_FENCE();
      if (eh != 0u) {
#pragma unroll
        for (int j = 0; j < nl; ++j) d[j] = ld16c(rs, doff + j * 32768);
        eh = 0u;
#pragma unroll
        for (int j = 0; j < nl; ++j) { eh |= (d[j][1] ^ tg.hi) | (d[j][3] ^ tg.hi); asm volatile("" : "+v"(eh)); }
      }
      if (__builtin_amdgcn_ballot_w64(eh != 0u) == 0ull) break;
    }
    STAMP(11);
    float y[4] = {bflo(d[0][0]), bfhi(d[0][0]), bflo(d[0][2]), bfhi(d[0][2])};
    if constexpr (nl == 2) { y[0] += bflo(d[1][0]); y[1] += bfhi(d[1][0]); y[2] += bflo(d[1][2]); y[3] += bfhi(d[1][2]); }
    float t = rs16(rs32(y[0], y[2]), rs32(y[1], y[3]));
    t += dpp<0x124>(t); t += dpp<0x128>(t);
    *reinterpret_cast<float*>(smem + fwo) = t;
    COMPILER_FENCE();
    const unsigned got = lds_arrive_nw(L_CNT + 24);
    f32x2 t2[4];
#pragma unroll
    for (int w = 0; w < 4; ++w) t2[w] = *reinterpret_cast<const f32x2*>(smem + fro + w * 64);
    SCHED();
    if (lds_done(got) == 3u) {
      const f32x2 r = (t2[0] + t2[1]) + (t2[2] + t2[3]);
      __builtin_amdgcn_raw_buffer_store_b32(pk_bf(r[0], r[1]), ro, (int)foo, 0, 0);
      if (bump) st8c(rs, lane == 0 ? WS_EP : OOB, u32x2{tlow, tg.hi});
    }
  };
  if (EPP < 0) { epw = ld8c(rs, WS_EP); SCHED(); }
  u32x4 Wa[16], Wb[16], xv[4];
  const u16* pb; size_t sb, sb2;
  unsigned tch = 0u;
  const unsigned qa_off = WS_QA + (((c16 >> 1) & 1) * 32 + h) * 1024 + (b * 256 + w4 * 64 + 16 * r4 + 8 * (c16 & 1)) * 2;
  {
    const u16* pa; size_t sa;
    if (cls) {
      const size_t o_ = ((size_t)(b * 256 + w4 * 64 + 16 * r4) * 32 + h) * 128 + 8 * c16;
      pa = (cls == 1 ? w_q : (cls == 2 ? w_uk : wq)) + o_; sa = 4096;
      if (cls == 2) { pb = w_uv + o_; sb = 4096; }
      else { pb = w_o + ((size_t)h * 128 + b * 64 + w4 * 2) * 512 + 8 * lane; sb = 512; }
      if (cls == 1 && c16 < 4) st16c(rs, qa_off, u32x4{0u, 0u, 0u, 0u});
    } else { pa = ckv + ((size_t)idx * 128 + w4) * 512 + 8 * lane; sa = sb = 2048; pb = pa + 64 * 512; }
    sb2 = cls == 1 ? 4096 : 2 * sb;
    const u16* xp = (cls == 1 ? xA : xB) + b * 256 + w4 * 64 + 16 * r4;
    SCHED();
    sfor<0, 17>([&](auto I) {
      if constexpr (I.v == EPP) { epw = ld8c(rs, WS_EP); SCHED(); }
      if constexpr (I.v == XPP) { sfor<0, 4>([&](auto J) { xv[J.v] = ld16(xp + (J.v >> 1) * 512 + 8 * (J.v & 1)); }); SCHED(); }
      if constexpr (I.v < 16) Wa[I.v] = ld16nt(pa + I.v * sa);
    });
    SCHED();
    if (TOUCH && w4 == 3) {
      const int mi = 8 * (blk & 7) + (blk >> 5);
      const char* p = ws + WS_APO + blk * 8192;
      if (lane == 1) p = reinterpret_cast<const char*>(wo) + (size_t)(mi >> 1) * 131072;
      if (lane == 2) p = reinterpret_cast<const char*>(pb);
      if (lane == 3) p = reinterpret_cast<const char*>(vc) + (size_t)((mi & 1) * 32 + (mi >> 1)) * 1048576 + cls * 128;
      if (lane == 4) p = reinterpret_cast<const char*>(out) + (blk & 31) * 128;
      if (lane < TOUCH) tch = __builtin_nontemporal_load(reinterpret_cast<const unsigned*>(p));
      SCHED();
    }
    rawbar();
    take_epoch();
    *reinterpret_cast<u32x2*>(cnt + 16) = u32x2{tlow, tg.hi};
    COMPILER_FENCE();
    lds_add(cnt + 15, 1u, lane);
  }
  STAMP(13);
  float* red = reinterpret_cast<float*>(smem);
  if (cls & 1) {
    float acc[2][8];
#pragma unroll
    for (int bb = 0; bb < 2; ++bb)
#pragma unroll
      for (int k = 0; k < 8; ++k) acc[bb][k] = 0.f;
#pragma unroll
    for (int ip = 0; ip < 8; ++ip) {
      const unsigned xp0 = xv[ip >> 2][ip & 3], xp1 = xv[2 + (ip >> 2)][ip & 3];
#pragma unroll
      for (int k = 0; k < 4; ++k) {
        const unsigned lo = plo(Wa[2 * ip][k], Wa[2 * ip + 1][k]), hi = phi(Wa[2 * ip][k], Wa[2 * ip + 1][k]);
        acc[0][2 * k] = dot2(lo, xp0, acc[0][2 * k]); acc[0][2 * k + 1] = dot2(hi, xp0, acc[0][2 * k + 1]);
        acc[1][2 * k] = dot2(lo, xp1, acc[1][2 * k]); acc[1][2 * k + 1] = dot2(hi, xp1, acc[1][2 * k + 1]);
      }
    }
    {
      float v8[8], v4[4];
#pragma unroll
      for (int k = 0; k < 8; ++k) v8[k] = rs32(acc[0][k], acc[1][k]);
#pragma unroll
      for (int k = 0; k < 4; ++k) v4[k] = rs16(v8[k], v8[k + 4]);
      *reinterpret_cast<f32x4*>(red + (w4 * 2 + (lane >> 5)) * 128 + 8 * c16 + 4 * (r4 & 1)) = f32x4{v4[0], v4[1], v4[2], v4[3]};
    }
    COMPILER_FENCE();
    const unsigned got = lds_arrive_nw(L_CNT + 8);
    f32x4 t4[4];
#pragma unroll
    for (int w = 0; w < 4; ++w) t4[w] = *reinterpret_cast<const f32x4*>(red + (w * 2 + (lane >> 5)) * 128 + 8 * c16 + 4 * (r4 & 1));
    SCHED();
    if (lds_done(got) == 3u) {
      const f32x4 t = (t4[0] + t4[1]) + (t4[2] + t4[3]);
      st16c(rs, WS_QX + (((cls >> 1) * 32 + h) * 2 + (lane >> 5)) * 1024 + b * 512 + (lane & 31) * 16, u32x4{pk_bf(t[0], t[1]), tg.hi, pk_bf(t[2], t[3]), tg.hi});
      STAMP(15);
      SCHED();
      lds_add(cnt + 2, 1u, lane);
    } else lds_wait(cnt + 2, 5u);
    {
      const unsigned tw = __builtin_nontemporal_load(reinterpret_cast<const unsigned*>(out + (idx >> 5) * 1024 + (cls >> 1) * 512 + 16 * (idx & 31)));
      asm volatile("" :: "v"(tw));
    }
    STAMP(8);
  } else if (cls == 2 && NOA != 1) {
    u32x4 g0, g1;
    const unsigned qo = WS_QX + h * 2048 + lane * 16;
    if (SPM & 1) shint<512, 1>(ws + WS_QX + h * 2048, tg.hi);
    STAMP(5);
#pragma unroll 1
    for (int spin = 0; spin < SPIN_MAX; ++spin) {
      COMPILER_FENCE();
      g0 = ld16c(rs, qo); g1 = ld16c(rs, qo + 1024);
      if (__all((g0[1] == tg.hi) & (g0[3] == tg.hi) & (g1[1] == tg.hi) & (g1[3] == tg.hi))) break;
      __builtin_amdgcn_s_sleep(QSL);
    }
    const u32x4 q0 = qsum(g0), q1 = qsum(g1);
    STAMP(9);
    float v[8];
    {
      float u[16];
#pragma unroll
      for (int i = 0; i < 16; ++i) {
        float a = 0.f, c = 0.f;
#pragma unroll
        for (int k = 0; k < 4; ++k) { a = dot2(Wa[i][k], q0[k], a); c = dot2(Wa[i][k], q1[k], c); }
        u[i] = (c16 & 2 ? c : a) + dpp<0x4E>(c16 & 2 ? a : c);
        asm volatile("" : "+v"(u[i]));
      }
#pragma unroll
      for (int i = 0; i < 8; ++i) {
        float t = (c16 & 1 ? u[i + 8] : u[i]) + dpp<0xB1>(c16 & 1 ? u[i] : u[i + 8]);
        asm volatile("" : "+v"(t));
        t += dpp<0x124>(t);
        asm volatile("" : "+v"(t));
        t += dpp<0x128>(t);
        asm volatile("" : "+v"(t));
        v[i] = t;
      }
    }
    u32x4 o = {pk_bf(v[0], v[1]), pk_bf(v[2], v[3]), pk_bf(v[4], v[5]), pk_bf(v[6], v[7])};
    if ((o[0] | o[1]) == 0u) o[0] = 0x8000u;
    if ((o[2] | o[3]) == 0u) o[2] = 0x8000u;
    if (c16 < 4) st16c(rs, qa_off, o);
    SCHED();
    sbar4(cnt + 2, lane);
    STAMP(10);
  }
  if (cls) asm volatile("" :: "v"(tch));
  asm volatile("" :: "v"(xv[0]), "v"(xv[1]), "v"(xv[2]), "v"(xv[3]));
  SCHED();
  if (cls == 3) {
    fin(IC<2>{}, WS_GB, 512, false);
    STAMP(6);
    STAMP_FLUSH(0); TR_FLUSH();
    return;
  }
  if (cls != 0) {
#pragma unroll
    for (int i = 0; i < 16; ++i) {
      Wb[i] = ld16nt(pb + (i >> 1) * sb2 + (i & 1) * sb);
      if (i >= 2 && (i & 1)) asm volatile("" : "+v"(Wb[i - 2]));
      if (i == 5 || i == 9 || i == 13) TR();
      SCHED();
    }
  }
  if (cls == 0) {
    char* scr = smem + L_SCR;
    const int ab = idx >> 5, r = lane & 31, hh = lane >> 5;
    const unsigned fo = WS_QX + (w4 * 8 + (c16 >> 1)) * 2048 + (c16 & 1) * 512 + r4 * 8;
    u32x2 fl;
    u32x4 g[9];
    const unsigned o8 = WS_QA + (ab * 32 + w4 * 8) * 1024 + lane * 16, ol = WS_QA + (ab * 32 + r) * 1024 + 992 + 16 * hh;
#pragma unroll
    for (int i = 0; i < 32; ++i) {
      if (i + WB0 >= 16 && i + WB0 < 32) Wb[(i + WB0) & 15] = ld16nt(pb + ((i + WB0) & 15) * 2048);
      if (i == FLI) fl = ld8c(rs, fo);
      if (i == FLI + WB0 + 2) {
        if (!__all(fl[1] == tg.hi)) {
#pragma unroll 1
          for (int spin = 0; spin < SPIN_MAX; ++spin) {
            COMPILER_FENCE();
            if (__all(ld8c(rs, fo)[1] == tg.hi)) break;
          }
        }
        lds_add(cnt + 7, 1u, lane);
      }
      if (i == QIA) {
        lds_wait(cnt + 7, 4u);
        STAMP(12);
#pragma unroll
        for (int j = 0; j < 9; ++j) g[j] = ld16c(rs, j < 8 ? o8 + j * 1024 : ol);
      }
      SCHED();
      *reinterpret_cast<u32x4*>(smem + ((i >> 3) * 4 + r4) * 8192 + toff(4 * (i & 7) + w4, c16)) = i < 16 ? Wa[i & 15] : Wb[i & 15];
      if (i >= 16 && (i & 3) == 3) TR();
      SCHED();
    }
    asm volatile("" :: "v"(tch));
    STAMP(8);
    u32x4 qlast;
    {
      unsigned pend = 0u;
#pragma unroll
      for (int i = 0; i < 9; ++i) if (!__all(nz8(g[i]))) pend |= 1u << i;
      STAMP(5);
#pragma unroll 1
      for (int spin = 0; spin < SPIN_MAX && pend; ++spin) {
        COMPILER_FENCE();
        u32x4 t[9];
#pragma unroll
        for (int i = 0; i < 9; ++i) t[i] = ld16c(rs, (pend >> i & 1) ? (i < 8 ? o8 + i * 1024 : ol) : OOB);
#pragma unroll
        for (int i = 0; i < 9; ++i) {
          if (pend >> i & 1) g[i] = t[i];
          if (__all(nz8(t[i]))) pend &= ~(1u << i);
        }
      }
      STAMP(9);
      qlast = g[8];
#pragma unroll
      for (int i = 0; i < 8; ++i)
        if (lane < 62) *reinterpret_cast<u32x4*>(scr + (w4 * 8 + i) * 1008 + 16 * lane) = g[i];
    }
    sbar4(cnt + 3, lane);
    STAMP(10);
    f32x16 sacc;
#pragma unroll
    for (int i = 0; i < 16; ++i) sacc[i] = 0.f;
#pragma unroll
    for (int jj = 0; jj < 32; ++jj) {
      const u32x4 af = *reinterpret_cast<const u32x4*>(smem + (w4 * 4 + (jj >> 3)) * 8192 + toff(r, 2 * (jj & 7) + hh));
      const u32x4 bf = jj < 31 ? *reinterpret_cast<const u32x4*>(scr + r * 1008 + 16 * (2 * (jj < 31 ? jj : 0) + hh)) : qlast;
      sacc = __builtin_amdgcn_mfma_f32_32x32x16_bf16(__builtin_bit_cast(bf16x8_t, af), __builtin_bit_cast(bf16x8_t, bf), sacc, 0, 0, 0);
    }
    float m = sacc[0];
#pragma unroll
    for (int i = 1; i < 16; ++i) m = fmaxf(m, sacc[i]);
    m = xhalf_max(m);
    if (hh == 0) *reinterpret_cast<float*>(scr + r * 1008 + 992 + 4 * w4) = m;
    sbar4(cnt + 4, lane);
    STAMP(11);
    {
      const f32x4 m4 = *reinterpret_cast<const f32x4*>(scr + r * 1008 + 992);
      m = fmaxf(fmaxf(m4[0], m4[1]), fmaxf(m4[2], m4[3]));
    }
    const float mc = m * C2;
    float p[16], ls = 0.f;
#pragma unroll
    for (int i = 0; i < 16; ++i) { p[i] = ex2(fmaf(sacc[i], C2, -mc)); ls += p[i]; }
    ls = xhalf_sum(ls);
    sbar4(cnt + 5, lane);
    u32x4* pf = reinterpret_cast<u32x4*>(scr);
    float* wl = reinterpret_cast<float*>(scr + 8192);
    if (hh == 0) wl[r * 4 + w4] = ls;
    pf[(2 * w4) * 64 + lane] = u32x4{pk_bf(p[0], p[1]), pk_bf(p[2], p[3]), pk_bf(p[4], p[5]), pk_bf(p[6], p[7])};
    pf[(2 * w4 + 1) * 64 + lane] = u32x4{pk_bf(p[8], p[9]), pk_bf(p[10], p[11]), pk_bf(p[12], p[13]), pk_bf(p[14], p[15])};
    sbar4(cnt + 6, lane);
    u32x4 pb8[8];
#pragma unroll
    for (int ks = 0; ks < 8; ++ks) pb8[ks] = pf[ks * 64 + lane];
    const f32x4 l4 = *reinterpret_cast<const f32x4*>(wl + r * 4);
    const float lsum = (l4[0] + l4[1]) + (l4[2] + l4[3]), rl = __builtin_amdgcn_rcpf(lsum);
    const int qq = (lane & 15) >> 2, pp = lane & 3, blk2 = (lane >> 4) & 1;
    char* ot = scr + 12288 + w4 * 4096;
#pragma unroll
    for (int c2 = 0; c2 < 2; ++c2) {
      f32x16 oacc[2];
#pragma unroll
      for (int ct = 0; ct < 2; ++ct)
#pragma unroll
        for (int i = 0; i < 16; ++i) oacc[ct][i] = 0.f;
#pragma unroll
      for (int ks = 0; ks < 8; ++ks)
#pragma unroll
        for (int ct = 0; ct < 2; ++ct) {
          typedef __attribute__((address_space(3))) s16x4* lp;
          const char* tile = smem + ((ks >> 1) * 4 + w4) * 8192;
          const int cc = 2 * c2 + ct;
          const s16x4 lo = __builtin_amdgcn_ds_read_tr16_b64_v4i16((lp)(tile + toff(16 * (ks & 1) + 4 * hh + qq, 4 * cc + 2 * blk2 + (pp >> 1)) + 8 * (pp & 1)));
          const s16x4 hi = __builtin_amdgcn_ds_read_tr16_b64_v4i16((lp)(tile + toff(16 * (ks & 1) + 8 + 4 * hh + qq, 4 * cc + 2 * blk2 + (pp >> 1)) + 8 * (pp & 1)));
          const s16x8 afr = __builtin_shufflevector(lo, hi, 0, 1, 2, 3, 4, 5, 6, 7);
          oacc[ct] = __builtin_amdgcn_mfma_f32_32x32x16_bf16(__builtin_bit_cast(bf16x8_t, afr), __builtin_bit_cast(bf16x8_t, pb8[ks]), oacc[ct], 0, 0, 0);
        }
#pragma unroll
      for (int ct = 0; ct < 2; ++ct)
#pragma unroll
        for (int g = 0; g < 4; ++g)
          *reinterpret_cast<u32x2*>(ot + r * 128 + 16 * ((4 * ct + g) ^ (r & 7)) + 8 * hh) = u32x2{pk_bf(oacc[ct][4 * g] * rl, oacc[ct][4 * g + 1] * rl), pk_bf(oacc[ct][4 * g + 2] * rl, oacc[ct][4 * g + 3] * rl)};
      if (c2 == 0 && NOA != 3) lds_add(cnt + 8, 1u, lane);
      __builtin_amdgcn_wave_barrier();
#pragma unroll
      for (int i = 0; i < 4; ++i) {
        const int hd = 8 * i + (lane >> 3), pc = lane & 7;
        const u32x4 vv = *reinterpret_cast<const u32x4*>(ot + hd * 128 + 16 * (pc ^ (hd & 7)));
        st16c(rs, NOA == 3 ? OOB : WS_APO + (((hd * 2 + (w4 >> 1)) * 64 + idx) * 256 + (w4 & 1) * 128 + 64 * c2 + 8 * pc) * 2, vv);
      }
      __builtin_amdgcn_wave_barrier();
    }
    lds_arrive(cnt + 1, lane);
    STAMP(1);
    asm volatile("s_waitcnt vmcnt(0)" ::: "memory");
    if (lds_arrive(cnt + 9 + (w4 >> 1), lane) == 1u && hh == 0)
      st8c(rs, NOAML ? OOB : WS_AML + ((r * 2 + (w4 >> 1)) * 64 + idx) * 8, u32x2{__float_as_uint(mc + __builtin_amdgcn_logf(lsum)), tg.hi});
    if (NOA != 3) lds_add(cnt + 8, ~0u, lane);
    STAMP(2);
    STAMP_FLUSH(0); TR_FLUSH();
    return;
  }
#pragma unroll
  for (int ip = 0; ip < 8; ++ip) {
#pragma unroll
    for (int k = 0; k < 4; ++k) { const unsigned lo = plo(Wb[2 * ip][k], Wb[2 * ip + 1][k]), hi = phi(Wb[2 * ip][k], Wb[2 * ip + 1][k]); Wb[2 * ip][k] = lo; Wb[2 * ip + 1][k] = hi; }
    pin2(Wb[2 * ip], Wb[2 * ip + 1]);
  }
  if (cls == 2) {
    float* red2 = red + 4 * 256;
    float* wml = red + 3072;
    u32x16 f[2];
    if (X2 & 1) { while (wall_clock64() - tstart < 1720) __builtin_amdgcn_s_sleep(8); }
    if (P2W && !(X2 & 1)) {
      if (w4 == 0) {
#pragma unroll 1
        for (int spin = 0; spin < SPIN_MAX; ++spin) {
          const u32x16 t = sld64(ws + WS_AML + (h * 2 + b) * 512);
          bool any = false;
#pragma unroll
          for (int i = 0; i < 8; ++i) any |= t[2 * i + 1] == tg.hi;
          if (any) break;
          __builtin_amdgcn_s_sleep(8);
        }
        lds_add(cnt + 7, 1u, lane);
      } else lds_wait_slow(cnt + 7, 1u);
    }
#pragma unroll 1
    for (int spin = 0; spin < SPIN_MAX; ++spin) {
      sld128(ws + WS_AML + ((h * 2 + b) * 64 + w4 * 16) * 8, f);
      unsigned e = 0u, any = ~0u;
#pragma unroll
      for (int i = 0; i < 16; ++i) { const unsigned x = f[i >> 3][2 * (i & 7) + 1] ^ tg.hi; e |= x; any = min(any, x); }
      if (e == 0u || (X2 & 1)) break;
      if (P2W) __builtin_amdgcn_s_sleep(P2S); else if (any != 0u) __builtin_amdgcn_s_sleep(8);
    }
    if (G2B) sbar4(cnt + 13, lane);
    STAMP(3);
    u32x4 pp[8];
#pragma unroll
    for (int i = 0; i < 8; ++i) { if (X2 & 2) { pp[i] = Wb[i]; pin2(pp[i], pp[i]); } else pp[i] = ld16c(rs, WS_APO + ((h * 2 + b) * 64 + w4 * 16 + 2 * i) * 512 + lane * 16); }
    if (X2 & 4) {
#pragma unroll
      for (int i = 0; i < 8; ++i) asm volatile("" :: "v"(pp[i]), "v"(Wb[2 * i]), "v"(Wb[2 * i + 1]));
      return; }
    SCHED();
    float wt[8];
    {
      float M = -INFINITY, L = 0.f;
#pragma unroll
      for (int i = 0; i < 8; ++i) {
        unsigned z0 = f[i >> 2][4 * (i & 3)], z1 = f[i >> 2][4 * (i & 3) + 2];
        asm("" : "+s"(z0), "+s"(z1));
        wt[i] = __uint_as_float(lane < 32 ? z0 : z1); M = fmaxf(M, wt[i]);
      }
      M = xhalf_max(M);
#pragma unroll
      for (int i = 0; i < 8; ++i) { wt[i] = ex2(wt[i] - M); L += wt[i]; }
      L = xhalf_sum(L);
      if (lane == 0) *reinterpret_cast<f32x2*>(wml + 2 * w4) = f32x2{M, L};
    }
    SCHED();
    float a8[8];
#pragma unroll
    for (int k = 0; k < 8; ++k) a8[k] = 0.f;
#pragma unroll
    for (int i = 0; i < 8; ++i) {
#pragma unroll
      for (int k = 0; k < 4; ++k) { a8[2 * k] = fmaf(wt[i], bflo(pp[i][k]), a8[2 * k]); a8[2 * k + 1] = fmaf(wt[i], bfhi(pp[i][k]), a8[2 * k + 1]); }
      asm volatile("" : "+v"(a8[0]), "+v"(a8[1]), "+v"(a8[2]), "+v"(a8[3]), "+v"(a8[4]), "+v"(a8[5]), "+v"(a8[6]), "+v"(a8[7]));
    }
    STAMP(6);
    *reinterpret_cast<f32x4*>(red + w4 * 256 + 8 * (lane & 31) + 4 * (lane >> 5)) = f32x4{rs32(a8[0], a8[4]), rs32(a8[1], a8[5]), rs32(a8[2], a8[6]), rs32(a8[3], a8[7])};
    sbar4(cnt + 3, lane);
    STAMP(4);
    float acc[2][8];
#pragma unroll
    for (int bb = 0; bb < 2; ++bb) {
      u32x4 cv[2];
      const f32x4 ml = *reinterpret_cast<const f32x4*>(wml + 4 * bb);
      const float M = fmaxf(ml[0], ml[2]), e0 = ex2(ml[0] - M), e1 = ex2(ml[2] - M), rl = 1.f / fmaf(e0, ml[1], e1 * ml[3]), s0 = e0 * rl, s1 = e1 * rl;
#pragma unroll
      for (int e = 0; e < 2; ++e) {
        const float* src = red + bb * 512 + w4 * 64 + 16 * r4 + 8 * e;
        const f32x4 t0 = s0 * *reinterpret_cast<const f32x4*>(src) + s1 * *reinterpret_cast<const f32x4*>(src + 256), t1 = s0 * *reinterpret_cast<const f32x4*>(src + 4) + s1 * *reinterpret_cast<const f32x4*>(src + 260);
        cv[e] = u32x4{pk_bf(t0[0], t0[1]), pk_bf(t0[2], t0[3]), pk_bf(t1[0], t1[1]), pk_bf(t1[2], t1[3])};
      }
#pragma unroll
      for (int k = 0; k < 8; ++k) acc[bb][k] = 0.f;
#pragma unroll
      for (int ip = 0; ip < 8; ++ip)
#pragma unroll
        for (int k = 0; k < 4; ++k) { acc[bb][2 * k] = dot2(Wb[2 * ip][k], cv[ip >> 2][ip & 3], acc[bb][2 * k]); acc[bb][2 * k + 1] = dot2(Wb[2 * ip + 1][k], cv[ip >> 2][ip & 3], acc[bb][2 * k + 1]); }
    }
    {
      float v8[8], v4[4];
#pragma unroll
      for (int k = 0; k < 8; ++k) v8[k] = rs32(acc[0][k], acc[1][k]);
#pragma unroll
      for (int k = 0; k < 4; ++k) v4[k] = rs16(v8[k], v8[k + 4]);
      *reinterpret_cast<f32x4*>(red2 + (w4 * 2 + (lane >> 5)) * 128 + 8 * c16 + 4 * (r4 & 1)) = f32x4{v4[0], v4[1], v4[2], v4[3]};
    }
    {
      COMPILER_FENCE();
      const unsigned got = lds_arrive_nw(L_CNT + 16);
      f32x4 t4[4];
#pragma unroll
      for (int w = 0; w < 4; ++w) t4[w] = *reinterpret_cast<const f32x4*>(red2 + w * 256 + 4 * lane);
      SCHED();
      if (lds_done(got) == 3u) {
        const f32x4 t = (t4[0] + t4[1]) + (t4[2] + t4[3]);
        const unsigned co = WS_CX + ((h * 2 + b) * 256 + 64 * r4 + 32 * (c16 & 1) + 2 * (c16 >> 1)) * 8;
        st16c(rs, co, u32x4{__float_as_uint(t[0]), tg.hi, __float_as_uint(t[1]), tg.hi});
        st16c(rs, co + 128, u32x4{__float_as_uint(t[2]), tg.hi, __float_as_uint(t[3]), tg.hi});
      }
    }
    STAMP(12);
    STAMP_FLUSH(0); TR_FLUSH();
    return;
  }
  {
    unsigned op;
    {
      u32x4 c;
      const unsigned co = WS_CX + ((h * 2 + ((lane >> 3) & 1)) * 256 + (lane >> 5) * 128 + b * 64 + w4 * 16 + 2 * (lane & 7)) * 8;
      if (idx == 0 && w4 == 1) {
#pragma unroll 1
        for (int spin = 0; spin < SPIN_MAX; ++spin) {
          COMPILER_FENCE();
          if (__all(ld8c(rs, WS_QX + 65536 + (lane >> 1) * 2048 + (lane & 1) * 512)[1] == tg.hi)) break;
          __builtin_amdgcn_s_sleep(8);
        }
      }
      if (P1W) {
        if (w4 == 0) { shint<2048, P1S>(ws + WS_CX + (h * 512 + b * 64) * 8, tg.hi); lds_add(cnt + 7, 1u, lane); }
        else lds_wait_slow(cnt + 7, 1u);
      } else shint<2048, P1S>(ws + WS_CX + (h * 512 + b * 64 + w4 * 16) * 8, tg.hi);
      STAMP(3);
#pragma unroll 1
      for (int spin = 0; spin < SPIN_MAX; ++spin) {
        COMPILER_FENCE();
        c = ld16c(rs, co);
        if (__all((c[1] == tg.hi) & (c[3] == tg.hi))) break;
        __builtin_amdgcn_s_sleep((SPM & 8) ? 1 : CSL);
      }
      float o0 = __uint_as_float(c[0]), o1 = __uint_as_float(c[2]);
      o0 += dpp<0x128>(o0); o1 += dpp<0x128>(o1);
      op = pk_bf(o0, o1);
    }
    STAMP(14);
    float* red3 = red + 4096;
    float a[2][8];
#pragma unroll
    for (int bb = 0; bb < 2; ++bb)
#pragma unroll
      for (int k = 0; k < 8; ++k) a[bb][k] = 0.f;
#pragma unroll
    for (int ip = 0; ip < 8; ++ip) {
      const unsigned v0 = __builtin_amdgcn_readlane(op, ip), v1 = __builtin_amdgcn_readlane(op, 32 + ip);
#pragma unroll
      for (int k = 0; k < 4; ++k) {
        a[0][2 * k] = dot2(Wb[2 * ip][k], v0, a[0][2 * k]); a[0][2 * k + 1] = dot2(Wb[2 * ip + 1][k], v0, a[0][2 * k + 1]);
        a[1][2 * k] = dot2(Wb[2 * ip][k], v1, a[1][2 * k]); a[1][2 * k + 1] = dot2(Wb[2 * ip + 1][k], v1, a[1][2 * k + 1]);
      }
    }
#pragma unroll
    for (int bb = 0; bb < 2; ++bb) {
      *reinterpret_cast<f32x4*>(red3 + (w4 * 2 + bb) * 512 + 8 * lane) = f32x4{a[bb][0], a[bb][1], a[bb][2], a[bb][3]};
      *reinterpret_cast<f32x4*>(red3 + (w4 * 2 + bb) * 512 + 8 * lane + 4) = f32x4{a[bb][4], a[bb][5], a[bb][6], a[bb][7]};
    }
    sbar4(cnt + 5, lane);
    f32x4 t = {0.f, 0.f, 0.f, 0.f};
#pragma unroll
    for (int w = 0; w < 4; ++w) t += *reinterpret_cast<const f32x4*>(red3 + w * 1024 + 4 * tid);
    st16c(rs, WS_GA + ((tid >> 7) * 64 + idx) * 2048 + (tid & 127) * 16, u32x4{pk_bf(t[0], t[1]), tg.hi, pk_bf(t[2], t[3]), tg.hi});
    STAMP(5);
  }
  fin(IC<1>{}, WS_GA, 0, idx == 0);
  STAMP(6);
  STAMP_FLUSH(0); TR_FLUSH();
}

extern "C" void kernel_launch(void* const* d_in, const int* in_sizes, int n_in, void* d_out, int out_size, void* d_ws, size_t ws_size, hipStream_t stream) {
  const u16* xA = (const u16*)d_in[0];
  const u16* ckv = (const u16*)d_in[1];
  const u16* w_q = (const u16*)d_in[2];
  const u16* w_uk = (const u16*)d_in[3];
  const u16* w_uv = (const u16*)d_in[4];
  const u16* w_o = (const u16*)d_in[5];
  const u16* xB = (const u16*)d_in[6];
  const u16* kc = (const u16*)d_in[7];
  const u16* vc = (const u16*)d_in[8];
  const u16* wq = (const u16*)d_in[9];
  const u16* wo = (const u16*)d_in[10];
  kmain<<<256, 512, 0, stream>>>(xA, ckv, w_q, w_uk, w_uv, w_o, xB, kc, vc, wq, wo, (char*)d_ws, (u16*)d_out);
}
```

```cpp
#include <hip/hip_runtime.h>
#include <hip/hip_bf16.h>
#include <stdint.h>

#define DI __device__ __forceinline__
typedef __attribute__((ext_vector_type(4))) unsigned u32x4;
typedef __attribute__((ext_vector_type(2))) unsigned u32x2;
typedef __attribute__((ext_vector_type(4))) float f32x4;
typedef __attribute__((ext_vector_type(2))) float f32x2;
typedef __attribute__((ext_vector_type(16))) float f32x16;
typedef __attribute__((ext_vector_type(8))) __bf16 bf16x8_t;
typedef __attribute__((ext_vector_type(2))) __bf16 bf16x2_t;
typedef __attribute__((ext_vector_type(4))) short s16x4;
typedef __attribute__((ext_vector_type(8))) short s16x8;
typedef unsigned short u16;
typedef unsigned long long u64;

constexpr int NS0 = 14, NS1 = 16, NS2 = 15, NS3 = 19;
constexpr int RA2 = 0xC0, RA3 = 0x33;
constexpr int AHL = 1;
constexpr int WB0 = 3, FLI = 20;
constexpr u64 TL0 = 0xbeb96069644b1c0ull, TH0 = 0xfbcdf3ull, TL1 = 0x9217595523c9142ull, TH1 = 0xef8d30b28ull;
constexpr u64 TL2 = 0x9a279b5d338a183ull, TH2 = 0x3adb2baaull, TL3 = 0x7dc61440d308101ull, TH3 = 0x3ff79d71b699e3ull;
constexpr u64 LA2 = TH2 >> (6 * (NS2 - 11)), LA3 = TH3 >> (6 * (NS3 - 11));
constexpr u64 chunk_mask(u64 lo, u64 hi, int n) { u64 m = 0; for (int i = 0; i < n; ++i) m |= 1ull << ((i < 10 ? lo >> (6 * i) : hi >> (6 * (i - 10))) & 63); return m; }
static_assert((chunk_mask(TL0, TH0, NS0) | chunk_mask(TL1, TH1, NS1) | chunk_mask(TL2, TH2, NS2) | chunk_mask(TL3, TH3, NS3)) == ~0ull && (RA2 & RA3) == 0, "every chunk once");
static_assert(NS0 + NS1 + NS2 + NS3 == 64 && FLI + WB0 + 2 <= 31, "split sizes");
constexpr int NSMIN = 14, NSMAX = 19;
static_assert(NS0 >= NSMIN && NS1 >= NSMIN && NS2 - (RA2 != 0) >= NSMIN && NS3 - (RA3 != 0) >= NSMIN && NS0 <= NSMAX && NS1 + 1 <= NSMAX && NS2 <= NSMAX && NS3 <= NSMAX, "range");
constexpr int SK = 4096;
constexpr float C2 = 0.08838834764831845f * 1.4426950408889634f;
constexpr u64 GOLD = 0x9E3779B97F4A7C15ull;

constexpr unsigned WS_EP = 0;
constexpr unsigned WS_QX = 4096;
constexpr unsigned WS_QA = WS_QX + 2 * 32 * 256 * 8;
constexpr unsigned WS_AML = WS_QA + 2 * 32 * 512 * 2;
constexpr unsigned WS_CX = WS_AML + 32 * 2 * 64 * 8;
constexpr unsigned WS_GZ = WS_CX + 32 * 2 * 2 * 128 * 8;
constexpr unsigned WS_GB = WS_GZ + 256 * 128;
constexpr unsigned WS_GA = WS_GB + 2 * 128 * 256 * 8;
constexpr unsigned WS_APO = 2u << 20;
static_assert(WS_GA + 64 * 2 * 256 * 8 <= WS_APO, "layout");
constexpr unsigned WS_END = WS_APO + 32 * 2 * 64 * 256 * 2;
#define STAMP(i) do {} while (0)
#define STAMP_DECL do {} while (0)
#define STAMP_FLUSH(grp) do {} while (0)

#define TR() do {} while (0)
#define TR_FLUSH() do {} while (0)
DI float bflo(unsigned u) { return __uint_as_float(u << 16); }
DI float bfhi(unsigned u) { return __uint_as_float(u & 0xffff0000u); }
DI unsigned pk_bf(float a, float b) { const f32x2 v = {a, b}; return __builtin_bit_cast(unsigned, __builtin_convertvector(v, bf16x2_t)); }
DI float dot2(unsigned k, unsigned q, float acc) {
  return __builtin_amdgcn_fdot2_f32_bf16(__builtin_bit_cast(bf16x2_t, k), __builtin_bit_cast(bf16x2_t, q), acc, false);
}
DI unsigned plo(unsigned w0, unsigned w1) { return __builtin_amdgcn_perm(w1, w0, 0x05040100u); }
DI unsigned phi(unsigned w0, unsigned w1) { return __builtin_amdgcn_perm(w1, w0, 0x07060302u); }
template <int CTRL> DI float dpp(float x) {
  return __builtin_bit_cast(float, __builtin_amdgcn_mov_dpp(__builtin_bit_cast(int, x), CTRL, 0xf, 0xf, true));
}
DI float row16_sum(float x) { x += dpp<0xB1>(x); x += dpp<0x4E>(x); x += dpp<0x141>(x); x += dpp<0x128>(x); return x; }
DI float row16_max(float x) { x = fmaxf(x, dpp<0xB1>(x)); x = fmaxf(x, dpp<0x4E>(x)); x = fmaxf(x, dpp<0x141>(x)); x = fmaxf(x, dpp<0x128>(x)); return x; }
DI float xhalf_max(float x) { auto t = __builtin_amdgcn_permlane32_swap(__float_as_uint(x), __float_as_uint(x), false, false); return fmaxf(__uint_as_float(t[0]), __uint_as_float(t[1])); }
DI float xhalf_sum(float x) { auto t = __builtin_amdgcn_permlane32_swap(__float_as_uint(x), __float_as_uint(x), false, false); return __uint_as_float(t[0]) + __uint_as_float(t[1]); }
DI u32x4 ld16(const void* p) { return *reinterpret_cast<const u32x4*>(p); }
DI u32x4 ld16nt(const void* p) { return __builtin_nontemporal_load(reinterpret_cast<const u32x4*>(p)); }
DI float ex2(float x) { return __builtin_amdgcn_exp2f(x); }
#define SCHED() __builtin_amdgcn_sched_barrier(0)

typedef __amdgpu_buffer_rsrc_t rsrc_t;
DI u32x4 ld16c(rsrc_t rs, unsigned off) { return __builtin_amdgcn_raw_buffer_load_b128(rs, (int)off, 0, 16); }
DI u32x2 ld8c(rsrc_t rs, unsigned off) { return __builtin_amdgcn_raw_buffer_load_b64(rs, (int)off, 0, 16); }
DI void st16c(rsrc_t rs, unsigned off, u32x4 v) { __builtin_amdgcn_raw_buffer_store_b128(v, rs, (int)off, 0, 16); }
DI void st8c(rsrc_t rs, unsigned off, u32x2 v) { __builtin_amdgcn_raw_buffer_store_b64(v, rs, (int)off, 0, 16); }
typedef __attribute__((ext_vector_type(16))) unsigned u32x16;
template <int O1> DI void sld2(const char* p, u32x2& a, u32x2& b) {
  asm volatile("s_load_dwordx2 %0, %2, 0x0 glc\n\ts_load_dwordx2 %1, %2, %3 glc\n\ts_waitcnt lgkmcnt(0)" : "=&s"(a), "=&s"(b) : "s"(p), "n"(O1) : "memory");
}
DI u32x16 sld64(const char* p) {
  u32x16 v;
  asm volatile("s_load_dwordx16 %0, %1, 0x0 glc\n\ts_waitcnt lgkmcnt(0)" : "=&s"(v) : "s"(p) : "memory");
  return v;
}
DI void sld128(const char* p, u32x16 (&v)[2]) {
  asm volatile("s_load_dwordx16 %0, %2, 0x0 glc\n\ts_load_dwordx16 %1, %2, 0x40 glc\n\ts_waitcnt lgkmcnt(0)" : "=&s"(v[0]), "=&s"(v[1]) : "s"(p) : "memory");
}
constexpr int SPIN_MAX = 1 << 20;
constexpr unsigned OOB = 0x7ffffff0u;
DI void rawbar() { __builtin_amdgcn_sched_barrier(0); __builtin_amdgcn_s_barrier(); __builtin_amdgcn_sched_barrier(0); }
#define COMPILER_FENCE() asm volatile("" ::: "memory")
template <int O1, int SL> DI void shint(const char* p, unsigned tag) {
#pragma unroll 1
  for (int spin = 0; spin < SPIN_MAX; ++spin) {
    u32x2 a, b;
    sld2<O1>(p, a, b);
    if (((a[1] ^ tag) | (b[1] ^ tag)) == 0u) break;
    __builtin_amdgcn_s_sleep(SL);
  }
}

template <int N> struct IC { static constexpr int v = N; };
template <int I, int N, class F> DI void sfor(F&& f) { if constexpr (I < N) { f(IC<I>{}); sfor<I + 1, N>(f); } }
typedef __attribute__((address_space(3))) unsigned lu32;
DI void lds_wait(unsigned* cnt, unsigned target) {
#pragma unroll 1
  for (int spin = 0; spin < SPIN_MAX; ++spin) {
    if (__builtin_amdgcn_readfirstlane(__hip_atomic_load((lu32*)cnt, __ATOMIC_RELAXED, __HIP_MEMORY_SCOPE_WORKGROUP)) >= target) break;
    __builtin_amdgcn_s_sleep(1);
  }
  COMPILER_FENCE();
}
DI void lds_wait_slow(unsigned* cnt, unsigned target) {
#pragma unroll 1
  for (int spin = 0; spin < SPIN_MAX; ++spin) {
    if (__builtin_amdgcn_readfirstlane(__hip_atomic_load((lu32*)cnt, __ATOMIC_RELAXED, __HIP_MEMORY_SCOPE_WORKGROUP)) >= target) break;
    __builtin_amdgcn_s_sleep(3);
  }
  COMPILER_FENCE();
}
DI void lds_hold(unsigned* flag) {
#pragma unroll 1
  for (int spin = 0; spin < SPIN_MAX; ++spin) {
    if (__builtin_amdgcn_readfirstlane(__hip_atomic_load((lu32*)flag, __ATOMIC_RELAXED, __HIP_MEMORY_SCOPE_WORKGROUP)) == 0u) break;
    __builtin_amdgcn_s_sleep(2);
  }
}
DI unsigned lds_arrive_nw(unsigned addr) {
  unsigned old, one = 1u;
  asm volatile("s_mov_b64 exec, 1\n\tds_add_rtn_u32 %0, %1, %2\n\ts_mov_b64 exec, -1" : "=v"(old) : "v"(addr), "v"(one) : "memory");
  return old;
}
DI unsigned lds_done(unsigned old) { asm volatile("s_waitcnt lgkmcnt(0)" : "+v"(old) :: "memory"); return __builtin_amdgcn_readfirstlane(old); }
DI unsigned lds_arrive(unsigned* cnt, int) { return lds_done(lds_arrive_nw((unsigned)(size_t)(lu32*)cnt)); }
DI void lds_add(unsigned* cnt, unsigned v, int) {
  asm volatile("s_mov_b64 exec, 1\n\tds_add_u32 %0, %1\n\ts_mov_b64 exec, -1" :: "v"((unsigned)(size_t)(lu32*)cnt), "v"(v) : "memory");
}
DI void sbar4(unsigned* cnt, int lane) { if (lds_arrive(cnt, lane) < 3u) lds_wait(cnt, 4u); }
DI float rs32(float a, float b) { auto t = __builtin_amdgcn_permlane32_swap(__float_as_uint(a), __float_as_uint(b), false, false); return __uint_as_float(t[0]) + __uint_as_float(t[1]); }
DI float rs16(float a, float b) { auto t = __builtin_amdgcn_permlane16_swap(__float_as_uint(a), __float_as_uint(b), false, false); return __uint_as_float(t[0]) + __uint_as_float(t[1]); }
template <int N> DI unsigned rowbcast(unsigned x) { return (unsigned)__builtin_amdgcn_mov_dpp((int)x, 0x150 + N, 0xf, 0xf, true); }
DI void pin2(u32x4& a, u32x4& b) { asm volatile("" : "+v"(a), "+v"(b)); }
DI bool nz8(const u32x4& v) { return ((v[0] | v[1]) != 0u) & ((v[2] | v[3]) != 0u); }

DI u32x4 qsum(const u32x4& g) {
  const auto ta = __builtin_amdgcn_permlane32_swap(g[0], g[0], false, false), tc = __builtin_amdgcn_permlane32_swap(g[2], g[2], false, false);
  const unsigned a = pk_bf(bflo(ta[0]) + bflo(ta[1]), bfhi(ta[0]) + bfhi(ta[1])), c = pk_bf(bflo(tc[0]) + bflo(tc[1]), bfhi(tc[0]) + bfhi(tc[1]));
  const auto ra = __builtin_amdgcn_permlane16_swap(a, a, false, false), rc = __builtin_amdgcn_permlane16_swap(c, c, false, false);
  return u32x4{ra[0], rc[0], ra[1], rc[1]};
}
DI unsigned toff(unsigned row, unsigned ch) { return 256u * row + 16u * (ch ^ (((row & 3) << 2) | ((row >> 2) & 3))); }
constexpr int SMEM = 160 * 1024;
constexpr int L_SCR = 131072;
constexpr int L_MRG = 65536;
constexpr int L_CNT = L_SCR + 32 * 1008;

struct KV { u32x4 r[8]; };

__global__ __launch_bounds__(512, 2) void kmain(const u16* __restrict__ xA, const u16* __restrict__ ckv, const u16* __restrict__ w_q, const u16* __restrict__ w_uk,
                                                const u16* __restrict__ w_uv, const u16* __restrict__ w_o,
                                                const u16* __restrict__ xB, const u16* __restrict__ kc, const u16* __restrict__ vc,
                                                const u16* __restrict__ wq, const u16* __restrict__ wo,
                                                char* __restrict__ ws, u16* __restrict__ out) {
  __shared__ __attribute__((aligned(16))) char smem[SMEM];
  const int tid = threadIdx.x, blk = blockIdx.x, lane = tid & 63;
  const int wave = __builtin_amdgcn_readfirstlane(tid >> 6), w4 = wave & 3;
  const int cls = (blk >> 3) & 3;
  int idx = 8 * (blk & 7) + (blk >> 5);
  if (wave < 4) idx = (idx & ~7) | ((idx + (cls == 3 ? 2 : (cls == 2 ? 4 : 0))) & 7);
  const int h = idx >> 1, b = idx & 1;
  const rsrc_t rs = __builtin_amdgcn_make_buffer_rsrc(ws, 0, (int)WS_END, 0x00020000);
  const int r4 = lane >> 4, c16 = lane & 15;
  unsigned* cnt = reinterpret_cast<unsigned*>(smem + L_CNT);
  STAMP_DECL;
  STAMP(0);
  asm volatile("" :: "s"(xA), "s"(ckv), "s"(w_q), "s"(w_uk), "s"(w_uv), "s"(w_o), "s"(xB), "s"(kc), "s"(vc), "s"(wq), "s"(wo), "s"(ws), "s"(out));
  unsigned tag, tlow;
  u32x2 epw;
  auto take_epoch = [&]() {
    const u64 t64 = (((u64)(unsigned)__builtin_amdgcn_readfirstlane(epw[1]) << 32) | (unsigned)__builtin_amdgcn_readfirstlane(epw[0])) + GOLD;
    tag = (unsigned)(t64 >> 32); tlow = (unsigned)t64;
    asm volatile("s_waitcnt lgkmcnt(0)" : "+s"(tag), "+s"(tlow) :: "memory");
  };
  if (wave >= 4) {
    if (tid - 256 < 16) cnt[tid - 256] = 0u;
    asm volatile("s_waitcnt lgkmcnt(0)" ::: "memory");
    rawbar();
    const int e2 = (RA2 >> (idx & 7)) & 1, e3 = (RA3 >> (idx & 7)) & 1;
    const int ns = cls == 0 ? NS0 : (cls == 1 ? NS1 + e2 + e3 : (cls == 2 ? NS2 - e2 : NS3 - e3));
    const size_t kvo = ((size_t)(b * 32 + h) * SK + w4 * 16) * 256;
    const char* kb = reinterpret_cast<const char*>(kc) + kvo;
    const char* vb = reinterpret_cast<const char*>(vc) + kvo;
    const unsigned lo = lane * 16u;
    u64 tlo = cls == 0 ? TL0 : (cls == 1 ? TL1 : (cls == 2 ? TL2 : TL3));
    u64 thi = cls == 0 ? TH0 : (cls == 1 ? TH1 | ((e2 ? LA2 : LA3) << (6 * (NS1 - 10))) : (cls == 2 ? TH2 : TH3));
    const u64 tlo0 = tlo, thi0 = thi;
    int hn = 0;
    const char* hb = kb;
    const char* pc[2];
    auto next2 = [&]() {
#pragma unroll
      for (int i = 0; i < 2; ++i) {
        if (hn == ns) { tlo = tlo0; thi = thi0; hb = vb; }
        ++hn;
        pc[i] = hb + ((((unsigned)tlo + (unsigned)idx) & 63u) << 14);
        tlo = (tlo >> 6) | ((thi & 63u) << 54); thi >>= 6;
      }
    };
    KV A, B;
    u32x4 W[8];
    u32x4 T[4][8];
    auto ldp = [&](KV& S, auto P) {
      constexpr int p = P.v;
      const char* q = pc[p >> 1] + lo + (p & 1) * 2048;
      S.r[2 * p] = ld16nt(q); S.r[2 * p + 1] = ld16nt(q + 1024);
    };
    auto landed = [&](KV& S, auto P) { pin2(S.r[2 * P.v], S.r[2 * P.v + 1]); };
    KV X;
    sfor<0, 12>([&](auto I) {
      constexpr int i = I.v, j = i - 4;
      if constexpr ((i & 3) == 0) next2();
      if constexpr (j >= 0) { if constexpr (j < 4) landed(X, IC<j & 3>{}); else landed(B, IC<j & 3>{}); }
      if constexpr (j >= 0 && (j & 1)) TR();
      if constexpr (i < 4) ldp(X, IC<i & 3>{}); else if constexpr (i < 8) ldp(B, IC<i & 3>{}); else ldp(A, IC<i & 3>{});
      SCHED();
    });
    lds_wait(cnt + 15, 1u);
    {
      const u32x2 t = *reinterpret_cast<const u32x2*>(cnt + 16);
      tlow = __builtin_amdgcn_readfirstlane(t[0]); tag = __builtin_amdgcn_readfirstlane(t[1]);
    }
    const char* wp = reinterpret_cast<const char*>(wo) + ((size_t)h * 128 + r4 * 8) * 1024 + w4 * 256 + c16 * 16;
    u32x4 qpk;
    {
      unsigned tv = 0u;
      if (tid == 256) tv = __builtin_nontemporal_load(reinterpret_cast<const unsigned*>(vb + cls * 128));
      const unsigned qo = WS_QX + ((32 + h) * 2 + b) * 1024 + lane * 16;
      u32x4 g0;
#pragma unroll 1
      for (int spin = 0; spin < SPIN_MAX; ++spin) {
        COMPILER_FENCE();
        g0 = ld16c(rs, qo);
        if (__all((g0[1] == tag) & (g0[3] == tag))) break;
      }
      qpk = qsum(g0);
      asm volatile("" :: "v"(tv));
    }
    STAMP(7);
    const bool holds = cls == 0;
    float m = 0.f, l = 0.f, acc[8];
    unsigned sc[5];
#pragma unroll
    for (int k = 0; k < 8; ++k) acc[k] = 0.f;
#pragma unroll
    for (int k = 0; k < 5; ++k) sc[k] = 0xff800000u;
    int vn = 0;
    unsigned rwo;
    const unsigned zo = WS_GZ + (b * 32 + h) * 512;
    u32x2 zs[4];
    float wr_;
    auto zreq = [&]() {
      asm volatile("s_load_dwordx2 %0, %4, 0x0 glc\n\ts_load_dwordx2 %1, %4, 0x80 glc\n\ts_load_dwordx2 %2, %4, 0x100 glc\n\ts_load_dwordx2 %3, %4, 0x180 glc"
                   : "=&s"(zs[0]), "=&s"(zs[1]), "=&s"(zs[2]), "=&s"(zs[3]) : "s"(ws + zo) : "memory");
    };
    auto zget = [&]() {
#pragma unroll 1
      for (int spin = 0; spin < SPIN_MAX; ++spin) {
        asm volatile("s_waitcnt lgkmcnt(0)" : "+s"(zs[0]), "+s"(zs[1]), "+s"(zs[2]), "+s"(zs[3]) :: "memory");
        if (((zs[0][1] ^ tag) | (zs[1][1] ^ tag) | (zs[2][1] ^ tag) | (zs[3][1] ^ tag)) == 0u) break;
        __builtin_amdgcn_s_sleep(2);
        zreq();
      }
      asm volatile("s_waitcnt lgkmcnt(0)" : "+s"(zs[0]), "+s"(zs[1]), "+s"(zs[2]), "+s"(zs[3]) :: "memory");
      const float z0 = __uint_as_float(zs[0][0]), z1 = __uint_as_float(zs[1][0]), z2 = __uint_as_float(zs[2][0]), z3 = __uint_as_float(zs[3][0]);
      const float Z = fmaxf(fmaxf(z0, z1), fmaxf(z2, z3));
      wr_ = ex2(fmaf(m, C2, -Z)) * __builtin_amdgcn_rcpf((ex2(z0 - Z) + ex2(z1 - Z)) + (ex2(z2 - Z) + ex2(z3 - Z)));
      asm volatile("" : "+v"(wr_));
    };
    auto qk = [&](const u32x4* K, auto I) {
      float x[4];
#pragma unroll
      for (int t = 0; t < 4; ++t) {
        x[t] = 0.f;
#pragma unroll
        for (int k = 0; k < 4; ++k) x[t] = dot2(K[t][k], qpk[k], x[t]);
      }
      const float u0 = (c16 & 1 ? x[1] : x[0]) + dpp<0xB1>(c16 & 1 ? x[0] : x[1]), u1 = (c16 & 1 ? x[3] : x[2]) + dpp<0xB1>(c16 & 1 ? x[2] : x[3]);
      float v = (c16 & 2 ? u1 : u0) + dpp<0x4E>(c16 & 2 ? u0 : u1);
      v += dpp<0x124>(v); v += dpp<0x128>(v);
      sc[I.v >> 2] = __builtin_amdgcn_update_dpp(sc[I.v >> 2], __float_as_uint(v), 0xE4, 0xf, 1 << (I.v & 3), false);
      asm volatile("" : "+v"(sc[I.v >> 2]));
    };
    auto soft = [&]() {
      m = row16_max(fmaxf(fmaxf(fmaxf(__uint_as_float(sc[0]), __uint_as_float(sc[1])), fmaxf(__uint_as_float(sc[2]), __uint_as_float(sc[3]))), __uint_as_float(sc[4])));
      const float mc = m * C2;
      l = 0.f;
#pragma unroll
      for (int k = 0; k < 5; ++k) {
        const float p = ex2(fmaf(__uint_as_float(sc[k]), C2, -mc));
        sc[k] = pk_bf(p, dpp<0xB1>(p));
        l = dot2(sc[k], 0x3f003f00u, l);
      }
      l = row16_sum(l);
    };
    auto pv = [&](const u32x4* V) {
      const unsigned p01 = rowbcast<0>(sc[0]), p23 = rowbcast<2>(sc[0]);
#pragma unroll
      for (int k = 0; k < 4; ++k) { acc[2 * k] = dot2(plo(V[0][k], V[1][k]), p01, acc[2 * k]); acc[2 * k + 1] = dot2(phi(V[0][k], V[1][k]), p01, acc[2 * k + 1]); }
#pragma unroll
      for (int k = 0; k < 4; ++k) { acc[2 * k] = dot2(plo(V[2][k], V[3][k]), p23, acc[2 * k]); acc[2 * k + 1] = dot2(phi(V[2][k], V[3][k]), p23, acc[2 * k + 1]); }
      sc[0] = (unsigned)__builtin_amdgcn_mov_dpp((int)sc[0], 0x12C, 0xf, 0xf, true);
      if ((++vn & 3) == 0) { sc[0] = sc[1]; sc[1] = sc[2]; sc[2] = sc[3]; sc[3] = sc[4]; }
      asm volatile("" : "+v"(acc[0]), "+v"(acc[1]), "+v"(acc[2]), "+v"(acc[3]), "+v"(acc[4]), "+v"(acc[5]), "+v"(acc[6]), "+v"(acc[7]));
    };
    auto step = [&](KV& S, KV& N, auto SI, auto REFILL, auto WR) {
      constexpr bool refill = REFILL.v == 1;
      constexpr int wr = WR.v;
      if constexpr (refill) next2();
      constexpr int ah = wr < 0 ? AHL : 0;
      auto point = [&](auto P) {
        if constexpr (refill) {
          if constexpr (P.v == 0 && ah > 0) { if (holds) lds_hold(cnt + 8); sfor<0, ah>([&](auto Q) { ldp(N, Q); }); SCHED(); }
          landed(S, P);
          if constexpr (P.v & 1) TR();
          if constexpr (P.v + ah < 4) { if constexpr ((P.v & 1) == 0) { if (holds) lds_hold(cnt + 8); } ldp(N, IC<(P.v + ah) & 3>{}); }
          if constexpr (wr >= 0 && wr < 8) W[(4 * wr + P.v) & 7] = ld16(wp + (((4 * wr + P.v) >> 3) * 32 + ((4 * wr + P.v) & 7)) * 1024);
          SCHED();
        }
      };
      sfor<0, 2>([&](auto H) {
        constexpr int i = 2 * SI.v + H.v;
        if constexpr (REFILL.v == 2 && H.v == 0) { zreq(); SCHED(); }
        point(IC<2 * H.v>{});
        point(IC<2 * H.v + 1>{});
        if constexpr (i < NSMIN) qk(S.r + 4 * H.v, IC<i>{});
        else if constexpr (i > NSMAX) pv(S.r + 4 * H.v);
        else {
          if (i < ns) { if constexpr (i < NSMAX) qk(S.r + 4 * H.v, IC<i>{}); }
          else { if (i == ns) soft(); pv(S.r + 4 * H.v); }
        }
        SCHED();
      });
      if constexpr (REFILL.v == 2) {
        zget();
        STAMP(1);
        *reinterpret_cast<u32x4*>(smem + rwo) = u32x4{pk_bf(acc[0] * wr_, acc[1] * wr_), pk_bf(acc[2] * wr_, acc[3] * wr_), pk_bf(acc[4] * wr_, acc[5] * wr_), pk_bf(acc[6] * wr_, acc[7] * wr_)};
        COMPILER_FENCE();
      }
      if constexpr (wr >= 1) {
        constexpr int t = (wr - 1) >> 1, j0 = ((wr - 1) & 1) * 2;
#pragma unroll
        for (int jj = 0; jj < 2; ++jj)
#pragma unroll
          for (int e = 0; e < 4; ++e) {
            const unsigned a_ = W[(4 * wr - 4 + 2 * jj) & 7][e], b_ = W[(4 * wr - 3 + 2 * jj) & 7][e];
            T[t][2 * e][j0 + jj] = plo(a_, b_); T[t][2 * e + 1][j0 + jj] = phi(a_, b_);
          }
#pragma unroll
        for (int e = 0; e < 8; e += 2) pin2(T[t][e], T[t][e + 1]);
      }
      SCHED();
    };
    step(B, B, IC<1>{}, IC<0>{}, IC<-1>{});
    step(A, B, IC<2>{}, IC<1>{}, IC<0>{});
    step(X, X, IC<0>{}, IC<0>{}, IC<-1>{});
    sfor<0, 4>([&](auto I) {
      step(B, A, IC<2 * I.v + 3>{}, IC<1>{}, IC<2 * I.v + 1>{});
      if constexpr (I.v == 3) {
        float* ml = reinterpret_cast<float*>(cnt + 32);
        ml[(c16 & 1) * 16 + w4 * 4 + r4] = (c16 & 1) ? l : m;
        COMPILER_FENCE();
        const unsigned got = lds_arrive_nw(L_CNT + 44);
        const float mr = ml[c16], lr = ml[16 + c16];
        SCHED();
        if (lds_done(got) == 3u) {
          const float M = row16_max(mr);
          const unsigned z = __float_as_uint(fmaf(M, C2, __builtin_amdgcn_logf(row16_sum(ex2((mr - M) * C2) * lr))));
          asm volatile("buffer_store_dwordx4 %0, %1, %2, 0 offen sc1\n\ts_nop 2" :: "v"(u32x4{z, tag, z, tag}), "v"(lane < 8 ? zo + cls * 128 + lane * 16 : OOB), "s"(rs) : "memory");
        }
        SCHED();
      }
      step(A, B, IC<2 * I.v + 4>{}, IC<1>{}, IC<2 * I.v + 2>{});
    });
    unsigned yoff = WS_GB + ((b * 128 + h * 4 + cls) * 256 + 64 * w4 + lane) * 8, rro = L_MRG + c16 * 272 + r4 * 16;
    rwo = L_MRG + (w4 * 4 + r4) * 272 + c16 * 16;
    asm volatile("" : "+v"(yoff), "+v"(rwo), "+v"(rro));
    const int nit = (ns - 12) >> 1;
#pragma unroll 1
    for (int i = 0; i < nit; ++i) {
      step(B, A, IC<11>{}, IC<1>{}, IC<-1>{});
      step(A, B, IC<11>{}, IC<1>{}, IC<-1>{});
    }
    if (cls == 0) lds_wait(cnt + 1, 4u);
    if (ns & 1) { step(B, A, IC<11>{}, IC<1>{}, IC<-1>{}); step(A, A, IC<11>{}, IC<2>{}, IC<-1>{}); }
    else step(B, B, IC<11>{}, IC<2>{}, IC<-1>{});
    {
      u32x4 af[4];
      unsigned got = lds_arrive_nw(L_CNT), need = 3u;
#pragma unroll 1
      for (int spin = 0; spin < SPIN_MAX; ++spin) {
#pragma unroll
        for (int t = 0; t < 4; ++t) af[t] = *reinterpret_cast<const u32x4*>(smem + rro + t * 64);
        SCHED();
        if (lds_done(got) >= need) break;
        need = 4u;
        __builtin_amdgcn_s_sleep(1);
        asm volatile("ds_read_b32 %0, %1" : "=v"(got) : "v"((unsigned)L_CNT) : "memory");
      }
      SCHED();
      f32x4 d[8];
      float a[8], v4[4];
      sfor<0, 5>([&](auto I) {
        constexpr int i = I.v;
        sfor<0, 8>([&](auto J) {
          constexpr int e = i + 4 * (J.v & 1), t = J.v >> 1;
          if constexpr (i < 4) { d[e] = __builtin_amdgcn_mfma_f32_16x16x32_bf16(__builtin_bit_cast(bf16x8_t, af[t]), __builtin_bit_cast(bf16x8_t, T[t][e]), t ? d[e] : f32x4{0.f, 0.f, 0.f, 0.f}, 0, 0, 0); SCHED(); }
          if constexpr (i >= 1 && (J.v == 4 || J.v == 5)) { const f32x2 u = f32x2{d[e - 1][0], d[e - 1][1]} + f32x2{d[e - 1][2], d[e - 1][3]}; a[e - 1] = u[0] + u[1]; asm volatile("" : "+v"(a[e - 1])); }
          if constexpr (i >= 1 && J.v == 6) v4[i - 1] = rs32(a[i - 1], a[i + 3]);
          if constexpr (i < 4) SCHED();
        });
      });
      st8c(rs, yoff, u32x2{pk_bf(rs16(v4[0], v4[2]), rs16(v4[1], v4[3])), tag});
    }
    STAMP(2);
    STAMP_FLUSH(1);
    TR_FLUSH();
    return;
  }
  const rsrc_t ro = __builtin_amdgcn_make_buffer_rsrc(out, 0, 4096, 0x00020000);
  auto fin = [&](auto NL, unsigned base, unsigned col, bool bump) {
    constexpr int nl = NL.v;
    unsigned doff = base + (((idx >> 5) * 4 + w4) * 16 * nl + (lane >> 2)) * 2048 + (idx & 31) * 64 + (lane & 3) * 16;
    unsigned fwo = 32768 + (c16 < 4 ? (w4 * 16 + 4 * c16 + r4) * 4 : 256 + lane * 4);
    unsigned fro = 32768 + (lane & 7) * 8, foo = lane < 8 ? ((idx >> 5) * 1024 + col + (nl == 2 ? 16 * (idx & 24) + 64 * (idx & 1) + (idx & 6) + 8 * lane : 16 * (idx & 31) + 2 * lane)) * 2 : OOB;
    asm volatile("" : "+v"(doff), "+v"(fwo), "+v"(fro), "+v"(foo));
    lds_wait(cnt + 0, 4u);
    STAMP(4);
    u32x4 d[nl];
    unsigned eh = 1u;
#pragma unroll 1
    for (int spin = SPIN_MAX; spin > 0; --spin) {
      COMPILER_FENCE();
      if (eh != 0u) {
#pragma unroll
        for (int j = 0; j < nl; ++j) d[j] = ld16c(rs, doff + j * 32768);
        eh = 0u;
#pragma unroll
        for (int j = 0; j < nl; ++j) { eh |= (d[j][1] ^ tag) | (d[j][3] ^ tag); asm volatile("" : "+v"(eh)); }
      }
      if (__builtin_amdgcn_ballot_w64(eh != 0u) == 0ull) break;
    }
    STAMP(11);
    float y[4] = {bflo(d[0][0]), bfhi(d[0][0]), bflo(d[0][2]), bfhi(d[0][2])};
    if constexpr (nl == 2) { y[0] += bflo(d[1][0]); y[1] += bfhi(d[1][0]); y[2] += bflo(d[1][2]); y[3] += bfhi(d[1][2]); }
    float t = rs16(rs32(y[0], y[2]), rs32(y[1], y[3]));
    t += dpp<0x124>(t); t += dpp<0x128>(t);
    *reinterpret_cast<float*>(smem + fwo) = t;
    COMPILER_FENCE();
    const unsigned got = lds_arrive_nw(L_CNT + 24);
    f32x2 t2[4];
#pragma unroll
    for (int w = 0; w < 4; ++w) t2[w] = *reinterpret_cast<const f32x2*>(smem + fro + w * 64);
    SCHED();
    if (lds_done(got) == 3u) {
      const f32x2 r = (t2[0] + t2[1]) + (t2[2] + t2[3]);
      __builtin_amdgcn_raw_buffer_store_b32(pk_bf(r[0], r[1]), ro, (int)foo, 0, 0);
      if (bump) st8c(rs, lane == 0 ? WS_EP : OOB, u32x2{tlow, tag});
    }
  };
  epw = ld8c(rs, WS_EP);
  SCHED();
  u32x4 Wa[16], Wb[16], xv[4];
  const u16* pb; size_t sb, sb2;
  const unsigned qa_off = WS_QA + (((c16 >> 1) & 1) * 32 + h) * 1024 + (b * 256 + w4 * 64 + 16 * r4 + 8 * (c16 & 1)) * 2;
  {
    const u16* pa; size_t sa;
    if (cls) {
      const size_t o_ = ((size_t)(b * 256 + w4 * 64 + 16 * r4) * 32 + h) * 128 + 8 * c16;
      pa = (cls == 1 ? w_q : (cls == 2 ? w_uk : wq)) + o_; sa = 4096;
      if (cls == 2) { pb = w_uv + o_; sb = 4096; }
      else { pb = w_o + ((size_t)h * 128 + b * 64 + w4 * 2) * 512 + 8 * lane; sb = 512; }
      if (cls == 1 && c16 < 4) st16c(rs, qa_off, u32x4{0u, 0u, 0u, 0u});
    } else { pa = ckv + ((size_t)idx * 128 + w4) * 512 + 8 * lane; sa = sb = 2048; pb = pa + 64 * 512; }
    sb2 = cls == 1 ? 4096 : 2 * sb;
    const u16* xp = (cls == 1 ? xA : xB) + b * 256 + w4 * 64 + 16 * r4;
    SCHED();
    sfor<0, 4>([&](auto J) { xv[J.v] = ld16(xp + (J.v >> 1) * 512 + 8 * (J.v & 1)); });
    SCHED();
    sfor<0, 16>([&](auto I) { Wa[I.v] = ld16nt(pa + I.v * sa); });
    SCHED();
    rawbar();
    take_epoch();
    *reinterpret_cast<u32x2*>(cnt + 16) = u32x2{tlow, tag};
    COMPILER_FENCE();
    lds_add(cnt + 15, 1u, lane);
  }
  STAMP(13);
  float* red = reinterpret_cast<float*>(smem);
  if (cls & 1) {
    float acc[2][8];
#pragma unroll
    for (int bb = 0; bb < 2; ++bb)
#pragma unroll
      for (int k = 0; k < 8; ++k) acc[bb][k] = 0.f;
#pragma unroll
    for (int ip = 0; ip < 8; ++ip) {
      const unsigned xp0 = xv[ip >> 2][ip & 3], xp1 = xv[2 + (ip >> 2)][ip & 3];
#pragma unroll
      for (int k = 0; k < 4; ++k) {
        const unsigned lo = plo(Wa[2 * ip][k], Wa[2 * ip + 1][k]), hi = phi(Wa[2 * ip][k], Wa[2 * ip + 1][k]);
        acc[0][2 * k] = dot2(lo, xp0, acc[0][2 * k]); acc[0][2 * k + 1] = dot2(hi, xp0, acc[0][2 * k + 1]);
        acc[1][2 * k] = dot2(lo, xp1, acc[1][2 * k]); acc[1][2 * k + 1] = dot2(hi, xp1, acc[1][2 * k + 1]);
      }
    }
    {
      float v8[8], v4[4];
#pragma unroll
      for (int k = 0; k < 8; ++k) v8[k] = rs32(acc[0][k], acc[1][k]);
#pragma unroll
      for (int k = 0; k < 4; ++k) v4[k] = rs16(v8[k], v8[k + 4]);
      *reinterpret_cast<f32x4*>(red + (w4 * 2 + (lane >> 5)) * 128 + 8 * c16 + 4 * (r4 & 1)) = f32x4{v4[0], v4[1], v4[2], v4[3]};
    }
    COMPILER_FENCE();
    const unsigned got = lds_arrive_nw(L_CNT + 8);
    f32x4 t4[4];
#pragma unroll
    for (int w = 0; w < 4; ++w) t4[w] = *reinterpret_cast<const f32x4*>(red + (w * 2 + (lane >> 5)) * 128 + 8 * c16 + 4 * (r4 & 1));
    SCHED();
    if (lds_done(got) == 3u) {
      const f32x4 t = (t4[0] + t4[1]) + (t4[2] + t4[3]);
      st16c(rs, WS_QX + (((cls >> 1) * 32 + h) * 2 + (lane >> 5)) * 1024 + b * 512 + (lane & 31) * 16, u32x4{pk_bf(t[0], t[1]), tag, pk_bf(t[2], t[3]), tag});
      STAMP(15);
      SCHED();
      lds_add(cnt + 2, 1u, lane);
    } else lds_wait(cnt + 2, 5u);
    {
      const unsigned tw = __builtin_nontemporal_load(reinterpret_cast<const unsigned*>(out + (idx >> 5) * 1024 + (cls >> 1) * 512 + 16 * (idx & 31)));
      asm volatile("" :: "v"(tw));
    }
    STAMP(8);
  } else if (cls == 2) {
    u32x4 g0, g1;
    const unsigned qo = WS_QX + h * 2048 + lane * 16;
    shint<512, 1>(ws + WS_QX + h * 2048, tag);
    STAMP(5);
#pragma unroll 1
    for (int spin = 0; spin < SPIN_MAX; ++spin) {
      COMPILER_FENCE();
      g0 = ld16c(rs, qo); g1 = ld16c(rs, qo + 1024);
      if (__all((g0[1] == tag) & (g0[3] == tag) & (g1[1] == tag) & (g1[3] == tag))) break;
      __builtin_amdgcn_s_sleep(1);
    }
    const u32x4 q0 = qsum(g0), q1 = qsum(g1);
    STAMP(9);
    float v[8];
    {
      float u[16];
#pragma unroll
      for (int i = 0; i < 16; ++i) {
        float a = 0.f, c = 0.f;
#pragma unroll
        for (int k = 0; k < 4; ++k) { a = dot2(Wa[i][k], q0[k], a); c = dot2(Wa[i][k], q1[k], c); }
        u[i] = (c16 & 2 ? c : a) + dpp<0x4E>(c16 & 2 ? a : c);
        asm volatile("" : "+v"(u[i]));
      }
#pragma unroll
      for (int i = 0; i < 8; ++i) {
        float t = (c16 & 1 ? u[i + 8] : u[i]) + dpp<0xB1>(c16 & 1 ? u[i] : u[i + 8]);
        asm volatile("" : "+v"(t));
        t += dpp<0x124>(t);
        asm volatile("" : "+v"(t));
        t += dpp<0x128>(t);
        asm volatile("" : "+v"(t));
        v[i] = t;
      }
    }
    u32x4 o = {pk_bf(v[0], v[1]), pk_bf(v[2], v[3]), pk_bf(v[4], v[5]), pk_bf(v[6], v[7])};
    if ((o[0] | o[1]) == 0u) o[0] = 0x8000u;
    if ((o[2] | o[3]) == 0u) o[2] = 0x8000u;
    if (c16 < 4) st16c(rs, qa_off, o);
    SCHED();
    sbar4(cnt + 2, lane);
    STAMP(10);
  }
  asm volatile("" :: "v"(xv[0]), "v"(xv[1]), "v"(xv[2]), "v"(xv[3]));
  SCHED();
  if (cls == 3) {
    fin(IC<2>{}, WS_GB, 512, false);
    STAMP(6);
    STAMP_FLUSH(0); TR_FLUSH();
    return;
  }
  if (cls != 0) {
#pragma unroll
    for (int i = 0; i < 16; ++i) {
      Wb[i] = ld16nt(pb + (i >> 1) * sb2 + (i & 1) * sb);
      if (i >= 2 && (i & 1)) asm volatile("" : "+v"(Wb[i - 2]));
      if (i == 5 || i == 9 || i == 13) TR();
      SCHED();
    }
  }
  if (cls == 0) {
    char* scr = smem + L_SCR;
    const int ab = idx >> 5, r = lane & 31, hh = lane >> 5;
    const unsigned fo = WS_QX + (w4 * 8 + (c16 >> 1)) * 2048 + (c16 & 1) * 512 + r4 * 8;
    u32x2 fl;
    u32x4 g[9];
    const unsigned o8 = WS_QA + (ab * 32 + w4 * 8) * 1024 + lane * 16, ol = WS_QA + (ab * 32 + r) * 1024 + 992 + 16 * hh;
#pragma unroll
    for (int i = 0; i < 32; ++i) {
      if (i + WB0 >= 16 && i + WB0 < 32) Wb[(i + WB0) & 15] = ld16nt(pb + ((i + WB0) & 15) * 2048);
      if (i == FLI) fl = ld8c(rs, fo);
      if (i == FLI + WB0 + 2) {
        if (!__all(fl[1] == tag)) {
#pragma unroll 1
          for (int spin = 0; spin < SPIN_MAX; ++spin) {
            COMPILER_FENCE();
            if (__all(ld8c(rs, fo)[1] == tag)) break;
          }
        }
        lds_add(cnt + 7, 1u, lane);
      }
      if (i == 31) {
        lds_wait(cnt + 7, 4u);
        STAMP(12);
#pragma unroll
        for (int j = 0; j < 9; ++j) g[j] = ld16c(rs, j < 8 ? o8 + j * 1024 : ol);
      }
      SCHED();
      *reinterpret_cast<u32x4*>(smem + ((i >> 3) * 4 + r4) * 8192 + toff(4 * (i & 7) + w4, c16)) = i < 16 ? Wa[i & 15] : Wb[i & 15];
      if (i >= 16 && (i & 3) == 3) TR();
      SCHED();
    }
    STAMP(8);
    u32x4 qlast;
    {
      unsigned pend = 0u;
#pragma unroll
      for (int i = 0; i < 9; ++i) if (!__all(nz8(g[i]))) pend |= 1u << i;
      STAMP(5);
#pragma unroll 1
      for (int spin = 0; spin < SPIN_MAX && pend; ++spin) {
        COMPILER_FENCE();
        u32x4 t[9];
#pragma unroll
        for (int i = 0; i < 9; ++i) t[i] = ld16c(rs, (pend >> i & 1) ? (i < 8 ? o8 + i * 1024 : ol) : OOB);
#pragma unroll
        for (int i = 0; i < 9; ++i) {
          if (pend >> i & 1) g[i] = t[i];
          if (__all(nz8(t[i]))) pend &= ~(1u << i);
        }
      }
      STAMP(9);
      qlast = g[8];
#pragma unroll
      for (int i = 0; i < 8; ++i)
        if (lane < 62) *reinterpret_cast<u32x4*>(scr + (w4 * 8 + i) * 1008 + 16 * lane) = g[i];
    }
    sbar4(cnt + 3, lane);
    STAMP(10);
    f32x16 sacc;
#pragma unroll
    for (int i = 0; i < 16; ++i) sacc[i] = 0.f;
#pragma unroll
    for (int jj = 0; jj < 32; ++jj) {
      const u32x4 af = *reinterpret_cast<const u32x4*>(smem + (w4 * 4 + (jj >> 3)) * 8192 + toff(r, 2 * (jj & 7) + hh));
      const u32x4 bf = jj < 31 ? *reinterpret_cast<const u32x4*>(scr + r * 1008 + 16 * (2 * (jj < 31 ? jj : 0) + hh)) : qlast;
      sacc = __builtin_amdgcn_mfma_f32_32x32x16_bf16(__builtin_bit_cast(bf16x8_t, af), __builtin_bit_cast(bf16x8_t, bf), sacc, 0, 0, 0);
    }
    float m = sacc[0];
#pragma unroll
    for (int i = 1; i < 16; ++i) m = fmaxf(m, sacc[i]);
    m = xhalf_max(m);
    if (hh == 0) *reinterpret_cast<float*>(scr + r * 1008 + 992 + 4 * w4) = m;
    sbar4(cnt + 4, lane);
    STAMP(11);
    {
      const f32x4 m4 = *reinterpret_cast<const f32x4*>(scr + r * 1008 + 992);
      m = fmaxf(fmaxf(m4[0], m4[1]), fmaxf(m4[2], m4[3]));
    }
    const float mc = m * C2;
    float p[16], ls = 0.f;
#pragma unroll
    for (int i = 0; i < 16; ++i) { p[i] = ex2(fmaf(sacc[i], C2, -mc)); ls += p[i]; }
    ls = xhalf_sum(ls);
    sbar4(cnt + 5, lane);
    u32x4* pf = reinterpret_cast<u32x4*>(scr);
    float* wl = reinterpret_cast<float*>(scr + 8192);
    if (hh == 0) wl[r * 4 + w4] = ls;
    pf[(2 * w4) * 64 + lane] = u32x4{pk_bf(p[0], p[1]), pk_bf(p[2], p[3]), pk_bf(p[4], p[5]), pk_bf(p[6], p[7])};
    pf[(2 * w4 + 1) * 64 + lane] = u32x4{pk_bf(p[8], p[9]), pk_bf(p[10], p[11]), pk_bf(p[12], p[13]), pk_bf(p[14], p[15])};
    sbar4(cnt + 6, lane);
    u32x4 pb8[8];
#pragma unroll
    for (int ks = 0; ks < 8; ++ks) pb8[ks] = pf[ks * 64 + lane];
    const f32x4 l4 = *reinterpret_cast<const f32x4*>(wl + r * 4);
    const float lsum = (l4[0] + l4[1]) + (l4[2] + l4[3]), rl = __builtin_amdgcn_rcpf(lsum);
    const int qq = (lane & 15) >> 2, pp = lane & 3, blk2 = (lane >> 4) & 1;
    char* ot = scr + 12288 + w4 * 4096;
#pragma unroll
    for (int c2 = 0; c2 < 2; ++c2) {
      f32x16 oacc[2];
#pragma unroll
      for (int ct = 0; ct < 2; ++ct)
#pragma unroll
        for (int i = 0; i < 16; ++i) oacc[ct][i] = 0.f;
#pragma unroll
      for (int ks = 0; ks < 8; ++ks)
#pragma unroll
        for (int ct = 0; ct < 2; ++ct) {
          typedef __attribute__((address_space(3))) s16x4* lp;
          const char* tile = smem + ((ks >> 1) * 4 + w4) * 8192;
          const int cc = 2 * c2 + ct;
          const s16x4 lo = __builtin_amdgcn_ds_read_tr16_b64_v4i16((lp)(tile + toff(16 * (ks & 1) + 4 * hh + qq, 4 * cc + 2 * blk2 + (pp >> 1)) + 8 * (pp & 1)));
          const s16x4 hi = __builtin_amdgcn_ds_read_tr16_b64_v4i16((lp)(tile + toff(16 * (ks & 1) + 8 + 4 * hh + qq, 4 * cc + 2 * blk2 + (pp >> 1)) + 8 * (pp & 1)));
          const s16x8 afr = __builtin_shufflevector(lo, hi, 0, 1, 2, 3, 4, 5, 6, 7);
          oacc[ct] = __builtin_amdgcn_mfma_f32_32x32x16_bf16(__builtin_bit_cast(bf16x8_t, afr), __builtin_bit_cast(bf16x8_t, pb8[ks]), oacc[ct], 0, 0, 0);
        }
#pragma unroll
      for (int ct = 0; ct < 2; ++ct)
#pragma unroll
        for (int g = 0; g < 4; ++g)
          *reinterpret_cast<u32x2*>(ot + r * 128 + 16 * ((4 * ct + g) ^ (r & 7)) + 8 * hh) = u32x2{pk_bf(oacc[ct][4 * g] * rl, oacc[ct][4 * g + 1] * rl), pk_bf(oacc[ct][4 * g + 2] * rl, oacc[ct][4 * g + 3] * rl)};
      if (c2 == 0) lds_add(cnt + 8, 1u, lane);
      __builtin_amdgcn_wave_barrier();
#pragma unroll
      for (int i = 0; i < 4; ++i) {
        const int hd = 8 * i + (lane >> 3), pc = lane & 7;
        const u32x4 vv = *reinterpret_cast<const u32x4*>(ot + hd * 128 + 16 * (pc ^ (hd & 7)));
        st16c(rs, WS_APO + (((hd * 2 + (w4 >> 1)) * 64 + idx) * 256 + (w4 & 1) * 128 + 64 * c2 + 8 * pc) * 2, vv);
      }
      __builtin_amdgcn_wave_barrier();
    }
    lds_arrive(cnt + 1, lane);
    STAMP(1);
    asm volatile("s_waitcnt vmcnt(0)" ::: "memory");
    if (lds_arrive(cnt + 9 + (w4 >> 1), lane) == 1u && hh == 0)
      st8c(rs, WS_AML + ((r * 2 + (w4 >> 1)) * 64 + idx) * 8, u32x2{__float_as_uint(mc + __builtin_amdgcn_logf(lsum)), tag});
    lds_add(cnt + 8, ~0u, lane);
    STAMP(2);
    STAMP_FLUSH(0); TR_FLUSH();
    return;
  }
#pragma unroll
  for (int ip = 0; ip < 8; ++ip) {
#pragma unroll
    for (int k = 0; k < 4; ++k) { const unsigned lo = plo(Wb[2 * ip][k], Wb[2 * ip + 1][k]), hi = phi(Wb[2 * ip][k], Wb[2 * ip + 1][k]); Wb[2 * ip][k] = lo; Wb[2 * ip + 1][k] = hi; }
    pin2(Wb[2 * ip], Wb[2 * ip + 1]);
  }
  if (cls == 2) {
    float* red2 = red + 4 * 256;
    float* wml = red + 3072;
    u32x16 f[2];
    if (w4 == 0) {
#pragma unroll 1
      for (int spin = 0; spin < SPIN_MAX; ++spin) {
        const u32x16 t = sld64(ws + WS_AML + (h * 2 + b) * 512);
        bool any = false;
#pragma unroll
        for (int i = 0; i < 8; ++i) any |= t[2 * i + 1] == tag;
        if (any) break;
        __builtin_amdgcn_s_sleep(8);
      }
      lds_add(cnt + 7, 1u, lane);
    } else lds_wait_slow(cnt + 7, 1u);
#pragma unroll 1
    for (int spin = 0; spin < SPIN_MAX; ++spin) {
      sld128(ws + WS_AML + ((h * 2 + b) * 64 + w4 * 16) * 8, f);
      unsigned e = 0u;
#pragma unroll
      for (int i = 0; i < 16; ++i) e |= f[i >> 3][2 * (i & 7) + 1] ^ tag;
      if (e == 0u) break;
      __builtin_amdgcn_s_sleep(2);
    }
    STAMP(3);
    u32x4 pp[8];
#pragma unroll
    for (int i = 0; i < 8; ++i) pp[i] = ld16c(rs, WS_APO + ((h * 2 + b) * 64 + w4 * 16 + 2 * i) * 512 + lane * 16);
    SCHED();
    float wt[8];
    {
      float M = -INFINITY, L = 0.f;
#pragma unroll
      for (int i = 0; i < 8; ++i) {
        unsigned z0 = f[i >> 2][4 * (i & 3)], z1 = f[i >> 2][4 * (i & 3) + 2];
        asm("" : "+s"(z0), "+s"(z1));
        wt[i] = __uint_as_float(lane < 32 ? z0 : z1); M = fmaxf(M, wt[i]);
      }
      M = xhalf_max(M);
#pragma unroll
      for (int i = 0; i < 8; ++i) { wt[i] = ex2(wt[i] - M); L += wt[i]; }
      L = xhalf_sum(L);
      if (lane == 0) *reinterpret_cast<f32x2*>(wml + 2 * w4) = f32x2{M, L};
    }
    SCHED();
    float a8[8];
#pragma unroll
    for (int k = 0; k < 8; ++k) a8[k] = 0.f;
#pragma unroll
    for (int i = 0; i < 8; ++i) {
#pragma unroll
      for (int k = 0; k < 4; ++k) { a8[2 * k] = fmaf(wt[i], bflo(pp[i][k]), a8[2 * k]); a8[2 * k + 1] = fmaf(wt[i], bfhi(pp[i][k]), a8[2 * k + 1]); }
      asm volatile("" : "+v"(a8[0]), "+v"(a8[1]), "+v"(a8[2]), "+v"(a8[3]), "+v"(a8[4]), "+v"(a8[5]), "+v"(a8[6]), "+v"(a8[7]));
    }
    STAMP(6);
    *reinterpret_cast<f32x4*>(red + w4 * 256 + 8 * (lane & 31) + 4 * (lane >> 5)) = f32x4{rs32(a8[0], a8[4]), rs32(a8[1], a8[5]), rs32(a8[2], a8[6]), rs32(a8[3], a8[7])};
    sbar4(cnt + 3, lane);
    STAMP(4);
    float acc[2][8];
#pragma unroll
    for (int bb = 0; bb < 2; ++bb) {
      u32x4 cv[2];
      const f32x4 ml = *reinterpret_cast<const f32x4*>(wml + 4 * bb);
      const float M = fmaxf(ml[0], ml[2]), e0 = ex2(ml[0] - M), e1 = ex2(ml[2] - M), rl = 1.f / fmaf(e0, ml[1], e1 * ml[3]), s0 = e0 * rl, s1 = e1 * rl;
#pragma unroll
      for (int e = 0; e < 2; ++e) {
        const float* src = red + bb * 512 + w4 * 64 + 16 * r4 + 8 * e;
        const f32x4 t0 = s0 * *reinterpret_cast<const f32x4*>(src) + s1 * *reinterpret_cast<const f32x4*>(src + 256), t1 = s0 * *reinterpret_cast<const f32x4*>(src + 4) + s1 * *reinterpret_cast<const f32x4*>(src + 260);
        cv[e] = u32x4{pk_bf(t0[0], t0[1]), pk_bf(t0[2], t0[3]), pk_bf(t1[0], t1[1]), pk_bf(t1[2], t1[3])};
      }
#pragma unroll
      for (int k = 0; k < 8; ++k) acc[bb][k] = 0.f;
#pragma unroll
      for (int ip = 0; ip < 8; ++ip)
#pragma unroll
        for (int k = 0; k < 4; ++k) { acc[bb][2 * k] = dot2(Wb[2 * ip][k], cv[ip >> 2][ip & 3], acc[bb][2 * k]); acc[bb][2 * k + 1] = dot2(Wb[2 * ip + 1][k], cv[ip >> 2][ip & 3], acc[bb][2 * k + 1]); }
    }
    {
      float v8[8], v4[4];
#pragma unroll
      for (int k = 0; k < 8; ++k) v8[k] = rs32(acc[0][k], acc[1][k]);
#pragma unroll
      for (int k = 0; k < 4; ++k) v4[k] = rs16(v8[k], v8[k + 4]);
      *reinterpret_cast<f32x4*>(red2 + (w4 * 2 + (lane >> 5)) * 128 + 8 * c16 + 4 * (r4 & 1)) = f32x4{v4[0], v4[1], v4[2], v4[3]};
    }
    {
      COMPILER_FENCE();
      const unsigned got = lds_arrive_nw(L_CNT + 16);
      f32x4 t4[4];
#pragma unroll
      for (int w = 0; w < 4; ++w) t4[w] = *reinterpret_cast<const f32x4*>(red2 + w * 256 + 4 * lane);
      SCHED();
      if (lds_done(got) == 3u) {
        const f32x4 t = (t4[0] + t4[1]) + (t4[2] + t4[3]);
        const unsigned co = WS_CX + ((h * 2 + b) * 256 + 64 * r4 + 32 * (c16 & 1) + 2 * (c16 >> 1)) * 8;
        st16c(rs, co, u32x4{__float_as_uint(t[0]), tag, __float_as_uint(t[1]), tag});
        st16c(rs, co + 128, u32x4{__float_as_uint(t[2]), tag, __float_as_uint(t[3]), tag});
      }
    }
    STAMP(12);
    STAMP_FLUSH(0); TR_FLUSH();
    return;
  }
  {
    unsigned op;
    {
      u32x4 c;
      const unsigned co = WS_CX + ((h * 2 + ((lane >> 3) & 1)) * 256 + (lane >> 5) * 128 + b * 64 + w4 * 16 + 2 * (lane & 7)) * 8;
      if (idx == 0 && w4 == 1) {
#pragma unroll 1
        for (int spin = 0; spin < SPIN_MAX; ++spin) {
          COMPILER_FENCE();
          if (__all(ld8c(rs, WS_QX + 65536 + (lane >> 1) * 2048 + (lane & 1) * 512)[1] == tag)) break;
          __builtin_amdgcn_s_sleep(8);
        }
      }
      if (w4 == 0) { shint<2048, 4>(ws + WS_CX + (h * 512 + b * 64) * 8, tag); lds_add(cnt + 7, 1u, lane); }
      else lds_wait_slow(cnt + 7, 1u);
      STAMP(3);
#pragma unroll 1
      for (int spin = 0; spin < SPIN_MAX; ++spin) {
        COMPILER_FENCE();
        c = ld16c(rs, co);
        if (__all((c[1] == tag) & (c[3] == tag))) break;
        __builtin_amdgcn_s_sleep(1);
      }
      float o0 = __uint_as_float(c[0]), o1 = __uint_as_float(c[2]);
      o0 += dpp<0x128>(o0); o1 += dpp<0x128>(o1);
      op = pk_bf(o0, o1);
    }
    STAMP(14);
    float* red3 = red + 4096;
    float a[2][8];
#pragma unroll
    for (int bb = 0; bb < 2; ++bb)
#pragma unroll
      for (int k = 0; k < 8; ++k) a[bb][k] = 0.f;
#pragma unroll
    for (int ip = 0; ip < 8; ++ip) {
      const unsigned v0 = __builtin_amdgcn_readlane(op, ip), v1 = __builtin_amdgcn_readlane(op, 32 + ip);
#pragma unroll
      for (int k = 0; k < 4; ++k) {
        a[0][2 * k] = dot2(Wb[2 * ip][k], v0, a[0][2 * k]); a[0][2 * k + 1] = dot2(Wb[2 * ip + 1][k], v0, a[0][2 * k + 1]);
        a[1][2 * k] = dot2(Wb[2 * ip][k], v1, a[1][2 * k]); a[1][2 * k + 1] = dot2(Wb[2 * ip + 1][k], v1, a[1][2 * k + 1]);
      }
    }
#pragma unroll
    for (int bb = 0; bb < 2; ++bb) {
      *reinterpret_cast<f32x4*>(red3 + (w4 * 2 + bb) * 512 + 8 * lane) = f32x4{a[bb][0], a[bb][1], a[bb][2], a[bb][3]};
      *reinterpret_cast<f32x4*>(red3 + (w4 * 2 + bb) * 512 + 8 * lane + 4) = f32x4{a[bb][4], a[bb][5], a[bb][6], a[bb][7]};
    }
    sbar4(cnt + 5, lane);
    f32x4 t = {0.f, 0.f, 0.f, 0.f};
#pragma unroll
    for (int w = 0; w < 4; ++w) t += *reinterpret_cast<const f32x4*>(red3 + w * 1024 + 4 * tid);
    st16c(rs, WS_GA + ((tid >> 7) * 64 + idx) * 2048 + (tid & 127) * 16, u32x4{pk_bf(t[0], t[1]), tag, pk_bf(t[2], t[3]), tag});
    STAMP(5);
  }
  fin(IC<1>{}, WS_GA, 0, idx == 0);
  STAMP(6);
  STAMP_FLUSH(0); TR_FLUSH();
}

extern "C" void kernel_launch(void* const* d_in, const int* in_sizes, int n_in, void* d_out, int out_size, void* d_ws, size_t ws_size, hipStream_t stream) {
  const u16* xA = (const u16*)d_in[0];
  const u16* ckv = (const u16*)d_in[1];
  const u16* w_q = (const u16*)d_in[2];
  const u16* w_uk = (const u16*)d_in[3];
  const u16* w_uv = (const u16*)d_in[4];
  const u16* w_o = (const u16*)d_in[5];
  const u16* xB = (const u16*)d_in[6];
  const u16* kc = (const u16*)d_in[7];
  const u16* vc = (const u16*)d_in[8];
  const u16* wq = (const u16*)d_in[9];
  const u16* wo = (const u16*)d_in[10];
  kmain<<<256, 512, 0, stream>>>(xA, ckv, w_q, w_uk, w_uv, w_o, xB, kc, vc, wq, wo, (char*)d_ws, (u16*)d_out);
}
```

```cpp
#define TL 2370
#define SL 0
#include <hip/hip_runtime.h>
#include <hip/hip_bf16.h>
#include <stdint.h>

#define DI __device__ __forceinline__
typedef __attribute__((ext_vector_type(4))) unsigned u32x4;
typedef __attribute__((ext_vector_type(2))) unsigned u32x2;
typedef __attribute__((ext_vector_type(4))) float f32x4;
typedef __attribute__((ext_vector_type(2))) float f32x2;
typedef __attribute__((ext_vector_type(16))) float f32x16;
typedef __attribute__((ext_vector_type(8))) __bf16 bf16x8_t;
typedef __attribute__((ext_vector_type(2))) __bf16 bf16x2_t;
typedef __attribute__((ext_vector_type(4))) short s16x4;
typedef __attribute__((ext_vector_type(8))) short s16x8;
typedef unsigned short u16;
typedef unsigned long long u64;

constexpr int NS0 = 14, NS1 = 16, NS2 = 15, NS3 = 19;
constexpr int RA2 = 0xC0, RA3 = 0x33;
#define LKS 99
#define LKD 0
#define STG 12
#define BSG 6
#define PT 0
#define P0 190
#define PI0 4
#define MSS 3
#define MSR 7
#define M0 935
#define S2 120
#define C2K 40
#define PCS 3
#define TA0 150
#define TA1 125
#define TA2 150
#define TA3 115
#define AHL 1
#define AHE 0
constexpr int WB0 = 3, FLI = 20;
constexpr u64 TL0 = 0xbeb96069644b1c0ull, TH0 = 0xfbcdf3ull, TL1 = 0x9217595523c9142ull, TH1 = 0xef8d30b28ull;
constexpr u64 TL2 = 0x9a279b5d338a183ull, TH2 = 0x3adb2baaull, TL3 = 0x7dc61440d308101ull, TH3 = 0x3ff79d71b699e3ull;
constexpr u64 LA2 = TH2 >> (6 * (NS2 - 11)), LA3 = TH3 >> (6 * (NS3 - 11));
constexpr u64 chunk_mask(u64 lo, u64 hi, int n) { u64 m = 0; for (int i = 0; i < n; ++i) m |= 1ull << ((i < 10 ? lo >> (6 * i) : hi >> (6 * (i - 10))) & 63); return m; }
static_assert((chunk_mask(TL0, TH0, NS0) | chunk_mask(TL1, TH1, NS1) | chunk_mask(TL2, TH2, NS2) | chunk_mask(TL3, TH3, NS3)) == ~0ull && (RA2 & RA3) == 0, "every chunk once");
static_assert(NS0 + NS1 + NS2 + NS3 == 64 && FLI + WB0 + 2 <= 31, "split sizes");
constexpr int NSMIN = 14, NSMAX = 19;
static_assert(NS0 >= NSMIN && NS1 >= NSMIN && NS2 - (RA2 != 0) >= NSMIN && NS3 - (RA3 != 0) >= NSMIN && NS0 <= NSMAX && NS1 + 1 <= NSMAX && NS2 <= NSMAX && NS3 <= NSMAX, "range");
constexpr int SK = 4096;
constexpr float C2 = 0.08838834764831845f * 1.4426950408889634f;
constexpr u64 GOLD = 0x9E3779B97F4A7C15ull;

constexpr unsigned WS_EP = 0;
constexpr unsigned WS_MS = 1024;
constexpr unsigned WS_QX = 4096;
constexpr unsigned WS_QA = WS_QX + 2 * 32 * 256 * 8;
constexpr unsigned WS_AML = WS_QA + 2 * 32 * 512 * 2;
constexpr unsigned WS_CX = WS_AML + 32 * 2 * 64 * 8;
constexpr unsigned WS_GZ = WS_CX + 32 * 2 * 2 * 128 * 8;
constexpr unsigned WS_GB = WS_GZ + 256 * 128;
constexpr unsigned WS_GA = WS_GB + 2 * 128 * 256 * 8;
constexpr unsigned WS_APO = 2u << 20;
static_assert(WS_GA + 64 * 2 * 256 * 8 <= WS_APO, "layout");
constexpr unsigned WS_END = WS_APO + 32 * 2 * 64 * 256 * 2;
#define STAMP(i) do {} while (0)
#define STAMP_DECL do {} while (0)
#define STAMP_FLUSH(grp) do {} while (0)

#define TR() do {} while (0)
#define TR_FLUSH() do {} while (0)
DI float bflo(unsigned u) { return __uint_as_float(u << 16); }
DI float bfhi(unsigned u) { return __uint_as_float(u & 0xffff0000u); }
DI unsigned pk_bf(float a, float b) { const f32x2 v = {a, b}; return __builtin_bit_cast(unsigned, __builtin_convertvector(v, bf16x2_t)); }
DI float dot2(unsigned k, unsigned q, float acc) {
  return __builtin_amdgcn_fdot2_f32_bf16(__builtin_bit_cast(bf16x2_t, k), __builtin_bit_cast(bf16x2_t, q), acc, false);
}
DI unsigned plo(unsigned w0, unsigned w1) { return __builtin_amdgcn_perm(w1, w0, 0x05040100u); }
DI unsigned phi(unsigned w0, unsigned w1) { return __builtin_amdgcn_perm(w1, w0, 0x07060302u); }
template <int CTRL> DI float dpp(float x) {
  return __builtin_bit_cast(float, __builtin_amdgcn_mov_dpp(__builtin_bit_cast(int, x), CTRL, 0xf, 0xf, true));
}
DI float row16_sum(float x) { x += dpp<0xB1>(x); x += dpp<0x4E>(x); x += dpp<0x141>(x); x += dpp<0x128>(x); return x; }
DI float row16_max(float x) { x = fmaxf(x, dpp<0xB1>(x)); x = fmaxf(x, dpp<0x4E>(x)); x = fmaxf(x, dpp<0x141>(x)); x = fmaxf(x, dpp<0x128>(x)); return x; }
DI float x16_sum(float x) { auto s = __builtin_amdgcn_permlane16_swap(__float_as_uint(x), __float_as_uint(x), false, false); return __uint_as_float(s[0]) + __uint_as_float(s[1]); }
DI float xhalf_max(float x) { auto t = __builtin_amdgcn_permlane32_swap(__float_as_uint(x), __float_as_uint(x), false, false); return fmaxf(__uint_as_float(t[0]), __uint_as_float(t[1])); }
DI float xhalf_sum(float x) { auto t = __builtin_amdgcn_permlane32_swap(__float_as_uint(x), __float_as_uint(x), false, false); return __uint_as_float(t[0]) + __uint_as_float(t[1]); }
DI u32x4 ld16(const void* p) { return *reinterpret_cast<const u32x4*>(p); }
DI u32x4 ld16nt(const void* p) { return __builtin_nontemporal_load(reinterpret_cast<const u32x4*>(p)); }
DI float ex2(float x) { return __builtin_amdgcn_exp2f(x); }
#define SCHED() __builtin_amdgcn_sched_barrier(0)

typedef __amdgpu_buffer_rsrc_t rsrc_t;
DI u32x4 ld16c(rsrc_t rs, unsigned off) { return __builtin_amdgcn_raw_buffer_load_b128(rs, (int)off, 0, 16); }
DI u32x2 ld8c(rsrc_t rs, unsigned off) { return __builtin_amdgcn_raw_buffer_load_b64(rs, (int)off, 0, 16); }
DI void st16c(rsrc_t rs, unsigned off, u32x4 v) { __builtin_amdgcn_raw_buffer_store_b128(v, rs, (int)off, 0, 16); }
DI void st8c(rsrc_t rs, unsigned off, u32x2 v) { __builtin_amdgcn_raw_buffer_store_b64(v, rs, (int)off, 0, 16); }
typedef __attribute__((ext_vector_type(16))) unsigned u32x16;
template <int O1> DI void sld2(const char* p, u32x2& a, u32x2& b) {
  asm volatile("s_load_dwordx2 %0, %2, 0x0 glc\n\ts_load_dwordx2 %1, %2, %3 glc\n\ts_waitcnt lgkmcnt(0)" : "=&s"(a), "=&s"(b) : "s"(p), "n"(O1) : "memory");
}
DI u32x16 sld64(const char* p) {
  u32x16 v;
  asm volatile("s_load_dwordx16 %0, %1, 0x0 glc\n\ts_waitcnt lgkmcnt(0)" : "=&s"(v) : "s"(p) : "memory");
  return v;
}
DI void sld128(const char* p, u32x16 (&v)[2]) {
  asm volatile("s_load_dwordx16 %0, %2, 0x0 glc\n\ts_load_dwordx16 %1, %2, 0x40 glc\n\ts_waitcnt lgkmcnt(0)" : "=&s"(v[0]), "=&s"(v[1]) : "s"(p) : "memory");
}
constexpr int SPIN_MAX = 1 << 20;
constexpr unsigned OOB = 0x7ffffff0u;
DI void rawbar() { __builtin_amdgcn_sched_barrier(0); __builtin_amdgcn_s_barrier(); __builtin_amdgcn_sched_barrier(0); }
#define COMPILER_FENCE() asm volatile("" ::: "memory")
template <int O1, int SLP> DI void shint(const char* p, unsigned tag) {
#pragma unroll 1
  for (int spin = 0; spin < SPIN_MAX; ++spin) {
    u32x2 a, b;
    sld2<O1>(p, a, b);
    if (((a[1] ^ tag) | (b[1] ^ tag)) == 0u) break;
    __builtin_amdgcn_s_sleep(SLP);
  }
}

template <int N> struct IC { static constexpr int v = N; };
template <int I, int N, class F> DI void sfor(F&& f) { if constexpr (I < N) { f(IC<I>{}); sfor<I + 1, N>(f); } }
typedef __attribute__((address_space(3))) unsigned lu32;
DI void lds_wait(unsigned* cnt, unsigned target) {
#pragma unroll 1
  for (int spin = 0; spin < SPIN_MAX; ++spin) {
    if (__builtin_amdgcn_readfirstlane(__hip_atomic_load((lu32*)cnt, __ATOMIC_RELAXED, __HIP_MEMORY_SCOPE_WORKGROUP)) >= target) break;
    __builtin_amdgcn_s_sleep(1);
  }
  COMPILER_FENCE();
}
DI void lds_wait_slow(unsigned* cnt, unsigned target) {
#pragma unroll 1
  for (int spin = 0; spin < SPIN_MAX; ++spin) {
    if (__builtin_amdgcn_readfirstlane(__hip_atomic_load((lu32*)cnt, __ATOMIC_RELAXED, __HIP_MEMORY_SCOPE_WORKGROUP)) >= target) break;
    __builtin_amdgcn_s_sleep(3);
  }
  COMPILER_FENCE();
}
DI void lds_hold(unsigned* flag) {
#pragma unroll 1
  for (int spin = 0; spin < SPIN_MAX; ++spin) {
    if (__builtin_amdgcn_readfirstlane(__hip_atomic_load((lu32*)flag, __ATOMIC_RELAXED, __HIP_MEMORY_SCOPE_WORKGROUP)) == 0u) break;
    __builtin_amdgcn_s_sleep(2);
  }
}
DI unsigned lds_arrive_nw(unsigned addr) {
  unsigned old, one = 1u;
  asm volatile("s_mov_b64 exec, 1\n\tds_add_rtn_u32 %0, %1, %2\n\ts_mov_b64 exec, -1" : "=v"(old) : "v"(addr), "v"(one) : "memory");
  return old;
}
DI unsigned lds_done(unsigned old) { asm volatile("s_waitcnt lgkmcnt(0)" : "+v"(old) :: "memory"); return __builtin_amdgcn_readfirstlane(old); }
DI unsigned lds_arrive(unsigned* cnt, int) { return lds_done(lds_arrive_nw((unsigned)(size_t)(lu32*)cnt)); }
DI void lds_add(unsigned* cnt, unsigned v, int) {
  asm volatile("s_mov_b64 exec, 1\n\tds_add_u32 %0, %1\n\ts_mov_b64 exec, -1" :: "v"((unsigned)(size_t)(lu32*)cnt), "v"(v) : "memory");
}
DI void sbar4(unsigned* cnt, int lane) { if (lds_arrive(cnt, lane) < 3u) lds_wait(cnt, 4u); }
DI float rs32(float a, float b) { auto t = __builtin_amdgcn_permlane32_swap(__float_as_uint(a), __float_as_uint(b), false, false); return __uint_as_float(t[0]) + __uint_as_float(t[1]); }
DI float rs16(float a, float b) { auto t = __builtin_amdgcn_permlane16_swap(__float_as_uint(a), __float_as_uint(b), false, false); return __uint_as_float(t[0]) + __uint_as_float(t[1]); }
template <int N> DI unsigned rowbcast(unsigned x) { return (unsigned)__builtin_amdgcn_mov_dpp((int)x, 0x150 + N, 0xf, 0xf, true); }
DI void pin2(u32x4& a, u32x4& b) { asm volatile("" : "+v"(a), "+v"(b)); }
DI bool nz8(const u32x4& v) { return ((v[0] | v[1]) != 0u) & ((v[2] | v[3]) != 0u); }

DI u32x4 qsum(const u32x4& g) {
  const auto ta = __builtin_amdgcn_permlane32_swap(g[0], g[0], false, false), tc = __builtin_amdgcn_permlane32_swap(g[2], g[2], false, false);
  const unsigned a = pk_bf(bflo(ta[0]) + bflo(ta[1]), bfhi(ta[0]) + bfhi(ta[1])), c = pk_bf(bflo(tc[0]) + bflo(tc[1]), bfhi(tc[0]) + bfhi(tc[1]));
  const auto ra = __builtin_amdgcn_permlane16_swap(a, a, false, false), rc = __builtin_amdgcn_permlane16_swap(c, c, false, false);
  return u32x4{ra[0], rc[0], ra[1], rc[1]};
}
DI unsigned toff(unsigned row, unsigned ch) { return 256u * row + 16u * (ch ^ (((row & 3) << 2) | ((row >> 2) & 3))); }
constexpr int SMEM = 160 * 1024;
constexpr int L_SCR = 131072;
constexpr int L_MRG = 65536;
constexpr int L_CNT = L_SCR + 32 * 1008;

struct KV { u32x4 r[8]; };

__global__ __launch_bounds__(512, 2) void kmain(const u16* __restrict__ xA, const u16* __restrict__ ckv, const u16* __restrict__ w_q, const u16* __restrict__ w_uk,
                                                const u16* __restrict__ w_uv, const u16* __restrict__ w_o,
                                                const u16* __restrict__ xB, const u16* __restrict__ kc, const u16* __restrict__ vc,
                                                const u16* __restrict__ wq, const u16* __restrict__ wo,
                                                char* __restrict__ ws, u16* __restrict__ out) {
  __shared__ __attribute__((aligned(16))) char smem[SMEM];
  const int tid = threadIdx.x, blk = blockIdx.x, lane = tid & 63;
  const int wave = __builtin_amdgcn_readfirstlane(tid >> 6), w4 = wave & 3;
  const int cls = (blk >> 3) & 3;
  int idx = 8 * (blk & 7) + (blk >> 5);
  if (wave < 4) idx = (idx & ~7) | ((idx + (cls == 3 ? 2 : (cls == 2 ? 4 : 0))) & 7);
  const int h = idx >> 1, b = idx & 1;
  const rsrc_t rs = __builtin_amdgcn_make_buffer_rsrc(ws, 0, (int)WS_END, 0x00020000);
  const int r4 = lane >> 4, c16 = lane & 15;
  unsigned* cnt = reinterpret_cast<unsigned*>(smem + L_CNT);
  STAMP_DECL;
  STAMP(0);
  asm volatile("" :: "s"(xA), "s"(ckv), "s"(w_q), "s"(w_uk), "s"(w_uv), "s"(w_o), "s"(xB), "s"(kc), "s"(vc), "s"(wq), "s"(wo), "s"(ws), "s"(out));
  unsigned tag, tlow;
  u32x2 epw;
  auto take_epoch = [&]() {
    const u64 t64 = (((u64)(unsigned)__builtin_amdgcn_readfirstlane(epw[1]) << 32) | (unsigned)__builtin_amdgcn_readfirstlane(epw[0])) + GOLD;
    tag = (unsigned)(t64 >> 32); tlow = (unsigned)t64;
    asm volatile("s_waitcnt lgkmcnt(0)" : "+s"(tag), "+s"(tlow) :: "memory");
  };
  if (wave >= 4) {
    if (tid - 256 < 16) cnt[tid - 256] = 0u;
    asm volatile("s_waitcnt lgkmcnt(0)" ::: "memory");
    const unsigned tstart = (unsigned)wall_clock64();
    SCHED();
    const unsigned pTL = TL, pTA = cls == 0 ? TA0 : (cls == 1 ? TA1 : (cls == 2 ? TA2 : TA3)), pOF = 0, pSTG = STG, pBSG = BSG, pRA2 = RA2, pRA3 = RA3, pM0 = M0, pSL = SL, pS2 = S2, pC2 = C2K, pOG = 100;
    rawbar();
    const int e2 = (pRA2 >> (idx & 7)) & 1, e3 = (pRA3 >> (idx & 7)) & 1;
    const int ns = cls == 0 ? NS0 : (cls == 1 ? NS1 + e2 + e3 : (cls == 2 ? NS2 - e2 : NS3 - e3));
    const size_t kvo = ((size_t)(b * 32 + h) * SK + w4 * 16) * 256;
    const char* kb = reinterpret_cast<const char*>(kc) + kvo;
    const char* vb = reinterpret_cast<const char*>(vc) + kvo;
    const unsigned lo = lane * 16u;
    u64 tlo = cls == 0 ? TL0 : (cls == 1 ? TL1 : (cls == 2 ? TL2 : TL3));
    u64 thi = cls == 0 ? TH0 : (cls == 1 ? TH1 | ((e2 ? LA2 : LA3) << (6 * (NS1 - 10))) : (cls == 2 ? TH2 : TH3));
    const u64 tlo0 = tlo, thi0 = thi;
    int hn = 0;
    const char* hb = kb;
    const char* pc[2];
    auto next2 = [&]() {
#pragma unroll
      for (int i = 0; i < 2; ++i) {
        if (hn == ns) { tlo = tlo0; thi = thi0; hb = vb; }
        ++hn;
        pc[i] = hb + ((((unsigned)tlo + (unsigned)idx) & 63u) << 14);
        tlo = (tlo >> 6) | ((thi & 63u) << 54); thi >>= 6;
      }
    };
    KV A, B;
    u32x4 W[8];
    u32x4 T[4][8];
    auto ldp = [&](KV& S, auto P) {
      constexpr int p = P.v;
      const char* q = pc[p >> 1] + lo + (p & 1) * 2048;
      S.r[2 * p] = ld16nt(q); S.r[2 * p + 1] = ld16nt(q + 1024);
    };
    auto landed = [&](KV& S, auto P) { pin2(S.r[2 * P.v], S.r[2 * P.v + 1]); };
    KV X;
    sfor<0, 12>([&](auto I) {
      constexpr int i = I.v, j = i - 4;
      if constexpr ((i & 3) == 0) next2();
      if constexpr (j >= 0) { if constexpr (j < 4) landed(X, IC<j & 3>{}); else landed(B, IC<j & 3>{}); }
      if constexpr (j >= 0 && (j & 1)) TR();
      if constexpr (i < 4) ldp(X, IC<i & 3>{}); else if constexpr (i < 8) ldp(B, IC<i & 3>{}); else ldp(A, IC<i & 3>{});
      SCHED();
    });
    lds_wait(cnt + 15, 1u);
    {
      const u32x2 t = *reinterpret_cast<const u32x2*>(cnt + 16);
      tlow = __builtin_amdgcn_readfirstlane(t[0]); tag = __builtin_amdgcn_readfirstlane(t[1]);
    }
    const char* wp = reinterpret_cast<const char*>(wo) + ((size_t)h * 128 + r4 * 8) * 1024 + w4 * 256 + c16 * 16;
    u32x4 qpk;
    {
      unsigned tv = 0u;
      if (tid == 256) tv = __builtin_nontemporal_load(reinterpret_cast<const unsigned*>(vb + cls * 128));
      const unsigned qo = WS_QX + ((32 + h) * 2 + b) * 1024 + lane * 16;
      u32x4 g0;
#pragma unroll 1
      for (int spin = 0; spin < SPIN_MAX; ++spin) {
        COMPILER_FENCE();
        g0 = ld16c(rs, qo);
        if (__all((g0[1] == tag) & (g0[3] == tag))) break;
      }
      qpk = qsum(g0);
      asm volatile("" :: "v"(tv));
    }
    STAMP(7);
    const bool holds = cls == 0;
    float m = 0.f, l = 0.f, acc[8];
    unsigned sc[5];
#pragma unroll
    for (int k = 0; k < 8; ++k) acc[k] = 0.f;
#pragma unroll
    for (int k = 0; k < 5; ++k) sc[k] = 0xff800000u;
    int vn = 0;
    u32x4 msg; unsigned mst;
    unsigned due = tstart + pTL - pOF - (unsigned)(ns - 5) * pTA - (3 - w4) * pSTG - ((idx * 5 + cls * 3) & 15) * pBSG;
    unsigned rwo;
    const unsigned zo = WS_GZ + (b * 32 + h) * 512;
    u32x2 zs[4];
    float wr_;
    auto zreq = [&]() {
      asm volatile("s_load_dwordx2 %0, %4, 0x0 glc\n\ts_load_dwordx2 %1, %4, 0x80 glc\n\ts_load_dwordx2 %2, %4, 0x100 glc\n\ts_load_dwordx2 %3, %4, 0x180 glc"
                   : "=&s"(zs[0]), "=&s"(zs[1]), "=&s"(zs[2]), "=&s"(zs[3]) : "s"(ws + zo) : "memory");
    };
    auto zget = [&]() {
#pragma unroll 1
      for (int spin = 0; spin < SPIN_MAX; ++spin) {
        asm volatile("s_waitcnt lgkmcnt(0)" : "+s"(zs[0]), "+s"(zs[1]), "+s"(zs[2]), "+s"(zs[3]) :: "memory");
        if (((zs[0][1] ^ tag) | (zs[1][1] ^ tag) | (zs[2][1] ^ tag) | (zs[3][1] ^ tag)) == 0u) break;
        __builtin_amdgcn_s_sleep(2);
        zreq();
      }
      asm volatile("s_waitcnt lgkmcnt(0)" : "+s"(zs[0]), "+s"(zs[1]), "+s"(zs[2]), "+s"(zs[3]) :: "memory");
      const float z0 = __uint_as_float(zs[0][0]), z1 = __uint_as_float(zs[1][0]), z2 = __uint_as_float(zs[2][0]), z3 = __uint_as_float(zs[3][0]);
      const float Z = fmaxf(fmaxf(z0, z1), fmaxf(z2, z3));
      wr_ = ex2(fmaf(m, C2, -Z)) * __builtin_amdgcn_rcpf((ex2(z0 - Z) + ex2(z1 - Z)) + (ex2(z2 - Z) + ex2(z3 - Z)));
      asm volatile("" : "+v"(wr_));
    };
    auto qk = [&](const u32x4* K, auto I) {
      float x[4];
#pragma unroll
      for (int t = 0; t < 4; ++t) {
        x[t] = 0.f;
#pragma unroll
        for (int k = 0; k < 4; ++k) x[t] = dot2(K[t][k], qpk[k], x[t]);
      }
      const float u0 = (c16 & 1 ? x[1] : x[0]) + dpp<0xB1>(c16 & 1 ? x[0] : x[1]), u1 = (c16 & 1 ? x[3] : x[2]) + dpp<0xB1>(c16 & 1 ? x[2] : x[3]);
      float v = (c16 & 2 ? u1 : u0) + dpp<0x4E>(c16 & 2 ? u0 : u1);
      v += dpp<0x124>(v); v += dpp<0x128>(v);
      sc[I.v >> 2] = __builtin_amdgcn_update_dpp(sc[I.v >> 2], __float_as_uint(v), 0xE4, 0xf, 1 << (I.v & 3), false);
      asm volatile("" : "+v"(sc[I.v >> 2]));
    };
    auto soft = [&]() {
      m = row16_max(fmaxf(fmaxf(fmaxf(__uint_as_float(sc[0]), __uint_as_float(sc[1])), fmaxf(__uint_as_float(sc[2]), __uint_as_float(sc[3]))), __uint_as_float(sc[4])));
      const float mc = m * C2;
      l = 0.f;
#pragma unroll
      for (int k = 0; k < 5; ++k) {
        const float p = ex2(fmaf(__uint_as_float(sc[k]), C2, -mc));
        sc[k] = pk_bf(p, dpp<0xB1>(p));
        l = dot2(sc[k], 0x3f003f00u, l);
      }
      l = row16_sum(l);
    };
    auto pv = [&](const u32x4* V) {
      const unsigned p01 = rowbcast<0>(sc[0]), p23 = rowbcast<2>(sc[0]);
#pragma unroll
      for (int k = 0; k < 4; ++k) { acc[2 * k] = dot2(plo(V[0][k], V[1][k]), p01, acc[2 * k]); acc[2 * k + 1] = dot2(phi(V[0][k], V[1][k]), p01, acc[2 * k + 1]); }
#pragma unroll
      for (int k = 0; k < 4; ++k) { acc[2 * k] = dot2(plo(V[2][k], V[3][k]), p23, acc[2 * k]); acc[2 * k + 1] = dot2(phi(V[2][k], V[3][k]), p23, acc[2 * k + 1]); }
      sc[0] = (unsigned)__builtin_amdgcn_mov_dpp((int)sc[0], 0x12C, 0xf, 0xf, true);
      if ((++vn & 3) == 0) { sc[0] = sc[1]; sc[1] = sc[2]; sc[2] = sc[3]; sc[3] = sc[4]; }
      asm volatile("" : "+v"(acc[0]), "+v"(acc[1]), "+v"(acc[2]), "+v"(acc[3]), "+v"(acc[4]), "+v"(acc[5]), "+v"(acc[6]), "+v"(acc[7]));
    };
    auto step = [&](KV& S, KV& N, auto SI, auto REFILL, auto WR) {
      constexpr bool refill = REFILL.v == 1;
      constexpr int wr = WR.v;
      if constexpr (refill) next2();
      if constexpr (refill && SI.v >= 3) {
        if constexpr (SI.v >= PCS) {
#pragma unroll 1
          for (int spin = 0; spin < 4096; ++spin) { if ((int)((unsigned)wall_clock64() - due) >= 0) break; __builtin_amdgcn_s_sleep(1); }
        }
        due += pTA;
      }
      constexpr int ah = wr < 0 ? AHL : AHE;
      auto point = [&](auto P) {
        if constexpr (refill) {
          if constexpr (P.v == 0 && ah > 0) { if (holds) lds_hold(cnt + 8); sfor<0, ah>([&](auto Q) { ldp(N, Q); }); SCHED(); }
          landed(S, P);
          if constexpr (P.v & 1) TR();
          if constexpr (P.v + ah < 4) { if constexpr ((P.v & 1) == 0) { if (holds) lds_hold(cnt + 8); } ldp(N, IC<(P.v + ah) & 3>{}); }
          if constexpr (wr >= 0 && wr < 8) W[(4 * wr + P.v) & 7] = ld16(wp + (((4 * wr + P.v) >> 3) * 32 + ((4 * wr + P.v) & 7)) * 1024);
          SCHED();
        }
      };
      sfor<0, 2>([&](auto H) {
        constexpr int i = 2 * SI.v + H.v;
        if constexpr (REFILL.v == 2 && H.v == 0) { zreq(); SCHED(); }
        point(IC<2 * H.v>{});
        point(IC<2 * H.v + 1>{});
        if constexpr (i < NSMIN) qk(S.r + 4 * H.v, IC<i>{});
        else if constexpr (i > NSMAX) pv(S.r + 4 * H.v);
        else {
          if (i < ns) { if constexpr (i < NSMAX) qk(S.r + 4 * H.v, IC<i>{}); }
          else { if (i == ns) soft(); pv(S.r + 4 * H.v); }
        }
        SCHED();
      });
      if constexpr (REFILL.v == 2) {
        zget();
        STAMP(1);
        *reinterpret_cast<u32x4*>(smem + rwo) = u32x4{pk_bf(acc[0] * wr_, acc[1] * wr_), pk_bf(acc[2] * wr_, acc[3] * wr_), pk_bf(acc[4] * wr_, acc[5] * wr_), pk_bf(acc[6] * wr_, acc[7] * wr_)};
        COMPILER_FENCE();
      }
      if constexpr (wr >= 1) {
        constexpr int t = (wr - 1) >> 1, j0 = ((wr - 1) & 1) * 2;
#pragma unroll
        for (int jj = 0; jj < 2; ++jj)
#pragma unroll
          for (int e = 0; e < 4; ++e) {
            const unsigned a_ = W[(4 * wr - 4 + 2 * jj) & 7][e], b_ = W[(4 * wr - 3 + 2 * jj) & 7][e];
            T[t][2 * e][j0 + jj] = plo(a_, b_); T[t][2 * e + 1][j0 + jj] = phi(a_, b_);
          }
#pragma unroll
        for (int e = 0; e < 8; e += 2) pin2(T[t][e], T[t][e + 1]);
      }
      SCHED();
    };
    step(B, B, IC<1>{}, IC<0>{}, IC<-1>{});
    step(A, B, IC<2>{}, IC<1>{}, IC<0>{});
    step(X, X, IC<0>{}, IC<0>{}, IC<-1>{});
    sfor<0, 4>([&](auto I) {
      step(B, A, IC<2 * I.v + 3>{}, IC<1>{}, IC<2 * I.v + 1>{});
      if constexpr (2 * I.v + 3 == MSS) {
        if (cls == 1 && (w4 & 1)) asm volatile("buffer_store_dwordx2 %0, %1, %2, 0 offen sc1" :: "v"(u32x2{(tstart << 16) | (((unsigned)wall_clock64() - tstart) & 0xffffu), tag}), "v"(lane == 0 ? WS_MS + idx * 16 + (w4 >> 1) * 8 : OOB), "s"(rs) : "memory");
        SCHED();
      }
      if constexpr (2 * I.v + 3 == MSR) {
        const unsigned m0 = msg[1] == tag ? msg[0] : mst, m1 = msg[3] == tag ? msg[2] : mst;
        const float t = (float)((m0 & 0xffffu) + (m1 & 0xffffu)), o = (float)(((int)(m0 - (tstart << 16)) >> 16) + ((int)(m1 - (tstart << 16)) >> 16));
        const float both = row16_sum(xhalf_sum(rs16(t, o))) * (1.f / 128);
        const float lag = __builtin_bit_cast(float, __builtin_amdgcn_readlane(__builtin_bit_cast(int, both), 0)) - (float)pM0;
        const float org = __builtin_bit_cast(float, __builtin_amdgcn_readlane(__builtin_bit_cast(int, both), 16)) * ((float)pOG * 0.01f);
        const float sh = fminf(fmaxf(fminf(lag * ((float)pSL * 0.01f), fmaf(lag, (float)pS2 * 0.01f, (float)pC2)), 0.f), 250.f) + fminf(org, 100.f);
        due += (unsigned)__builtin_amdgcn_readfirstlane((int)sh);
        SCHED();
      }
      if constexpr (I.v == 3) STAMP(9);
      if constexpr (I.v == 3) {
        float* ml = reinterpret_cast<float*>(cnt + 32);
        ml[(c16 & 1) * 16 + w4 * 4 + r4] = (c16 & 1) ? l : m;
        COMPILER_FENCE();
        const unsigned got = lds_arrive_nw(L_CNT + 44);
        const float mr = ml[c16], lr = ml[16 + c16];
        SCHED();
        if (lds_done(got) == 3u) {
          const float M = row16_max(mr);
          const unsigned z = __float_as_uint(fmaf(M, C2, __builtin_amdgcn_logf(row16_sum(ex2((mr - M) * C2) * lr))));
          asm volatile("buffer_store_dwordx4 %0, %1, %2, 0 offen sc1\n\ts_nop 2" :: "v"(u32x4{z, tag, z, tag}), "v"(lane < 8 ? zo + cls * 128 + lane * 16 : OOB), "s"(rs) : "memory");
        }
        SCHED();
      }
      step(A, B, IC<2 * I.v + 4>{}, IC<1>{}, IC<2 * I.v + 2>{});
      if constexpr (2 * I.v + 5 == MSR) { msg = ld16c(rs, WS_MS + lane * 16); mst = (tstart << 16) | (((unsigned)wall_clock64() - tstart - 70u) & 0xffffu); SCHED(); }
    });
    unsigned yoff = WS_GB + ((b * 128 + h * 4 + cls) * 256 + 64 * w4 + lane) * 8, rro = L_MRG + c16 * 272 + r4 * 16;
    rwo = L_MRG + (w4 * 4 + r4) * 272 + c16 * 16;
    asm volatile("" : "+v"(yoff), "+v"(rwo), "+v"(rro));
    const int nit = (ns - 12) >> 1;
#pragma unroll 1
    for (int i = 0; i < nit; ++i) {
      step(B, A, IC<11>{}, IC<1>{}, IC<-1>{});
      step(A, B, IC<11>{}, IC<1>{}, IC<-1>{});
    }
    if (cls == 0) lds_wait(cnt + 1, 4u);
    if (ns & 1) { step(B, A, IC<11>{}, IC<1>{}, IC<-1>{}); step(A, A, IC<11>{}, IC<2>{}, IC<-1>{}); }
    else step(B, B, IC<11>{}, IC<2>{}, IC<-1>{});
    {
      u32x4 af[4];
      unsigned got = lds_arrive_nw(L_CNT), need = 3u;
#pragma unroll 1
      for (int spin = 0; spin < SPIN_MAX; ++spin) {
#pragma unroll
        for (int t = 0; t < 4; ++t) af[t] = *reinterpret_cast<const u32x4*>(smem + rro + t * 64);
        SCHED();
        if (lds_done(got) >= need) break;
        need = 4u;
        __builtin_amdgcn_s_sleep(1);
        asm volatile("ds_read_b32 %0, %1" : "=v"(got) : "v"((unsigned)L_CNT) : "memory");
      }
      SCHED();
      f32x4 d[8];
      float a[8], v4[4];
      sfor<0, 5>([&](auto I) {
        constexpr int i = I.v;
        sfor<0, 8>([&](auto J) {
          constexpr int e = i + 4 * (J.v & 1), t = J.v >> 1;
          if constexpr (i < 4) { d[e] = __builtin_amdgcn_mfma_f32_16x16x32_bf16(__builtin_bit_cast(bf16x8_t, af[t]), __builtin_bit_cast(bf16x8_t, T[t][e]), t ? d[e] : f32x4{0.f, 0.f, 0.f, 0.f}, 0, 0, 0); SCHED(); }
          if constexpr (i >= 1 && (J.v == 4 || J.v == 5)) { const f32x2 u = f32x2{d[e - 1][0], d[e - 1][1]} + f32x2{d[e - 1][2], d[e - 1][3]}; a[e - 1] = u[0] + u[1]; asm volatile("" : "+v"(a[e - 1])); }
          if constexpr (i >= 1 && J.v == 6) v4[i - 1] = rs32(a[i - 1], a[i + 3]);
          if constexpr (i < 4) SCHED();
        });
      });
      st8c(rs, yoff, u32x2{pk_bf(rs16(v4[0], v4[2]), rs16(v4[1], v4[3])), tag});
    }
    STAMP(2);
    STAMP_FLUSH(1);
    TR_FLUSH();
    return;
  }
  const rsrc_t ro = __builtin_amdgcn_make_buffer_rsrc(out, 0, 4096, 0x00020000);
  auto fin = [&](auto NL, unsigned base, unsigned col, bool bump) {
    constexpr int nl = NL.v;
    unsigned doff = base + (((idx >> 5) * 4 + w4) * 16 * nl + (lane >> 2)) * 2048 + (idx & 31) * 64 + (lane & 3) * 16;
    unsigned fwo = 32768 + (c16 < 4 ? (w4 * 16 + 4 * c16 + r4) * 4 : 256 + lane * 4);
    unsigned fro = 32768 + (lane & 7) * 8, foo = lane < 8 ? ((idx >> 5) * 1024 + col + (nl == 2 ? 16 * (idx & 24) + 64 * (idx & 1) + (idx & 6) + 8 * lane : 16 * (idx & 31) + 2 * lane)) * 2 : OOB;
    asm volatile("" : "+v"(doff), "+v"(fwo), "+v"(fro), "+v"(foo));
    lds_wait(cnt + 0, 4u);
    STAMP(4);
    u32x4 d[nl];
    unsigned eh = 1u;
#pragma unroll 1
    for (int spin = SPIN_MAX; spin > 0; --spin) {
      COMPILER_FENCE();
      if (eh != 0u) {
#pragma unroll
        for (int j = 0; j < nl; ++j) d[j] = ld16c(rs, doff + j * 32768);
        eh = 0u;
#pragma unroll
        for (int j = 0; j < nl; ++j) { eh |= (d[j][1] ^ tag) | (d[j][3] ^ tag); asm volatile("" : "+v"(eh)); }
      }
      if (__builtin_amdgcn_ballot_w64(eh != 0u) == 0ull) break;
    }
    STAMP(11);
    float y[4] = {bflo(d[0][0]), bfhi(d[0][0]), bflo(d[0][2]), bfhi(d[0][2])};
    if constexpr (nl == 2) { y[0] += bflo(d[1][0]); y[1] += bfhi(d[1][0]); y[2] += bflo(d[1][2]); y[3] += bfhi(d[1][2]); }
    float t = rs16(rs32(y[0], y[2]), rs32(y[1], y[3]));
    t += dpp<0x124>(t); t += dpp<0x128>(t);
    *reinterpret_cast<float*>(smem + fwo) = t;
    COMPILER_FENCE();
    const unsigned got = lds_arrive_nw(L_CNT + 24);
    f32x2 t2[4];
#pragma unroll
    for (int w = 0; w < 4; ++w) t2[w] = *reinterpret_cast<const f32x2*>(smem + fro + w * 64);
    SCHED();
    if (lds_done(got) == 3u) {
      const f32x2 r = (t2[0] + t2[1]) + (t2[2] + t2[3]);
      __builtin_amdgcn_raw_buffer_store_b32(pk_bf(r[0], r[1]), ro, (int)foo, 0, 0);
      if (bump) st8c(rs, lane == 0 ? WS_EP : OOB, u32x2{tlow, tag});
    }
  };
  epw = ld8c(rs, WS_EP);
  SCHED();
  u32x4 Wa[16], Wb[16], xv[4];
  const u16* pb; size_t sb, sb2;
  const unsigned qa_off = WS_QA + (((c16 >> 1) & 1) * 32 + h) * 1024 + (b * 256 + w4 * 64 + 16 * r4 + 8 * (c16 & 1)) * 2;
  {
    const u16* pa; size_t sa;
    if (cls) {
      const size_t o_ = ((size_t)(b * 256 + w4 * 64 + 16 * r4) * 32 + h) * 128 + 8 * c16;
      pa = (cls == 1 ? w_q : (cls == 2 ? w_uk : wq)) + o_; sa = 4096;
      if (cls == 2) { pb = w_uv + o_; sb = 4096; }
      else { pb = w_o + ((size_t)h * 128 + b * 64 + w4 * 2) * 512 + 8 * lane; sb = 512; }
      if (cls == 1 && c16 < 4) st16c(rs, qa_off, u32x4{0u, 0u, 0u, 0u});
    } else { pa = ckv + ((size_t)idx * 128 + w4) * 512 + 8 * lane; sa = sb = 2048; pb = pa + 64 * 512; }
    sb2 = cls == 1 ? 4096 : 2 * sb;
    const u16* xp = (cls == 1 ? xA : xB) + b * 256 + w4 * 64 + 16 * r4;
    SCHED();
    sfor<0, 4>([&](auto J) { xv[J.v] = ld16(xp + (J.v >> 1) * 512 + 8 * (J.v & 1)); });
    SCHED();
    sfor<0, 16>([&](auto I) { Wa[I.v] = ld16nt(pa + I.v * sa); });
    SCHED();
    rawbar();
    take_epoch();
    *reinterpret_cast<u32x2*>(cnt + 16) = u32x2{tlow, tag};
    COMPILER_FENCE();
    lds_add(cnt + 15, 1u, lane);
  }
  STAMP(13);
  float* red = reinterpret_cast<float*>(smem);
  if (cls & 1) {
    float acc[2][8];
#pragma unroll
    for (int bb = 0; bb < 2; ++bb)
#pragma unroll
      for (int k = 0; k < 8; ++k) acc[bb][k] = 0.f;
#pragma unroll
    for (int ip = 0; ip < 8; ++ip) {
      const unsigned xp0 = xv[ip >> 2][ip & 3], xp1 = xv[2 + (ip >> 2)][ip & 3];
#pragma unroll
      for (int k = 0; k < 4; ++k) {
        const unsigned lo = plo(Wa[2 * ip][k], Wa[2 * ip + 1][k]), hi = phi(Wa[2 * ip][k], Wa[2 * ip + 1][k]);
        acc[0][2 * k] = dot2(lo, xp0, acc[0][2 * k]); acc[0][2 * k + 1] = dot2(hi, xp0, acc[0][2 * k + 1]);
        acc[1][2 * k] = dot2(lo, xp1, acc[1][2 * k]); acc[1][2 * k + 1] = dot2(hi, xp1, acc[1][2 * k + 1]);
      }
    }
    {
      float v8[8], v4[4];
#pragma unroll
      for (int k = 0; k < 8; ++k) v8[k] = rs32(acc[0][k], acc[1][k]);
#pragma unroll
      for (int k = 0; k < 4; ++k) v4[k] = rs16(v8[k], v8[k + 4]);
      *reinterpret_cast<f32x4*>(red + (w4 * 2 + (lane >> 5)) * 128 + 8 * c16 + 4 * (r4 & 1)) = f32x4{v4[0], v4[1], v4[2], v4[3]};
    }
    COMPILER_FENCE();
    const unsigned got = lds_arrive_nw(L_CNT + 8);
    f32x4 t4[4];
#pragma unroll
    for (int w = 0; w < 4; ++w) t4[w] = *reinterpret_cast<const f32x4*>(red + (w * 2 + (lane >> 5)) * 128 + 8 * c16 + 4 * (r4 & 1));
    SCHED();
    if (lds_done(got) == 3u) {
      const f32x4 t = (t4[0] + t4[1]) + (t4[2] + t4[3]);
      st16c(rs, WS_QX + (((cls >> 1) * 32 + h) * 2 + (lane >> 5)) * 1024 + b * 512 + (lane & 31) * 16, u32x4{pk_bf(t[0], t[1]), tag, pk_bf(t[2], t[3]), tag});
      STAMP(15);
      SCHED();
      lds_add(cnt + 2, 1u, lane);
    } else lds_wait(cnt + 2, 5u);
    {
      const unsigned tw = __builtin_nontemporal_load(reinterpret_cast<const unsigned*>(out + (idx >> 5) * 1024 + (cls >> 1) * 512 + 16 * (idx & 31)));
      asm volatile("" :: "v"(tw));
    }
    STAMP(8);
  } else if (cls == 2) {
    u32x4 g0, g1;
    const unsigned qo = WS_QX + h * 2048 + lane * 16;
    shint<512, 1>(ws + WS_QX + h * 2048, tag);
    STAMP(5);
#pragma unroll 1
    for (int spin = 0; spin < SPIN_MAX; ++spin) {
      COMPILER_FENCE();
      g0 = ld16c(rs, qo); g1 = ld16c(rs, qo + 1024);
      if (__all((g0[1] == tag) & (g0[3] == tag) & (g1[1] == tag) & (g1[3] == tag))) break;
      __builtin_amdgcn_s_sleep(1);
    }
    const u32x4 q0 = qsum(g0), q1 = qsum(g1);
    STAMP(9);
    float v[8];
    {
      float u[16];
#pragma unroll
      for (int i = 0; i < 16; ++i) {
        float a = 0.f, c = 0.f;
#pragma unroll
        for (int k = 0; k < 4; ++k) { a = dot2(Wa[i][k], q0[k], a); c = dot2(Wa[i][k], q1[k], c); }
        u[i] = (c16 & 2 ? c : a) + dpp<0x4E>(c16 & 2 ? a : c);
        asm volatile("" : "+v"(u[i]));
      }
#pragma unroll
      for (int i = 0; i < 8; ++i) {
        float t = (c16 & 1 ? u[i + 8] : u[i]) + dpp<0xB1>(c16 & 1 ? u[i] : u[i + 8]);
        asm volatile("" : "+v"(t));
        t += dpp<0x124>(t);
        asm volatile("" : "+v"(t));
        t += dpp<0x128>(t);
        asm volatile("" : "+v"(t));
        v[i] = t;
      }
    }
    u32x4 o = {pk_bf(v[0], v[1]), pk_bf(v[2], v[3]), pk_bf(v[4], v[5]), pk_bf(v[6], v[7])};
    if ((o[0] | o[1]) == 0u) o[0] = 0x8000u;
    if ((o[2] | o[3]) == 0u) o[2] = 0x8000u;
    if (c16 < 4) st16c(rs, qa_off, o);
    SCHED();
    sbar4(cnt + 2, lane);
    STAMP(10);
  }
  asm volatile("" :: "v"(xv[0]), "v"(xv[1]), "v"(xv[2]), "v"(xv[3]));
  SCHED();
  if (cls == 3) {
    fin(IC<2>{}, WS_GB, 512, false);
    STAMP(6);
    STAMP_FLUSH(0); TR_FLUSH();
    return;
  }
  if (cls != 0) {
#pragma unroll
    for (int i = 0; i < 16; ++i) {
      Wb[i] = ld16nt(pb + (i >> 1) * sb2 + (i & 1) * sb);
      if (i >= 2 && (i & 1)) asm volatile("" : "+v"(Wb[i - 2]));
      if (i == 5 || i == 9 || i == 13) TR();
      SCHED();
    }
  }
  if (cls == 0) {
    char* scr = smem + L_SCR;
    const int ab = idx >> 5, r = lane & 31, hh = lane >> 5;
    const unsigned fo = WS_QX + (w4 * 8 + (c16 >> 1)) * 2048 + (c16 & 1) * 512 + r4 * 8;
    u32x2 fl;
    u32x4 g[9];
    const unsigned o8 = WS_QA + (ab * 32 + w4 * 8) * 1024 + lane * 16, ol = WS_QA + (ab * 32 + r) * 1024 + 992 + 16 * hh;
#pragma unroll
    for (int i = 0; i < 32; ++i) {
      if (i + WB0 >= 16 && i + WB0 < 32) Wb[(i + WB0) & 15] = ld16nt(pb + ((i + WB0) & 15) * 2048);
      if (i == FLI) fl = ld8c(rs, fo);
      if (i == FLI + WB0 + 2) {
        if (!__all(fl[1] == tag)) {
#pragma unroll 1
          for (int spin = 0; spin < SPIN_MAX; ++spin) {
            COMPILER_FENCE();
            if (__all(ld8c(rs, fo)[1] == tag)) break;
          }
        }
        lds_add(cnt + 7, 1u, lane);
      }
      if (i == 31) {
        lds_wait(cnt + 7, 4u);
        STAMP(12);
#pragma unroll
        for (int j = 0; j < 9; ++j) g[j] = ld16c(rs, j < 8 ? o8 + j * 1024 : ol);
      }
      SCHED();
      *reinterpret_cast<u32x4*>(smem + ((i >> 3) * 4 + r4) * 8192 + toff(4 * (i & 7) + w4, c16)) = i < 16 ? Wa[i & 15] : Wb[i & 15];
      if (i >= 16 && (i & 3) == 3) TR();
      SCHED();
    }
    STAMP(8);
    u32x4 qlast;
    {
      unsigned pend = 0u;
#pragma unroll
      for (int i = 0; i < 9; ++i) if (!__all(nz8(g[i]))) pend |= 1u << i;
      STAMP(5);
#pragma unroll 1
      for (int spin = 0; spin < SPIN_MAX && pend; ++spin) {
        COMPILER_FENCE();
        u32x4 t[9];
#pragma unroll
        for (int i = 0; i < 9; ++i) t[i] = ld16c(rs, (pend >> i & 1) ? (i < 8 ? o8 + i * 1024 : ol) : OOB);
#pragma unroll
        for (int i = 0; i < 9; ++i) {
          if (pend >> i & 1) g[i] = t[i];
          if (__all(nz8(t[i]))) pend &= ~(1u << i);
        }
      }
      STAMP(9);
      qlast = g[8];
#pragma unroll
      for (int i = 0; i < 8; ++i)
        if (lane < 62) *reinterpret_cast<u32x4*>(scr + (w4 * 8 + i) * 1008 + 16 * lane) = g[i];
    }
    sbar4(cnt + 3, lane);
    STAMP(10);
    f32x16 sacc;
#pragma unroll
    for (int i = 0; i < 16; ++i) sacc[i] = 0.f;
#pragma unroll
    for (int jj = 0; jj < 32; ++jj) {
      const u32x4 af = *reinterpret_cast<const u32x4*>(smem + (w4 * 4 + (jj >> 3)) * 8192 + toff(r, 2 * (jj & 7) + hh));
      const u32x4 bf = jj < 31 ? *reinterpret_cast<const u32x4*>(scr + r * 1008 + 16 * (2 * (jj < 31 ? jj : 0) + hh)) : qlast;
      sacc = __builtin_amdgcn_mfma_f32_32x32x16_bf16(__builtin_bit_cast(bf16x8_t, af), __builtin_bit_cast(bf16x8_t, bf), sacc, 0, 0, 0);
    }
    float m = sacc[0];
#pragma unroll
    for (int i = 1; i < 16; ++i) m = fmaxf(m, sacc[i]);
    m = xhalf_max(m);
    if (hh == 0) *reinterpret_cast<float*>(scr + r * 1008 + 992 + 4 * w4) = m;
    sbar4(cnt + 4, lane);
    STAMP(11);
    {
      const f32x4 m4 = *reinterpret_cast<const f32x4*>(scr + r * 1008 + 992);
      m = fmaxf(fmaxf(m4[0], m4[1]), fmaxf(m4[2], m4[3]));
    }
    const float mc = m * C2;
    float p[16], ls = 0.f;
#pragma unroll
    for (int i = 0; i < 16; ++i) { p[i] = ex2(fmaf(sacc[i], C2, -mc)); ls += p[i]; }
    ls = xhalf_sum(ls);
    sbar4(cnt + 5, lane);
    u32x4* pf = reinterpret_cast<u32x4*>(scr);
    float* wl = reinterpret_cast<float*>(scr + 8192);
    if (hh == 0) wl[r * 4 + w4] = ls;
    pf[(2 * w4) * 64 + lane] = u32x4{pk_bf(p[0], p[1]), pk_bf(p[2], p[3]), pk_bf(p[4], p[5]), pk_bf(p[6], p[7])};
    pf[(2 * w4 + 1) * 64 + lane] = u32x4{pk_bf(p[8], p[9]), pk_bf(p[10], p[11]), pk_bf(p[12], p[13]), pk_bf(p[14], p[15])};
    sbar4(cnt + 6, lane);
    u32x4 pb8[8];
#pragma unroll
    for (int ks = 0; ks < 8; ++ks) pb8[ks] = pf[ks * 64 + lane];
    const f32x4 l4 = *reinterpret_cast<const f32x4*>(wl + r * 4);
    const float lsum = (l4[0] + l4[1]) + (l4[2] + l4[3]), rl = __builtin_amdgcn_rcpf(lsum);
    const int qq = (lane & 15) >> 2, pp = lane & 3, blk2 = (lane >> 4) & 1;
    char* ot = scr + 12288 + w4 * 4096;
#pragma unroll
    for (int c2 = 0; c2 < 2; ++c2) {
      f32x16 oacc[2];
#pragma unroll
      for (int ct = 0; ct < 2; ++ct)
#pragma unroll
        for (int i = 0; i < 16; ++i) oacc[ct][i] = 0.f;
#pragma unroll
      for (int ks = 0; ks < 8; ++ks)
#pragma unroll
        for (int ct = 0; ct < 2; ++ct) {
          typedef __attribute__((address_space(3))) s16x4* lp;
          const char* tile = smem + ((ks >> 1) * 4 + w4) * 8192;
          const int cc = 2 * c2 + ct;
          const s16x4 lo = __builtin_amdgcn_ds_read_tr16_b64_v4i16((lp)(tile + toff(16 * (ks & 1) + 4 * hh + qq, 4 * cc + 2 * blk2 + (pp >> 1)) + 8 * (pp & 1)));
          const s16x4 hi = __builtin_amdgcn_ds_read_tr16_b64_v4i16((lp)(tile + toff(16 * (ks & 1) + 8 + 4 * hh + qq, 4 * cc + 2 * blk2 + (pp >> 1)) + 8 * (pp & 1)));
          const s16x8 afr = __builtin_shufflevector(lo, hi, 0, 1, 2, 3, 4, 5, 6, 7);
          oacc[ct] = __builtin_amdgcn_mfma_f32_32x32x16_bf16(__builtin_bit_cast(bf16x8_t, afr), __builtin_bit_cast(bf16x8_t, pb8[ks]), oacc[ct], 0, 0, 0);
        }
#pragma unroll
      for (int ct = 0; ct < 2; ++ct)
#pragma unroll
        for (int g = 0; g < 4; ++g)
          *reinterpret_cast<u32x2*>(ot + r * 128 + 16 * ((4 * ct + g) ^ (r & 7)) + 8 * hh) = u32x2{pk_bf(oacc[ct][4 * g] * rl, oacc[ct][4 * g + 1] * rl), pk_bf(oacc[ct][4 * g + 2] * rl, oacc[ct][4 * g + 3] * rl)};
      if (c2 == 0) lds_add(cnt + 8, 1u, lane);
      __builtin_amdgcn_wave_barrier();
#pragma unroll
      for (int i = 0; i < 4; ++i) {
        const int hd = 8 * i + (lane >> 3), pc = lane & 7;
        const u32x4 vv = *reinterpret_cast<const u32x4*>(ot + hd * 128 + 16 * (pc ^ (hd & 7)));
        st16c(rs, WS_APO + (((hd * 2 + (w4 >> 1)) * 64 + idx) * 256 + (w4 & 1) * 128 + 64 * c2 + 8 * pc) * 2, vv);
      }
      __builtin_amdgcn_wave_barrier();
    }
    lds_arrive(cnt + 1, lane);
    STAMP(1);
    asm volatile("s_waitcnt vmcnt(0)" ::: "memory");
    if (lds_arrive(cnt + 9 + (w4 >> 1), lane) == 1u && hh == 0)
      st8c(rs, WS_AML + ((r * 2 + (w4 >> 1)) * 64 + idx) * 8, u32x2{__float_as_uint(mc + __builtin_amdgcn_logf(lsum)), tag});
    lds_add(cnt + 8, ~0u, lane);
    STAMP(2);
    STAMP_FLUSH(0); TR_FLUSH();
    return;
  }
#pragma unroll
  for (int ip = 0; ip < 8; ++ip) {
#pragma unroll
    for (int k = 0; k < 4; ++k) { const unsigned lo = plo(Wb[2 * ip][k], Wb[2 * ip + 1][k]), hi = phi(Wb[2 * ip][k], Wb[2 * ip + 1][k]); Wb[2 * ip][k] = lo; Wb[2 * ip + 1][k] = hi; }
    pin2(Wb[2 * ip], Wb[2 * ip + 1]);
  }
  if (cls == 2) {
    float* red2 = red + 4 * 256;
    float* wml = red + 3072;
    u32x16 f[2];
    if (w4 == 0) {
#pragma unroll 1
      for (int spin = 0; spin < SPIN_MAX; ++spin) {
        const u32x16 t = sld64(ws + WS_AML + (h * 2 + b) * 512);
        bool any = false;
#pragma unroll
        for (int i = 0; i < 8; ++i) any |= t[2 * i + 1] == tag;
        if (any) break;
        __builtin_amdgcn_s_sleep(8);
      }
      lds_add(cnt + 7, 1u, lane);
    } else lds_wait_slow(cnt + 7, 1u);
#pragma unroll 1
    for (int spin = 0; spin < SPIN_MAX; ++spin) {
      sld128(ws + WS_AML + ((h * 2 + b) * 64 + w4 * 16) * 8, f);
      unsigned e = 0u;
#pragma unroll
      for (int i = 0; i < 16; ++i) e |= f[i >> 3][2 * (i & 7) + 1] ^ tag;
      if (e == 0u) break;
      __builtin_amdgcn_s_sleep(2);
    }
    STAMP(3);
    u32x4 pp[8];
#pragma unroll
    for (int i = 0; i < 8; ++i) pp[i] = ld16c(rs, WS_APO + ((h * 2 + b) * 64 + w4 * 16 + 2 * i) * 512 + lane * 16);
    SCHED();
    float wt[8];
    {
      float M = -INFINITY, L = 0.f;
#pragma unroll
      for (int i = 0; i < 8; ++i) {
        unsigned z0 = f[i >> 2][4 * (i & 3)], z1 = f[i >> 2][4 * (i & 3) + 2];
        asm("" : "+s"(z0), "+s"(z1));
        wt[i] = __uint_as_float(lane < 32 ? z0 : z1); M = fmaxf(M, wt[i]);
      }
      M = xhalf_max(M);
#pragma unroll
      for (int i = 0; i < 8; ++i) { wt[i] = ex2(wt[i] - M); L += wt[i]; }
      L = xhalf_sum(L);
      if (lane == 0) *reinterpret_cast<f32x2*>(wml + 2 * w4) = f32x2{M, L};
    }
    SCHED();
    float a8[8];
#pragma unroll
    for (int k = 0; k < 8; ++k) a8[k] = 0.f;
#pragma unroll
    for (int i = 0; i < 8; ++i) {
#pragma unroll
      for (int k = 0; k < 4; ++k) { a8[2 * k] = fmaf(wt[i], bflo(pp[i][k]), a8[2 * k]); a8[2 * k + 1] = fmaf(wt[i], bfhi(pp[i][k]), a8[2 * k + 1]); }
      asm volatile("" : "+v"(a8[0]), "+v"(a8[1]), "+v"(a8[2]), "+v"(a8[3]), "+v"(a8[4]), "+v"(a8[5]), "+v"(a8[6]), "+v"(a8[7]));
    }
    STAMP(6);
    *reinterpret_cast<f32x4*>(red + w4 * 256 + 8 * (lane & 31) + 4 * (lane >> 5)) = f32x4{rs32(a8[0], a8[4]), rs32(a8[1], a8[5]), rs32(a8[2], a8[6]), rs32(a8[3], a8[7])};
    sbar4(cnt + 3, lane);
    STAMP(4);
    float acc[2][8];
#pragma unroll
    for (int bb = 0; bb < 2; ++bb) {
      u32x4 cv[2];
      const f32x4 ml = *reinterpret_cast<const f32x4*>(wml + 4 * bb);
      const float M = fmaxf(ml[0], ml[2]), e0 = ex2(ml[0] - M), e1 = ex2(ml[2] - M), rl = 1.f / fmaf(e0, ml[1], e1 * ml[3]), s0 = e0 * rl, s1 = e1 * rl;
#pragma unroll
      for (int e = 0; e < 2; ++e) {
        const float* src = red + bb * 512 + w4 * 64 + 16 * r4 + 8 * e;
        const f32x4 t0 = s0 * *reinterpret_cast<const f32x4*>(src) + s1 * *reinterpret_cast<const f32x4*>(src + 256), t1 = s0 * *reinterpret_cast<const f32x4*>(src + 4) + s1 * *reinterpret_cast<const f32x4*>(src + 260);
        cv[e] = u32x4{pk_bf(t0[0], t0[1]), pk_bf(t0[2], t0[3]), pk_bf(t1[0], t1[1]), pk_bf(t1[2], t1[3])};
      }
#pragma unroll
      for (int k = 0; k < 8; ++k) acc[bb][k] = 0.f;
#pragma unroll
      for (int ip = 0; ip < 8; ++ip)
#pragma unroll
        for (int k = 0; k < 4; ++k) { acc[bb][2 * k] = dot2(Wb[2 * ip][k], cv[ip >> 2][ip & 3], acc[bb][2 * k]); acc[bb][2 * k + 1] = dot2(Wb[2 * ip + 1][k], cv[ip >> 2][ip & 3], acc[bb][2 * k + 1]); }
    }
    {
      float v8[8], v4[4];
#pragma unroll
      for (int k = 0; k < 8; ++k) v8[k] = rs32(acc[0][k], acc[1][k]);
#pragma unroll
      for (int k = 0; k < 4; ++k) v4[k] = rs16(v8[k], v8[k + 4]);
      *reinterpret_cast<f32x4*>(red2 + (w4 * 2 + (lane >> 5)) * 128 + 8 * c16 + 4 * (r4 & 1)) = f32x4{v4[0], v4[1], v4[2], v4[3]};
    }
    {
      COMPILER_FENCE();
      const unsigned got = lds_arrive_nw(L_CNT + 16);
      f32x4 t4[4];
#pragma unroll
      for (int w = 0; w < 4; ++w) t4[w] = *reinterpret_cast<const f32x4*>(red2 + w * 256 + 4 * lane);
      SCHED();
      if (lds_done(got) == 3u) {
        const f32x4 t = (t4[0] + t4[1]) + (t4[2] + t4[3]);
        const unsigned co = WS_CX + ((h * 2 + b) * 256 + 64 * r4 + 32 * (c16 & 1) + 2 * (c16 >> 1)) * 8;
        st16c(rs, co, u32x4{__float_as_uint(t[0]), tag, __float_as_uint(t[1]), tag});
        st16c(rs, co + 128, u32x4{__float_as_uint(t[2]), tag, __float_as_uint(t[3]), tag});
      }
    }
    STAMP(12);
    STAMP_FLUSH(0); TR_FLUSH();
    return;
  }
  {
    unsigned op;
    {
      u32x4 c;
      const unsigned co = WS_CX + ((h * 2 + ((lane >> 3) & 1)) * 256 + (lane >> 5) * 128 + b * 64 + w4 * 16 + 2 * (lane & 7)) * 8;
      if (idx == 0 && w4 == 1) {
#pragma unroll 1
        for (int spin = 0; spin < SPIN_MAX; ++spin) {
          COMPILER_FENCE();
          if (__all(ld8c(rs, WS_QX + 65536 + (lane >> 1) * 2048 + (lane & 1) * 512)[1] == tag)) break;
          __builtin_amdgcn_s_sleep(8);
        }
      }
      if (w4 == 0) { shint<2048, 4>(ws + WS_CX + (h * 512 + b * 64) * 8, tag); lds_add(cnt + 7, 1u, lane); }
      else lds_wait_slow(cnt + 7, 1u);
      STAMP(3);
#pragma unroll 1
      for (int spin = 0; spin < SPIN_MAX; ++spin) {
        COMPILER_FENCE();
        c = ld16c(rs, co);
        if (__all((c[1] == tag) & (c[3] == tag))) break;
        __builtin_amdgcn_s_sleep(1);
      }
      float o0 = __uint_as_float(c[0]), o1 = __uint_as_float(c[2]);
      o0 += dpp<0x128>(o0); o1 += dpp<0x128>(o1);
      op = pk_bf(o0, o1);
    }
    STAMP(14);
    float* red3 = red + 4096;
    float a[2][8];
#pragma unroll
    for (int bb = 0; bb < 2; ++bb)
#pragma unroll
      for (int k = 0; k < 8; ++k) a[bb][k] = 0.f;
#pragma unroll
    for (int ip = 0; ip < 8; ++ip) {
      const unsigned v0 = __builtin_amdgcn_readlane(op, ip), v1 = __builtin_amdgcn_readlane(op, 32 + ip);
#pragma unroll
      for (int k = 0; k < 4; ++k) {
        a[0][2 * k] = dot2(Wb[2 * ip][k], v0, a[0][2 * k]); a[0][2 * k + 1] = dot2(Wb[2 * ip + 1][k], v0, a[0][2 * k + 1]);
        a[1][2 * k] = dot2(Wb[2 * ip][k], v1, a[1][2 * k]); a[1][2 * k + 1] = dot2(Wb[2 * ip + 1][k], v1, a[1][2 * k + 1]);
      }
    }
#pragma unroll
    for (int bb = 0; bb < 2; ++bb) {
      *reinterpret_cast<f32x4*>(red3 + (w4 * 2 + bb) * 512 + 8 * lane) = f32x4{a[bb][0], a[bb][1], a[bb][2], a[bb][3]};
      *reinterpret_cast<f32x4*>(red3 + (w4 * 2 + bb) * 512 + 8 * lane + 4) = f32x4{a[bb][4], a[bb][5], a[bb][6], a[bb][7]};
    }
    sbar4(cnt + 5, lane);
    f32x4 t = {0.f, 0.f, 0.f, 0.f};
#pragma unroll
    for (int w = 0; w < 4; ++w) t += *reinterpret_cast<const f32x4*>(red3 + w * 1024 + 4 * tid);
    st16c(rs, WS_GA + ((tid >> 7) * 64 + idx) * 2048 + (tid & 127) * 16, u32x4{pk_bf(t[0], t[1]), tag, pk_bf(t[2], t[3]), tag});
    STAMP(5);
  }
  fin(IC<1>{}, WS_GA, 0, idx == 0);
  STAMP(6);
  STAMP_FLUSH(0); TR_FLUSH();
}

extern "C" void kernel_launch(void* const* d_in, const int* in_sizes, int n_in, void* d_out, int out_size, void* d_ws, size_t ws_size, hipStream_t stream) {
  const u16* xA = (const u16*)d_in[0];
  const u16* ckv = (const u16*)d_in[1];
  const u16* w_q = (const u16*)d_in[2];
  const u16* w_uk = (const u16*)d_in[3];
  const u16* w_uv = (const u16*)d_in[4];
  const u16* w_o = (const u16*)d_in[5];
  const u16* xB = (const u16*)d_in[6];
  const u16* kc = (const u16*)d_in[7];
  const u16* vc = (const u16*)d_in[8];
  const u16* wq = (const u16*)d_in[9];
  const u16* wo = (const u16*)d_in[10];
  kmain<<<256, 512, 0, stream>>>(xA, ckv, w_q, w_uk, w_uv, w_o, xB, kc, vc, wq, wo, (char*)d_ws, (u16*)d_out);
}
```

```cpp
#include <hip/hip_runtime.h>
#include <hip/hip_bf16.h>
#include <stdint.h>

#define DI __device__ __forceinline__
typedef __attribute__((ext_vector_type(4))) unsigned u32x4;
typedef __attribute__((ext_vector_type(2))) unsigned u32x2;
typedef __attribute__((ext_vector_type(4))) float f32x4;
typedef __attribute__((ext_vector_type(2))) float f32x2;
typedef __attribute__((ext_vector_type(16))) float f32x16;
typedef __attribute__((ext_vector_type(8))) __bf16 bf16x8_t;
typedef __attribute__((ext_vector_type(2))) __bf16 bf16x2_t;
typedef __attribute__((ext_vector_type(4))) short s16x4;
typedef __attribute__((ext_vector_type(8))) short s16x8;
typedef unsigned short u16;
typedef unsigned long long u64;

constexpr int NS0 = 14, NS1 = 17, NS2 = 16, NS3 = 17;
constexpr u64 TL0 = 0xb2991b5d33cb1c1ull, TH0 = 0xf78d30ull, TL1 = 0x8e0759551349142ull, TH1 = 0x3eef7cefae7ull;
constexpr u64 TL2 = 0x96179a59238a183ull, TH2 = 0xffad71b68ull, TL3 = 0x89f718510308100ull, TH3 = 0x3ce76caeaa6ull;
constexpr int RA2 = 0xC0, RA3 = 0;
#define AHL 2
#define QPE 5
#define WBW 4
#define WB0 7
#define PCS 3
#define TL 2410
constexpr unsigned TA0 = 150, TA1 = 120, TA2 = 130, TA3 = 120, STG = 12, BSG = 6;
constexpr u64 LA2 = TH2 >> (6 * (NS2 - 11)), LA3 = TH3 >> (6 * (NS3 - 11));
constexpr u64 chunk_mask(u64 lo, u64 hi, int n) { u64 m = 0; for (int i = 0; i < n; ++i) m |= 1ull << ((i < 10 ? lo >> (6 * i) : hi >> (6 * (i - 10))) & 63); return m; }
static_assert((chunk_mask(TL0, TH0, NS0) | chunk_mask(TL1, TH1, NS1) | chunk_mask(TL2, TH2, NS2) | chunk_mask(TL3, TH3, NS3)) == ~0ull && (RA2 & RA3) == 0, "every chunk once");
static_assert(NS0 + NS1 + NS2 + NS3 == 64, "split sizes");
constexpr int NSMIN = 14, NSMAX = 19;
static_assert(NS0 >= NSMIN && NS1 >= NSMIN && NS2 - 1 >= NSMIN && NS3 - 1 >= NSMIN && NS0 <= NSMAX && NS1 + 1 <= NSMAX && NS2 <= NSMAX && NS3 <= NSMAX, "range");
constexpr int SK = 4096;
constexpr float C2 = 0.08838834764831845f * 1.4426950408889634f;
constexpr u64 GOLD = 0x9E3779B97F4A7C15ull;

constexpr unsigned WS_EP = 0;
constexpr unsigned WS_QX = 4096;
constexpr unsigned WS_QA = WS_QX + 2 * 32 * 256 * 8;
constexpr unsigned WS_AML = WS_QA + 2 * 32 * 512 * 2;
constexpr unsigned WS_CX = WS_AML + 32 * 2 * 64 * 8;
constexpr unsigned WS_GZ = WS_CX + 32 * 2 * 2 * 128 * 8;
constexpr unsigned WS_GB = WS_GZ + 256 * 128;
constexpr unsigned WS_GA = WS_GB + 2 * 128 * 256 * 8;
constexpr unsigned WS_APO = 2u << 20;
static_assert(WS_GA + 64 * 2 * 256 * 8 <= WS_APO, "layout");
constexpr unsigned WS_END = WS_APO + 32 * 2 * 64 * 256 * 2;
#define STAMP(i) do {} while (0)
#define STAMP_DECL do {} while (0)
#define STAMP_FLUSH(grp) do {} while (0)

#define TR() do {} while (0)
#define TR_FLUSH() do {} while (0)
DI float bflo(unsigned u) { return __uint_as_float(u << 16); }
DI float bfhi(unsigned u) { return __uint_as_float(u & 0xffff0000u); }
DI unsigned pk_bf(float a, float b) { const f32x2 v = {a, b}; return __builtin_bit_cast(unsigned, __builtin_convertvector(v, bf16x2_t)); }
DI float dot2(unsigned k, unsigned q, float acc) {
  return __builtin_amdgcn_fdot2_f32_bf16(__builtin_bit_cast(bf16x2_t, k), __builtin_bit_cast(bf16x2_t, q), acc, false);
}
DI unsigned plo(unsigned w0, unsigned w1) { return __builtin_amdgcn_perm(w1, w0, 0x05040100u); }
DI unsigned phi(unsigned w0, unsigned w1) { return __builtin_amdgcn_perm(w1, w0, 0x07060302u); }
template <int CTRL> DI float dpp(float x) {
  return __builtin_bit_cast(float, __builtin_amdgcn_mov_dpp(__builtin_bit_cast(int, x), CTRL, 0xf, 0xf, true));
}
DI float row16_sum(float x) { x += dpp<0xB1>(x); x += dpp<0x4E>(x); x += dpp<0x141>(x); x += dpp<0x128>(x); return x; }
DI float row16_max(float x) { x = fmaxf(x, dpp<0xB1>(x)); x = fmaxf(x, dpp<0x4E>(x)); x = fmaxf(x, dpp<0x141>(x)); x = fmaxf(x, dpp<0x128>(x)); return x; }
DI float xhalf_max(float x) { auto t = __builtin_amdgcn_permlane32_swap(__float_as_uint(x), __float_as_uint(x), false, false); return fmaxf(__uint_as_float(t[0]), __uint_as_float(t[1])); }
DI float xhalf_sum(float x) { auto t = __builtin_amdgcn_permlane32_swap(__float_as_uint(x), __float_as_uint(x), false, false); return __uint_as_float(t[0]) + __uint_as_float(t[1]); }
DI u32x4 ld16(const void* p) { return *reinterpret_cast<const u32x4*>(p); }
DI u32x4 ld16nt(const void* p) { return __builtin_nontemporal_load(reinterpret_cast<const u32x4*>(p)); }
DI float ex2(float x) { return __builtin_amdgcn_exp2f(x); }
#define SCHED() __builtin_amdgcn_sched_barrier(0)

typedef __amdgpu_buffer_rsrc_t rsrc_t;
DI u32x4 ld16c(rsrc_t rs, unsigned off) { return __builtin_amdgcn_raw_buffer_load_b128(rs, (int)off, 0, 16); }
DI u32x2 ld8c(rsrc_t rs, unsigned off) { return __builtin_amdgcn_raw_buffer_load_b64(rs, (int)off, 0, 16); }
DI void st16c(rsrc_t rs, unsigned off, u32x4 v) { __builtin_amdgcn_raw_buffer_store_b128(v, rs, (int)off, 0, 16); }
DI void st8c(rsrc_t rs, unsigned off, u32x2 v) { __builtin_amdgcn_raw_buffer_store_b64(v, rs, (int)off, 0, 16); }
typedef __attribute__((ext_vector_type(16))) unsigned u32x16;
template <int O1> DI void sld2(const char* p, u32x2& a, u32x2& b) {
  asm volatile("s_load_dwordx2 %0, %2, 0x0 glc\n\ts_load_dwordx2 %1, %2, %3 glc\n\ts_waitcnt lgkmcnt(0)" : "=&s"(a), "=&s"(b) : "s"(p), "n"(O1) : "memory");
}
DI u32x16 sld64(const char* p) {
  u32x16 v;
  asm volatile("s_load_dwordx16 %0, %1, 0x0 glc\n\ts_waitcnt lgkmcnt(0)" : "=&s"(v) : "s"(p) : "memory");
  return v;
}
DI void sld128(const char* p, u32x16 (&v)[2]) {
  asm volatile("s_load_dwordx16 %0, %2, 0x0 glc\n\ts_load_dwordx16 %1, %2, 0x40 glc\n\ts_waitcnt lgkmcnt(0)" : "=&s"(v[0]), "=&s"(v[1]) : "s"(p) : "memory");
}
constexpr int SPIN_MAX = 1 << 20;
constexpr unsigned OOB = 0x7ffffff0u;
DI void rawbar() { __builtin_amdgcn_sched_barrier(0); __builtin_amdgcn_s_barrier(); __builtin_amdgcn_sched_barrier(0); }
#define COMPILER_FENCE() asm volatile("" ::: "memory")
template <int O1, int SL> DI void shint(const char* p, unsigned tag) {
#pragma unroll 1
  for (int spin = 0; spin < SPIN_MAX; ++spin) {
    u32x2 a, b;
    sld2<O1>(p, a, b);
    if (((a[1] ^ tag) | (b[1] ^ tag)) == 0u) break;
    __builtin_amdgcn_s_sleep(SL);
  }
}

template <int N> struct IC { static constexpr int v = N; };
template <int I, int N, class F> DI void sfor(F&& f) { if constexpr (I < N) { f(IC<I>{}); sfor<I + 1, N>(f); } }
typedef __attribute__((address_space(3))) unsigned lu32;
DI void lds_wait(unsigned* cnt, unsigned target) {
#pragma unroll 1
  for (int spin = 0; spin < SPIN_MAX; ++spin) {
    if (__builtin_amdgcn_readfirstlane(__hip_atomic_load((lu32*)cnt, __ATOMIC_RELAXED, __HIP_MEMORY_SCOPE_WORKGROUP)) >= target) break;
    __builtin_amdgcn_s_sleep(1);
  }
  COMPILER_FENCE();
}
DI void lds_wait_slow(unsigned* cnt, unsigned target) {
#pragma unroll 1
  for (int spin = 0; spin < SPIN_MAX; ++spin) {
    if (__builtin_amdgcn_readfirstlane(__hip_atomic_load((lu32*)cnt, __ATOMIC_RELAXED, __HIP_MEMORY_SCOPE_WORKGROUP)) >= target) break;
    __builtin_amdgcn_s_sleep(3);
  }
  COMPILER_FENCE();
}
DI void lds_hold(unsigned* flag) {
#pragma unroll 1
  for (int spin = 0; spin < SPIN_MAX; ++spin) {
    if (__builtin_amdgcn_readfirstlane(__hip_atomic_load((lu32*)flag, __ATOMIC_RELAXED, __HIP_MEMORY_SCOPE_WORKGROUP)) == 0u) break;
    __builtin_amdgcn_s_sleep(2);
  }
}
DI unsigned lds_arrive_nw(unsigned addr) {
  unsigned old, one = 1u;
  asm volatile("s_mov_b64 exec, 1\n\tds_add_rtn_u32 %0, %1, %2\n\ts_mov_b64 exec, -1" : "=v"(old) : "v"(addr), "v"(one) : "memory");
  return old;
}
DI unsigned lds_done(unsigned old) { asm volatile("s_waitcnt lgkmcnt(0)" : "+v"(old) :: "memory"); return __builtin_amdgcn_readfirstlane(old); }
DI unsigned lds_arrive(unsigned* cnt, int) { return lds_done(lds_arrive_nw((unsigned)(size_t)(lu32*)cnt)); }
DI void lds_add(unsigned* cnt, unsigned v, int) {
  asm volatile("s_mov_b64 exec, 1\n\tds_add_u32 %0, %1\n\ts_mov_b64 exec, -1" :: "v"((unsigned)(size_t)(lu32*)cnt), "v"(v) : "memory");
}
DI void sbar4(unsigned* cnt, int lane) { if (lds_arrive(cnt, lane) < 3u) lds_wait(cnt, 4u); }
DI float rs32(float a, float b) { auto t = __builtin_amdgcn_permlane32_swap(__float_as_uint(a), __float_as_uint(b), false, false); return __uint_as_float(t[0]) + __uint_as_float(t[1]); }
DI float rs16(float a, float b) { auto t = __builtin_amdgcn_permlane16_swap(__float_as_uint(a), __float_as_uint(b), false, false); return __uint_as_float(t[0]) + __uint_as_float(t[1]); }
template <int N> DI unsigned rowbcast(unsigned x) { return (unsigned)__builtin_amdgcn_mov_dpp((int)x, 0x150 + N, 0xf, 0xf, true); }
DI void pin2(u32x4& a, u32x4& b) { asm volatile("" : "+v"(a), "+v"(b)); }
DI void use2(const u32x4& a, const u32x4& b) { asm volatile("" :: "v"(a), "v"(b)); }
DI bool nz8(const u32x4& v) { return ((v[0] | v[1]) != 0u) & ((v[2] | v[3]) != 0u); }

DI u32x4 qsum(const u32x4& g) {
  const auto ta = __builtin_amdgcn_permlane32_swap(g[0], g[0], false, false), tc = __builtin_amdgcn_permlane32_swap(g[2], g[2], false, false);
  const unsigned a = pk_bf(bflo(ta[0]) + bflo(ta[1]), bfhi(ta[0]) + bfhi(ta[1])), c = pk_bf(bflo(tc[0]) + bflo(tc[1]), bfhi(tc[0]) + bfhi(tc[1]));
  const auto ra = __builtin_amdgcn_permlane16_swap(a, a, false, false), rc = __builtin_amdgcn_permlane16_swap(c, c, false, false);
  return u32x4{ra[0], rc[0], ra[1], rc[1]};
}
DI unsigned toff(unsigned row, unsigned ch) { return 256u * row + 16u * (ch ^ (((row & 3) << 2) | ((row >> 2) & 3))); }
constexpr int SMEM = 160 * 1024;
constexpr int L_SCR = 131072;
constexpr int L_MRG = 65536;
constexpr int L_CNT = L_SCR + 32 * 1008;

struct KV { u32x4 r[8]; };

__global__ __launch_bounds__(512, 2) void kmain(const u16* __restrict__ xA, const u16* __restrict__ ckv, const u16* __restrict__ w_q, const u16* __restrict__ w_uk,
                                                const u16* __restrict__ w_uv, const u16* __restrict__ w_o,
                                                const u16* __restrict__ xB, const u16* __restrict__ kc, const u16* __restrict__ vc,
                                                const u16* __restrict__ wq, const u16* __restrict__ wo,
                                                char* __restrict__ ws, u16* __restrict__ out) {
  const unsigned tstart = (unsigned)wall_clock64();
  SCHED();
  __shared__ __attribute__((aligned(16))) char smem[SMEM];
  const int tid = threadIdx.x, blk = blockIdx.x, lane = tid & 63;
  const int wave = __builtin_amdgcn_readfirstlane(tid >> 6), w4 = wave & 3;
  const int cls = (blk >> 3) & 3;
  int idx = 8 * (blk & 7) + (blk >> 5);
  if (wave < 4) idx = (idx & ~7) | ((idx + (cls == 3 ? 2 : (cls == 2 ? 4 : 0))) & 7);
  const int h = idx >> 1, b = idx & 1;
  const rsrc_t rs = __builtin_amdgcn_make_buffer_rsrc(ws, 0, (int)WS_END, 0x00020000);
  const int r4 = lane >> 4, c16 = lane & 15;
  unsigned* cnt = reinterpret_cast<unsigned*>(smem + L_CNT);
  STAMP_DECL;
  STAMP(0);
  asm volatile("" :: "s"(xA), "s"(ckv), "s"(w_q), "s"(w_uk), "s"(w_uv), "s"(w_o), "s"(xB), "s"(kc), "s"(vc), "s"(wq), "s"(wo), "s"(ws), "s"(out), "s"(tstart));
  unsigned tag, tlow;
  u32x2 epw;
  auto take_epoch = [&]() {
    asm volatile("" : "+v"(epw));
    const u64 t64 = (((u64)(unsigned)__builtin_amdgcn_readfirstlane(epw[1]) << 32) | (unsigned)__builtin_amdgcn_readfirstlane(epw[0])) + GOLD;
    tag = (unsigned)(t64 >> 32); tlow = (unsigned)t64;
    asm volatile("s_waitcnt lgkmcnt(0)" : "+s"(tag), "+s"(tlow) :: "memory");
  };
  if (wave >= 4) {
    if (tid - 256 < 16) cnt[tid - 256] = 0u;
    asm volatile("s_waitcnt lgkmcnt(0)" ::: "memory");
    const unsigned pTL = TL, pTA = cls == 0 ? TA0 : (cls == 1 ? TA1 : (cls == 2 ? TA2 : TA3)), pOF = 0, pSTG = STG, pBSG = BSG, pRA2 = RA2, pRA3 = RA3;
    rawbar();
    const int e2 = (pRA2 >> (idx & 7)) & 1, e3 = (pRA3 >> (idx & 7)) & 1;
    const int ns = cls == 0 ? NS0 : (cls == 1 ? NS1 + e2 + e3 : (cls == 2 ? NS2 - e2 : NS3 - e3));
    const size_t kvo = ((size_t)(b * 32 + h) * SK + w4 * 16) * 256;
    const char* kb = reinterpret_cast<const char*>(kc) + kvo;
    const char* vb = reinterpret_cast<const char*>(vc) + kvo;
    const unsigned lo = lane * 16u;
    u64 tlo = cls == 0 ? TL0 : (cls == 1 ? TL1 : (cls == 2 ? TL2 : TL3));
    u64 thi = cls == 0 ? TH0 : (cls == 1 ? TH1 | ((e2 ? LA2 : LA3) << (6 * (NS1 - 10))) : (cls == 2 ? TH2 : TH3));
    const u64 tlo0 = tlo, thi0 = thi;
    int hn = 0;
    const char* hb = kb;
    const char* pc[2];
    auto next2 = [&]() {
#pragma unroll
      for (int i = 0; i < 2; ++i) {
        if (hn == ns) { tlo = tlo0; thi = thi0; hb = vb; }
        ++hn;
        pc[i] = hb + ((((unsigned)tlo + (unsigned)idx) & 63u) << 14);
        tlo = (tlo >> 6) | ((thi & 63u) << 54); thi >>= 6;
      }
    };
    KV A, B;
    u32x4 W[8];
    u32x4 T[4][8];
    auto ldp = [&](KV& S, auto P) {
      constexpr int p = P.v;
      const char* q = pc[p >> 1] + lo + (p & 1) * 2048;
      S.r[2 * p] = ld16nt(q); S.r[2 * p + 1] = ld16nt(q + 1024);
    };
    auto landed = [&](KV& S, auto P) { pin2(S.r[2 * P.v], S.r[2 * P.v + 1]); };
    KV X;
    const unsigned qo = WS_QX + ((32 + h) * 2 + b) * 1024 + lane * 16;
    u32x4 g0 = {0u, 0u, 0u, 0u};
    {
      unsigned tk = 0u;
      if (tid == 256) tk = __builtin_nontemporal_load(reinterpret_cast<const unsigned*>(kb + cls * 128));
      lds_wait_slow(cnt + 14, 4u);
      asm volatile("" :: "v"(tk));
    }
    STAMP(10);
    sfor<0, 12>([&](auto I) {
      constexpr int i = I.v, j = i - 4;
      if constexpr ((i & 3) == 0) next2();
      if constexpr (j >= 0) { if constexpr (j < 4) landed(X, IC<j & 3>{}); else landed(B, IC<j & 3>{}); }
      if constexpr (j >= 0 && (j & 1)) TR();
      if constexpr (i == QPE) g0 = ld16c(rs, qo);
      if constexpr (i < 4) ldp(X, IC<i & 3>{}); else if constexpr (i < 8) ldp(B, IC<i & 3>{}); else ldp(A, IC<i & 3>{});
      SCHED();
    });
    {
      const u32x2 t = *reinterpret_cast<const u32x2*>(cnt + 16);
      tlow = __builtin_amdgcn_readfirstlane(t[0]); tag = __builtin_amdgcn_readfirstlane(t[1]);
    }
    const char* wp = reinterpret_cast<const char*>(wo) + ((size_t)h * 128 + r4 * 8) * 1024 + w4 * 256 + c16 * 16;
    u32x4 qpk;
    unsigned tvk;
    {
      unsigned tv = 0u;
      if (tid == 256) tv = __builtin_nontemporal_load(reinterpret_cast<const unsigned*>(vb + cls * 128));
      if (QPE >= 12 || !__all((g0[1] == tag) & (g0[3] == tag))) {
#pragma unroll 1
        for (int spin = 0; spin < SPIN_MAX; ++spin) {
          COMPILER_FENCE();
          g0 = ld16c(rs, qo);
          if (__all((g0[1] == tag) & (g0[3] == tag))) break;
        }
      }
      qpk = qsum(g0);
      tvk = tv;
    }
    STAMP(7);
    const bool holds = cls == 0;
    float m = 0.f, l = 0.f, acc[8];
    unsigned sc[5];
#pragma unroll
    for (int k = 0; k < 8; ++k) acc[k] = 0.f;
#pragma unroll
    for (int k = 0; k < 5; ++k) sc[k] = 0xff800000u;
    int vn = 0;
    unsigned due = tstart + pTL - pOF - (unsigned)(ns - 5) * pTA - (3 - w4) * pSTG - ((idx * 5 + cls * 3) & 15) * pBSG;
    unsigned rwo;
    const unsigned zo = WS_GZ + (b * 32 + h) * 512;
    u32x2 zs[4];
    float wr_;
    auto zreq = [&]() {
      asm volatile("s_load_dwordx2 %0, %4, 0x0 glc\n\ts_load_dwordx2 %1, %4, 0x80 glc\n\ts_load_dwordx2 %2, %4, 0x100 glc\n\ts_load_dwordx2 %3, %4, 0x180 glc"
                   : "=&s"(zs[0]), "=&s"(zs[1]), "=&s"(zs[2]), "=&s"(zs[3]) : "s"(ws + zo) : "memory");
    };
    auto zget = [&]() {
#pragma unroll 1
      for (int spin = 0; spin < SPIN_MAX; ++spin) {
        asm volatile("s_waitcnt lgkmcnt(0)" : "+s"(zs[0]), "+s"(zs[1]), "+s"(zs[2]), "+s"(zs[3]) :: "memory");
        if (((zs[0][1] ^ tag) | (zs[1][1] ^ tag) | (zs[2][1] ^ tag) | (zs[3][1] ^ tag)) == 0u) break;
        __builtin_amdgcn_s_sleep(2);
        zreq();
      }
      asm volatile("s_waitcnt lgkmcnt(0)" : "+s"(zs[0]), "+s"(zs[1]), "+s"(zs[2]), "+s"(zs[3]) :: "memory");
      const float z0 = __uint_as_float(zs[0][0]), z1 = __uint_as_float(zs[1][0]), z2 = __uint_as_float(zs[2][0]), z3 = __uint_as_float(zs[3][0]);
      const float Z = fmaxf(fmaxf(z0, z1), fmaxf(z2, z3));
      wr_ = ex2(fmaf(m, C2, -Z)) * __builtin_amdgcn_rcpf((ex2(z0 - Z) + ex2(z1 - Z)) + (ex2(z2 - Z) + ex2(z3 - Z)));
      asm volatile("" : "+v"(wr_));
    };
    auto qk = [&](const u32x4* K, auto I) {
      float x[4];
#pragma unroll
      for (int t = 0; t < 4; ++t) {
        x[t] = 0.f;
#pragma unroll
        for (int k = 0; k < 4; ++k) x[t] = dot2(K[t][k], qpk[k], x[t]);
      }
      const float u0 = (c16 & 1 ? x[1] : x[0]) + dpp<0xB1>(c16 & 1 ? x[0] : x[1]), u1 = (c16 & 1 ? x[3] : x[2]) + dpp<0xB1>(c16 & 1 ? x[2] : x[3]);
      float v = (c16 & 2 ? u1 : u0) + dpp<0x4E>(c16 & 2 ? u0 : u1);
      v += dpp<0x124>(v); v += dpp<0x128>(v);
      sc[I.v >> 2] = __builtin_amdgcn_update_dpp(sc[I.v >> 2], __float_as_uint(v), 0xE4, 0xf, 1 << (I.v & 3), false);
      asm volatile("" : "+v"(sc[I.v >> 2]));
    };
    auto soft = [&]() {
      m = row16_max(fmaxf(fmaxf(fmaxf(__uint_as_float(sc[0]), __uint_as_float(sc[1])), fmaxf(__uint_as_float(sc[2]), __uint_as_float(sc[3]))), __uint_as_float(sc[4])));
      const float mc = m * C2;
      l = 0.f;
#pragma unroll
      for (int k = 0; k < 5; ++k) {
        const float p = ex2(fmaf(__uint_as_float(sc[k]), C2, -mc));
        sc[k] = pk_bf(p, dpp<0xB1>(p));
        l = dot2(sc[k], 0x3f003f00u, l);
      }
      l = row16_sum(l);
    };
    auto pv = [&](const u32x4* V) {
      const unsigned p01 = rowbcast<0>(sc[0]), p23 = rowbcast<2>(sc[0]);
#pragma unroll
      for (int k = 0; k < 4; ++k) { acc[2 * k] = dot2(plo(V[0][k], V[1][k]), p01, acc[2 * k]); acc[2 * k + 1] = dot2(phi(V[0][k], V[1][k]), p01, acc[2 * k + 1]); }
#pragma unroll
      for (int k = 0; k < 4; ++k) { acc[2 * k] = dot2(plo(V[2][k], V[3][k]), p23, acc[2 * k]); acc[2 * k + 1] = dot2(phi(V[2][k], V[3][k]), p23, acc[2 * k + 1]); }
      sc[0] = (unsigned)__builtin_amdgcn_mov_dpp((int)sc[0], 0x12C, 0xf, 0xf, true);
      if ((++vn & 3) == 0) { sc[0] = sc[1]; sc[1] = sc[2]; sc[2] = sc[3]; sc[3] = sc[4]; }
      asm volatile("" : "+v"(acc[0]), "+v"(acc[1]), "+v"(acc[2]), "+v"(acc[3]), "+v"(acc[4]), "+v"(acc[5]), "+v"(acc[6]), "+v"(acc[7]));
    };
    auto step = [&](KV& S, KV& N, auto SI, auto REFILL, auto WR) {
      constexpr bool refill = REFILL.v == 1;
      constexpr int wr = WR.v;
      if constexpr (refill) next2();
      if constexpr (refill && SI.v >= 3) {
        if constexpr (SI.v >= PCS) {
#pragma unroll 1
          for (int spin = 0; spin < 4096; ++spin) { if ((int)((unsigned)wall_clock64() - due) >= 0) break; __builtin_amdgcn_s_sleep(1); }
        }
        due += pTA;
      }
      constexpr int ah = wr < 0 ? AHL : 0;
      auto point = [&](auto P) {
        if constexpr (refill) {
          if constexpr (P.v == 0 && ah > 0) { if constexpr (wr >= 0) { if (holds) lds_hold(cnt + 8); } sfor<0, ah>([&](auto Q) { ldp(N, Q); }); SCHED(); }
          landed(S, P);
          if constexpr (P.v & 1) TR();
          if constexpr (P.v + ah < 4) { if constexpr ((P.v & 1) == 0 && wr >= 0) { if (holds) lds_hold(cnt + 8); } ldp(N, IC<(P.v + ah) & 3>{}); }
          if constexpr (wr >= 0 && wr < 8) W[(4 * wr + P.v) & 7] = ld16(wp + (((4 * wr + P.v) >> 3) * 32 + ((4 * wr + P.v) & 7)) * 1024);
          SCHED();
        }
      };
      sfor<0, 2>([&](auto H) {
        constexpr int i = 2 * SI.v + H.v;
        if constexpr (REFILL.v == 2 && H.v == 0) { zreq(); SCHED(); }
        point(IC<2 * H.v>{});
        point(IC<2 * H.v + 1>{});
        if constexpr (i < NSMIN) qk(S.r + 4 * H.v, IC<i>{});
        else if constexpr (i > NSMAX) pv(S.r + 4 * H.v);
        else {
          if (i < ns) { if constexpr (i < NSMAX) qk(S.r + 4 * H.v, IC<i>{}); }
          else { if (i == ns) soft(); pv(S.r + 4 * H.v); }
        }
        SCHED();
      });
      if constexpr (REFILL.v == 2) {
        zget();
        STAMP(1);
        *reinterpret_cast<u32x4*>(smem + rwo) = u32x4{pk_bf(acc[0] * wr_, acc[1] * wr_), pk_bf(acc[2] * wr_, acc[3] * wr_), pk_bf(acc[4] * wr_, acc[5] * wr_), pk_bf(acc[6] * wr_, acc[7] * wr_)};
        COMPILER_FENCE();
      }
      if constexpr (wr >= 1) {
        constexpr int t = (wr - 1) >> 1, j0 = ((wr - 1) & 1) * 2;
#pragma unroll
        for (int jj = 0; jj < 2; ++jj)
#pragma unroll
          for (int e = 0; e < 4; ++e) {
            const unsigned a_ = W[(4 * wr - 4 + 2 * jj) & 7][e], b_ = W[(4 * wr - 3 + 2 * jj) & 7][e];
            T[t][2 * e][j0 + jj] = plo(a_, b_); T[t][2 * e + 1][j0 + jj] = phi(a_, b_);
          }
#pragma unroll
        for (int e = 0; e < 8; e += 2) pin2(T[t][e], T[t][e + 1]);
      }
      SCHED();
    };
    step(B, B, IC<1>{}, IC<0>{}, IC<-1>{});
    step(A, B, IC<2>{}, IC<1>{}, IC<0>{});
    asm volatile("" :: "v"(tvk));
    step(X, X, IC<0>{}, IC<0>{}, IC<-1>{});
    sfor<0, 4>([&](auto I) {
      step(B, A, IC<2 * I.v + 3>{}, IC<1>{}, IC<2 * I.v + 1>{});
      if constexpr (I.v == 1) STAMP(8);
      if constexpr (I.v == 3) STAMP(9);
      if constexpr (I.v == 3) {
        float* ml = reinterpret_cast<float*>(cnt + 32);
        ml[(c16 & 1) * 16 + w4 * 4 + r4] = (c16 & 1) ? l : m;
        COMPILER_FENCE();
        const unsigned got = lds_arrive_nw(L_CNT + 44);
        const float mr = ml[c16], lr = ml[16 + c16];
        SCHED();
        if (lds_done(got) == 3u) {
          const float M = row16_max(mr);
          const unsigned z = __float_as_uint(fmaf(M, C2, __builtin_amdgcn_logf(row16_sum(ex2((mr - M) * C2) * lr))));
          asm volatile("buffer_store_dwordx4 %0, %1, %2, 0 offen sc1\n\ts_nop 2" :: "v"(u32x4{z, tag, z, tag}), "v"(lane < 8 ? zo + cls * 128 + lane * 16 : OOB), "s"(rs) : "memory");
        }
        SCHED();
      }
      step(A, B, IC<2 * I.v + 4>{}, IC<1>{}, IC<2 * I.v + 2>{});
    });
    unsigned yoff = WS_GB + ((b * 128 + h * 4 + cls) * 256 + 64 * w4 + lane) * 8, rro = L_MRG + c16 * 272 + r4 * 16;
    rwo = L_MRG + (w4 * 4 + r4) * 272 + c16 * 16;
    asm volatile("" : "+v"(yoff), "+v"(rwo), "+v"(rro));
    if (cls == 0) lds_wait(cnt + 1, 4u);
    const int nit = (ns - 12) >> 1;
#pragma unroll 1
    for (int i = 0; i < nit; ++i) {
      step(B, A, IC<11>{}, IC<1>{}, IC<-1>{});
      step(A, B, IC<11>{}, IC<1>{}, IC<-1>{});
    }
    if (ns & 1) { step(B, A, IC<11>{}, IC<1>{}, IC<-1>{}); step(A, A, IC<11>{}, IC<2>{}, IC<-1>{}); }
    else step(B, B, IC<11>{}, IC<2>{}, IC<-1>{});
    {
      u32x4 af[4];
      unsigned got = lds_arrive_nw(L_CNT), need = 3u;
#pragma unroll 1
      for (int spin = 0; spin < SPIN_MAX; ++spin) {
#pragma unroll
        for (int t = 0; t < 4; ++t) af[t] = *reinterpret_cast<const u32x4*>(smem + rro + t * 64);
        SCHED();
        if (lds_done(got) >= need) break;
        need = 4u;
        __builtin_amdgcn_s_sleep(1);
        asm volatile("ds_read_b32 %0, %1" : "=v"(got) : "v"((unsigned)L_CNT) : "memory");
      }
      SCHED();
      f32x4 d[8];
      float a[8], v4[4];
      sfor<0, 5>([&](auto I) {
        constexpr int i = I.v;
        sfor<0, 8>([&](auto J) {
          constexpr int e = i + 4 * (J.v & 1), t = J.v >> 1;
          if constexpr (i < 4) { d[e] = __builtin_amdgcn_mfma_f32_16x16x32_bf16(__builtin_bit_cast(bf16x8_t, af[t]), __builtin_bit_cast(bf16x8_t, T[t][e]), t ? d[e] : f32x4{0.f, 0.f, 0.f, 0.f}, 0, 0, 0); SCHED(); }
          if constexpr (i >= 1 && (J.v == 4 || J.v == 5)) { const f32x2 u = f32x2{d[e - 1][0], d[e - 1][1]} + f32x2{d[e - 1][2], d[e - 1][3]}; a[e - 1] = u[0] + u[1]; asm volatile("" : "+v"(a[e - 1])); }
          if constexpr (i >= 1 && J.v == 6) v4[i - 1] = rs32(a[i - 1], a[i + 3]);
          if constexpr (i < 4) SCHED();
        });
      });
      st8c(rs, yoff, u32x2{pk_bf(rs16(v4[0], v4[2]), rs16(v4[1], v4[3])), tag});
    }
    STAMP(2);
    STAMP_FLUSH(1);
    TR_FLUSH();
    return;
  }
  const rsrc_t ro = __builtin_amdgcn_make_buffer_rsrc(out, 0, 4096, 0x00020000);
  auto fin = [&](auto NL, unsigned base, unsigned col, bool bump) {
    constexpr int nl = NL.v;
    unsigned doff = base + (((idx >> 5) * 4 + w4) * 16 * nl + (lane >> 2)) * 2048 + (idx & 31) * 64 + (lane & 3) * 16;
    unsigned fwo = 32768 + (c16 < 4 ? (w4 * 16 + 4 * c16 + r4) * 4 : 256 + lane * 4);
    unsigned fro = 32768 + (lane & 7) * 8, foo = lane < 8 ? ((idx >> 5) * 1024 + col + (nl == 2 ? 16 * (idx & 24) + 64 * (idx & 1) + (idx & 6) + 8 * lane : 16 * (idx & 31) + 2 * lane)) * 2 : OOB;
    asm volatile("" : "+v"(doff), "+v"(fwo), "+v"(fro), "+v"(foo));
    lds_wait(cnt + 0, 4u);
    STAMP(4);
    u32x4 d[nl];
    unsigned eh = 1u;
#pragma unroll 1
    for (int spin = SPIN_MAX; spin > 0; --spin) {
      COMPILER_FENCE();
      if (eh != 0u) {
#pragma unroll
        for (int j = 0; j < nl; ++j) d[j] = ld16c(rs, doff + j * 32768);
        eh = 0u;
#pragma unroll
        for (int j = 0; j < nl; ++j) { eh |= (d[j][1] ^ tag) | (d[j][3] ^ tag); asm volatile("" : "+v"(eh)); }
      }
      if (__builtin_amdgcn_ballot_w64(eh != 0u) == 0ull) break;
    }
    STAMP(11);
    float y[4] = {bflo(d[0][0]), bfhi(d[0][0]), bflo(d[0][2]), bfhi(d[0][2])};
    if constexpr (nl == 2) { y[0] += bflo(d[1][0]); y[1] += bfhi(d[1][0]); y[2] += bflo(d[1][2]); y[3] += bfhi(d[1][2]); }
    float t = rs16(rs32(y[0], y[2]), rs32(y[1], y[3]));
    t += dpp<0x124>(t); t += dpp<0x128>(t);
    *reinterpret_cast<float*>(smem + fwo) = t;
    COMPILER_FENCE();
    const unsigned got = lds_arrive_nw(L_CNT + 24);
    f32x2 t2[4];
#pragma unroll
    for (int w = 0; w < 4; ++w) t2[w] = *reinterpret_cast<const f32x2*>(smem + fro + w * 64);
    SCHED();
    if (lds_done(got) == 3u) {
      const f32x2 r = (t2[0] + t2[1]) + (t2[2] + t2[3]);
      __builtin_amdgcn_raw_buffer_store_b32(pk_bf(r[0], r[1]), ro, (int)foo, 0, 0);
      if (bump) st8c(rs, lane == 0 ? WS_EP : OOB, u32x2{tlow, tag});
    }
  };
  epw = ld8c(rs, WS_EP);
  SCHED();
  u32x4 Wa[16], Wb[16], xv[4];
  const u16* pb;
  const unsigned qa_off = WS_QA + (((c16 >> 1) & 1) * 32 + h) * 1024 + (b * 256 + w4 * 64 + 16 * r4 + 8 * (c16 & 1)) * 2;
  {
    const u16* pa; size_t sa;
    if (cls) {
      const size_t o_ = ((size_t)(b * 256 + w4 * 64 + 16 * r4) * 32 + h) * 128 + 8 * c16;
      pa = (cls == 1 ? w_q : (cls == 2 ? w_uk : wq)) + o_; sa = 4096;
      pb = cls == 2 ? w_uv + o_ : (cls == 1 ? w_o : wo) + ((size_t)h * 128 + b * 64 + w4 * 2) * 512 + 8 * lane;
      if (cls == 1 && c16 < 4) st16c(rs, qa_off, u32x4{0u, 0u, 0u, 0u});
    } else { pa = ckv + ((size_t)idx * 128 + w4) * 512 + 8 * lane; sa = 2048; pb = pa + 64 * 512; }
    const u16* xp = (cls == 1 ? xA : xB) + b * 256 + w4 * 64 + 16 * r4;
    SCHED();
    sfor<0, 4>([&](auto J) { xv[J.v] = ld16(xp + (J.v >> 1) * 512 + 8 * (J.v & 1)); });
    SCHED();
    sfor<0, 16>([&](auto I) { Wa[I.v] = ld16nt(pa + I.v * sa); });
    SCHED();
    rawbar();
    take_epoch();
    *reinterpret_cast<u32x2*>(cnt + 16) = u32x2{tlow, tag};
    COMPILER_FENCE();
  }
  STAMP(13);
  float* red = reinterpret_cast<float*>(smem);
  if (cls & 1) {
    float acc[2][8];
#pragma unroll
    for (int bb = 0; bb < 2; ++bb)
#pragma unroll
      for (int k = 0; k < 8; ++k) acc[bb][k] = 0.f;
    sfor<0, 8>([&](auto IP) {
      constexpr int ip = IP.v, j = ip - (8 - WBW);
      use2(Wa[2 * ip], Wa[2 * ip + 1]);
      if constexpr (ip & 1) TR();
      if constexpr (j >= 0) { Wb[2 * j] = ld16(pb + j * 4096); Wb[2 * j + 1] = ld16(pb + j * 4096 + 512); }
      SCHED();
      const unsigned xp0 = xv[ip >> 2][ip & 3], xp1 = xv[2 + (ip >> 2)][ip & 3];
#pragma unroll
      for (int k = 0; k < 4; ++k) {
        const unsigned lo = plo(Wa[2 * ip][k], Wa[2 * ip + 1][k]), hi = phi(Wa[2 * ip][k], Wa[2 * ip + 1][k]);
        acc[0][2 * k] = dot2(lo, xp0, acc[0][2 * k]); acc[0][2 * k + 1] = dot2(hi, xp0, acc[0][2 * k + 1]);
        acc[1][2 * k] = dot2(lo, xp1, acc[1][2 * k]); acc[1][2 * k + 1] = dot2(hi, xp1, acc[1][2 * k + 1]);
      }
      SCHED();
    });
    {
      float v8[8], v4[4];
#pragma unroll
      for (int k = 0; k < 8; ++k) v8[k] = rs32(acc[0][k], acc[1][k]);
#pragma unroll
      for (int k = 0; k < 4; ++k) v4[k] = rs16(v8[k], v8[k + 4]);
      *reinterpret_cast<f32x4*>(red + (w4 * 2 + (lane >> 5)) * 128 + 8 * c16 + 4 * (r4 & 1)) = f32x4{v4[0], v4[1], v4[2], v4[3]};
    }
    COMPILER_FENCE();
    const unsigned got = lds_arrive_nw(L_CNT + 8);
    f32x4 t4[4];
#pragma unroll
    for (int w = 0; w < 4; ++w) t4[w] = *reinterpret_cast<const f32x4*>(red + (w * 2 + (lane >> 5)) * 128 + 8 * c16 + 4 * (r4 & 1));
    SCHED();
    if (lds_done(got) == 3u) {
      const f32x4 t = (t4[0] + t4[1]) + (t4[2] + t4[3]);
      st16c(rs, WS_QX + (((cls >> 1) * 32 + h) * 2 + (lane >> 5)) * 1024 + b * 512 + (lane & 31) * 16, u32x4{pk_bf(t[0], t[1]), tag, pk_bf(t[2], t[3]), tag});
      STAMP(15);
      SCHED();
      lds_add(cnt + 2, 1u, lane);
    } else lds_wait(cnt + 2, 5u);
    sfor<WBW, 8>([&](auto J) {
      constexpr int j = J.v;
      use2(Wb[2 * (j - WBW)], Wb[2 * (j - WBW) + 1]);
      if constexpr ((j - WBW) & 1) TR();
      Wb[2 * j] = ld16(pb + j * 4096); Wb[2 * j + 1] = ld16(pb + j * 4096 + 512);
      if constexpr (j == 7) lds_add(cnt + 14, 1u, lane);
      SCHED();
    });
    {
      const unsigned tw = __builtin_nontemporal_load(reinterpret_cast<const unsigned*>(out + (idx >> 5) * 1024 + (cls >> 1) * 512 + 16 * (idx & 31)));
      sfor<8 - WBW, 8>([&](auto J) { use2(Wb[2 * J.v], Wb[2 * J.v + 1]); if constexpr (J.v & 1) TR(); });
      asm volatile("" :: "v"(tw));
    }
    STAMP(8);
  } else if (cls == 2) {
    sfor<0, 16 - WBW>([&](auto IP) {
      constexpr int ip = IP.v, j = ip - (8 - WBW);
      if constexpr (ip < 8) use2(Wa[2 * ip], Wa[2 * ip + 1]); else use2(Wb[2 * ip - 16], Wb[2 * ip - 15]);
      if constexpr (ip & 1) TR();
      if constexpr (j >= 0) { Wb[2 * j] = ld16nt(pb + j * 8192); Wb[2 * j + 1] = ld16nt(pb + j * 8192 + 4096); }
      SCHED();
    });
    u32x4 g0, g1;
    const unsigned qo = WS_QX + h * 2048 + lane * 16;
    shint<512, 1>(ws + WS_QX + h * 2048, tag);
    STAMP(5);
#pragma unroll 1
    for (int spin = 0; spin < SPIN_MAX; ++spin) {
      COMPILER_FENCE();
      g0 = ld16c(rs, qo); g1 = ld16c(rs, qo + 1024);
      SCHED();
      lds_add(cnt + 14, spin == 0, lane);
      SCHED();
      if (__all((g0[1] == tag) & (g0[3] == tag) & (g1[1] == tag) & (g1[3] == tag))) break;
      __builtin_amdgcn_s_sleep(1);
    }
    sfor<8 - WBW, 8>([&](auto J) { if constexpr (J.v & 1) TR(); });
    const u32x4 q0 = qsum(g0), q1 = qsum(g1);
    STAMP(9);
    float v[8];
    {
      float u[16];
#pragma unroll
      for (int i = 0; i < 16; ++i) {
        float a = 0.f, c = 0.f;
#pragma unroll
        for (int k = 0; k < 4; ++k) { a = dot2(Wa[i][k], q0[k], a); c = dot2(Wa[i][k], q1[k], c); }
        u[i] = (c16 & 2 ? c : a) + dpp<0x4E>(c16 & 2 ? a : c);
        asm volatile("" : "+v"(u[i]));
      }
#pragma unroll
      for (int i = 0; i < 8; ++i) {
        float t = (c16 & 1 ? u[i + 8] : u[i]) + dpp<0xB1>(c16 & 1 ? u[i] : u[i + 8]);
        asm volatile("" : "+v"(t));
        t += dpp<0x124>(t);
        asm volatile("" : "+v"(t));
        t += dpp<0x128>(t);
        asm volatile("" : "+v"(t));
        v[i] = t;
      }
    }
    u32x4 o = {pk_bf(v[0], v[1]), pk_bf(v[2], v[3]), pk_bf(v[4], v[5]), pk_bf(v[6], v[7])};
    if ((o[0] | o[1]) == 0u) o[0] = 0x8000u;
    if ((o[2] | o[3]) == 0u) o[2] = 0x8000u;
    if (c16 < 4) st16c(rs, qa_off, o);
    STAMP(10);
  }
  asm volatile("" :: "v"(xv[0]), "v"(xv[1]), "v"(xv[2]), "v"(xv[3]));
  SCHED();
  if (cls == 3) {
    fin(IC<2>{}, WS_GB, 512, false);
    STAMP(6);
    STAMP_FLUSH(0); TR_FLUSH();
    return;
  }
  if (cls == 0) {
    char* scr = smem + L_SCR;
    const int ab = idx >> 5, r = lane & 31, hh = lane >> 5;
    const unsigned fo = WS_QX + (w4 * 8 + (c16 >> 1)) * 2048 + (c16 & 1) * 512 + r4 * 8;
    u32x2 fl;
    u32x4 g[9];
    const unsigned o8 = WS_QA + (ab * 32 + w4 * 8) * 1024 + lane * 16, ol = WS_QA + (ab * 32 + r) * 1024 + 992 + 16 * hh;
#pragma unroll
    for (int i = 0; i < 32; ++i) {
      if (i + WB0 >= 16 && i + WB0 < 32) Wb[(i + WB0) & 15] = ld16nt(pb + ((i + WB0) & 15) * 2048);
      if (i + WB0 == 31) lds_add(cnt + 14, 1u, lane);
      if (i == 30) fl = ld8c(rs, fo);
      SCHED();
      *reinterpret_cast<u32x4*>(smem + ((i >> 3) * 4 + r4) * 8192 + toff(4 * (i & 7) + w4, c16)) = i < 16 ? Wa[i & 15] : Wb[i & 15];
      if ((i & 3) == 3) TR();
      SCHED();
    }
    STAMP(8);
    u32x4 qlast;
    {
      if (!__all(fl[1] == tag)) {
#pragma unroll 1
        for (int spin = 0; spin < SPIN_MAX; ++spin) {
          COMPILER_FENCE();
          if (__all(ld8c(rs, fo)[1] == tag)) break;
        }
      }
      {
        const char* hp = ws + WS_QA + ab * 32768 + w4 * 8192 + ((idx + w4 * 11) & 31) * 128;
#pragma unroll 1
        for (int spin = 0; spin < SPIN_MAX; ++spin) {
          u32x2 a, c;
          sld2<4096>(hp, a, c);
          if ((a[0] | a[1]) != 0u && (c[0] | c[1]) != 0u) break;
          __builtin_amdgcn_s_sleep(2);
        }
      }
      STAMP(5);
      sbar4(cnt + 7, lane);
      STAMP(12);
      unsigned pend = 0x1ffu;
#pragma unroll 1
      for (int spin = 0; spin < SPIN_MAX && pend; ++spin) {
        COMPILER_FENCE();
        u32x4 t[9];
#pragma unroll
        for (int i = 0; i < 9; ++i) t[i] = ld16c(rs, (pend >> i & 1) ? (i < 8 ? o8 + i * 1024 : ol) : OOB);
#pragma unroll
        for (int i = 0; i < 9; ++i) {
          if (pend >> i & 1) g[i] = t[i];
          if (__all(nz8(t[i]))) pend &= ~(1u << i);
        }
      }
      STAMP(9);
      qlast = g[8];
#pragma unroll
      for (int i = 0; i < 8; ++i)
        if (lane < 62) *reinterpret_cast<u32x4*>(scr + (w4 * 8 + i) * 1008 + 16 * lane) = g[i];
    }
    sbar4(cnt + 3, lane);
    STAMP(10);
    f32x16 sacc;
#pragma unroll
    for (int i = 0; i < 16; ++i) sacc[i] = 0.f;
#pragma unroll
    for (int jj = 0; jj < 32; ++jj) {
      const u32x4 af = *reinterpret_cast<const u32x4*>(smem + (w4 * 4 + (jj >> 3)) * 8192 + toff(r, 2 * (jj & 7) + hh));
      const u32x4 bf = jj < 31 ? *reinterpret_cast<const u32x4*>(scr + r * 1008 + 16 * (2 * (jj < 31 ? jj : 0) + hh)) : qlast;
      sacc = __builtin_amdgcn_mfma_f32_32x32x16_bf16(__builtin_bit_cast(bf16x8_t, af), __builtin_bit_cast(bf16x8_t, bf), sacc, 0, 0, 0);
    }
    float m = sacc[0];
#pragma unroll
    for (int i = 1; i < 16; ++i) m = fmaxf(m, sacc[i]);
    m = xhalf_max(m);
    if (hh == 0) *reinterpret_cast<float*>(scr + r * 1008 + 992 + 4 * w4) = m;
    sbar4(cnt + 4, lane);
    STAMP(11);
    {
      const f32x4 m4 = *reinterpret_cast<const f32x4*>(scr + r * 1008 + 992);
      m = fmaxf(fmaxf(m4[0], m4[1]), fmaxf(m4[2], m4[3]));
    }
    const float mc = m * C2;
    float p[16], ls = 0.f;
#pragma unroll
    for (int i = 0; i < 16; ++i) { p[i] = ex2(fmaf(sacc[i], C2, -mc)); ls += p[i]; }
    ls = xhalf_sum(ls);
    sbar4(cnt + 5, lane);
    u32x4* pf = reinterpret_cast<u32x4*>(scr);
    float* wl = reinterpret_cast<float*>(scr + 8192);
    if (hh == 0) wl[r * 4 + w4] = ls;
    pf[(2 * w4) * 64 + lane] = u32x4{pk_bf(p[0], p[1]), pk_bf(p[2], p[3]), pk_bf(p[4], p[5]), pk_bf(p[6], p[7])};
    pf[(2 * w4 + 1) * 64 + lane] = u32x4{pk_bf(p[8], p[9]), pk_bf(p[10], p[11]), pk_bf(p[12], p[13]), pk_bf(p[14], p[15])};
    sbar4(cnt + 6, lane);
    u32x4 pb8[8];
#pragma unroll
    for (int ks = 0; ks < 8; ++ks) pb8[ks] = pf[ks * 64 + lane];
    const f32x4 l4 = *reinterpret_cast<const f32x4*>(wl + r * 4);
    const float lsum = (l4[0] + l4[1]) + (l4[2] + l4[3]), rl = __builtin_amdgcn_rcpf(lsum);
    const int qq = (lane & 15) >> 2, pp = lane & 3, blk2 = (lane >> 4) & 1;
    char* ot = scr + 12288 + w4 * 4096;
#pragma unroll
    for (int c2 = 0; c2 < 2; ++c2) {
      f32x16 oacc[2];
#pragma unroll
      for (int ct = 0; ct < 2; ++ct)
#pragma unroll
        for (int i = 0; i < 16; ++i) oacc[ct][i] = 0.f;
#pragma unroll
      for (int ks = 0; ks < 8; ++ks)
#pragma unroll
        for (int ct = 0; ct < 2; ++ct) {
          typedef __attribute__((address_space(3))) s16x4* lp;
          const char* tile = smem + ((ks >> 1) * 4 + w4) * 8192;
          const int cc = 2 * c2 + ct;
          const s16x4 lo = __builtin_amdgcn_ds_read_tr16_b64_v4i16((lp)(tile + toff(16 * (ks & 1) + 4 * hh + qq, 4 * cc + 2 * blk2 + (pp >> 1)) + 8 * (pp & 1)));
          const s16x4 hi = __builtin_amdgcn_ds_read_tr16_b64_v4i16((lp)(tile + toff(16 * (ks & 1) + 8 + 4 * hh + qq, 4 * cc + 2 * blk2 + (pp >> 1)) + 8 * (pp & 1)));
          const s16x8 afr = __builtin_shufflevector(lo, hi, 0, 1, 2, 3, 4, 5, 6, 7);
          oacc[ct] = __builtin_amdgcn_mfma_f32_32x32x16_bf16(__builtin_bit_cast(bf16x8_t, afr), __builtin_bit_cast(bf16x8_t, pb8[ks]), oacc[ct], 0, 0, 0);
        }
#pragma unroll
      for (int ct = 0; ct < 2; ++ct)
#pragma unroll
        for (int g = 0; g < 4; ++g)
          *reinterpret_cast<u32x2*>(ot + r * 128 + 16 * ((4 * ct + g) ^ (r & 7)) + 8 * hh) = u32x2{pk_bf(oacc[ct][4 * g] * rl, oacc[ct][4 * g + 1] * rl), pk_bf(oacc[ct][4 * g + 2] * rl, oacc[ct][4 * g + 3] * rl)};
      if (c2 == 0) lds_add(cnt + 8, 1u, lane);
      __builtin_amdgcn_wave_barrier();
#pragma unroll
      for (int i = 0; i < 4; ++i) {
        const int hd = 8 * i + (lane >> 3), pc = lane & 7;
        const u32x4 vv = *reinterpret_cast<const u32x4*>(ot + hd * 128 + 16 * (pc ^ (hd & 7)));
        st16c(rs, WS_APO + (((hd * 2 + (w4 >> 1)) * 64 + idx) * 256 + (w4 & 1) * 128 + 64 * c2 + 8 * pc) * 2, vv);
      }
      __builtin_amdgcn_wave_barrier();
    }
    lds_arrive(cnt + 1, lane);
    STAMP(1);
    asm volatile("s_waitcnt vmcnt(0)" ::: "memory");
    if (lds_arrive(cnt + 9 + (w4 >> 1), lane) == 1u && hh == 0)
      st8c(rs, WS_AML + ((r * 2 + (w4 >> 1)) * 64 + idx) * 8, u32x2{__float_as_uint(mc + __builtin_amdgcn_logf(lsum)), tag});
    lds_add(cnt + 8, ~0u, lane);
    STAMP(2);
    STAMP_FLUSH(0); TR_FLUSH();
    return;
  }
#pragma unroll
  for (int ip = 0; ip < 8; ++ip) {
#pragma unroll
    for (int k = 0; k < 4; ++k) { const unsigned lo = plo(Wb[2 * ip][k], Wb[2 * ip + 1][k]), hi = phi(Wb[2 * ip][k], Wb[2 * ip + 1][k]); Wb[2 * ip][k] = lo; Wb[2 * ip + 1][k] = hi; }
    pin2(Wb[2 * ip], Wb[2 * ip + 1]);
  }
  if (cls == 2) {
    float* red2 = red + 4 * 256;
    float* wml = red + 3072;
    u32x16 f[2];
    if (w4 == 0) {
#pragma unroll 1
      for (int spin = 0; spin < SPIN_MAX; ++spin) {
        const u32x16 t = sld64(ws + WS_AML + (h * 2 + b) * 512);
        bool any = false;
#pragma unroll
        for (int i = 0; i < 8; ++i) any |= t[2 * i + 1] == tag;
        if (any) break;
        __builtin_amdgcn_s_sleep(8);
      }
      lds_add(cnt + 7, 1u, lane);
    } else lds_wait_slow(cnt + 7, 1u);
#pragma unroll 1
    for (int spin = 0; spin < SPIN_MAX; ++spin) {
      sld128(ws + WS_AML + ((h * 2 + b) * 64 + w4 * 16) * 8, f);
      unsigned e = 0u;
#pragma unroll
      for (int i = 0; i < 16; ++i) e |= f[i >> 3][2 * (i & 7) + 1] ^ tag;
      if (e == 0u) break;
      __builtin_amdgcn_s_sleep(2);
    }
    STAMP(3);
    u32x4 pp[8];
#pragma unroll
    for (int i = 0; i < 8; ++i) pp[i] = ld16c(rs, WS_APO + ((h * 2 + b) * 64 + w4 * 16 + 2 * i) * 512 + lane * 16);
    SCHED();
    float wt[8];
    {
      float M = -INFINITY, L = 0.f;
#pragma unroll
      for (int i = 0; i < 8; ++i) {
        unsigned z0 = f[i >> 2][4 * (i & 3)], z1 = f[i >> 2][4 * (i & 3) + 2];
        asm("" : "+s"(z0), "+s"(z1));
        wt[i] = __uint_as_float(lane < 32 ? z0 : z1); M = fmaxf(M, wt[i]);
      }
      M = xhalf_max(M);
#pragma unroll
      for (int i = 0; i < 8; ++i) { wt[i] = ex2(wt[i] - M); L += wt[i]; }
      L = xhalf_sum(L);
      if (lane == 0) *reinterpret_cast<f32x2*>(wml + 2 * w4) = f32x2{M, L};
    }
    SCHED();
    float a8[8];
#pragma unroll
    for (int k = 0; k < 8; ++k) a8[k] = 0.f;
#pragma unroll
    for (int i = 0; i < 8; ++i) {
#pragma unroll
      for (int k = 0; k < 4; ++k) { a8[2 * k] = fmaf(wt[i], bflo(pp[i][k]), a8[2 * k]); a8[2 * k + 1] = fmaf(wt[i], bfhi(pp[i][k]), a8[2 * k + 1]); }
      asm volatile("" : "+v"(a8[0]), "+v"(a8[1]), "+v"(a8[2]), "+v"(a8[3]), "+v"(a8[4]), "+v"(a8[5]), "+v"(a8[6]), "+v"(a8[7]));
    }
    STAMP(6);
    *reinterpret_cast<f32x4*>(red + w4 * 256 + 8 * (lane & 31) + 4 * (lane >> 5)) = f32x4{rs32(a8[0], a8[4]), rs32(a8[1], a8[5]), rs32(a8[2], a8[6]), rs32(a8[3], a8[7])};
    sbar4(cnt + 3, lane);
    STAMP(4);
    float acc[2][8];
#pragma unroll
    for (int bb = 0; bb < 2; ++bb) {
      u32x4 cv[2];
      const f32x4 ml = *reinterpret_cast<const f32x4*>(wml + 4 * bb);
      const float M = fmaxf(ml[0], ml[2]), e0 = ex2(ml[0] - M), e1 = ex2(ml[2] - M), rl = 1.f / fmaf(e0, ml[1], e1 * ml[3]), s0 = e0 * rl, s1 = e1 * rl;
#pragma unroll
      for (int e = 0; e < 2; ++e) {
        const float* src = red + bb * 512 + w4 * 64 + 16 * r4 + 8 * e;
        const f32x4 t0 = s0 * *reinterpret_cast<const f32x4*>(src) + s1 * *reinterpret_cast<const f32x4*>(src + 256), t1 = s0 * *reinterpret_cast<const f32x4*>(src + 4) + s1 * *reinterpret_cast<const f32x4*>(src + 260);
        cv[e] = u32x4{pk_bf(t0[0], t0[1]), pk_bf(t0[2], t0[3]), pk_bf(t1[0], t1[1]), pk_bf(t1[2], t1[3])};
      }
#pragma unroll
      for (int k = 0; k < 8; ++k) acc[bb][k] = 0.f;
#pragma unroll
      for (int ip = 0; ip < 8; ++ip)
#pragma unroll
        for (int k = 0; k < 4; ++k) { acc[bb][2 * k] = dot2(Wb[2 * ip][k], cv[ip >> 2][ip & 3], acc[bb][2 * k]); acc[bb][2 * k + 1] = dot2(Wb[2 * ip + 1][k], cv[ip >> 2][ip & 3], acc[bb][2 * k + 1]); }
    }
    {
      float v8[8], v4[4];
#pragma unroll
      for (int k = 0; k < 8; ++k) v8[k] = rs32(acc[0][k], acc[1][k]);
#pragma unroll
      for (int k = 0; k < 4; ++k) v4[k] = rs16(v8[k], v8[k + 4]);
      *reinterpret_cast<f32x4*>(red2 + (w4 * 2 + (lane >> 5)) * 128 + 8 * c16 + 4 * (r4 & 1)) = f32x4{v4[0], v4[1], v4[2], v4[3]};
    }
    {
      COMPILER_FENCE();
      const unsigned got = lds_arrive_nw(L_CNT + 16);
      f32x4 t4[4];
#pragma unroll
      for (int w = 0; w < 4; ++w) t4[w] = *reinterpret_cast<const f32x4*>(red2 + w * 256 + 4 * lane);
      SCHED();
      if (lds_done(got) == 3u) {
        const f32x4 t = (t4[0] + t4[1]) + (t4[2] + t4[3]);
        const unsigned co = WS_CX + ((h * 2 + b) * 256 + 64 * r4 + 32 * (c16 & 1) + 2 * (c16 >> 1)) * 8;
        st16c(rs, co, u32x4{__float_as_uint(t[0]), tag, __float_as_uint(t[1]), tag});
        st16c(rs, co + 128, u32x4{__float_as_uint(t[2]), tag, __float_as_uint(t[3]), tag});
      }
    }
    STAMP(12);
    STAMP_FLUSH(0); TR_FLUSH();
    return;
  }
  {
    unsigned op;
    {
      u32x4 c;
      const unsigned co = WS_CX + ((h * 2 + ((lane >> 3) & 1)) * 256 + (lane >> 5) * 128 + b * 64 + w4 * 16 + 2 * (lane & 7)) * 8;
      if (idx == 0 && w4 == 1) {
#pragma unroll 1
        for (int spin = 0; spin < SPIN_MAX; ++spin) {
          COMPILER_FENCE();
          if (__all(ld8c(rs, WS_QX + 65536 + (lane >> 1) * 2048 + (lane & 1) * 512)[1] == tag)) break;
          __builtin_amdgcn_s_sleep(8);
        }
      }
      if (w4 == 0) { shint<2048, 4>(ws + WS_CX + (h * 512 + b * 64) * 8, tag); lds_add(cnt + 7, 1u, lane); }
      else lds_wait_slow(cnt + 7, 1u);
      STAMP(3);
#pragma unroll 1
      for (int spin = 0; spin < SPIN_MAX; ++spin) {
        COMPILER_FENCE();
        c = ld16c(rs, co);
        if (__all((c[1] == tag) & (c[3] == tag))) break;
        __builtin_amdgcn_s_sleep(1);
      }
      float o0 = __uint_as_float(c[0]), o1 = __uint_as_float(c[2]);
      o0 += dpp<0x128>(o0); o1 += dpp<0x128>(o1);
      op = pk_bf(o0, o1);
    }
    STAMP(14);
    float* red3 = red + 4096;
    float a[2][8];
#pragma unroll
    for (int bb = 0; bb < 2; ++bb)
#pragma unroll
      for (int k = 0; k < 8; ++k) a[bb][k] = 0.f;
#pragma unroll
    for (int ip = 0; ip < 8; ++ip) {
      const unsigned v0 = __builtin_amdgcn_readlane(op, ip), v1 = __builtin_amdgcn_readlane(op, 32 + ip);
#pragma unroll
      for (int k = 0; k < 4; ++k) {
        a[0][2 * k] = dot2(Wb[2 * ip][k], v0, a[0][2 * k]); a[0][2 * k + 1] = dot2(Wb[2 * ip + 1][k], v0, a[0][2 * k + 1]);
        a[1][2 * k] = dot2(Wb[2 * ip][k], v1, a[1][2 * k]); a[1][2 * k + 1] = dot2(Wb[2 * ip + 1][k], v1, a[1][2 * k + 1]);
      }
    }
#pragma unroll
    for (int bb = 0; bb < 2; ++bb) {
      *reinterpret_cast<f32x4*>(red3 + (w4 * 2 + bb) * 512 + 8 * lane) = f32x4{a[bb][0], a[bb][1], a[bb][2], a[bb][3]};
      *reinterpret_cast<f32x4*>(red3 + (w4 * 2 + bb) * 512 + 8 * lane + 4) = f32x4{a[bb][4], a[bb][5], a[bb][6], a[bb][7]};
    }
    sbar4(cnt + 5, lane);
    f32x4 t = {0.f, 0.f, 0.f, 0.f};
#pragma unroll
    for (int w = 0; w < 4; ++w) t += *reinterpret_cast<const f32x4*>(red3 + w * 1024 + 4 * tid);
    st16c(rs, WS_GA + ((tid >> 7) * 64 + idx) * 2048 + (tid & 127) * 16, u32x4{pk_bf(t[0], t[1]), tag, pk_bf(t[2], t[3]), tag});
    STAMP(5);
  }
  fin(IC<1>{}, WS_GA, 0, idx == 0);
  STAMP(6);
  STAMP_FLUSH(0); TR_FLUSH();
}

extern "C" void kernel_launch(void* const* d_in, const int* in_sizes, int n_in, void* d_out, int out_size, void* d_ws, size_t ws_size, hipStream_t stream) {
  const u16* xA = (const u16*)d_in[0];
  const u16* ckv = (const u16*)d_in[1];
  const u16* w_q = (const u16*)d_in[2];
  const u16* w_uk = (const u16*)d_in[3];
  const u16* w_uv = (const u16*)d_in[4];
  const u16* w_o = (const u16*)d_in[5];
  const u16* xB = (const u16*)d_in[6];
  const u16* kc = (const u16*)d_in[7];
  const u16* vc = (const u16*)d_in[8];
  const u16* wq = (const u16*)d_in[9];
  const u16* wo = (const u16*)d_in[10];
  kmain<<<256, 512, 0, stream>>>(xA, ckv, w_q, w_uk, w_uv, w_o, xB, kc, vc, wq, wo, (char*)d_ws, (u16*)d_out);
}
```

```cpp
#include <hip/hip_runtime.h>
#include <hip/hip_bf16.h>
#include <stdint.h>

#define DI __device__ __forceinline__
typedef __attribute__((ext_vector_type(4))) unsigned u32x4;
typedef __attribute__((ext_vector_type(2))) unsigned u32x2;
typedef __attribute__((ext_vector_type(4))) float f32x4;
typedef __attribute__((ext_vector_type(2))) float f32x2;
typedef __attribute__((ext_vector_type(16))) float f32x16;
typedef __attribute__((ext_vector_type(8))) __bf16 bf16x8_t;
typedef __attribute__((ext_vector_type(2))) __bf16 bf16x2_t;
typedef __attribute__((ext_vector_type(4))) short s16x4;
typedef __attribute__((ext_vector_type(8))) short s16x8;
typedef unsigned short u16;
typedef unsigned long long u64;

constexpr int NS0 = 14, NS1 = 17, NS2 = 16, NS3 = 17;
constexpr u64 TL0 = 0xb2991b5d33cb1c1ull, TH0 = 0xf78d30ull, TL1 = 0x8e0759551349142ull, TH1 = 0x3eef7cefae7ull;
constexpr u64 TL2 = 0x96179a59238a183ull, TH2 = 0xffad71b68ull, TL3 = 0x89f718510308100ull, TH3 = 0x3ce76caeaa6ull;
constexpr int RA2 = 0xC0, RA3 = 0;
constexpr int AHL = 2;
constexpr int QPE = 5;
constexpr int WBW = 4, WB0 = 7;
#define TL 2390
constexpr unsigned TA0 = 135, TA1 = 120, TA2 = 120, TA3 = 120, STG = 12, BSG = 6, TW = 1640, HL = 40, TP0 = 1810, TP2 = 2060, PA = 150;
#define PAR(i, v) (v)
constexpr u64 LA2 = TH2 >> (6 * (NS2 - 11)), LA3 = TH3 >> (6 * (NS3 - 11));
constexpr u64 chunk_mask(u64 lo, u64 hi, int n) { u64 m = 0; for (int i = 0; i < n; ++i) m |= 1ull << ((i < 10 ? lo >> (6 * i) : hi >> (6 * (i - 10))) & 63); return m; }
static_assert((chunk_mask(TL0, TH0, NS0) | chunk_mask(TL1, TH1, NS1) | chunk_mask(TL2, TH2, NS2) | chunk_mask(TL3, TH3, NS3)) == ~0ull && (RA2 & RA3) == 0, "every chunk once");
static_assert(NS0 + NS1 + NS2 + NS3 == 64, "split sizes");
constexpr int NSMIN = 14, NSMAX = 18;
static_assert(NS0 >= NSMIN && NS1 >= NSMIN && NS2 - 1 >= NSMIN && NS3 - 1 >= NSMIN && NS0 <= NSMAX && NS1 + 1 <= NSMAX && NS2 <= NSMAX && NS3 <= NSMAX, "range");
constexpr int SK = 4096;
constexpr float C2 = 0.08838834764831845f * 1.4426950408889634f;
constexpr u64 GOLD = 0x9E3779B97F4A7C15ull;

constexpr unsigned WS_EP = 0;
constexpr unsigned WS_QX = 4096;
constexpr unsigned WS_QA = WS_QX + 2 * 32 * 256 * 8;
constexpr unsigned WS_AML = WS_QA + 2 * 32 * 512 * 2;
constexpr unsigned WS_CX = WS_AML + 32 * 2 * 64 * 8;
constexpr unsigned WS_GZ = WS_CX + 32 * 2 * 2 * 128 * 8;
constexpr unsigned WS_GB = WS_GZ + 256 * 128;
constexpr unsigned WS_GA = WS_GB + 2 * 128 * 256 * 8;
constexpr unsigned WS_APO = 2u << 20;
static_assert(WS_GA + 64 * 2 * 256 * 8 <= WS_APO, "layout");
constexpr unsigned WS_END = WS_APO + 32 * 2 * 64 * 256 * 2;
#define STAMP(i) do {} while (0)
#define STAMP_DECL do {} while (0)
#define STAMP_FLUSH(grp) do {} while (0)

#define TR() do {} while (0)
#define TR_FLUSH() do {} while (0)
DI float bflo(unsigned u) { return __uint_as_float(u << 16); }
DI float bfhi(unsigned u) { return __uint_as_float(u & 0xffff0000u); }
DI unsigned pk_bf(float a, float b) { const f32x2 v = {a, b}; return __builtin_bit_cast(unsigned, __builtin_convertvector(v, bf16x2_t)); }
DI float dot2(unsigned k, unsigned q, float acc) {
  return __builtin_amdgcn_fdot2_f32_bf16(__builtin_bit_cast(bf16x2_t, k), __builtin_bit_cast(bf16x2_t, q), acc, false);
}
DI unsigned plo(unsigned w0, unsigned w1) { return __builtin_amdgcn_perm(w1, w0, 0x05040100u); }
DI unsigned phi(unsigned w0, unsigned w1) { return __builtin_amdgcn_perm(w1, w0, 0x07060302u); }
template <int CTRL> DI float dpp(float x) {
  return __builtin_bit_cast(float, __builtin_amdgcn_mov_dpp(__builtin_bit_cast(int, x), CTRL, 0xf, 0xf, true));
}
DI float row16_sum(float x) { x += dpp<0xB1>(x); x += dpp<0x4E>(x); x += dpp<0x141>(x); x += dpp<0x128>(x); return x; }
DI float row16_max(float x) { x = fmaxf(x, dpp<0xB1>(x)); x = fmaxf(x, dpp<0x4E>(x)); x = fmaxf(x, dpp<0x141>(x)); x = fmaxf(x, dpp<0x128>(x)); return x; }
DI float xhalf_max(float x) { auto t = __builtin_amdgcn_permlane32_swap(__float_as_uint(x), __float_as_uint(x), false, false); return fmaxf(__uint_as_float(t[0]), __uint_as_float(t[1])); }
DI float xhalf_sum(float x) { auto t = __builtin_amdgcn_permlane32_swap(__float_as_uint(x), __float_as_uint(x), false, false); return __uint_as_float(t[0]) + __uint_as_float(t[1]); }
DI u32x4 ld16(const void* p) { return *reinterpret_cast<const u32x4*>(p); }
DI u32x4 ld16nt(const void* p) { return __builtin_nontemporal_load(reinterpret_cast<const u32x4*>(p)); }
DI float ex2(float x) { return __builtin_amdgcn_exp2f(x); }
#define SCHED() __builtin_amdgcn_sched_barrier(0)

typedef __amdgpu_buffer_rsrc_t rsrc_t;
DI u32x4 ld16c(rsrc_t rs, unsigned off) { return __builtin_amdgcn_raw_buffer_load_b128(rs, (int)off, 0, 16); }
DI u32x2 ld8c(rsrc_t rs, unsigned off) { return __builtin_amdgcn_raw_buffer_load_b64(rs, (int)off, 0, 16); }
DI void st16c(rsrc_t rs, unsigned off, u32x4 v) { __builtin_amdgcn_raw_buffer_store_b128(v, rs, (int)off, 0, 16); }
DI void st8c(rsrc_t rs, unsigned off, u32x2 v) { __builtin_amdgcn_raw_buffer_store_b64(v, rs, (int)off, 0, 16); }
typedef __attribute__((ext_vector_type(16))) unsigned u32x16;
template <int O1> DI void sld2(const char* p, u32x2& a, u32x2& b) {
  asm volatile("s_load_dwordx2 %0, %2, 0x0 glc\n\ts_load_dwordx2 %1, %2, %3 glc\n\ts_waitcnt lgkmcnt(0)" : "=&s"(a), "=&s"(b) : "s"(p), "n"(O1) : "memory");
}
DI u32x16 sld64(const char* p) {
  u32x16 v;
  asm volatile("s_load_dwordx16 %0, %1, 0x0 glc\n\ts_waitcnt lgkmcnt(0)" : "=&s"(v) : "s"(p) : "memory");
  return v;
}
DI void sld128(const char* p, u32x16 (&v)[2]) {
  asm volatile("s_load_dwordx16 %0, %2, 0x0 glc\n\ts_load_dwordx16 %1, %2, 0x40 glc\n\ts_waitcnt lgkmcnt(0)" : "=&s"(v[0]), "=&s"(v[1]) : "s"(p) : "memory");
}
constexpr int SPIN_MAX = 1 << 20;
constexpr unsigned OOB = 0x7ffffff0u;
DI void rawbar() { __builtin_amdgcn_sched_barrier(0); __builtin_amdgcn_s_barrier(); __builtin_amdgcn_sched_barrier(0); }
#define COMPILER_FENCE() asm volatile("" ::: "memory")
template <int O1, int SL> DI void shint(const char* p, unsigned tag) {
#pragma unroll 1
  for (int spin = 0; spin < SPIN_MAX; ++spin) {
    u32x2 a, b;
    sld2<O1>(p, a, b);
    if (((a[1] ^ tag) | (b[1] ^ tag)) == 0u) break;
    __builtin_amdgcn_s_sleep(SL);
  }
}

template <int N> struct IC { static constexpr int v = N; };
template <int I, int N, class F> DI void sfor(F&& f) { if constexpr (I < N) { f(IC<I>{}); sfor<I + 1, N>(f); } }
typedef __attribute__((address_space(3))) unsigned lu32;
DI void lds_wait(unsigned* cnt, unsigned target) {
#pragma unroll 1
  for (int spin = 0; spin < SPIN_MAX; ++spin) {
    if (__builtin_amdgcn_readfirstlane(__hip_atomic_load((lu32*)cnt, __ATOMIC_RELAXED, __HIP_MEMORY_SCOPE_WORKGROUP)) >= target) break;
    __builtin_amdgcn_s_sleep(1);
  }
  COMPILER_FENCE();
}
DI void lds_wait_slow(unsigned* cnt, unsigned target) {
#pragma unroll 1
  for (int spin = 0; spin < SPIN_MAX; ++spin) {
    if (__builtin_amdgcn_readfirstlane(__hip_atomic_load((lu32*)cnt, __ATOMIC_RELAXED, __HIP_MEMORY_SCOPE_WORKGROUP)) >= target) break;
    __builtin_amdgcn_s_sleep(3);
  }
  COMPILER_FENCE();
}
DI void until(unsigned t) {
#pragma unroll 1
  for (int spin = 0; spin < 1024; ++spin) { if ((int)((unsigned)wall_clock64() - t) >= 0) break; __builtin_amdgcn_s_sleep(1); }
}
DI void lds_hold(unsigned* flag) {
#pragma unroll 1
  for (int spin = 0; spin < SPIN_MAX; ++spin) {
    if (__builtin_amdgcn_readfirstlane(__hip_atomic_load((lu32*)flag, __ATOMIC_RELAXED, __HIP_MEMORY_SCOPE_WORKGROUP)) == 0u) break;
    __builtin_amdgcn_s_sleep(2);
  }
}
DI unsigned lds_arrive_nw(unsigned addr) {
  unsigned old, one = 1u;
  asm volatile("s_mov_b64 exec, 1\n\tds_add_rtn_u32 %0, %1, %2\n\ts_mov_b64 exec, -1" : "=v"(old) : "v"(addr), "v"(one) : "memory");
  return old;
}
DI unsigned lds_done(unsigned old) { asm volatile("s_waitcnt lgkmcnt(0)" : "+v"(old) :: "memory"); return __builtin_amdgcn_readfirstlane(old); }
DI unsigned lds_arrive(unsigned* cnt, int) { return lds_done(lds_arrive_nw((unsigned)(size_t)(lu32*)cnt)); }
DI void lds_add(unsigned* cnt, unsigned v, int) {
  asm volatile("s_mov_b64 exec, 1\n\tds_add_u32 %0, %1\n\ts_mov_b64 exec, -1" :: "v"((unsigned)(size_t)(lu32*)cnt), "v"(v) : "memory");
}
DI void sbar4(unsigned* cnt, int lane) { if (lds_arrive(cnt, lane) < 3u) lds_wait(cnt, 4u); }
DI float rs32(float a, float b) { auto t = __builtin_amdgcn_permlane32_swap(__float_as_uint(a), __float_as_uint(b), false, false); return __uint_as_float(t[0]) + __uint_as_float(t[1]); }
DI float rs16(float a, float b) { auto t = __builtin_amdgcn_permlane16_swap(__float_as_uint(a), __float_as_uint(b), false, false); return __uint_as_float(t[0]) + __uint_as_float(t[1]); }
template <int N> DI unsigned rowbcast(unsigned x) { return (unsigned)__builtin_amdgcn_mov_dpp((int)x, 0x150 + N, 0xf, 0xf, true); }
DI void pin2(u32x4& a, u32x4& b) { asm volatile("" : "+v"(a), "+v"(b)); }
DI void use2(const u32x4& a, const u32x4& b) { asm volatile("" :: "v"(a), "v"(b)); }
DI bool nz8(const u32x4& v) { return ((v[0] | v[1]) != 0u) & ((v[2] | v[3]) != 0u); }

DI u32x4 qsum(const u32x4& g) {
  const auto ta = __builtin_amdgcn_permlane32_swap(g[0], g[0], false, false), tc = __builtin_amdgcn_permlane32_swap(g[2], g[2], false, false);
  const unsigned a = pk_bf(bflo(ta[0]) + bflo(ta[1]), bfhi(ta[0]) + bfhi(ta[1])), c = pk_bf(bflo(tc[0]) + bflo(tc[1]), bfhi(tc[0]) + bfhi(tc[1]));
  const auto ra = __builtin_amdgcn_permlane16_swap(a, a, false, false), rc = __builtin_amdgcn_permlane16_swap(c, c, false, false);
  return u32x4{ra[0], rc[0], ra[1], rc[1]};
}
DI unsigned toff(unsigned row, unsigned ch) { return 256u * row + 16u * (ch ^ (((row & 3) << 2) | ((row >> 2) & 3))); }
constexpr int SMEM = 160 * 1024;
constexpr int L_SCR = 131072;
constexpr int L_MRG = 65536;
constexpr int L_CNT = L_SCR + 32 * 1008;

struct KV { u32x4 r[8]; };

__global__ __launch_bounds__(512, 2) void kmain(const u16* __restrict__ xA, const u16* __restrict__ ckv, const u16* __restrict__ w_q, const u16* __restrict__ w_uk,
                                                const u16* __restrict__ w_uv, const u16* __restrict__ w_o,
                                                const u16* __restrict__ xB, const u16* __restrict__ kc, const u16* __restrict__ vc,
                                                const u16* __restrict__ wq, const u16* __restrict__ wo,
                                                char* __restrict__ ws, u16* __restrict__ out) {
  const unsigned tstart = (unsigned)wall_clock64();
  SCHED();
  __shared__ __attribute__((aligned(16))) char smem[SMEM];
  const int tid = threadIdx.x, blk = blockIdx.x, lane = tid & 63;
  const int wave = __builtin_amdgcn_readfirstlane(tid >> 6), w4 = wave & 3;
  const int cls = (blk >> 3) & 3;
  int idx = 8 * (blk & 7) + (blk >> 5);
  if (wave < 4) idx = (idx & ~7) | ((idx + (cls == 3 ? 2 : (cls == 2 ? 4 : 0))) & 7);
  const int h = idx >> 1, b = idx & 1;
  const rsrc_t rs = __builtin_amdgcn_make_buffer_rsrc(ws, 0, (int)WS_END, 0x00020000);
  const int r4 = lane >> 4, c16 = lane & 15;
  unsigned* cnt = reinterpret_cast<unsigned*>(smem + L_CNT);
  STAMP_DECL;
  STAMP(0);
  asm volatile("" :: "s"(xA), "s"(ckv), "s"(w_q), "s"(w_uk), "s"(w_uv), "s"(w_o), "s"(xB), "s"(kc), "s"(vc), "s"(wq), "s"(wo), "s"(ws), "s"(out), "s"(tstart));
  unsigned tag, tlow;
  u32x2 epw;
  auto take_epoch = [&]() {
    asm volatile("" : "+v"(epw));
    const u64 t64 = (((u64)(unsigned)__builtin_amdgcn_readfirstlane(epw[1]) << 32) | (unsigned)__builtin_amdgcn_readfirstlane(epw[0])) + GOLD;
    tag = (unsigned)(t64 >> 32); tlow = (unsigned)t64;
    asm volatile("s_waitcnt lgkmcnt(0)" : "+s"(tag), "+s"(tlow) :: "memory");
  };
  if (wave >= 4) {
    if (tid - 256 < 16) cnt[tid - 256] = 0u;
    asm volatile("s_waitcnt lgkmcnt(0)" ::: "memory");
    const unsigned pTL = PAR(0, TL), pTA = PAR(1 + cls, cls == 0 ? TA0 : (cls == 1 ? TA1 : (cls == 2 ? TA2 : TA3))), pSTG = PAR(9, STG), pBSG = PAR(10, BSG);
    const unsigned pRA2 = PAR(11, RA2), pRA3 = PAR(12, RA3), pTP = PAR(15 + cls, cls == 0 ? TP0 : TP2), pPA = PAR(19 + cls, (cls & 1) ? 0u : PA);
    rawbar();
    const int e2 = (pRA2 >> (idx & 7)) & 1, e3 = (pRA3 >> (idx & 7)) & 1;
    const int ns = cls == 0 ? NS0 : (cls == 1 ? NS1 + e2 + e3 : (cls == 2 ? NS2 - e2 : NS3 - e3));
    const size_t kvo = ((size_t)(b * 32 + h) * SK + w4 * 16) * 256;
    const char* kb = reinterpret_cast<const char*>(kc) + kvo;
    const char* vb = reinterpret_cast<const char*>(vc) + kvo;
    const unsigned lo = lane * 16u;
    u64 tlo = cls == 0 ? TL0 : (cls == 1 ? TL1 : (cls == 2 ? TL2 : TL3));
    u64 thi = cls == 0 ? TH0 : (cls == 1 ? TH1 | ((e2 ? LA2 : LA3) << (6 * (NS1 - 10))) : (cls == 2 ? TH2 : TH3));
    const u64 tlo0 = tlo, thi0 = thi;
    int hn = 0;
    const char* hb = kb;
    const char* pc[2];
    auto next2 = [&]() {
#pragma unroll
      for (int i = 0; i < 2; ++i) {
        if (hn == ns) { tlo = tlo0; thi = thi0; hb = vb; }
        ++hn;
        pc[i] = hb + ((((unsigned)tlo + (unsigned)idx) & 63u) << 14);
        tlo = (tlo >> 6) | ((thi & 63u) << 54); thi >>= 6;
      }
    };
    KV A, B;
    u32x4 W[8];
    u32x4 T[4][8];
    auto ldp = [&](KV& S, auto P) {
      constexpr int p = P.v;
      const char* q = pc[p >> 1] + lo + (p & 1) * 2048;
      S.r[2 * p] = ld16nt(q); S.r[2 * p + 1] = ld16nt(q + 1024);
    };
    auto landed = [&](KV& S, auto P) { pin2(S.r[2 * P.v], S.r[2 * P.v + 1]); };
    KV X;
    const unsigned qo = WS_QX + ((32 + h) * 2 + b) * 1024 + lane * 16;
    u32x4 g0;
    {
      unsigned tk = 0u;
      if (tid == 256) tk = __builtin_nontemporal_load(reinterpret_cast<const unsigned*>(kb + cls * 128));
      lds_wait_slow(cnt + 14, 4u);
      asm volatile("" :: "v"(tk));
    }
    STAMP(10);
    sfor<0, 12>([&](auto I) {
      constexpr int i = I.v, j = i - 4;
      if constexpr ((i & 3) == 0) next2();
      if constexpr (j >= 0) { if constexpr (j < 4) landed(X, IC<j & 3>{}); else landed(B, IC<j & 3>{}); }
      if constexpr (j >= 0 && (j & 1)) TR();
      if constexpr (i == QPE) g0 = ld16c(rs, qo);
      if constexpr (i < 4) ldp(X, IC<i & 3>{}); else if constexpr (i < 8) ldp(B, IC<i & 3>{}); else ldp(A, IC<i & 3>{});
      SCHED();
    });
    {
      const u32x2 t = *reinterpret_cast<const u32x2*>(cnt + 16);
      tlow = __builtin_amdgcn_readfirstlane(t[0]); tag = __builtin_amdgcn_readfirstlane(t[1]);
    }
    const char* wp = reinterpret_cast<const char*>(wo) + ((size_t)h * 128 + r4 * 8) * 1024 + w4 * 256 + c16 * 16;
    u32x4 qpk;
    unsigned tvk;
    {
      unsigned tv = 0u;
      if (tid == 256) tv = __builtin_nontemporal_load(reinterpret_cast<const unsigned*>(vb + cls * 128));
      if (!__all((g0[1] == tag) & (g0[3] == tag))) {
#pragma unroll 1
        for (int spin = 0; spin < SPIN_MAX; ++spin) {
          COMPILER_FENCE();
          g0 = ld16c(rs, qo);
          if (__all((g0[1] == tag) & (g0[3] == tag))) break;
        }
      }
      qpk = qsum(g0);
      tvk = tv;
    }
    STAMP(7);
    const bool holds = cls == 0;
    float m = 0.f, l = 0.f, acc[8];
    unsigned sc[5];
#pragma unroll
    for (int k = 0; k < 8; ++k) acc[k] = 0.f;
#pragma unroll
    for (int k = 0; k < 5; ++k) sc[k] = 0xff800000u;
    int vn = 0;
    unsigned due = tstart + pTL - (unsigned)(ns - 5) * pTA - (3 - w4) * pSTG - ((idx * 5 + cls * 3) & 15) * pBSG;
    unsigned rwo;
    const unsigned zo = WS_GZ + (b * 32 + h) * 512;
    u32x2 zs[4];
    float wr_;
    auto zreq = [&]() {
      asm volatile("s_load_dwordx2 %0, %4, 0x0 glc\n\ts_load_dwordx2 %1, %4, 0x80 glc\n\ts_load_dwordx2 %2, %4, 0x100 glc\n\ts_load_dwordx2 %3, %4, 0x180 glc"
                   : "=&s"(zs[0]), "=&s"(zs[1]), "=&s"(zs[2]), "=&s"(zs[3]) : "s"(ws + zo) : "memory");
    };
    auto zget = [&]() {
#pragma unroll 1
      for (int spin = 0; spin < SPIN_MAX; ++spin) {
        asm volatile("s_waitcnt lgkmcnt(0)" : "+s"(zs[0]), "+s"(zs[1]), "+s"(zs[2]), "+s"(zs[3]) :: "memory");
        if (((zs[0][1] ^ tag) | (zs[1][1] ^ tag) | (zs[2][1] ^ tag) | (zs[3][1] ^ tag)) == 0u) break;
        __builtin_amdgcn_s_sleep(2);
        zreq();
      }
      asm volatile("s_waitcnt lgkmcnt(0)" : "+s"(zs[0]), "+s"(zs[1]), "+s"(zs[2]), "+s"(zs[3]) :: "memory");
      const float z0 = __uint_as_float(zs[0][0]), z1 = __uint_as_float(zs[1][0]), z2 = __uint_as_float(zs[2][0]), z3 = __uint_as_float(zs[3][0]);
      const float Z = fmaxf(fmaxf(z0, z1), fmaxf(z2, z3));
      wr_ = ex2(fmaf(m, C2, -Z)) * __builtin_amdgcn_rcpf((ex2(z0 - Z) + ex2(z1 - Z)) + (ex2(z2 - Z) + ex2(z3 - Z)));
      asm volatile("" : "+v"(wr_));
    };
    auto qk = [&](const u32x4* K, auto I) {
      float x[4];
#pragma unroll
      for (int t = 0; t < 4; ++t) {
        x[t] = 0.f;
#pragma unroll
        for (int k = 0; k < 4; ++k) x[t] = dot2(K[t][k], qpk[k], x[t]);
      }
      const float u0 = (c16 & 1 ? x[1] : x[0]) + dpp<0xB1>(c16 & 1 ? x[0] : x[1]), u1 = (c16 & 1 ? x[3] : x[2]) + dpp<0xB1>(c16 & 1 ? x[2] : x[3]);
      float v = (c16 & 2 ? u1 : u0) + dpp<0x4E>(c16 & 2 ? u0 : u1);
      v += dpp<0x124>(v); v += dpp<0x128>(v);
      sc[I.v >> 2] = __builtin_amdgcn_update_dpp(sc[I.v >> 2], __float_as_uint(v), 0xE4, 0xf, 1 << (I.v & 3), false);
      asm volatile("" : "+v"(sc[I.v >> 2]));
    };
    auto soft = [&]() {
      m = row16_max(fmaxf(fmaxf(fmaxf(__uint_as_float(sc[0]), __uint_as_float(sc[1])), fmaxf(__uint_as_float(sc[2]), __uint_as_float(sc[3]))), __uint_as_float(sc[4])));
      const float mc = m * C2;
      l = 0.f;
#pragma unroll
      for (int k = 0; k < 5; ++k) {
        const float p = ex2(fmaf(__uint_as_float(sc[k]), C2, -mc));
        sc[k] = pk_bf(p, dpp<0xB1>(p));
        l = dot2(sc[k], 0x3f003f00u, l);
      }
      l = row16_sum(l);
    };
    auto pv = [&](const u32x4* V) {
      const unsigned p01 = rowbcast<0>(sc[0]), p23 = rowbcast<2>(sc[0]);
#pragma unroll
      for (int k = 0; k < 4; ++k) { acc[2 * k] = dot2(plo(V[0][k], V[1][k]), p01, acc[2 * k]); acc[2 * k + 1] = dot2(phi(V[0][k], V[1][k]), p01, acc[2 * k + 1]); }
#pragma unroll
      for (int k = 0; k < 4; ++k) { acc[2 * k] = dot2(plo(V[2][k], V[3][k]), p23, acc[2 * k]); acc[2 * k + 1] = dot2(phi(V[2][k], V[3][k]), p23, acc[2 * k + 1]); }
      sc[0] = (unsigned)__builtin_amdgcn_mov_dpp((int)sc[0], 0x12C, 0xf, 0xf, true);
      if ((++vn & 3) == 0) { sc[0] = sc[1]; sc[1] = sc[2]; sc[2] = sc[3]; sc[3] = sc[4]; }
      asm volatile("" : "+v"(acc[0]), "+v"(acc[1]), "+v"(acc[2]), "+v"(acc[3]), "+v"(acc[4]), "+v"(acc[5]), "+v"(acc[6]), "+v"(acc[7]));
    };
    auto step = [&](KV& S, KV& N, auto SI, auto REFILL, auto WR) {
      constexpr bool refill = REFILL.v == 1;
      constexpr int wr = WR.v;
      if constexpr (refill) next2();
      if constexpr (refill && SI.v >= 3) { until((int)(due - tstart) < (int)pTP ? due - pPA : due); due += pTA; }
      constexpr int ah = wr < 0 ? AHL : 0;
      auto point = [&](auto P) {
        if constexpr (refill) {
          if constexpr (P.v == 0 && ah > 0) { if constexpr (wr >= 0) { if (holds) lds_hold(cnt + 8); } sfor<0, ah>([&](auto Q) { ldp(N, Q); }); SCHED(); }
          landed(S, P);
          if constexpr (P.v & 1) TR();
          if constexpr (P.v + ah < 4) { if constexpr ((P.v & 1) == 0 && wr >= 0) { if (holds) lds_hold(cnt + 8); } ldp(N, IC<(P.v + ah) & 3>{}); }
          if constexpr (wr >= 0 && wr < 8) W[(4 * wr + P.v) & 7] = ld16(wp + (((4 * wr + P.v) >> 3) * 32 + ((4 * wr + P.v) & 7)) * 1024);
          SCHED();
        }
      };
      sfor<0, 2>([&](auto H) {
        constexpr int i = 2 * SI.v + H.v;
        if constexpr (REFILL.v == 2 && H.v == 0) { zreq(); SCHED(); }
        point(IC<2 * H.v>{});
        point(IC<2 * H.v + 1>{});
        if constexpr (i < NSMIN) qk(S.r + 4 * H.v, IC<i>{});
        else if constexpr (i > NSMAX) pv(S.r + 4 * H.v);
        else {
          if (i < ns) { if constexpr (i < NSMAX) qk(S.r + 4 * H.v, IC<i>{}); }
          else { if (i == ns) soft(); pv(S.r + 4 * H.v); }
        }
        SCHED();
      });
      if constexpr (REFILL.v == 2) {
        zget();
        STAMP(1);
        *reinterpret_cast<u32x4*>(smem + rwo) = u32x4{pk_bf(acc[0] * wr_, acc[1] * wr_), pk_bf(acc[2] * wr_, acc[3] * wr_), pk_bf(acc[4] * wr_, acc[5] * wr_), pk_bf(acc[6] * wr_, acc[7] * wr_)};
        COMPILER_FENCE();
      }
      if constexpr (wr >= 1) {
        constexpr int t = (wr - 1) >> 1, j0 = ((wr - 1) & 1) * 2;
#pragma unroll
        for (int jj = 0; jj < 2; ++jj)
#pragma unroll
          for (int e = 0; e < 4; ++e) {
            const unsigned a_ = W[(4 * wr - 4 + 2 * jj) & 7][e], b_ = W[(4 * wr - 3 + 2 * jj) & 7][e];
            T[t][2 * e][j0 + jj] = plo(a_, b_); T[t][2 * e + 1][j0 + jj] = phi(a_, b_);
          }
#pragma unroll
        for (int e = 0; e < 8; e += 2) pin2(T[t][e], T[t][e + 1]);
      }
      SCHED();
    };
    step(B, B, IC<1>{}, IC<0>{}, IC<-1>{});
    step(A, B, IC<2>{}, IC<1>{}, IC<0>{});
    asm volatile("" :: "v"(tvk));
    step(X, X, IC<0>{}, IC<0>{}, IC<-1>{});
    sfor<0, 4>([&](auto I) {
      step(B, A, IC<2 * I.v + 3>{}, IC<1>{}, IC<2 * I.v + 1>{});
      if constexpr (I.v == 3) {
        float* ml = reinterpret_cast<float*>(cnt + 32);
        ml[(c16 & 1) * 16 + w4 * 4 + r4] = (c16 & 1) ? l : m;
        COMPILER_FENCE();
        const unsigned got = lds_arrive_nw(L_CNT + 44);
        const float mr = ml[c16], lr = ml[16 + c16];
        SCHED();
        if (lds_done(got) == 3u) {
          const float M = row16_max(mr);
          const unsigned z = __float_as_uint(fmaf(M, C2, __builtin_amdgcn_logf(row16_sum(ex2((mr - M) * C2) * lr))));
          asm volatile("buffer_store_dwordx4 %0, %1, %2, 0 offen sc1\n\ts_nop 2" :: "v"(u32x4{z, tag, z, tag}), "v"(lane < 8 ? zo + cls * 128 + lane * 16 : OOB), "s"(rs) : "memory");
        }
        SCHED();
      }
      step(A, B, IC<2 * I.v + 4>{}, IC<1>{}, IC<2 * I.v + 2>{});
    });
    unsigned yoff = WS_GB + ((b * 128 + h * 4 + cls) * 256 + 64 * w4 + lane) * 8, rro = L_MRG + c16 * 272 + r4 * 16;
    rwo = L_MRG + (w4 * 4 + r4) * 272 + c16 * 16;
    asm volatile("" : "+v"(yoff), "+v"(rwo), "+v"(rro));
    if (cls == 0) lds_wait(cnt + 1, 4u);
    const int nit = (ns - 12) >> 1;
#pragma unroll 1
    for (int i = 0; i < nit; ++i) {
      step(B, A, IC<11>{}, IC<1>{}, IC<-1>{});
      step(A, B, IC<11>{}, IC<1>{}, IC<-1>{});
    }
    if (ns & 1) { step(B, A, IC<11>{}, IC<1>{}, IC<-1>{}); step(A, A, IC<11>{}, IC<2>{}, IC<-1>{}); }
    else step(B, B, IC<11>{}, IC<2>{}, IC<-1>{});
    {
      u32x4 af[4];
      unsigned got = lds_arrive_nw(L_CNT), need = 3u;
#pragma unroll 1
      for (int spin = 0; spin < SPIN_MAX; ++spin) {
#pragma unroll
        for (int t = 0; t < 4; ++t) af[t] = *reinterpret_cast<const u32x4*>(smem + rro + t * 64);
        SCHED();
        if (lds_done(got) >= need) break;
        need = 4u;
        __builtin_amdgcn_s_sleep(1);
        asm volatile("ds_read_b32 %0, %1" : "=v"(got) : "v"((unsigned)L_CNT) : "memory");
      }
      SCHED();
      f32x4 d[8];
      float a[8], v4[4];
      sfor<0, 5>([&](auto I) {
        constexpr int i = I.v;
        sfor<0, 8>([&](auto J) {
          constexpr int e = i + 4 * (J.v & 1), t = J.v >> 1;
          if constexpr (i < 4) { d[e] = __builtin_amdgcn_mfma_f32_16x16x32_bf16(__builtin_bit_cast(bf16x8_t, af[t]), __builtin_bit_cast(bf16x8_t, T[t][e]), t ? d[e] : f32x4{0.f, 0.f, 0.f, 0.f}, 0, 0, 0); SCHED(); }
          if constexpr (i >= 1 && (J.v == 4 || J.v == 5)) { const f32x2 u = f32x2{d[e - 1][0], d[e - 1][1]} + f32x2{d[e - 1][2], d[e - 1][3]}; a[e - 1] = u[0] + u[1]; asm volatile("" : "+v"(a[e - 1])); }
          if constexpr (i >= 1 && J.v == 6) v4[i - 1] = rs32(a[i - 1], a[i + 3]);
          if constexpr (i < 4) SCHED();
        });
      });
      st8c(rs, yoff, u32x2{pk_bf(rs16(v4[0], v4[2]), rs16(v4[1], v4[3])), tag});
    }
    STAMP(2);
    STAMP_FLUSH(1);
    TR_FLUSH();
    return;
  }
  const rsrc_t ro = __builtin_amdgcn_make_buffer_rsrc(out, 0, 4096, 0x00020000);
  auto fin = [&](auto NL, unsigned base, unsigned col, bool bump) {
    constexpr int nl = NL.v;
    unsigned doff = base + (((idx >> 5) * 4 + w4) * 16 * nl + (lane >> 2)) * 2048 + (idx & 31) * 64 + (lane & 3) * 16;
    unsigned fwo = 32768 + (c16 < 4 ? (w4 * 16 + 4 * c16 + r4) * 4 : 256 + lane * 4);
    unsigned fro = 32768 + (lane & 7) * 8, foo = lane < 8 ? ((idx >> 5) * 1024 + col + (nl == 2 ? 16 * (idx & 24) + 64 * (idx & 1) + (idx & 6) + 8 * lane : 16 * (idx & 31) + 2 * lane)) * 2 : OOB;
    asm volatile("" : "+v"(doff), "+v"(fwo), "+v"(fro), "+v"(foo));
    lds_wait(cnt + 0, 4u);
    STAMP(4);
    u32x4 d[nl];
    unsigned eh = 1u;
#pragma unroll 1
    for (int spin = SPIN_MAX; spin > 0; --spin) {
      COMPILER_FENCE();
      if (eh != 0u) {
#pragma unroll
        for (int j = 0; j < nl; ++j) d[j] = ld16c(rs, doff + j * 32768);
        eh = 0u;
#pragma unroll
        for (int j = 0; j < nl; ++j) { eh |= (d[j][1] ^ tag) | (d[j][3] ^ tag); asm volatile("" : "+v"(eh)); }
      }
      if (__builtin_amdgcn_ballot_w64(eh != 0u) == 0ull) break;
    }
    STAMP(11);
    float y[4] = {bflo(d[0][0]), bfhi(d[0][0]), bflo(d[0][2]), bfhi(d[0][2])};
    if constexpr (nl == 2) { y[0] += bflo(d[1][0]); y[1] += bfhi(d[1][0]); y[2] += bflo(d[1][2]); y[3] += bfhi(d[1][2]); }
    float t = rs16(rs32(y[0], y[2]), rs32(y[1], y[3]));
    t += dpp<0x124>(t); t += dpp<0x128>(t);
    *reinterpret_cast<float*>(smem + fwo) = t;
    COMPILER_FENCE();
    const unsigned got = lds_arrive_nw(L_CNT + 24);
    f32x2 t2[4];
#pragma unroll
    for (int w = 0; w < 4; ++w) t2[w] = *reinterpret_cast<const f32x2*>(smem + fro + w * 64);
    SCHED();
    if (lds_done(got) == 3u) {
      const f32x2 r = (t2[0] + t2[1]) + (t2[2] + t2[3]);
      __builtin_amdgcn_raw_buffer_store_b32(pk_bf(r[0], r[1]), ro, (int)foo, 0, 0);
      if (bump) st8c(rs, lane == 0 ? WS_EP : OOB, u32x2{tlow, tag});
    }
  };
  epw = ld8c(rs, WS_EP);
  SCHED();
  u32x4 Wa[16], Wb[16], xv[4];
  const u16* pb;
  const unsigned qa_off = WS_QA + (((c16 >> 1) & 1) * 32 + h) * 1024 + (b * 256 + w4 * 64 + 16 * r4 + 8 * (c16 & 1)) * 2;
  {
    const u16* pa; size_t sa;
    if (cls) {
      const size_t o_ = ((size_t)(b * 256 + w4 * 64 + 16 * r4) * 32 + h) * 128 + 8 * c16;
      pa = (cls == 1 ? w_q : (cls == 2 ? w_uk : wq)) + o_; sa = 4096;
      pb = cls == 2 ? w_uv + o_ : (cls == 1 ? w_o : wo) + ((size_t)h * 128 + b * 64 + w4 * 2) * 512 + 8 * lane;
      if (cls == 1 && c16 < 4) st16c(rs, qa_off, u32x4{0u, 0u, 0u, 0u});
    } else { pa = ckv + ((size_t)idx * 128 + w4) * 512 + 8 * lane; sa = 2048; pb = pa + 64 * 512; }
    const u16* xp = (cls == 1 ? xA : xB) + b * 256 + w4 * 64 + 16 * r4;
    SCHED();
    sfor<0, 4>([&](auto J) { xv[J.v] = ld16(xp + (J.v >> 1) * 512 + 8 * (J.v & 1)); });
    SCHED();
    sfor<0, 16>([&](auto I) { Wa[I.v] = ld16nt(pa + I.v * sa); });
    SCHED();
    rawbar();
    take_epoch();
    *reinterpret_cast<u32x2*>(cnt + 16) = u32x2{tlow, tag};
    COMPILER_FENCE();
  }
  STAMP(13);
  float* red = reinterpret_cast<float*>(smem);
  if (cls & 1) {
    float acc[2][8];
#pragma unroll
    for (int bb = 0; bb < 2; ++bb)
#pragma unroll
      for (int k = 0; k < 8; ++k) acc[bb][k] = 0.f;
    sfor<0, 8>([&](auto IP) {
      constexpr int ip = IP.v, j = ip - (8 - WBW);
      use2(Wa[2 * ip], Wa[2 * ip + 1]);
      if constexpr (ip & 1) TR();
      if constexpr (j >= 0) { Wb[2 * j] = ld16(pb + j * 4096); Wb[2 * j + 1] = ld16(pb + j * 4096 + 512); }
      SCHED();
      const unsigned xp0 = xv[ip >> 2][ip & 3], xp1 = xv[2 + (ip >> 2)][ip & 3];
#pragma unroll
      for (int k = 0; k < 4; ++k) {
        const unsigned lo = plo(Wa[2 * ip][k], Wa[2 * ip + 1][k]), hi = phi(Wa[2 * ip][k], Wa[2 * ip + 1][k]);
        acc[0][2 * k] = dot2(lo, xp0, acc[0][2 * k]); acc[0][2 * k + 1] = dot2(hi, xp0, acc[0][2 * k + 1]);
        acc[1][2 * k] = dot2(lo, xp1, acc[1][2 * k]); acc[1][2 * k + 1] = dot2(hi, xp1, acc[1][2 * k + 1]);
      }
      SCHED();
    });
    {
      float v8[8], v4[4];
#pragma unroll
      for (int k = 0; k < 8; ++k) v8[k] = rs32(acc[0][k], acc[1][k]);
#pragma unroll
      for (int k = 0; k < 4; ++k) v4[k] = rs16(v8[k], v8[k + 4]);
      *reinterpret_cast<f32x4*>(red + (w4 * 2 + (lane >> 5)) * 128 + 8 * c16 + 4 * (r4 & 1)) = f32x4{v4[0], v4[1], v4[2], v4[3]};
    }
    COMPILER_FENCE();
    const unsigned got = lds_arrive_nw(L_CNT + 8);
    f32x4 t4[4];
#pragma unroll
    for (int w = 0; w < 4; ++w) t4[w] = *reinterpret_cast<const f32x4*>(red + (w * 2 + (lane >> 5)) * 128 + 8 * c16 + 4 * (r4 & 1));
    SCHED();
    if (lds_done(got) == 3u) {
      const f32x4 t = (t4[0] + t4[1]) + (t4[2] + t4[3]);
      st16c(rs, WS_QX + (((cls >> 1) * 32 + h) * 2 + (lane >> 5)) * 1024 + b * 512 + (lane & 31) * 16, u32x4{pk_bf(t[0], t[1]), tag, pk_bf(t[2], t[3]), tag});
      STAMP(15);
      SCHED();
      lds_add(cnt + 2, 1u, lane);
    } else lds_wait(cnt + 2, 5u);
    sfor<WBW, 8>([&](auto J) {
      constexpr int j = J.v;
      use2(Wb[2 * (j - WBW)], Wb[2 * (j - WBW) + 1]);
      if constexpr ((j - WBW) & 1) TR();
      Wb[2 * j] = ld16(pb + j * 4096); Wb[2 * j + 1] = ld16(pb + j * 4096 + 512);
      if constexpr (j == 7) lds_add(cnt + 14, 1u, lane);
      SCHED();
    });
    {
      const unsigned tw = __builtin_nontemporal_load(reinterpret_cast<const unsigned*>(out + (idx >> 5) * 1024 + (cls >> 1) * 512 + 16 * (idx & 31)));
      sfor<8 - WBW, 8>([&](auto J) { use2(Wb[2 * J.v], Wb[2 * J.v + 1]); if constexpr (J.v & 1) TR(); });
      asm volatile("" :: "v"(tw));
    }
    STAMP(8);
  } else if (cls == 2) {
    sfor<0, 16 - WBW>([&](auto IP) {
      constexpr int ip = IP.v, j = ip - (8 - WBW);
      if constexpr (ip < 8) use2(Wa[2 * ip], Wa[2 * ip + 1]); else use2(Wb[2 * ip - 16], Wb[2 * ip - 15]);
      if constexpr (ip & 1) TR();
      if constexpr (j >= 0) { Wb[2 * j] = ld16nt(pb + j * 8192); Wb[2 * j + 1] = ld16nt(pb + j * 8192 + 4096); }
      SCHED();
    });
    u32x4 g0, g1;
    const unsigned qo = WS_QX + h * 2048 + lane * 16;
    shint<512, 1>(ws + WS_QX + h * 2048, tag);
    STAMP(5);
#pragma unroll 1
    for (int spin = 0; spin < SPIN_MAX; ++spin) {
      COMPILER_FENCE();
      g0 = ld16c(rs, qo); g1 = ld16c(rs, qo + 1024);
      SCHED();
      lds_add(cnt + 14, spin == 0, lane);
      SCHED();
      if (__all((g0[1] == tag) & (g0[3] == tag) & (g1[1] == tag) & (g1[3] == tag))) break;
      __builtin_amdgcn_s_sleep(1);
    }
    sfor<8 - WBW, 8>([&](auto J) { if constexpr (J.v & 1) TR(); });
    const u32x4 q0 = qsum(g0), q1 = qsum(g1);
    STAMP(9);
    float v[8];
    {
      float u[16];
#pragma unroll
      for (int i = 0; i < 16; ++i) {
        float a = 0.f, c = 0.f;
#pragma unroll
        for (int k = 0; k < 4; ++k) { a = dot2(Wa[i][k], q0[k], a); c = dot2(Wa[i][k], q1[k], c); }
        u[i] = (c16 & 2 ? c : a) + dpp<0x4E>(c16 & 2 ? a : c);
        asm volatile("" : "+v"(u[i]));
      }
#pragma unroll
      for (int i = 0; i < 8; ++i) {
        float t = (c16 & 1 ? u[i + 8] : u[i]) + dpp<0xB1>(c16 & 1 ? u[i] : u[i + 8]);
        asm volatile("" : "+v"(t));
        t += dpp<0x124>(t);
        asm volatile("" : "+v"(t));
        t += dpp<0x128>(t);
        asm volatile("" : "+v"(t));
        v[i] = t;
      }
    }
    u32x4 o = {pk_bf(v[0], v[1]), pk_bf(v[2], v[3]), pk_bf(v[4], v[5]), pk_bf(v[6], v[7])};
    if ((o[0] | o[1]) == 0u) o[0] = 0x8000u;
    if ((o[2] | o[3]) == 0u) o[2] = 0x8000u;
    if (c16 < 4) st16c(rs, qa_off, o);
    STAMP(10);
  }
  asm volatile("" :: "v"(xv[0]), "v"(xv[1]), "v"(xv[2]), "v"(xv[3]));
  SCHED();
  if (cls == 3) {
    fin(IC<2>{}, WS_GB, 512, false);
    STAMP(6);
    STAMP_FLUSH(0); TR_FLUSH();
    return;
  }
  if (cls == 0) {
    char* scr = smem + L_SCR;
    const int ab = idx >> 5, r = lane & 31, hh = lane >> 5;
    const unsigned fo = WS_QX + (w4 * 8 + (c16 >> 1)) * 2048 + (c16 & 1) * 512 + r4 * 8;
    u32x2 fl;
    u32x4 g[9];
    const unsigned o8 = WS_QA + (ab * 32 + w4 * 8) * 1024 + lane * 16, ol = WS_QA + (ab * 32 + r) * 1024 + 992 + 16 * hh;
#pragma unroll
    for (int i = 0; i < 32; ++i) {
      if (i + WB0 >= 16 && i + WB0 < 32) Wb[(i + WB0) & 15] = ld16nt(pb + ((i + WB0) & 15) * 2048);
      if (i + WB0 == 31) lds_add(cnt + 14, 1u, lane);
      if (i == 30) fl = ld8c(rs, fo);
      SCHED();
      *reinterpret_cast<u32x4*>(smem + ((i >> 3) * 4 + r4) * 8192 + toff(4 * (i & 7) + w4, c16)) = i < 16 ? Wa[i & 15] : Wb[i & 15];
      if ((i & 3) == 3) TR();
      SCHED();
    }
    STAMP(8);
    u32x4 qlast;
    {
      if (!__all(fl[1] == tag)) {
#pragma unroll 1
        for (int spin = 0; spin < SPIN_MAX; ++spin) {
          COMPILER_FENCE();
          if (__all(ld8c(rs, fo)[1] == tag)) break;
        }
      }
      {
        const char* hp = ws + WS_QA + ab * 32768 + w4 * 8192 + ((idx + w4 * 11) & 31) * 128;
#pragma unroll 1
        for (int spin = 0; spin < SPIN_MAX; ++spin) {
          u32x2 a, c;
          sld2<4096>(hp, a, c);
          if ((a[0] | a[1]) != 0u && (c[0] | c[1]) != 0u) break;
          __builtin_amdgcn_s_sleep(2);
        }
      }
      STAMP(5);
      sbar4(cnt + 7, lane);
      STAMP(12);
      unsigned pend = 0x1ffu;
#pragma unroll 1
      for (int spin = 0; spin < SPIN_MAX && pend; ++spin) {
        COMPILER_FENCE();
        u32x4 t[9];
#pragma unroll
        for (int i = 0; i < 9; ++i) t[i] = ld16c(rs, (pend >> i & 1) ? (i < 8 ? o8 + i * 1024 : ol) : OOB);
#pragma unroll
        for (int i = 0; i < 9; ++i) {
          if (pend >> i & 1) g[i] = t[i];
          if (__all(nz8(t[i]))) pend &= ~(1u << i);
        }
      }
      STAMP(9);
      qlast = g[8];
#pragma unroll
      for (int i = 0; i < 8; ++i)
        if (lane < 62) *reinterpret_cast<u32x4*>(scr + (w4 * 8 + i) * 1008 + 16 * lane) = g[i];
    }
    sbar4(cnt + 3, lane);
    STAMP(10);
    f32x16 sacc;
#pragma unroll
    for (int i = 0; i < 16; ++i) sacc[i] = 0.f;
#pragma unroll
    for (int jj = 0; jj < 32; ++jj) {
      const u32x4 af = *reinterpret_cast<const u32x4*>(smem + (w4 * 4 + (jj >> 3)) * 8192 + toff(r, 2 * (jj & 7) + hh));
      const u32x4 bf = jj < 31 ? *reinterpret_cast<const u32x4*>(scr + r * 1008 + 16 * (2 * (jj < 31 ? jj : 0) + hh)) : qlast;
      sacc = __builtin_amdgcn_mfma_f32_32x32x16_bf16(__builtin_bit_cast(bf16x8_t, af), __builtin_bit_cast(bf16x8_t, bf), sacc, 0, 0, 0);
    }
    float m = sacc[0];
#pragma unroll
    for (int i = 1; i < 16; ++i) m = fmaxf(m, sacc[i]);
    m = xhalf_max(m);
    if (hh == 0) *reinterpret_cast<float*>(scr + r * 1008 + 992 + 4 * w4) = m;
    sbar4(cnt + 4, lane);
    STAMP(11);
    {
      const f32x4 m4 = *reinterpret_cast<const f32x4*>(scr + r * 1008 + 992);
      m = fmaxf(fmaxf(m4[0], m4[1]), fmaxf(m4[2], m4[3]));
    }
    const float mc = m * C2;
    float p[16], ls = 0.f;
#pragma unroll
    for (int i = 0; i < 16; ++i) { p[i] = ex2(fmaf(sacc[i], C2, -mc)); ls += p[i]; }
    ls = xhalf_sum(ls);
    sbar4(cnt + 5, lane);
    u32x4* pf = reinterpret_cast<u32x4*>(scr);
    float* wl = reinterpret_cast<float*>(scr + 8192);
    if (hh == 0) wl[r * 4 + w4] = ls;
    pf[(2 * w4) * 64 + lane] = u32x4{pk_bf(p[0], p[1]), pk_bf(p[2], p[3]), pk_bf(p[4], p[5]), pk_bf(p[6], p[7])};
    pf[(2 * w4 + 1) * 64 + lane] = u32x4{pk_bf(p[8], p[9]), pk_bf(p[10], p[11]), pk_bf(p[12], p[13]), pk_bf(p[14], p[15])};
    sbar4(cnt + 6, lane);
    u32x4 pb8[8];
#pragma unroll
    for (int ks = 0; ks < 8; ++ks) pb8[ks] = pf[ks * 64 + lane];
    const f32x4 l4 = *reinterpret_cast<const f32x4*>(wl + r * 4);
    const float lsum = (l4[0] + l4[1]) + (l4[2] + l4[3]), rl = __builtin_amdgcn_rcpf(lsum);
    const int qq = (lane & 15) >> 2, pp = lane & 3, blk2 = (lane >> 4) & 1;
    const unsigned tw = tstart + PAR(13, TW), th = tw - PAR(14, HL);
    u32x4 vv[2][4];
    bool held = false;
    char* ot = scr + 12288 + w4 * 4096;
#pragma unroll
    for (int c2 = 0; c2 < 2; ++c2) {
      f32x16 oacc[2];
#pragma unroll
      for (int ct = 0; ct < 2; ++ct)
#pragma unroll
        for (int i = 0; i < 16; ++i) oacc[ct][i] = 0.f;
#pragma unroll
      for (int ks = 0; ks < 8; ++ks)
#pragma unroll
        for (int ct = 0; ct < 2; ++ct) {
          typedef __attribute__((address_space(3))) s16x4* lp;
          const char* tile = smem + ((ks >> 1) * 4 + w4) * 8192;
          const int cc = 2 * c2 + ct;
          const s16x4 lo = __builtin_amdgcn_ds_read_tr16_b64_v4i16((lp)(tile + toff(16 * (ks & 1) + 4 * hh + qq, 4 * cc + 2 * blk2 + (pp >> 1)) + 8 * (pp & 1)));
          const s16x4 hi = __builtin_amdgcn_ds_read_tr16_b64_v4i16((lp)(tile + toff(16 * (ks & 1) + 8 + 4 * hh + qq, 4 * cc + 2 * blk2 + (pp >> 1)) + 8 * (pp & 1)));
          const s16x8 afr = __builtin_shufflevector(lo, hi, 0, 1, 2, 3, 4, 5, 6, 7);
          oacc[ct] = __builtin_amdgcn_mfma_f32_32x32x16_bf16(__builtin_bit_cast(bf16x8_t, afr), __builtin_bit_cast(bf16x8_t, pb8[ks]), oacc[ct], 0, 0, 0);
        }
#pragma unroll
      for (int ct = 0; ct < 2; ++ct)
#pragma unroll
        for (int g = 0; g < 4; ++g)
          *reinterpret_cast<u32x2*>(ot + r * 128 + 16 * ((4 * ct + g) ^ (r & 7)) + 8 * hh) = u32x2{pk_bf(oacc[ct][4 * g] * rl, oacc[ct][4 * g + 1] * rl), pk_bf(oacc[ct][4 * g + 2] * rl, oacc[ct][4 * g + 3] * rl)};
      if (c2 == 0) { held = (int)((unsigned)wall_clock64() + 30u - th) >= 0; if (held) lds_add(cnt + 8, 1u, lane); }
      __builtin_amdgcn_wave_barrier();
#pragma unroll
      for (int i = 0; i < 4; ++i) { vv[c2][i] = *reinterpret_cast<const u32x4*>(ot + (8 * i + (lane >> 3)) * 128 + 16 * ((lane ^ (lane >> 3)) & 7)); asm volatile("" : "+v"(vv[c2][i])); }
      __builtin_amdgcn_wave_barrier();
    }
    lds_arrive(cnt + 1, lane);
    if (!held) { until(th); lds_add(cnt + 8, 1u, lane); }
    until(tw);
#pragma unroll
    for (int i = 0; i < 8; ++i) st16c(rs, WS_APO + ((((8 * (i >> 1) + (lane >> 3)) * 2 + (w4 >> 1)) * 64 + idx) * 256 + (w4 & 1) * 128 + 64 * (i & 1) + 8 * (lane & 7)) * 2, vv[i & 1][i >> 1]);
    STAMP(1);
    asm volatile("s_waitcnt vmcnt(0)" ::: "memory");
    if (lds_arrive(cnt + 9 + (w4 >> 1), lane) == 1u && hh == 0)
      st8c(rs, WS_AML + ((r * 2 + (w4 >> 1)) * 64 + idx) * 8, u32x2{__float_as_uint(mc + __builtin_amdgcn_logf(lsum)), tag});
    lds_add(cnt + 8, ~0u, lane);
    STAMP(2);
    STAMP_FLUSH(0); TR_FLUSH();
    return;
  }
#pragma unroll
  for (int ip = 0; ip < 8; ++ip) {
#pragma unroll
    for (int k = 0; k < 4; ++k) { const unsigned lo = plo(Wb[2 * ip][k], Wb[2 * ip + 1][k]), hi = phi(Wb[2 * ip][k], Wb[2 * ip + 1][k]); Wb[2 * ip][k] = lo; Wb[2 * ip + 1][k] = hi; }
    pin2(Wb[2 * ip], Wb[2 * ip + 1]);
  }
  if (cls == 2) {
    float* red2 = red + 4 * 256;
    float* wml = red + 3072;
    u32x16 f[2];
    if (w4 == 0) {
#pragma unroll 1
      for (int spin = 0; spin < SPIN_MAX; ++spin) {
        const u32x16 t = sld64(ws + WS_AML + (h * 2 + b) * 512);
        bool any = false;
#pragma unroll
        for (int i = 0; i < 8; ++i) any |= t[2 * i + 1] == tag;
        if (any) break;
        __builtin_amdgcn_s_sleep(8);
      }
      lds_add(cnt + 7, 1u, lane);
    } else lds_wait_slow(cnt + 7, 1u);
#pragma unroll 1
    for (int spin = 0; spin < SPIN_MAX; ++spin) {
      sld128(ws + WS_AML + ((h * 2 + b) * 64 + w4 * 16) * 8, f);
      unsigned e = 0u;
#pragma unroll
      for (int i = 0; i < 16; ++i) e |= f[i >> 3][2 * (i & 7) + 1] ^ tag;
      if (e == 0u) break;
      __builtin_amdgcn_s_sleep(2);
    }
    STAMP(3);
    u32x4 pp[8];
#pragma unroll
    for (int i = 0; i < 8; ++i) pp[i] = ld16c(rs, WS_APO + ((h * 2 + b) * 64 + w4 * 16 + 2 * i) * 512 + lane * 16);
    SCHED();
    float wt[8];
    {
      float M = -INFINITY, L = 0.f;
#pragma unroll
      for (int i = 0; i < 8; ++i) {
        unsigned z0 = f[i >> 2][4 * (i & 3)], z1 = f[i >> 2][4 * (i & 3) + 2];
        asm("" : "+s"(z0), "+s"(z1));
        wt[i] = __uint_as_float(lane < 32 ? z0 : z1); M = fmaxf(M, wt[i]);
      }
      M = xhalf_max(M);
#pragma unroll
      for (int i = 0; i < 8; ++i) { wt[i] = ex2(wt[i] - M); L += wt[i]; }
      L = xhalf_sum(L);
      if (lane == 0) *reinterpret_cast<f32x2*>(wml + 2 * w4) = f32x2{M, L};
    }
    SCHED();
    float a8[8];
#pragma unroll
    for (int k = 0; k < 8; ++k) a8[k] = 0.f;
#pragma unroll
    for (int i = 0; i < 8; ++i) {
#pragma unroll
      for (int k = 0; k < 4; ++k) { a8[2 * k] = fmaf(wt[i], bflo(pp[i][k]), a8[2 * k]); a8[2 * k + 1] = fmaf(wt[i], bfhi(pp[i][k]), a8[2 * k + 1]); }
      asm volatile("" : "+v"(a8[0]), "+v"(a8[1]), "+v"(a8[2]), "+v"(a8[3]), "+v"(a8[4]), "+v"(a8[5]), "+v"(a8[6]), "+v"(a8[7]));
    }
    STAMP(6);
    *reinterpret_cast<f32x4*>(red + w4 * 256 + 8 * (lane & 31) + 4 * (lane >> 5)) = f32x4{rs32(a8[0], a8[4]), rs32(a8[1], a8[5]), rs32(a8[2], a8[6]), rs32(a8[3], a8[7])};
    sbar4(cnt + 3, lane);
    STAMP(4);
    float acc[2][8];
#pragma unroll
    for (int bb = 0; bb < 2; ++bb) {
      u32x4 cv[2];
      const f32x4 ml = *reinterpret_cast<const f32x4*>(wml + 4 * bb);
      const float M = fmaxf(ml[0], ml[2]), e0 = ex2(ml[0] - M), e1 = ex2(ml[2] - M), rl = 1.f / fmaf(e0, ml[1], e1 * ml[3]), s0 = e0 * rl, s1 = e1 * rl;
#pragma unroll
      for (int e = 0; e < 2; ++e) {
        const float* src = red + bb * 512 + w4 * 64 + 16 * r4 + 8 * e;
        const f32x4 t0 = s0 * *reinterpret_cast<const f32x4*>(src) + s1 * *reinterpret_cast<const f32x4*>(src + 256), t1 = s0 * *reinterpret_cast<const f32x4*>(src + 4) + s1 * *reinterpret_cast<const f32x4*>(src + 260);
        cv[e] = u32x4{pk_bf(t0[0], t0[1]), pk_bf(t0[2], t0[3]), pk_bf(t1[0], t1[1]), pk_bf(t1[2], t1[3])};
      }
#pragma unroll
      for (int k = 0; k < 8; ++k) acc[bb][k] = 0.f;
#pragma unroll
      for (int ip = 0; ip < 8; ++ip)
#pragma unroll
        for (int k = 0; k < 4; ++k) { acc[bb][2 * k] = dot2(Wb[2 * ip][k], cv[ip >> 2][ip & 3], acc[bb][2 * k]); acc[bb][2 * k + 1] = dot2(Wb[2 * ip + 1][k], cv[ip >> 2][ip & 3], acc[bb][2 * k + 1]); }
    }
    {
      float v8[8], v4[4];
#pragma unroll
      for (int k = 0; k < 8; ++k) v8[k] = rs32(acc[0][k], acc[1][k]);
#pragma unroll
      for (int k = 0; k < 4; ++k) v4[k] = rs16(v8[k], v8[k + 4]);
      *reinterpret_cast<f32x4*>(red2 + (w4 * 2 + (lane >> 5)) * 128 + 8 * c16 + 4 * (r4 & 1)) = f32x4{v4[0], v4[1], v4[2], v4[3]};
    }
    {
      COMPILER_FENCE();
      const unsigned got = lds_arrive_nw(L_CNT + 16);
      f32x4 t4[4];
#pragma unroll
      for (int w = 0; w < 4; ++w) t4[w] = *reinterpret_cast<const f32x4*>(red2 + w * 256 + 4 * lane);
      SCHED();
      if (lds_done(got) == 3u) {
        const f32x4 t = (t4[0] + t4[1]) + (t4[2] + t4[3]);
        const unsigned co = WS_CX + ((h * 2 + b) * 256 + 64 * r4 + 32 * (c16 & 1) + 2 * (c16 >> 1)) * 8;
        st16c(rs, co, u32x4{__float_as_uint(t[0]), tag, __float_as_uint(t[1]), tag});
        st16c(rs, co + 128, u32x4{__float_as_uint(t[2]), tag, __float_as_uint(t[3]), tag});
      }
    }
    STAMP(12);
    STAMP_FLUSH(0); TR_FLUSH();
    return;
  }
  {
    unsigned op;
    {
      u32x4 c;
      const unsigned co = WS_CX + ((h * 2 + ((lane >> 3) & 1)) * 256 + (lane >> 5) * 128 + b * 64 + w4 * 16 + 2 * (lane & 7)) * 8;
      if (idx == 0 && w4 == 1) {
#pragma unroll 1
        for (int spin = 0; spin < SPIN_MAX; ++spin) {
          COMPILER_FENCE();
          if (__all(ld8c(rs, WS_QX + 65536 + (lane >> 1) * 2048 + (lane & 1) * 512)[1] == tag)) break;
          __builtin_amdgcn_s_sleep(8);
        }
      }
      if (w4 == 0) { shint<2048, 4>(ws + WS_CX + (h * 512 + b * 64) * 8, tag); lds_add(cnt + 7, 1u, lane); }
      else lds_wait_slow(cnt + 7, 1u);
      STAMP(3);
#pragma unroll 1
      for (int spin = 0; spin < SPIN_MAX; ++spin) {
        COMPILER_FENCE();
        c = ld16c(rs, co);
        if (__all((c[1] == tag) & (c[3] == tag))) break;
        __builtin_amdgcn_s_sleep(1);
      }
      float o0 = __uint_as_float(c[0]), o1 = __uint_as_float(c[2]);
      o0 += dpp<0x128>(o0); o1 += dpp<0x128>(o1);
      op = pk_bf(o0, o1);
    }
    STAMP(14);
    float* red3 = red + 4096;
    float a[2][8];
#pragma unroll
    for (int bb = 0; bb < 2; ++bb)
#pragma unroll
      for (int k = 0; k < 8; ++k) a[bb][k] = 0.f;
#pragma unroll
    for (int ip = 0; ip < 8; ++ip) {
      const unsigned v0 = __builtin_amdgcn_readlane(op, ip), v1 = __builtin_amdgcn_readlane(op, 32 + ip);
#pragma unroll
      for (int k = 0; k < 4; ++k) {
        a[0][2 * k] = dot2(Wb[2 * ip][k], v0, a[0][2 * k]); a[0][2 * k + 1] = dot2(Wb[2 * ip + 1][k], v0, a[0][2 * k + 1]);
        a[1][2 * k] = dot2(Wb[2 * ip][k], v1, a[1][2 * k]); a[1][2 * k + 1] = dot2(Wb[2 * ip + 1][k], v1, a[1][2 * k + 1]);
      }
    }
#pragma unroll
    for (int bb = 0; bb < 2; ++bb) {
      *reinterpret_cast<f32x4*>(red3 + (w4 * 2 + bb) * 512 + 8 * lane) = f32x4{a[bb][0], a[bb][1], a[bb][2], a[bb][3]};
      *reinterpret_cast<f32x4*>(red3 + (w4 * 2 + bb) * 512 + 8 * lane + 4) = f32x4{a[bb][4], a[bb][5], a[bb][6], a[bb][7]};
    }
    sbar4(cnt + 5, lane);
    f32x4 t = {0.f, 0.f, 0.f, 0.f};
#pragma unroll
    for (int w = 0; w < 4; ++w) t += *reinterpret_cast<const f32x4*>(red3 + w * 1024 + 4 * tid);
    st16c(rs, WS_GA + ((tid >> 7) * 64 + idx) * 2048 + (tid & 127) * 16, u32x4{pk_bf(t[0], t[1]), tag, pk_bf(t[2], t[3]), tag});
    STAMP(5);
  }
  fin(IC<1>{}, WS_GA, 0, idx == 0);
  STAMP(6);
  STAMP_FLUSH(0); TR_FLUSH();
}

extern "C" void kernel_launch(void* const* d_in, const int* in_sizes, int n_in, void* d_out, int out_size, void* d_ws, size_t ws_size, hipStream_t stream) {
  const u16* xA = (const u16*)d_in[0];
  const u16* ckv = (const u16*)d_in[1];
  const u16* w_q = (const u16*)d_in[2];
  const u16* w_uk = (const u16*)d_in[3];
  const u16* w_uv = (const u16*)d_in[4];
  const u16* w_o = (const u16*)d_in[5];
  const u16* xB = (const u16*)d_in[6];
  const u16* kc = (const u16*)d_in[7];
  const u16* vc = (const u16*)d_in[8];
  const u16* wq = (const u16*)d_in[9];
  const u16* wo = (const u16*)d_in[10];
  kmain<<<256, 512, 0, stream>>>(xA, ckv, w_q, w_uk, w_uv, w_o, xB, kc, vc, wq, wo, (char*)d_ws, (u16*)d_out);
}
```

```cpp
#define PROBE 100
#include <hip/hip_runtime.h>
#include <hip/hip_bf16.h>
#include <stdint.h>

#define DI __device__ __forceinline__
typedef __attribute__((ext_vector_type(4))) unsigned u32x4;
typedef __attribute__((ext_vector_type(2))) unsigned u32x2;
typedef __attribute__((ext_vector_type(4))) float f32x4;
typedef __attribute__((ext_vector_type(2))) float f32x2;
typedef __attribute__((ext_vector_type(16))) float f32x16;
typedef __attribute__((ext_vector_type(8))) __bf16 bf16x8_t;
typedef __attribute__((ext_vector_type(2))) __bf16 bf16x2_t;
typedef __attribute__((ext_vector_type(4))) short s16x4;
typedef __attribute__((ext_vector_type(8))) short s16x8;
typedef unsigned short u16;
typedef unsigned long long u64;

constexpr int NS0 = 14, NS1 = 17, NS2 = 16, NS3 = 17;
constexpr u64 TL0 = 0xb2991b5d33cb1c1ull, TH0 = 0xf78d30ull, TL1 = 0x8e0759551349142ull, TH1 = 0x3eef7cefae7ull;
constexpr u64 TL2 = 0x96179a59238a183ull, TH2 = 0xffad71b68ull, TL3 = 0x89f718510308100ull, TH3 = 0x3ce76caeaa6ull;
constexpr int RA2 = 0xC0, RA3 = 0;
constexpr int AHL = 2;
constexpr int QPE = 5;
constexpr int WBW = 4, WB0 = 7;
#define TL 2390
constexpr unsigned TA0 = 135, TA1 = 120, TA2 = 120, TA3 = 120, STG = 12, BSG = 6, TW = 1640, HL = 40, TP0 = 1810, TP2 = 2060, PA = 150;
#define PAR(i, v) (v)
constexpr u64 LA2 = TH2 >> (6 * (NS2 - 11)), LA3 = TH3 >> (6 * (NS3 - 11));
constexpr u64 chunk_mask(u64 lo, u64 hi, int n) { u64 m = 0; for (int i = 0; i < n; ++i) m |= 1ull << ((i < 10 ? lo >> (6 * i) : hi >> (6 * (i - 10))) & 63); return m; }
static_assert((chunk_mask(TL0, TH0, NS0) | chunk_mask(TL1, TH1, NS1) | chunk_mask(TL2, TH2, NS2) | chunk_mask(TL3, TH3, NS3)) == ~0ull && (RA2 & RA3) == 0, "every chunk once");
static_assert(NS0 + NS1 + NS2 + NS3 == 64, "split sizes");
constexpr int NSMIN = 14, NSMAX = 18;
static_assert(NS0 >= NSMIN && NS1 >= NSMIN && NS2 - 1 >= NSMIN && NS3 - 1 >= NSMIN && NS0 <= NSMAX && NS1 + 1 <= NSMAX && NS2 <= NSMAX && NS3 <= NSMAX, "range");
constexpr int SK = 4096;
constexpr float C2 = 0.08838834764831845f * 1.4426950408889634f;
constexpr u64 GOLD = 0x9E3779B97F4A7C15ull;

constexpr unsigned WS_EP = 0;
constexpr unsigned WS_QX = 4096;
constexpr unsigned WS_QA = WS_QX + 2 * 32 * 256 * 8;
constexpr unsigned WS_AML = WS_QA + 2 * 32 * 512 * 2;
constexpr unsigned WS_CX = WS_AML + 32 * 2 * 64 * 8;
constexpr unsigned WS_GZ = WS_CX + 32 * 2 * 2 * 128 * 8;
constexpr unsigned WS_GB = WS_GZ + 256 * 128;
constexpr unsigned WS_GA = WS_GB + 2 * 128 * 256 * 8;
constexpr unsigned WS_APO = 2u << 20;
static_assert(WS_GA + 64 * 2 * 256 * 8 <= WS_APO, "layout");
constexpr unsigned WS_END = WS_APO + 32 * 2 * 64 * 256 * 2;
#define STAMP(i) do { if (i == 0) { __builtin_amdgcn_sched_barrier(0); st0 = wall_clock64(); __builtin_amdgcn_sched_barrier(0); } } while (0)
#define STAMP_DECL u64 st0 = 0
#define STAMP_FLUSH(grp) do { const u64 tn_ = wall_clock64(); if (blockIdx.x == 0 && threadIdx.x == 0) st8c(rs, 2048, u32x2{(unsigned)st0, tag}); \
    u32x2 g_; for (int sp_ = 0; sp_ < SPIN_MAX; ++sp_) { COMPILER_FENCE(); g_ = ld8c(rs, 2048); if (g_[1] == tag) break; __builtin_amdgcn_s_sleep(64); } \
    const int d_ = (int)((unsigned)st0 - g_[0]); const u64 t0_ = st0 - (long long)d_; \
    long long ex_ = 0; \
    if (PROBE == 101) ex_ = 10 * (500 + d_); else if (PROBE == 102) ex_ = 10 * (500 - d_); else if (PROBE == 103) ex_ = 10 * (long long)(tn_ - t0_); \
    const u64 tend = t0_ + 6000 + ex_; while (wall_clock64() < tend) __builtin_amdgcn_s_sleep(8); } while (0)

#define TR() do {} while (0)
#define TR_FLUSH() do {} while (0)
DI float bflo(unsigned u) { return __uint_as_float(u << 16); }
DI float bfhi(unsigned u) { return __uint_as_float(u & 0xffff0000u); }
DI unsigned pk_bf(float a, float b) { const f32x2 v = {a, b}; return __builtin_bit_cast(unsigned, __builtin_convertvector(v, bf16x2_t)); }
DI float dot2(unsigned k, unsigned q, float acc) {
  return __builtin_amdgcn_fdot2_f32_bf16(__builtin_bit_cast(bf16x2_t, k), __builtin_bit_cast(bf16x2_t, q), acc, false);
}
DI unsigned plo(unsigned w0, unsigned w1) { return __builtin_amdgcn_perm(w1, w0, 0x05040100u); }
DI unsigned phi(unsigned w0, unsigned w1) { return __builtin_amdgcn_perm(w1, w0, 0x07060302u); }
template <int CTRL> DI float dpp(float x) {
  return __builtin_bit_cast(float, __builtin_amdgcn_mov_dpp(__builtin_bit_cast(int, x), CTRL, 0xf, 0xf, true));
}
DI float row16_sum(float x) { x += dpp<0xB1>(x); x += dpp<0x4E>(x); x += dpp<0x141>(x); x += dpp<0x128>(x); return x; }
DI float row16_max(float x) { x = fmaxf(x, dpp<0xB1>(x)); x = fmaxf(x, dpp<0x4E>(x)); x = fmaxf(x, dpp<0x141>(x)); x = fmaxf(x, dpp<0x128>(x)); return x; }
DI float xhalf_max(float x) { auto t = __builtin_amdgcn_permlane32_swap(__float_as_uint(x), __float_as_uint(x), false, false); return fmaxf(__uint_as_float(t[0]), __uint_as_float(t[1])); }
DI float xhalf_sum(float x) { auto t = __builtin_amdgcn_permlane32_swap(__float_as_uint(x), __float_as_uint(x), false, false); return __uint_as_float(t[0]) + __uint_as_float(t[1]); }
DI u32x4 ld16(const void* p) { return *reinterpret_cast<const u32x4*>(p); }
DI u32x4 ld16nt(const void* p) { return __builtin_nontemporal_load(reinterpret_cast<const u32x4*>(p)); }
DI float ex2(float x) { return __builtin_amdgcn_exp2f(x); }
#define SCHED() __builtin_amdgcn_sched_barrier(0)

typedef __amdgpu_buffer_rsrc_t rsrc_t;
DI u32x4 ld16c(rsrc_t rs, unsigned off) { return __builtin_amdgcn_raw_buffer_load_b128(rs, (int)off, 0, 16); }
DI u32x2 ld8c(rsrc_t rs, unsigned off) { return __builtin_amdgcn_raw_buffer_load_b64(rs, (int)off, 0, 16); }
DI void st16c(rsrc_t rs, unsigned off, u32x4 v) { __builtin_amdgcn_raw_buffer_store_b128(v, rs, (int)off, 0, 16); }
DI void st8c(rsrc_t rs, unsigned off, u32x2 v) { __builtin_amdgcn_raw_buffer_store_b64(v, rs, (int)off, 0, 16); }
typedef __attribute__((ext_vector_type(16))) unsigned u32x16;
template <int O1> DI void sld2(const char* p, u32x2& a, u32x2& b) {
  asm volatile("s_load_dwordx2 %0, %2, 0x0 glc\n\ts_load_dwordx2 %1, %2, %3 glc\n\ts_waitcnt lgkmcnt(0)" : "=&s"(a), "=&s"(b) : "s"(p), "n"(O1) : "memory");
}
DI u32x16 sld64(const char* p) {
  u32x16 v;
  asm volatile("s_load_dwordx16 %0, %1, 0x0 glc\n\ts_waitcnt lgkmcnt(0)" : "=&s"(v) : "s"(p) : "memory");
  return v;
}
DI void sld128(const char* p, u32x16 (&v)[2]) {
  asm volatile("s_load_dwordx16 %0, %2, 0x0 glc\n\ts_load_dwordx16 %1, %2, 0x40 glc\n\ts_waitcnt lgkmcnt(0)" : "=&s"(v[0]), "=&s"(v[1]) : "s"(p) : "memory");
}
constexpr int SPIN_MAX = 1 << 20;
constexpr unsigned OOB = 0x7ffffff0u;
DI void rawbar() { __builtin_amdgcn_sched_barrier(0); __builtin_amdgcn_s_barrier(); __builtin_amdgcn_sched_barrier(0); }
#define COMPILER_FENCE() asm volatile("" ::: "memory")
template <int O1, int SL> DI void shint(const char* p, unsigned tag) {
#pragma unroll 1
  for (int spin = 0; spin < SPIN_MAX; ++spin) {
    u32x2 a, b;
    sld2<O1>(p, a, b);
    if (((a[1] ^ tag) | (b[1] ^ tag)) == 0u) break;
    __builtin_amdgcn_s_sleep(SL);
  }
}

template <int N> struct IC { static constexpr int v = N; };
template <int I, int N, class F> DI void sfor(F&& f) { if constexpr (I < N) { f(IC<I>{}); sfor<I + 1, N>(f); } }
typedef __attribute__((address_space(3))) unsigned lu32;
DI void lds_wait(unsigned* cnt, unsigned target) {
#pragma unroll 1
  for (int spin = 0; spin < SPIN_MAX; ++spin) {
    if (__builtin_amdgcn_readfirstlane(__hip_atomic_load((lu32*)cnt, __ATOMIC_RELAXED, __HIP_MEMORY_SCOPE_WORKGROUP)) >= target) break;
    __builtin_amdgcn_s_sleep(1);
  }
  COMPILER_FENCE();
}
DI void lds_wait_slow(unsigned* cnt, unsigned target) {
#pragma unroll 1
  for (int spin = 0; spin < SPIN_MAX; ++spin) {
    if (__builtin_amdgcn_readfirstlane(__hip_atomic_load((lu32*)cnt, __ATOMIC_RELAXED, __HIP_MEMORY_SCOPE_WORKGROUP)) >= target) break;
    __builtin_amdgcn_s_sleep(3);
  }
  COMPILER_FENCE();
}
DI void until(unsigned t) {
#pragma unroll 1
  for (int spin = 0; spin < 1024; ++spin) { if ((int)((unsigned)wall_clock64() - t) >= 0) break; __builtin_amdgcn_s_sleep(1); }
}
DI void lds_hold(unsigned* flag) {
#pragma unroll 1
  for (int spin = 0; spin < SPIN_MAX; ++spin) {
    if (__builtin_amdgcn_readfirstlane(__hip_atomic_load((lu32*)flag, __ATOMIC_RELAXED, __HIP_MEMORY_SCOPE_WORKGROUP)) == 0u) break;
    __builtin_amdgcn_s_sleep(2);
  }
}
DI unsigned lds_arrive_nw(unsigned addr) {
  unsigned old, one = 1u;
  asm volatile("s_mov_b64 exec, 1\n\tds_add_rtn_u32 %0, %1, %2\n\ts_mov_b64 exec, -1" : "=v"(old) : "v"(addr), "v"(one) : "memory");
  return old;
}
DI unsigned lds_done(unsigned old) { asm volatile("s_waitcnt lgkmcnt(0)" : "+v"(old) :: "memory"); return __builtin_amdgcn_readfirstlane(old); }
DI unsigned lds_arrive(unsigned* cnt, int) { return lds_done(lds_arrive_nw((unsigned)(size_t)(lu32*)cnt)); }
DI void lds_add(unsigned* cnt, unsigned v, int) {
  asm volatile("s_mov_b64 exec, 1\n\tds_add_u32 %0, %1\n\ts_mov_b64 exec, -1" :: "v"((unsigned)(size_t)(lu32*)cnt), "v"(v) : "memory");
}
DI void sbar4(unsigned* cnt, int lane) { if (lds_arrive(cnt, lane) < 3u) lds_wait(cnt, 4u); }
DI float rs32(float a, float b) { auto t = __builtin_amdgcn_permlane32_swap(__float_as_uint(a), __float_as_uint(b), false, false); return __uint_as_float(t[0]) + __uint_as_float(t[1]); }
DI float rs16(float a, float b) { auto t = __builtin_amdgcn_permlane16_swap(__float_as_uint(a), __float_as_uint(b), false, false); return __uint_as_float(t[0]) + __uint_as_float(t[1]); }
template <int N> DI unsigned rowbcast(unsigned x) { return (unsigned)__builtin_amdgcn_mov_dpp((int)x, 0x150 + N, 0xf, 0xf, true); }
DI void pin2(u32x4& a, u32x4& b) { asm volatile("" : "+v"(a), "+v"(b)); }
DI void use2(const u32x4& a, const u32x4& b) { asm volatile("" :: "v"(a), "v"(b)); }
DI bool nz8(const u32x4& v) { return ((v[0] | v[1]) != 0u) & ((v[2] | v[3]) != 0u); }

DI u32x4 qsum(const u32x4& g) {
  const auto ta = __builtin_amdgcn_permlane32_swap(g[0], g[0], false, false), tc = __builtin_amdgcn_permlane32_swap(g[2], g[2], false, false);
  const unsigned a = pk_bf(bflo(ta[0]) + bflo(ta[1]), bfhi(ta[0]) + bfhi(ta[1])), c = pk_bf(bflo(tc[0]) + bflo(tc[1]), bfhi(tc[0]) + bfhi(tc[1]));
  const auto ra = __builtin_amdgcn_permlane16_swap(a, a, false, false), rc = __builtin_amdgcn_permlane16_swap(c, c, false, false);
  return u32x4{ra[0], rc[0], ra[1], rc[1]};
}
DI unsigned toff(unsigned row, unsigned ch) { return 256u * row + 16u * (ch ^ (((row & 3) << 2) | ((row >> 2) & 3))); }
constexpr int SMEM = 160 * 1024;
constexpr int L_SCR = 131072;
constexpr int L_MRG = 65536;
constexpr int L_CNT = L_SCR + 32 * 1008;

struct KV { u32x4 r[8]; };

__global__ __launch_bounds__(512, 2) void kmain(const u16* __restrict__ xA, const u16* __restrict__ ckv, const u16* __restrict__ w_q, const u16* __restrict__ w_uk,
                                                const u16* __restrict__ w_uv, const u16* __restrict__ w_o,
                                                const u16* __restrict__ xB, const u16* __restrict__ kc, const u16* __restrict__ vc,
                                                const u16* __restrict__ wq, const u16* __restrict__ wo,
                                                char* __restrict__ ws, u16* __restrict__ out) {
  const unsigned tstart = (unsigned)wall_clock64();
  SCHED();
  __shared__ __attribute__((aligned(16))) char smem[SMEM];
  const int tid = threadIdx.x, blk = blockIdx.x, lane = tid & 63;
  const int wave = __builtin_amdgcn_readfirstlane(tid >> 6), w4 = wave & 3;
  const int cls = (blk >> 3) & 3;
  int idx = 8 * (blk & 7) + (blk >> 5);
  if (wave < 4) idx = (idx & ~7) | ((idx + (cls == 3 ? 2 : (cls == 2 ? 4 : 0))) & 7);
  const int h = idx >> 1, b = idx & 1;
  const rsrc_t rs = __builtin_amdgcn_make_buffer_rsrc(ws, 0, (int)WS_END, 0x00020000);
  const int r4 = lane >> 4, c16 = lane & 15;
  unsigned* cnt = reinterpret_cast<unsigned*>(smem + L_CNT);
  STAMP_DECL;
  STAMP(0);
  asm volatile("" :: "s"(xA), "s"(ckv), "s"(w_q), "s"(w_uk), "s"(w_uv), "s"(w_o), "s"(xB), "s"(kc), "s"(vc), "s"(wq), "s"(wo), "s"(ws), "s"(out), "s"(tstart));
  unsigned tag, tlow;
  u32x2 epw;
  auto take_epoch = [&]() {
    asm volatile("" : "+v"(epw));
    const u64 t64 = (((u64)(unsigned)__builtin_amdgcn_readfirstlane(epw[1]) << 32) | (unsigned)__builtin_amdgcn_readfirstlane(epw[0])) + GOLD;
    tag = (unsigned)(t64 >> 32); tlow = (unsigned)t64;
    asm volatile("s_waitcnt lgkmcnt(0)" : "+s"(tag), "+s"(tlow) :: "memory");
  };
  if (wave >= 4) {
    if (tid - 256 < 16) cnt[tid - 256] = 0u;
    asm volatile("s_waitcnt lgkmcnt(0)" ::: "memory");
    const unsigned pTL = PAR(0, TL), pTA = PAR(1 + cls, cls == 0 ? TA0 : (cls == 1 ? TA1 : (cls == 2 ? TA2 : TA3))), pSTG = PAR(9, STG), pBSG = PAR(10, BSG);
    const unsigned pRA2 = PAR(11, RA2), pRA3 = PAR(12, RA3), pTP = PAR(15 + cls, cls == 0 ? TP0 : TP2), pPA = PAR(19 + cls, (cls & 1) ? 0u : PA);
    rawbar();
    const int e2 = (pRA2 >> (idx & 7)) & 1, e3 = (pRA3 >> (idx & 7)) & 1;
    const int ns = cls == 0 ? NS0 : (cls == 1 ? NS1 + e2 + e3 : (cls == 2 ? NS2 - e2 : NS3 - e3));
    const size_t kvo = ((size_t)(b * 32 + h) * SK + w4 * 16) * 256;
    const char* kb = reinterpret_cast<const char*>(kc) + kvo;
    const char* vb = reinterpret_cast<const char*>(vc) + kvo;
    const unsigned lo = lane * 16u;
    u64 tlo = cls == 0 ? TL0 : (cls == 1 ? TL1 : (cls == 2 ? TL2 : TL3));
    u64 thi = cls == 0 ? TH0 : (cls == 1 ? TH1 | ((e2 ? LA2 : LA3) << (6 * (NS1 - 10))) : (cls == 2 ? TH2 : TH3));
    const u64 tlo0 = tlo, thi0 = thi;
    int hn = 0;
    const char* hb = kb;
    const char* pc[2];
    auto next2 = [&]() {
#pragma unroll
      for (int i = 0; i < 2; ++i) {
        if (hn == ns) { tlo = tlo0; thi = thi0; hb = vb; }
        ++hn;
        pc[i] = hb + ((((unsigned)tlo + (unsigned)idx) & 63u) << 14);
        tlo = (tlo >> 6) | ((thi & 63u) << 54); thi >>= 6;
      }
    };
    KV A, B;
    u32x4 W[8];
    u32x4 T[4][8];
    auto ldp = [&](KV& S, auto P) {
      constexpr int p = P.v;
      const char* q = pc[p >> 1] + lo + (p & 1) * 2048;
      S.r[2 * p] = ld16nt(q); S.r[2 * p + 1] = ld16nt(q + 1024);
    };
    auto landed = [&](KV& S, auto P) { pin2(S.r[2 * P.v], S.r[2 * P.v + 1]); };
    KV X;
    const unsigned qo = WS_QX + ((32 + h) * 2 + b) * 1024 + lane * 16;
    u32x4 g0;
    {
      unsigned tk = 0u;
      if (tid == 256) tk = __builtin_nontemporal_load(reinterpret_cast<const unsigned*>(kb + cls * 128));
      lds_wait_slow(cnt + 14, 4u);
      asm volatile("" :: "v"(tk));
    }
    STAMP(10);
    sfor<0, 12>([&](auto I) {
      constexpr int i = I.v, j = i - 4;
      if constexpr ((i & 3) == 0) next2();
      if constexpr (j >= 0) { if constexpr (j < 4) landed(X, IC<j & 3>{}); else landed(B, IC<j & 3>{}); }
      if constexpr (j >= 0 && (j & 1)) TR();
      if constexpr (i == QPE) g0 = ld16c(rs, qo);
      if constexpr (i < 4) ldp(X, IC<i & 3>{}); else if constexpr (i < 8) ldp(B, IC<i & 3>{}); else ldp(A, IC<i & 3>{});
      SCHED();
    });
    {
      const u32x2 t = *reinterpret_cast<const u32x2*>(cnt + 16);
      tlow = __builtin_amdgcn_readfirstlane(t[0]); tag = __builtin_amdgcn_readfirstlane(t[1]);
    }
    const char* wp = reinterpret_cast<const char*>(wo) + ((size_t)h * 128 + r4 * 8) * 1024 + w4 * 256 + c16 * 16;
    u32x4 qpk;
    unsigned tvk;
    {
      unsigned tv = 0u;
      if (tid == 256) tv = __builtin_nontemporal_load(reinterpret_cast<const unsigned*>(vb + cls * 128));
      if (!__all((g0[1] == tag) & (g0[3] == tag))) {
#pragma unroll 1
        for (int spin = 0; spin < SPIN_MAX; ++spin) {
          COMPILER_FENCE();
          g0 = ld16c(rs, qo);
          if (__all((g0[1] == tag) & (g0[3] == tag))) break;
        }
      }
      qpk = qsum(g0);
      tvk = tv;
    }
    STAMP(7);
    const bool holds = cls == 0;
    float m = 0.f, l = 0.f, acc[8];
    unsigned sc[5];
#pragma unroll
    for (int k = 0; k < 8; ++k) acc[k] = 0.f;
#pragma unroll
    for (int k = 0; k < 5; ++k) sc[k] = 0xff800000u;
    int vn = 0;
    unsigned due = tstart + pTL - (unsigned)(ns - 5) * pTA - (3 - w4) * pSTG - ((idx * 5 + cls * 3) & 15) * pBSG;
    unsigned rwo;
    const unsigned zo = WS_GZ + (b * 32 + h) * 512;
    u32x2 zs[4];
    float wr_;
    auto zreq = [&]() {
      asm volatile("s_load_dwordx2 %0, %4, 0x0 glc\n\ts_load_dwordx2 %1, %4, 0x80 glc\n\ts_load_dwordx2 %2, %4, 0x100 glc\n\ts_load_dwordx2 %3, %4, 0x180 glc"
                   : "=&s"(zs[0]), "=&s"(zs[1]), "=&s"(zs[2]), "=&s"(zs[3]) : "s"(ws + zo) : "memory");
    };
    auto zget = [&]() {
#pragma unroll 1
      for (int spin = 0; spin < SPIN_MAX; ++spin) {
        asm volatile("s_waitcnt lgkmcnt(0)" : "+s"(zs[0]), "+s"(zs[1]), "+s"(zs[2]), "+s"(zs[3]) :: "memory");
        if (((zs[0][1] ^ tag) | (zs[1][1] ^ tag) | (zs[2][1] ^ tag) | (zs[3][1] ^ tag)) == 0u) break;
        __builtin_amdgcn_s_sleep(2);
        zreq();
      }
      asm volatile("s_waitcnt lgkmcnt(0)" : "+s"(zs[0]), "+s"(zs[1]), "+s"(zs[2]), "+s"(zs[3]) :: "memory");
      const float z0 = __uint_as_float(zs[0][0]), z1 = __uint_as_float(zs[1][0]), z2 = __uint_as_float(zs[2][0]), z3 = __uint_as_float(zs[3][0]);
      const float Z = fmaxf(fmaxf(z0, z1), fmaxf(z2, z3));
      wr_ = ex2(fmaf(m, C2, -Z)) * __builtin_amdgcn_rcpf((ex2(z0 - Z) + ex2(z1 - Z)) + (ex2(z2 - Z) + ex2(z3 - Z)));
      asm volatile("" : "+v"(wr_));
    };
    auto qk = [&](const u32x4* K, auto I) {
      float x[4];
#pragma unroll
      for (int t = 0; t < 4; ++t) {
        x[t] = 0.f;
#pragma unroll
        for (int k = 0; k < 4; ++k) x[t] = dot2(K[t][k], qpk[k], x[t]);
      }
      const float u0 = (c16 & 1 ? x[1] : x[0]) + dpp<0xB1>(c16 & 1 ? x[0] : x[1]), u1 = (c16 & 1 ? x[3] : x[2]) + dpp<0xB1>(c16 & 1 ? x[2] : x[3]);
      float v = (c16 & 2 ? u1 : u0) + dpp<0x4E>(c16 & 2 ? u0 : u1);
      v += dpp<0x124>(v); v += dpp<0x128>(v);
      sc[I.v >> 2] = __builtin_amdgcn_update_dpp(sc[I.v >> 2], __float_as_uint(v), 0xE4, 0xf, 1 << (I.v & 3), false);
      asm volatile("" : "+v"(sc[I.v >> 2]));
    };
    auto soft = [&]() {
      m = row16_max(fmaxf(fmaxf(fmaxf(__uint_as_float(sc[0]), __uint_as_float(sc[1])), fmaxf(__uint_as_float(sc[2]), __uint_as_float(sc[3]))), __uint_as_float(sc[4])));
      const float mc = m * C2;
      l = 0.f;
#pragma unroll
      for (int k = 0; k < 5; ++k) {
        const float p = ex2(fmaf(__uint_as_float(sc[k]), C2, -mc));
        sc[k] = pk_bf(p, dpp<0xB1>(p));
        l = dot2(sc[k], 0x3f003f00u, l);
      }
      l = row16_sum(l);
    };
    auto pv = [&](const u32x4* V) {
      const unsigned p01 = rowbcast<0>(sc[0]), p23 = rowbcast<2>(sc[0]);
#pragma unroll
      for (int k = 0; k < 4; ++k) { acc[2 * k] = dot2(plo(V[0][k], V[1][k]), p01, acc[2 * k]); acc[2 * k + 1] = dot2(phi(V[0][k], V[1][k]), p01, acc[2 * k + 1]); }
#pragma unroll
      for (int k = 0; k < 4; ++k) { acc[2 * k] = dot2(plo(V[2][k], V[3][k]), p23, acc[2 * k]); acc[2 * k + 1] = dot2(phi(V[2][k], V[3][k]), p23, acc[2 * k + 1]); }
      sc[0] = (unsigned)__builtin_amdgcn_mov_dpp((int)sc[0], 0x12C, 0xf, 0xf, true);
      if ((++vn & 3) == 0) { sc[0] = sc[1]; sc[1] = sc[2]; sc[2] = sc[3]; sc[3] = sc[4]; }
      asm volatile("" : "+v"(acc[0]), "+v"(acc[1]), "+v"(acc[2]), "+v"(acc[3]), "+v"(acc[4]), "+v"(acc[5]), "+v"(acc[6]), "+v"(acc[7]));
    };
    auto step = [&](KV& S, KV& N, auto SI, auto REFILL, auto WR) {
      constexpr bool refill = REFILL.v == 1;
      constexpr int wr = WR.v;
      if constexpr (refill) next2();
      if constexpr (refill && SI.v >= 3) { until((int)(due - tstart) < (int)pTP ? due - pPA : due); due += pTA; }
      constexpr int ah = wr < 0 ? AHL : 0;
      auto point = [&](auto P) {
        if constexpr (refill) {
          if constexpr (P.v == 0 && ah > 0) { if constexpr (wr >= 0) { if (holds) lds_hold(cnt + 8); } sfor<0, ah>([&](auto Q) { ldp(N, Q); }); SCHED(); }
          landed(S, P);
          if constexpr (P.v & 1) TR();
          if constexpr (P.v + ah < 4) { if constexpr ((P.v & 1) == 0 && wr >= 0) { if (holds) lds_hold(cnt + 8); } ldp(N, IC<(P.v + ah) & 3>{}); }
          if constexpr (wr >= 0 && wr < 8) W[(4 * wr + P.v) & 7] = ld16(wp + (((4 * wr + P.v) >> 3) * 32 + ((4 * wr + P.v) & 7)) * 1024);
          SCHED();
        }
      };
      sfor<0, 2>([&](auto H) {
        constexpr int i = 2 * SI.v + H.v;
        if constexpr (REFILL.v == 2 && H.v == 0) { zreq(); SCHED(); }
        point(IC<2 * H.v>{});
        point(IC<2 * H.v + 1>{});
        if constexpr (i < NSMIN) qk(S.r + 4 * H.v, IC<i>{});
        else if constexpr (i > NSMAX) pv(S.r + 4 * H.v);
        else {
          if (i < ns) { if constexpr (i < NSMAX) qk(S.r + 4 * H.v, IC<i>{}); }
          else { if (i == ns) soft(); pv(S.r + 4 * H.v); }
        }
        SCHED();
      });
      if constexpr (REFILL.v == 2) {
        zget();
        STAMP(1);
        *reinterpret_cast<u32x4*>(smem + rwo) = u32x4{pk_bf(acc[0] * wr_, acc[1] * wr_), pk_bf(acc[2] * wr_, acc[3] * wr_), pk_bf(acc[4] * wr_, acc[5] * wr_), pk_bf(acc[6] * wr_, acc[7] * wr_)};
        COMPILER_FENCE();
      }
      if constexpr (wr >= 1) {
        constexpr int t = (wr - 1) >> 1, j0 = ((wr - 1) & 1) * 2;
#pragma unroll
        for (int jj = 0; jj < 2; ++jj)
#pragma unroll
          for (int e = 0; e < 4; ++e) {
            const unsigned a_ = W[(4 * wr - 4 + 2 * jj) & 7][e], b_ = W[(4 * wr - 3 + 2 * jj) & 7][e];
            T[t][2 * e][j0 + jj] = plo(a_, b_); T[t][2 * e + 1][j0 + jj] = phi(a_, b_);
          }
#pragma unroll
        for (int e = 0; e < 8; e += 2) pin2(T[t][e], T[t][e + 1]);
      }
      SCHED();
    };
    step(B, B, IC<1>{}, IC<0>{}, IC<-1>{});
    step(A, B, IC<2>{}, IC<1>{}, IC<0>{});
    asm volatile("" :: "v"(tvk));
    step(X, X, IC<0>{}, IC<0>{}, IC<-1>{});
    sfor<0, 4>([&](auto I) {
      step(B, A, IC<2 * I.v + 3>{}, IC<1>{}, IC<2 * I.v + 1>{});
      if constexpr (I.v == 3) {
        float* ml = reinterpret_cast<float*>(cnt + 32);
        ml[(c16 & 1) * 16 + w4 * 4 + r4] = (c16 & 1) ? l : m;
        COMPILER_FENCE();
        const unsigned got = lds_arrive_nw(L_CNT + 44);
        const float mr = ml[c16], lr = ml[16 + c16];
        SCHED();
        if (lds_done(got) == 3u) {
          const float M = row16_max(mr);
          const unsigned z = __float_as_uint(fmaf(M, C2, __builtin_amdgcn_logf(row16_sum(ex2((mr - M) * C2) * lr))));
          asm volatile("buffer_store_dwordx4 %0, %1, %2, 0 offen sc1\n\ts_nop 2" :: "v"(u32x4{z, tag, z, tag}), "v"(lane < 8 ? zo + cls * 128 + lane * 16 : OOB), "s"(rs) : "memory");
        }
        SCHED();
      }
      step(A, B, IC<2 * I.v + 4>{}, IC<1>{}, IC<2 * I.v + 2>{});
    });
    unsigned yoff = WS_GB + ((b * 128 + h * 4 + cls) * 256 + 64 * w4 + lane) * 8, rro = L_MRG + c16 * 272 + r4 * 16;
    rwo = L_MRG + (w4 * 4 + r4) * 272 + c16 * 16;
    asm volatile("" : "+v"(yoff), "+v"(rwo), "+v"(rro));
    if (cls == 0) lds_wait(cnt + 1, 4u);
    const int nit = (ns - 12) >> 1;
#pragma unroll 1
    for (int i = 0; i < nit; ++i) {
      step(B, A, IC<11>{}, IC<1>{}, IC<-1>{});
      step(A, B, IC<11>{}, IC<1>{}, IC<-1>{});
    }
    if (ns & 1) { step(B, A, IC<11>{}, IC<1>{}, IC<-1>{}); step(A, A, IC<11>{}, IC<2>{}, IC<-1>{}); }
    else step(B, B, IC<11>{}, IC<2>{}, IC<-1>{});
    {
      u32x4 af[4];
      unsigned got = lds_arrive_nw(L_CNT), need = 3u;
#pragma unroll 1
      for (int spin = 0; spin < SPIN_MAX; ++spin) {
#pragma unroll
        for (int t = 0; t < 4; ++t) af[t] = *reinterpret_cast<const u32x4*>(smem + rro + t * 64);
        SCHED();
        if (lds_done(got) >= need) break;
        need = 4u;
        __builtin_amdgcn_s_sleep(1);
        asm volatile("ds_read_b32 %0, %1" : "=v"(got) : "v"((unsigned)L_CNT) : "memory");
      }
      SCHED();
      f32x4 d[8];
      float a[8], v4[4];
      sfor<0, 5>([&](auto I) {
        constexpr int i = I.v;
        sfor<0, 8>([&](auto J) {
          constexpr int e = i + 4 * (J.v & 1), t = J.v >> 1;
          if constexpr (i < 4) { d[e] = __builtin_amdgcn_mfma_f32_16x16x32_bf16(__builtin_bit_cast(bf16x8_t, af[t]), __builtin_bit_cast(bf16x8_t, T[t][e]), t ? d[e] : f32x4{0.f, 0.f, 0.f, 0.f}, 0, 0, 0); SCHED(); }
          if constexpr (i >= 1 && (J.v == 4 || J.v == 5)) { const f32x2 u = f32x2{d[e - 1][0], d[e - 1][1]} + f32x2{d[e - 1][2], d[e - 1][3]}; a[e - 1] = u[0] + u[1]; asm volatile("" : "+v"(a[e - 1])); }
          if constexpr (i >= 1 && J.v == 6) v4[i - 1] = rs32(a[i - 1], a[i + 3]);
          if constexpr (i < 4) SCHED();
        });
      });
      st8c(rs, yoff, u32x2{pk_bf(rs16(v4[0], v4[2]), rs16(v4[1], v4[3])), tag});
    }
    STAMP(2);
    STAMP_FLUSH(1);
    TR_FLUSH();
    return;
  }
  const rsrc_t ro = __builtin_amdgcn_make_buffer_rsrc(out, 0, 4096, 0x00020000);
  auto fin = [&](auto NL, unsigned base, unsigned col, bool bump) {
    constexpr int nl = NL.v;
    unsigned doff = base + (((idx >> 5) * 4 + w4) * 16 * nl + (lane >> 2)) * 2048 + (idx & 31) * 64 + (lane & 3) * 16;
    unsigned fwo = 32768 + (c16 < 4 ? (w4 * 16 + 4 * c16 + r4) * 4 : 256 + lane * 4);
    unsigned fro = 32768 + (lane & 7) * 8, foo = lane < 8 ? ((idx >> 5) * 1024 + col + (nl == 2 ? 16 * (idx & 24) + 64 * (idx & 1) + (idx & 6) + 8 * lane : 16 * (idx & 31) + 2 * lane)) * 2 : OOB;
    asm volatile("" : "+v"(doff), "+v"(fwo), "+v"(fro), "+v"(foo));
    lds_wait(cnt + 0, 4u);
    STAMP(4);
    u32x4 d[nl];
    unsigned eh = 1u;
#pragma unroll 1
    for (int spin = SPIN_MAX; spin > 0; --spin) {
      COMPILER_FENCE();
      if (eh != 0u) {
#pragma unroll
        for (int j = 0; j < nl; ++j) d[j] = ld16c(rs, doff + j * 32768);
        eh = 0u;
#pragma unroll
        for (int j = 0; j < nl; ++j) { eh |= (d[j][1] ^ tag) | (d[j][3] ^ tag); asm volatile("" : "+v"(eh)); }
      }
      if (__builtin_amdgcn_ballot_w64(eh != 0u) == 0ull) break;
    }
    STAMP(11);
    float y[4] = {bflo(d[0][0]), bfhi(d[0][0]), bflo(d[0][2]), bfhi(d[0][2])};
    if constexpr (nl == 2) { y[0] += bflo(d[1][0]); y[1] += bfhi(d[1][0]); y[2] += bflo(d[1][2]); y[3] += bfhi(d[1][2]); }
    float t = rs16(rs32(y[0], y[2]), rs32(y[1], y[3]));
    t += dpp<0x124>(t); t += dpp<0x128>(t);
    *reinterpret_cast<float*>(smem + fwo) = t;
    COMPILER_FENCE();
    const unsigned got = lds_arrive_nw(L_CNT + 24);
    f32x2 t2[4];
#pragma unroll
    for (int w = 0; w < 4; ++w) t2[w] = *reinterpret_cast<const f32x2*>(smem + fro + w * 64);
    SCHED();
    if (lds_done(got) == 3u) {
      const f32x2 r = (t2[0] + t2[1]) + (t2[2] + t2[3]);
      __builtin_amdgcn_raw_buffer_store_b32(pk_bf(r[0], r[1]), ro, (int)foo, 0, 0);
      if (bump) st8c(rs, lane == 0 ? WS_EP : OOB, u32x2{tlow, tag});
    }
  };
  epw = ld8c(rs, WS_EP);
  SCHED();
  u32x4 Wa[16], Wb[16], xv[4];
  const u16* pb;
  const unsigned qa_off = WS_QA + (((c16 >> 1) & 1) * 32 + h) * 1024 + (b * 256 + w4 * 64 + 16 * r4 + 8 * (c16 & 1)) * 2;
  {
    const u16* pa; size_t sa;
    if (cls) {
      const size_t o_ = ((size_t)(b * 256 + w4 * 64 + 16 * r4) * 32 + h) * 128 + 8 * c16;
      pa = (cls == 1 ? w_q : (cls == 2 ? w_uk : wq)) + o_; sa = 4096;
      pb = cls == 2 ? w_uv + o_ : (cls == 1 ? w_o : wo) + ((size_t)h * 128 + b * 64 + w4 * 2) * 512 + 8 * lane;
      if (cls == 1 && c16 < 4) st16c(rs, qa_off, u32x4{0u, 0u, 0u, 0u});
    } else { pa = ckv + ((size_t)idx * 128 + w4) * 512 + 8 * lane; sa = 2048; pb = pa + 64 * 512; }
    const u16* xp = (cls == 1 ? xA : xB) + b * 256 + w4 * 64 + 16 * r4;
    SCHED();
    sfor<0, 4>([&](auto J) { xv[J.v] = ld16(xp + (J.v >> 1) * 512 + 8 * (J.v & 1)); });
    SCHED();
    sfor<0, 16>([&](auto I) { Wa[I.v] = ld16nt(pa + I.v * sa); });
    SCHED();
    rawbar();
    take_epoch();
    *reinterpret_cast<u32x2*>(cnt + 16) = u32x2{tlow, tag};
    COMPILER_FENCE();
  }
  STAMP(13);
  float* red = reinterpret_cast<float*>(smem);
  if (cls & 1) {
    float acc[2][8];
#pragma unroll
    for (int bb = 0; bb < 2; ++bb)
#pragma unroll
      for (int k = 0; k < 8; ++k) acc[bb][k] = 0.f;
    sfor<0, 8>([&](auto IP) {
      constexpr int ip = IP.v, j = ip - (8 - WBW);
      use2(Wa[2 * ip], Wa[2 * ip + 1]);
      if constexpr (ip & 1) TR();
      if constexpr (j >= 0) { Wb[2 * j] = ld16(pb + j * 4096); Wb[2 * j + 1] = ld16(pb + j * 4096 + 512); }
      SCHED();
      const unsigned xp0 = xv[ip >> 2][ip & 3], xp1 = xv[2 + (ip >> 2)][ip & 3];
#pragma unroll
      for (int k = 0; k < 4; ++k) {
        const unsigned lo = plo(Wa[2 * ip][k], Wa[2 * ip + 1][k]), hi = phi(Wa[2 * ip][k], Wa[2 * ip + 1][k]);
        acc[0][2 * k] = dot2(lo, xp0, acc[0][2 * k]); acc[0][2 * k + 1] = dot2(hi, xp0, acc[0][2 * k + 1]);
        acc[1][2 * k] = dot2(lo, xp1, acc[1][2 * k]); acc[1][2 * k + 1] = dot2(hi, xp1, acc[1][2 * k + 1]);
      }
      SCHED();
    });
    {
      float v8[8], v4[4];
#pragma unroll
      for (int k = 0; k < 8; ++k) v8[k] = rs32(acc[0][k], acc[1][k]);
#pragma unroll
      for (int k = 0; k < 4; ++k) v4[k] = rs16(v8[k], v8[k + 4]);
      *reinterpret_cast<f32x4*>(red + (w4 * 2 + (lane >> 5)) * 128 + 8 * c16 + 4 * (r4 & 1)) = f32x4{v4[0], v4[1], v4[2], v4[3]};
    }
    COMPILER_FENCE();
    const unsigned got = lds_arrive_nw(L_CNT + 8);
    f32x4 t4[4];
#pragma unroll
    for (int w = 0; w < 4; ++w) t4[w] = *reinterpret_cast<const f32x4*>(red + (w * 2 + (lane >> 5)) * 128 + 8 * c16 + 4 * (r4 & 1));
    SCHED();
    if (lds_done(got) == 3u) {
      const f32x4 t = (t4[0] + t4[1]) + (t4[2] + t4[3]);
      st16c(rs, WS_QX + (((cls >> 1) * 32 + h) * 2 + (lane >> 5)) * 1024 + b * 512 + (lane & 31) * 16, u32x4{pk_bf(t[0], t[1]), tag, pk_bf(t[2], t[3]), tag});
      STAMP(15);
      SCHED();
      lds_add(cnt + 2, 1u, lane);
    } else lds_wait(cnt + 2, 5u);
    sfor<WBW, 8>([&](auto J) {
      constexpr int j = J.v;
      use2(Wb[2 * (j - WBW)], Wb[2 * (j - WBW) + 1]);
      if constexpr ((j - WBW) & 1) TR();
      Wb[2 * j] = ld16(pb + j * 4096); Wb[2 * j + 1] = ld16(pb + j * 4096 + 512);
      if constexpr (j == 7) lds_add(cnt + 14, 1u, lane);
      SCHED();
    });
    {
      const unsigned tw = __builtin_nontemporal_load(reinterpret_cast<const unsigned*>(out + (idx >> 5) * 1024 + (cls >> 1) * 512 + 16 * (idx & 31)));
      sfor<8 - WBW, 8>([&](auto J) { use2(Wb[2 * J.v], Wb[2 * J.v + 1]); if constexpr (J.v & 1) TR(); });
      asm volatile("" :: "v"(tw));
    }
    STAMP(8);
  } else if (cls == 2) {
    sfor<0, 16 - WBW>([&](auto IP) {
      constexpr int ip = IP.v, j = ip - (8 - WBW);
      if constexpr (ip < 8) use2(Wa[2 * ip], Wa[2 * ip + 1]); else use2(Wb[2 * ip - 16], Wb[2 * ip - 15]);
      if constexpr (ip & 1) TR();
      if constexpr (j >= 0) { Wb[2 * j] = ld16nt(pb + j * 8192); Wb[2 * j + 1] = ld16nt(pb + j * 8192 + 4096); }
      SCHED();
    });
    u32x4 g0, g1;
    const unsigned qo = WS_QX + h * 2048 + lane * 16;
    shint<512, 1>(ws + WS_QX + h * 2048, tag);
    STAMP(5);
#pragma unroll 1
    for (int spin = 0; spin < SPIN_MAX; ++spin) {
      COMPILER_FENCE();
      g0 = ld16c(rs, qo); g1 = ld16c(rs, qo + 1024);
      SCHED();
      lds_add(cnt + 14, spin == 0, lane);
      SCHED();
      if (__all((g0[1] == tag) & (g0[3] == tag) & (g1[1] == tag) & (g1[3] == tag))) break;
      __builtin_amdgcn_s_sleep(1);
    }
    sfor<8 - WBW, 8>([&](auto J) { if constexpr (J.v & 1) TR(); });
    const u32x4 q0 = qsum(g0), q1 = qsum(g1);
    STAMP(9);
    float v[8];
    {
      float u[16];
#pragma unroll
      for (int i = 0; i < 16; ++i) {
        float a = 0.f, c = 0.f;
#pragma unroll
        for (int k = 0; k < 4; ++k) { a = dot2(Wa[i][k], q0[k], a); c = dot2(Wa[i][k], q1[k], c); }
        u[i] = (c16 & 2 ? c : a) + dpp<0x4E>(c16 & 2 ? a : c);
        asm volatile("" : "+v"(u[i]));
      }
#pragma unroll
      for (int i = 0; i < 8; ++i) {
        float t = (c16 & 1 ? u[i + 8] : u[i]) + dpp<0xB1>(c16 & 1 ? u[i] : u[i + 8]);
        asm volatile("" : "+v"(t));
        t += dpp<0x124>(t);
        asm volatile("" : "+v"(t));
        t += dpp<0x128>(t);
        asm volatile("" : "+v"(t));
        v[i] = t;
      }
    }
    u32x4 o = {pk_bf(v[0], v[1]), pk_bf(v[2], v[3]), pk_bf(v[4], v[5]), pk_bf(v[6], v[7])};
    if ((o[0] | o[1]) == 0u) o[0] = 0x8000u;
    if ((o[2] | o[3]) == 0u) o[2] = 0x8000u;
    if (c16 < 4) st16c(rs, qa_off, o);
    STAMP(10);
  }
  asm volatile("" :: "v"(xv[0]), "v"(xv[1]), "v"(xv[2]), "v"(xv[3]));
  SCHED();
  if (cls == 3) {
    fin(IC<2>{}, WS_GB, 512, false);
    STAMP(6);
    STAMP_FLUSH(0); TR_FLUSH();
    return;
  }
  if (cls == 0) {
    char* scr = smem + L_SCR;
    const int ab = idx >> 5, r = lane & 31, hh = lane >> 5;
    const unsigned fo = WS_QX + (w4 * 8 + (c16 >> 1)) * 2048 + (c16 & 1) * 512 + r4 * 8;
    u32x2 fl;
    u32x4 g[9];
    const unsigned o8 = WS_QA + (ab * 32 + w4 * 8) * 1024 + lane * 16, ol = WS_QA + (ab * 32 + r) * 1024 + 992 + 16 * hh;
#pragma unroll
    for (int i = 0; i < 32; ++i) {
      if (i + WB0 >= 16 && i + WB0 < 32) Wb[(i + WB0) & 15] = ld16nt(pb + ((i + WB0) & 15) * 2048);
      if (i + WB0 == 31) lds_add(cnt + 14, 1u, lane);
      if (i == 30) fl = ld8c(rs, fo);
      SCHED();
      *reinterpret_cast<u32x4*>(smem + ((i >> 3) * 4 + r4) * 8192 + toff(4 * (i & 7) + w4, c16)) = i < 16 ? Wa[i & 15] : Wb[i & 15];
      if ((i & 3) == 3) TR();
      SCHED();
    }
    STAMP(8);
    u32x4 qlast;
    {
      if (!__all(fl[1] == tag)) {
#pragma unroll 1
        for (int spin = 0; spin < SPIN_MAX; ++spin) {
          COMPILER_FENCE();
          if (__all(ld8c(rs, fo)[1] == tag)) break;
        }
      }
      {
        const char* hp = ws + WS_QA + ab * 32768 + w4 * 8192 + ((idx + w4 * 11) & 31) * 128;
#pragma unroll 1
        for (int spin = 0; spin < SPIN_MAX; ++spin) {
          u32x2 a, c;
          sld2<4096>(hp, a, c);
          if ((a[0] | a[1]) != 0u && (c[0] | c[1]) != 0u) break;
          __builtin_amdgcn_s_sleep(2);
        }
      }
      STAMP(5);
      sbar4(cnt + 7, lane);
      STAMP(12);
      unsigned pend = 0x1ffu;
#pragma unroll 1
      for (int spin = 0; spin < SPIN_MAX && pend; ++spin) {
        COMPILER_FENCE();
        u32x4 t[9];
#pragma unroll
        for (int i = 0; i < 9; ++i) t[i] = ld16c(rs, (pend >> i & 1) ? (i < 8 ? o8 + i * 1024 : ol) : OOB);
#pragma unroll
        for (int i = 0; i < 9; ++i) {
          if (pend >> i & 1) g[i] = t[i];
          if (__all(nz8(t[i]))) pend &= ~(1u << i);
        }
      }
      STAMP(9);
      qlast = g[8];
#pragma unroll
      for (int i = 0; i < 8; ++i)
        if (lane < 62) *reinterpret_cast<u32x4*>(scr + (w4 * 8 + i) * 1008 + 16 * lane) = g[i];
    }
    sbar4(cnt + 3, lane);
    STAMP(10);
    f32x16 sacc;
#pragma unroll
    for (int i = 0; i < 16; ++i) sacc[i] = 0.f;
#pragma unroll
    for (int jj = 0; jj < 32; ++jj) {
      const u32x4 af = *reinterpret_cast<const u32x4*>(smem + (w4 * 4 + (jj >> 3)) * 8192 + toff(r, 2 * (jj & 7) + hh));
      const u32x4 bf = jj < 31 ? *reinterpret_cast<const u32x4*>(scr + r * 1008 + 16 * (2 * (jj < 31 ? jj : 0) + hh)) : qlast;
      sacc = __builtin_amdgcn_mfma_f32_32x32x16_bf16(__builtin_bit_cast(bf16x8_t, af), __builtin_bit_cast(bf16x8_t, bf), sacc, 0, 0, 0);
    }
    float m = sacc[0];
#pragma unroll
    for (int i = 1; i < 16; ++i) m = fmaxf(m, sacc[i]);
    m = xhalf_max(m);
    if (hh == 0) *reinterpret_cast<float*>(scr + r * 1008 + 992 + 4 * w4) = m;
    sbar4(cnt + 4, lane);
    STAMP(11);
    {
      const f32x4 m4 = *reinterpret_cast<const f32x4*>(scr + r * 1008 + 992);
      m = fmaxf(fmaxf(m4[0], m4[1]), fmaxf(m4[2], m4[3]));
    }
    const float mc = m * C2;
    float p[16], ls = 0.f;
#pragma unroll
    for (int i = 0; i < 16; ++i) { p[i] = ex2(fmaf(sacc[i], C2, -mc)); ls += p[i]; }
    ls = xhalf_sum(ls);
    sbar4(cnt + 5, lane);
    u32x4* pf = reinterpret_cast<u32x4*>(scr);
    float* wl = reinterpret_cast<float*>(scr + 8192);
    if (hh == 0) wl[r * 4 + w4] = ls;
    pf[(2 * w4) * 64 + lane] = u32x4{pk_bf(p[0], p[1]), pk_bf(p[2], p[3]), pk_bf(p[4], p[5]), pk_bf(p[6], p[7])};
    pf[(2 * w4 + 1) * 64 + lane] = u32x4{pk_bf(p[8], p[9]), pk_bf(p[10], p[11]), pk_bf(p[12], p[13]), pk_bf(p[14], p[15])};
    sbar4(cnt + 6, lane);
    u32x4 pb8[8];
#pragma unroll
    for (int ks = 0; ks < 8; ++ks) pb8[ks] = pf[ks * 64 + lane];
    const f32x4 l4 = *reinterpret_cast<const f32x4*>(wl + r * 4);
    const float lsum = (l4[0] + l4[1]) + (l4[2] + l4[3]), rl = __builtin_amdgcn_rcpf(lsum);
    const int qq = (lane & 15) >> 2, pp = lane & 3, blk2 = (lane >> 4) & 1;
    const unsigned tw = tstart + PAR(13, TW), th = tw - PAR(14, HL);
    u32x4 vv[2][4];
    bool held = false;
    char* ot = scr + 12288 + w4 * 4096;
#pragma unroll
    for (int c2 = 0; c2 < 2; ++c2) {
      f32x16 oacc[2];
#pragma unroll
      for (int ct = 0; ct < 2; ++ct)
#pragma unroll
        for (int i = 0; i < 16; ++i) oacc[ct][i] = 0.f;
#pragma unroll
      for (int ks = 0; ks < 8; ++ks)
#pragma unroll
        for (int ct = 0; ct < 2; ++ct) {
          typedef __attribute__((address_space(3))) s16x4* lp;
          const char* tile = smem + ((ks >> 1) * 4 + w4) * 8192;
          const int cc = 2 * c2 + ct;
          const s16x4 lo = __builtin_amdgcn_ds_read_tr16_b64_v4i16((lp)(tile + toff(16 * (ks & 1) + 4 * hh + qq, 4 * cc + 2 * blk2 + (pp >> 1)) + 8 * (pp & 1)));
          const s16x4 hi = __builtin_amdgcn_ds_read_tr16_b64_v4i16((lp)(tile + toff(16 * (ks & 1) + 8 + 4 * hh + qq, 4 * cc + 2 * blk2 + (pp >> 1)) + 8 * (pp & 1)));
          const s16x8 afr = __builtin_shufflevector(lo, hi, 0, 1, 2, 3, 4, 5, 6, 7);
          oacc[ct] = __builtin_amdgcn_mfma_f32_32x32x16_bf16(__builtin_bit_cast(bf16x8_t, afr), __builtin_bit_cast(bf16x8_t, pb8[ks]), oacc[ct], 0, 0, 0);
        }
#pragma unroll
      for (int ct = 0; ct < 2; ++ct)
#pragma unroll
        for (int g = 0; g < 4; ++g)
          *reinterpret_cast<u32x2*>(ot + r * 128 + 16 * ((4 * ct + g) ^ (r & 7)) + 8 * hh) = u32x2{pk_bf(oacc[ct][4 * g] * rl, oacc[ct][4 * g + 1] * rl), pk_bf(oacc[ct][4 * g + 2] * rl, oacc[ct][4 * g + 3] * rl)};
      if (c2 == 0) { held = (int)((unsigned)wall_clock64() + 30u - th) >= 0; if (held) lds_add(cnt + 8, 1u, lane); }
      __builtin_amdgcn_wave_barrier();
#pragma unroll
      for (int i = 0; i < 4; ++i) { vv[c2][i] = *reinterpret_cast<const u32x4*>(ot + (8 * i + (lane >> 3)) * 128 + 16 * ((lane ^ (lane >> 3)) & 7)); asm volatile("" : "+v"(vv[c2][i])); }
      __builtin_amdgcn_wave_barrier();
    }
    lds_arrive(cnt + 1, lane);
    if (!held) { until(th); lds_add(cnt + 8, 1u, lane); }
    until(tw);
#pragma unroll
    for (int i = 0; i < 8; ++i) st16c(rs, WS_APO + ((((8 * (i >> 1) + (lane >> 3)) * 2 + (w4 >> 1)) * 64 + idx) * 256 + (w4 & 1) * 128 + 64 * (i & 1) + 8 * (lane & 7)) * 2, vv[i & 1][i >> 1]);
    STAMP(1);
    asm volatile("s_waitcnt vmcnt(0)" ::: "memory");
    if (lds_arrive(cnt + 9 + (w4 >> 1), lane) == 1u && hh == 0)
      st8c(rs, WS_AML + ((r * 2 + (w4 >> 1)) * 64 + idx) * 8, u32x2{__float_as_uint(mc + __builtin_amdgcn_logf(lsum)), tag});
    lds_add(cnt + 8, ~0u, lane);
    STAMP(2);
    STAMP_FLUSH(0); TR_FLUSH();
    return;
  }
#pragma unroll
  for (int ip = 0; ip < 8; ++ip) {
#pragma unroll
    for (int k = 0; k < 4; ++k) { const unsigned lo = plo(Wb[2 * ip][k], Wb[2 * ip + 1][k]), hi = phi(Wb[2 * ip][k], Wb[2 * ip + 1][k]); Wb[2 * ip][k] = lo; Wb[2 * ip + 1][k] = hi; }
    pin2(Wb[2 * ip], Wb[2 * ip + 1]);
  }
  if (cls == 2) {
    float* red2 = red + 4 * 256;
    float* wml = red + 3072;
    u32x16 f[2];
    if (w4 == 0) {
#pragma unroll 1
      for (int spin = 0; spin < SPIN_MAX; ++spin) {
        const u32x16 t = sld64(ws + WS_AML + (h * 2 + b) * 512);
        bool any = false;
#pragma unroll
        for (int i = 0; i < 8; ++i) any |= t[2 * i + 1] == tag;
        if (any) break;
        __builtin_amdgcn_s_sleep(8);
      }
      lds_add(cnt + 7, 1u, lane);
    } else lds_wait_slow(cnt + 7, 1u);
#pragma unroll 1
    for (int spin = 0; spin < SPIN_MAX; ++spin) {
      sld128(ws + WS_AML + ((h * 2 + b) * 64 + w4 * 16) * 8, f);
      unsigned e = 0u;
#pragma unroll
      for (int i = 0; i < 16; ++i) e |= f[i >> 3][2 * (i & 7) + 1] ^ tag;
      if (e == 0u) break;
      __builtin_amdgcn_s_sleep(2);
    }
    STAMP(3);
    u32x4 pp[8];
#pragma unroll
    for (int i = 0; i < 8; ++i) pp[i] = ld16c(rs, WS_APO + ((h * 2 + b) * 64 + w4 * 16 + 2 * i) * 512 + lane * 16);
    SCHED();
    float wt[8];
    {
      float M = -INFINITY, L = 0.f;
#pragma unroll
      for (int i = 0; i < 8; ++i) {
        unsigned z0 = f[i >> 2][4 * (i & 3)], z1 = f[i >> 2][4 * (i & 3) + 2];
        asm("" : "+s"(z0), "+s"(z1));
        wt[i] = __uint_as_float(lane < 32 ? z0 : z1); M = fmaxf(M, wt[i]);
      }
      M = xhalf_max(M);
#pragma unroll
      for (int i = 0; i < 8; ++i) { wt[i] = ex2(wt[i] - M); L += wt[i]; }
      L = xhalf_sum(L);
      if (lane == 0) *reinterpret_cast<f32x2*>(wml + 2 * w4) = f32x2{M, L};
    }
    SCHED();
    float a8[8];
#pragma unroll
    for (int k = 0; k < 8; ++k) a8[k] = 0.f;
#pragma unroll
    for (int i = 0; i < 8; ++i) {
#pragma unroll
      for (int k = 0; k < 4; ++k) { a8[2 * k] = fmaf(wt[i], bflo(pp[i][k]), a8[2 * k]); a8[2 * k + 1] = fmaf(wt[i], bfhi(pp[i][k]), a8[2 * k + 1]); }
      asm volatile("" : "+v"(a8[0]), "+v"(a8[1]), "+v"(a8[2]), "+v"(a8[3]), "+v"(a8[4]), "+v"(a8[5]), "+v"(a8[6]), "+v"(a8[7]));
    }
    STAMP(6);
    *reinterpret_cast<f32x4*>(red + w4 * 256 + 8 * (lane & 31) + 4 * (lane >> 5)) = f32x4{rs32(a8[0], a8[4]), rs32(a8[1], a8[5]), rs32(a8[2], a8[6]), rs32(a8[3], a8[7])};
    sbar4(cnt + 3, lane);
    STAMP(4);
    float acc[2][8];
#pragma unroll
    for (int bb = 0; bb < 2; ++bb) {
      u32x4 cv[2];
      const f32x4 ml = *reinterpret_cast<const f32x4*>(wml + 4 * bb);
      const float M = fmaxf(ml[0], ml[2]), e0 = ex2(ml[0] - M), e1 = ex2(ml[2] - M), rl = 1.f / fmaf(e0, ml[1], e1 * ml[3]), s0 = e0 * rl, s1 = e1 * rl;
#pragma unroll
      for (int e = 0; e < 2; ++e) {
        const float* src = red + bb * 512 + w4 * 64 + 16 * r4 + 8 * e;
        const f32x4 t0 = s0 * *reinterpret_cast<const f32x4*>(src) + s1 * *reinterpret_cast<const f32x4*>(src + 256), t1 = s0 * *reinterpret_cast<const f32x4*>(src + 4) + s1 * *reinterpret_cast<const f32x4*>(src + 260);
        cv[e] = u32x4{pk_bf(t0[0], t0[1]), pk_bf(t0[2], t0[3]), pk_bf(t1[0], t1[1]), pk_bf(t1[2], t1[3])};
      }
#pragma unroll
      for (int k = 0; k < 8; ++k) acc[bb][k] = 0.f;
#pragma unroll
      for (int ip = 0; ip < 8; ++ip)
#pragma unroll
        for (int k = 0; k < 4; ++k) { acc[bb][2 * k] = dot2(Wb[2 * ip][k], cv[ip >> 2][ip & 3], acc[bb][2 * k]); acc[bb][2 * k + 1] = dot2(Wb[2 * ip + 1][k], cv[ip >> 2][ip & 3], acc[bb][2 * k + 1]); }
    }
    {
      float v8[8], v4[4];
#pragma unroll
      for (int k = 0; k < 8; ++k) v8[k] = rs32(acc[0][k], acc[1][k]);
#pragma unroll
      for (int k = 0; k < 4; ++k) v4[k] = rs16(v8[k], v8[k + 4]);
      *reinterpret_cast<f32x4*>(red2 + (w4 * 2 + (lane >> 5)) * 128 + 8 * c16 + 4 * (r4 & 1)) = f32x4{v4[0], v4[1], v4[2], v4[3]};
    }
    {
      COMPILER_FENCE();
      const unsigned got = lds_arrive_nw(L_CNT + 16);
      f32x4 t4[4];
#pragma unroll
      for (int w = 0; w < 4; ++w) t4[w] = *reinterpret_cast<const f32x4*>(red2 + w * 256 + 4 * lane);
      SCHED();
      if (lds_done(got) == 3u) {
        const f32x4 t = (t4[0] + t4[1]) + (t4[2] + t4[3]);
        const unsigned co = WS_CX + ((h * 2 + b) * 256 + 64 * r4 + 32 * (c16 & 1) + 2 * (c16 >> 1)) * 8;
        st16c(rs, co, u32x4{__float_as_uint(t[0]), tag, __float_as_uint(t[1]), tag});
        st16c(rs, co + 128, u32x4{__float_as_uint(t[2]), tag, __float_as_uint(t[3]), tag});
      }
    }
    STAMP(12);
    STAMP_FLUSH(0); TR_FLUSH();
    return;
  }
  {
    unsigned op;
    {
      u32x4 c;
      const unsigned co = WS_CX + ((h * 2 + ((lane >> 3) & 1)) * 256 + (lane >> 5) * 128 + b * 64 + w4 * 16 + 2 * (lane & 7)) * 8;
      if (idx == 0 && w4 == 1) {
#pragma unroll 1
        for (int spin = 0; spin < SPIN_MAX; ++spin) {
          COMPILER_FENCE();
          if (__all(ld8c(rs, WS_QX + 65536 + (lane >> 1) * 2048 + (lane & 1) * 512)[1] == tag)) break;
          __builtin_amdgcn_s_sleep(8);
        }
      }
      if (w4 == 0) { shint<2048, 4>(ws + WS_CX + (h * 512 + b * 64) * 8, tag); lds_add(cnt + 7, 1u, lane); }
      else lds_wait_slow(cnt + 7, 1u);
      STAMP(3);
#pragma unroll 1
      for (int spin = 0; spin < SPIN_MAX; ++spin) {
        COMPILER_FENCE();
        c = ld16c(rs, co);
        if (__all((c[1] == tag) & (c[3] == tag))) break;
        __builtin_amdgcn_s_sleep(1);
      }
      float o0 = __uint_as_float(c[0]), o1 = __uint_as_float(c[2]);
      o0 += dpp<0x128>(o0); o1 += dpp<0x128>(o1);
      op = pk_bf(o0, o1);
    }
    STAMP(14);
    float* red3 = red + 4096;
    float a[2][8];
#pragma unroll
    for (int bb = 0; bb < 2; ++bb)
#pragma unroll
      for (int k = 0; k < 8; ++k) a[bb][k] = 0.f;
#pragma unroll
    for (int ip = 0; ip < 8; ++ip) {
      const unsigned v0 = __builtin_amdgcn_readlane(op, ip), v1 = __builtin_amdgcn_readlane(op, 32 + ip);
#pragma unroll
      for (int k = 0; k < 4; ++k) {
        a[0][2 * k] = dot2(Wb[2 * ip][k], v0, a[0][2 * k]); a[0][2 * k + 1] = dot2(Wb[2 * ip + 1][k], v0, a[0][2 * k + 1]);
        a[1][2 * k] = dot2(Wb[2 * ip][k], v1, a[1][2 * k]); a[1][2 * k + 1] = dot2(Wb[2 * ip + 1][k], v1, a[1][2 * k + 1]);
      }
    }
#pragma unroll
    for (int bb = 0; bb < 2; ++bb) {
      *reinterpret_cast<f32x4*>(red3 + (w4 * 2 + bb) * 512 + 8 * lane) = f32x4{a[bb][0], a[bb][1], a[bb][2], a[bb][3]};
      *reinterpret_cast<f32x4*>(red3 + (w4 * 2 + bb) * 512 + 8 * lane + 4) = f32x4{a[bb][4], a[bb][5], a[bb][6], a[bb][7]};
    }
    sbar4(cnt + 5, lane);
    f32x4 t = {0.f, 0.f, 0.f, 0.f};
#pragma unroll
    for (int w = 0; w < 4; ++w) t += *reinterpret_cast<const f32x4*>(red3 + w * 1024 + 4 * tid);
    st16c(rs, WS_GA + ((tid >> 7) * 64 + idx) * 2048 + (tid & 127) * 16, u32x4{pk_bf(t[0], t[1]), tag, pk_bf(t[2], t[3]), tag});
    STAMP(5);
  }
  fin(IC<1>{}, WS_GA, 0, idx == 0);
  STAMP(6);
  STAMP_FLUSH(0); TR_FLUSH();
}

extern "C" void kernel_launch(void* const* d_in, const int* in_sizes, int n_in, void* d_out, int out_size, void* d_ws, size_t ws_size, hipStream_t stream) {
  const u16* xA = (const u16*)d_in[0];
  const u16* ckv = (const u16*)d_in[1];
  const u16* w_q = (const u16*)d_in[2];
  const u16* w_uk = (const u16*)d_in[3];
  const u16* w_uv = (const u16*)d_in[4];
  const u16* w_o = (const u16*)d_in[5];
  const u16* xB = (const u16*)d_in[6];
  const u16* kc = (const u16*)d_in[7];
  const u16* vc = (const u16*)d_in[8];
  const u16* wq = (const u16*)d_in[9];
  const u16* wo = (const u16*)d_in[10];
  kmain<<<256, 512, 0, stream>>>(xA, ckv, w_q, w_uk, w_uv, w_o, xB, kc, vc, wq, wo, (char*)d_ws, (u16*)d_out);
}
```

```cpp
#include <hip/hip_runtime.h>
#include <hip/hip_bf16.h>
#include <stdint.h>

#define DI __device__ __forceinline__
typedef __attribute__((ext_vector_type(4))) unsigned u32x4;
typedef __attribute__((ext_vector_type(2))) unsigned u32x2;
typedef __attribute__((ext_vector_type(4))) float f32x4;
typedef __attribute__((ext_vector_type(2))) float f32x2;
typedef __attribute__((ext_vector_type(16))) float f32x16;
typedef __attribute__((ext_vector_type(8))) __bf16 bf16x8_t;
typedef __attribute__((ext_vector_type(2))) __bf16 bf16x2_t;
typedef __attribute__((ext_vector_type(4))) short s16x4;
typedef __attribute__((ext_vector_type(8))) short s16x8;
typedef unsigned short u16;
typedef unsigned long long u64;

constexpr int NS0 = 14, NS1 = 17, NS2 = 16, NS3 = 17;
constexpr u64 TL0 = 0xb2991b5d33cb1c1ull, TH0 = 0xf78d30ull, TL1 = 0x8e0759551349142ull, TH1 = 0x3eef7cefae7ull;
constexpr u64 TL2 = 0x96179a59238a183ull, TH2 = 0xffad71b68ull, TL3 = 0x89f718510308100ull, TH3 = 0x3ce76caeaa6ull;
constexpr int RA2 = 0xC0, RA3 = 0;
constexpr int AHL = 2;
constexpr int QPE = 5;
constexpr int WBW = 4, WB0 = 7;
#define TL 2402
#define TW 1660
constexpr unsigned TA0 = 135, TA1 = 120, TA2 = 120, TA3 = 120, STG = 12, BSG = 6, HL = 40, TP0 = TW + 170, TP2 = TW + 420, PA = 150;
#define PAR(i, v) (v)
constexpr u64 LA2 = TH2 >> (6 * (NS2 - 11)), LA3 = TH3 >> (6 * (NS3 - 11));
constexpr u64 chunk_mask(u64 lo, u64 hi, int n) { u64 m = 0; for (int i = 0; i < n; ++i) m |= 1ull << ((i < 10 ? lo >> (6 * i) : hi >> (6 * (i - 10))) & 63); return m; }
static_assert((chunk_mask(TL0, TH0, NS0) | chunk_mask(TL1, TH1, NS1) | chunk_mask(TL2, TH2, NS2) | chunk_mask(TL3, TH3, NS3)) == ~0ull && (RA2 & RA3) == 0, "every chunk once");
static_assert(NS0 + NS1 + NS2 + NS3 == 64, "split sizes");
constexpr int NSMIN = 14, NSMAX = 18;
static_assert(NS0 >= NSMIN && NS1 >= NSMIN && NS2 - 1 >= NSMIN && NS3 - 1 >= NSMIN && NS0 <= NSMAX && NS1 + 1 <= NSMAX && NS2 <= NSMAX && NS3 <= NSMAX, "range");
constexpr int SK = 4096;
constexpr float C2 = 0.08838834764831845f * 1.4426950408889634f;
constexpr u64 GOLD = 0x9E3779B97F4A7C15ull;

constexpr unsigned WS_EP = 0;
constexpr unsigned WS_QX = 4096;
constexpr unsigned WS_QA = WS_QX + 2 * 32 * 256 * 8;
constexpr unsigned WS_AML = WS_QA + 2 * 32 * 512 * 2;
constexpr unsigned WS_CX = WS_AML + 32 * 2 * 64 * 8;
constexpr unsigned WS_GZ = WS_CX + 32 * 2 * 2 * 128 * 8;
constexpr unsigned WS_T0 = WS_GZ + 256 * 128;
constexpr unsigned WS_GB = WS_T0 + 64 * 128;
constexpr unsigned WS_GA = WS_GB + 2 * 128 * 256 * 8;
constexpr unsigned WS_APO = 2u << 20;
static_assert(WS_GA + 64 * 2 * 256 * 8 <= WS_APO, "layout");
constexpr unsigned WS_END = WS_APO + 32 * 2 * 64 * 256 * 2;
#define STAMP(i) do {} while (0)
#define STAMP_DECL do {} while (0)
#define STAMP_FLUSH(grp) do {} while (0)

#define TR() do {} while (0)
#define TR_FLUSH() do {} while (0)
DI float bflo(unsigned u) { return __uint_as_float(u << 16); }
DI float bfhi(unsigned u) { return __uint_as_float(u & 0xffff0000u); }
DI unsigned pk_bf(float a, float b) { const f32x2 v = {a, b}; return __builtin_bit_cast(unsigned, __builtin_convertvector(v, bf16x2_t)); }
DI float dot2(unsigned k, unsigned q, float acc) {
  return __builtin_amdgcn_fdot2_f32_bf16(__builtin_bit_cast(bf16x2_t, k), __builtin_bit_cast(bf16x2_t, q), acc, false);
}
DI unsigned plo(unsigned w0, unsigned w1) { return __builtin_amdgcn_perm(w1, w0, 0x05040100u); }
DI unsigned phi(unsigned w0, unsigned w1) { return __builtin_amdgcn_perm(w1, w0, 0x07060302u); }
template <int CTRL> DI float dpp(float x) {
  return __builtin_bit_cast(float, __builtin_amdgcn_mov_dpp(__builtin_bit_cast(int, x), CTRL, 0xf, 0xf, true));
}
DI float row16_sum(float x) { x += dpp<0xB1>(x); x += dpp<0x4E>(x); x += dpp<0x141>(x); x += dpp<0x128>(x); return x; }
DI float row16_max(float x) { x = fmaxf(x, dpp<0xB1>(x)); x = fmaxf(x, dpp<0x4E>(x)); x = fmaxf(x, dpp<0x141>(x)); x = fmaxf(x, dpp<0x128>(x)); return x; }
DI float xhalf_max(float x) { auto t = __builtin_amdgcn_permlane32_swap(__float_as_uint(x), __float_as_uint(x), false, false); return fmaxf(__uint_as_float(t[0]), __uint_as_float(t[1])); }
DI float xhalf_sum(float x) { auto t = __builtin_amdgcn_permlane32_swap(__float_as_uint(x), __float_as_uint(x), false, false); return __uint_as_float(t[0]) + __uint_as_float(t[1]); }
DI u32x4 ld16(const void* p) { return *reinterpret_cast<const u32x4*>(p); }
DI u32x4 ld16nt(const void* p) { return __builtin_nontemporal_load(reinterpret_cast<const u32x4*>(p)); }
DI float ex2(float x) { return __builtin_amdgcn_exp2f(x); }
#define SCHED() __builtin_amdgcn_sched_barrier(0)

typedef __amdgpu_buffer_rsrc_t rsrc_t;
DI u32x4 ld16c(rsrc_t rs, unsigned off) { return __builtin_amdgcn_raw_buffer_load_b128(rs, (int)off, 0, 16); }
DI u32x2 ld8c(rsrc_t rs, unsigned off) { return __builtin_amdgcn_raw_buffer_load_b64(rs, (int)off, 0, 16); }
DI void st16c(rsrc_t rs, unsigned off, u32x4 v) { __builtin_amdgcn_raw_buffer_store_b128(v, rs, (int)off, 0, 16); }
DI void st8c(rsrc_t rs, unsigned off, u32x2 v) { __builtin_amdgcn_raw_buffer_store_b64(v, rs, (int)off, 0, 16); }
typedef __attribute__((ext_vector_type(16))) unsigned u32x16;
template <int O1> DI void sld2(const char* p, u32x2& a, u32x2& b) {
  asm volatile("s_load_dwordx2 %0, %2, 0x0 glc\n\ts_load_dwordx2 %1, %2, %3 glc\n\ts_waitcnt lgkmcnt(0)" : "=&s"(a), "=&s"(b) : "s"(p), "n"(O1) : "memory");
}
DI u32x16 sld64(const char* p) {
  u32x16 v;
  asm volatile("s_load_dwordx16 %0, %1, 0x0 glc\n\ts_waitcnt lgkmcnt(0)" : "=&s"(v) : "s"(p) : "memory");
  return v;
}
DI void sld128(const char* p, u32x16 (&v)[2]) {
  asm volatile("s_load_dwordx16 %0, %2, 0x0 glc\n\ts_load_dwordx16 %1, %2, 0x40 glc\n\ts_waitcnt lgkmcnt(0)" : "=&s"(v[0]), "=&s"(v[1]) : "s"(p) : "memory");
}
constexpr int SPIN_MAX = 1 << 20;
constexpr unsigned OOB = 0x7ffffff0u;
DI void rawbar() { __builtin_amdgcn_sched_barrier(0); __builtin_amdgcn_s_barrier(); __builtin_amdgcn_sched_barrier(0); }
#define COMPILER_FENCE() asm volatile("" ::: "memory")
template <int O1, int SL> DI void shint(const char* p, unsigned tag) {
#pragma unroll 1
  for (int spin = 0; spin < SPIN_MAX; ++spin) {
    u32x2 a, b;
    sld2<O1>(p, a, b);
    if (((a[1] ^ tag) | (b[1] ^ tag)) == 0u) break;
    __builtin_amdgcn_s_sleep(SL);
  }
}

template <int N> struct IC { static constexpr int v = N; };
template <int I, int N, class F> DI void sfor(F&& f) { if constexpr (I < N) { f(IC<I>{}); sfor<I + 1, N>(f); } }
typedef __attribute__((address_space(3))) unsigned lu32;
DI void lds_wait(unsigned* cnt, unsigned target) {
#pragma unroll 1
  for (int spin = 0; spin < SPIN_MAX; ++spin) {
    if (__builtin_amdgcn_readfirstlane(__hip_atomic_load((lu32*)cnt, __ATOMIC_RELAXED, __HIP_MEMORY_SCOPE_WORKGROUP)) >= target) break;
    __builtin_amdgcn_s_sleep(1);
  }
  COMPILER_FENCE();
}
DI void lds_wait_slow(unsigned* cnt, unsigned target) {
#pragma unroll 1
  for (int spin = 0; spin < SPIN_MAX; ++spin) {
    if (__builtin_amdgcn_readfirstlane(__hip_atomic_load((lu32*)cnt, __ATOMIC_RELAXED, __HIP_MEMORY_SCOPE_WORKGROUP)) >= target) break;
    __builtin_amdgcn_s_sleep(3);
  }
  COMPILER_FENCE();
}
DI void until(unsigned t) {
#pragma unroll 1
  for (int spin = 0; spin < 1024; ++spin) { if ((int)((unsigned)wall_clock64() - t) >= 0) break; __builtin_amdgcn_s_sleep(1); }
}
DI void lds_hold(unsigned* flag) {
#pragma unroll 1
  for (int spin = 0; spin < SPIN_MAX; ++spin) {
    if (__builtin_amdgcn_readfirstlane(__hip_atomic_load((lu32*)flag, __ATOMIC_RELAXED, __HIP_MEMORY_SCOPE_WORKGROUP)) == 0u) break;
    __builtin_amdgcn_s_sleep(2);
  }
}
DI unsigned lds_arrive_nw(unsigned addr) {
  unsigned old, one = 1u;
  asm volatile("s_mov_b64 exec, 1\n\tds_add_rtn_u32 %0, %1, %2\n\ts_mov_b64 exec, -1" : "=v"(old) : "v"(addr), "v"(one) : "memory");
  return old;
}
DI unsigned lds_done(unsigned old) { asm volatile("s_waitcnt lgkmcnt(0)" : "+v"(old) :: "memory"); return __builtin_amdgcn_readfirstlane(old); }
DI unsigned lds_arrive(unsigned* cnt, int) { return lds_done(lds_arrive_nw((unsigned)(size_t)(lu32*)cnt)); }
DI void lds_add(unsigned* cnt, unsigned v, int) {
  asm volatile("s_mov_b64 exec, 1\n\tds_add_u32 %0, %1\n\ts_mov_b64 exec, -1" :: "v"((unsigned)(size_t)(lu32*)cnt), "v"(v) : "memory");
}
DI void sbar4(unsigned* cnt, int lane) { if (lds_arrive(cnt, lane) < 3u) lds_wait(cnt, 4u); }
DI float rs32(float a, float b) { auto t = __builtin_amdgcn_permlane32_swap(__float_as_uint(a), __float_as_uint(b), false, false); return __uint_as_float(t[0]) + __uint_as_float(t[1]); }
DI float rs16(float a, float b) { auto t = __builtin_amdgcn_permlane16_swap(__float_as_uint(a), __float_as_uint(b), false, false); return __uint_as_float(t[0]) + __uint_as_float(t[1]); }
template <int N> DI unsigned rowbcast(unsigned x) { return (unsigned)__builtin_amdgcn_mov_dpp((int)x, 0x150 + N, 0xf, 0xf, true); }
DI void pin2(u32x4& a, u32x4& b) { asm volatile("" : "+v"(a), "+v"(b)); }
DI void use2(const u32x4& a, const u32x4& b) { asm volatile("" :: "v"(a), "v"(b)); }
DI bool nz8(const u32x4& v) { return ((v[0] | v[1]) != 0u) & ((v[2] | v[3]) != 0u); }

DI u32x4 qsum(const u32x4& g) {
  const auto ta = __builtin_amdgcn_permlane32_swap(g[0], g[0], false, false), tc = __builtin_amdgcn_permlane32_swap(g[2], g[2], false, false);
  const unsigned a = pk_bf(bflo(ta[0]) + bflo(ta[1]), bfhi(ta[0]) + bfhi(ta[1])), c = pk_bf(bflo(tc[0]) + bflo(tc[1]), bfhi(tc[0]) + bfhi(tc[1]));
  const auto ra = __builtin_amdgcn_permlane16_swap(a, a, false, false), rc = __builtin_amdgcn_permlane16_swap(c, c, false, false);
  return u32x4{ra[0], rc[0], ra[1], rc[1]};
}
DI unsigned toff(unsigned row, unsigned ch) { return 256u * row + 16u * (ch ^ (((row & 3) << 2) | ((row >> 2) & 3))); }
constexpr int SMEM = 160 * 1024;
constexpr int L_SCR = 131072;
constexpr int L_MRG = 65536;
constexpr int L_CNT = L_SCR + 32 * 1008;

struct KV { u32x4 r[8]; };

__global__ __launch_bounds__(512, 2) void kmain(const u16* __restrict__ xA, const u16* __restrict__ ckv, const u16* __restrict__ w_q, const u16* __restrict__ w_uk,
                                                const u16* __restrict__ w_uv, const u16* __restrict__ w_o,
                                                const u16* __restrict__ xB, const u16* __restrict__ kc, const u16* __restrict__ vc,
                                                const u16* __restrict__ wq, const u16* __restrict__ wo,
                                                char* __restrict__ ws, u16* __restrict__ out) {
  const unsigned tstart = (unsigned)wall_clock64();
  SCHED();
  __shared__ __attribute__((aligned(16))) char smem[SMEM];
  const int tid = threadIdx.x, blk = blockIdx.x, lane = tid & 63;
  const int wave = __builtin_amdgcn_readfirstlane(tid >> 6), w4 = wave & 3;
  const int cls = (blk >> 3) & 3;
  int idx = 8 * (blk & 7) + (blk >> 5);
  if (wave < 4) idx = (idx & ~7) | ((idx + (cls == 3 ? 2 : (cls == 2 ? 4 : 0))) & 7);
  const int h = idx >> 1, b = idx & 1;
  const rsrc_t rs = __builtin_amdgcn_make_buffer_rsrc(ws, 0, (int)WS_END, 0x00020000);
  const int r4 = lane >> 4, c16 = lane & 15;
  unsigned* cnt = reinterpret_cast<unsigned*>(smem + L_CNT);
  STAMP_DECL;
  STAMP(0);
  asm volatile("" :: "s"(xA), "s"(ckv), "s"(w_q), "s"(w_uk), "s"(w_uv), "s"(w_o), "s"(xB), "s"(kc), "s"(vc), "s"(wq), "s"(wo), "s"(ws), "s"(out), "s"(tstart));
  unsigned tag, tlow;
  u32x2 epw;
  auto take_epoch = [&]() {
    asm volatile("" : "+v"(epw));
    const u64 t64 = (((u64)(unsigned)__builtin_amdgcn_readfirstlane(epw[1]) << 32) | (unsigned)__builtin_amdgcn_readfirstlane(epw[0])) + GOLD;
    tag = (unsigned)(t64 >> 32); tlow = (unsigned)t64;
    asm volatile("s_waitcnt lgkmcnt(0)" : "+s"(tag), "+s"(tlow) :: "memory");
  };
  if (wave >= 4) {
    if (tid - 256 < 16) cnt[tid - 256] = 0u;
    asm volatile("s_waitcnt lgkmcnt(0)" ::: "memory");
    const unsigned pTL = PAR(0, TL), pTA = PAR(1 + cls, cls == 0 ? TA0 : (cls == 1 ? TA1 : (cls == 2 ? TA2 : TA3))), pSTG = PAR(9, STG), pBSG = PAR(10, BSG);
    const unsigned pRA2 = PAR(11, RA2), pRA3 = PAR(12, RA3), pTP = PAR(15 + cls, cls == 0 ? TP0 : TP2), pPA = PAR(19 + cls, (cls & 1) ? 0u : PA);
    rawbar();
    const int e2 = (pRA2 >> (idx & 7)) & 1, e3 = (pRA3 >> (idx & 7)) & 1;
    const int ns = cls == 0 ? NS0 : (cls == 1 ? NS1 + e2 + e3 : (cls == 2 ? NS2 - e2 : NS3 - e3));
    const size_t kvo = ((size_t)(b * 32 + h) * SK + w4 * 16) * 256;
    const char* kb = reinterpret_cast<const char*>(kc) + kvo;
    const char* vb = reinterpret_cast<const char*>(vc) + kvo;
    const unsigned lo = lane * 16u;
    u64 tlo = cls == 0 ? TL0 : (cls == 1 ? TL1 : (cls == 2 ? TL2 : TL3));
    u64 thi = cls == 0 ? TH0 : (cls == 1 ? TH1 | ((e2 ? LA2 : LA3) << (6 * (NS1 - 10))) : (cls == 2 ? TH2 : TH3));
    const u64 tlo0 = tlo, thi0 = thi;
    int hn = 0;
    const char* hb = kb;
    const char* pc[2];
    auto next2 = [&]() {
#pragma unroll
      for (int i = 0; i < 2; ++i) {
        if (hn == ns) { tlo = tlo0; thi = thi0; hb = vb; }
        ++hn;
        pc[i] = hb + ((((unsigned)tlo + (unsigned)idx) & 63u) << 14);
        tlo = (tlo >> 6) | ((thi & 63u) << 54); thi >>= 6;
      }
    };
    KV A, B;
    u32x4 W[8];
    u32x4 T[4][8];
    auto ldp = [&](KV& S, auto P) {
      constexpr int p = P.v;
      const char* q = pc[p >> 1] + lo + (p & 1) * 2048;
      S.r[2 * p] = ld16nt(q); S.r[2 * p + 1] = ld16nt(q + 1024);
    };
    auto landed = [&](KV& S, auto P) { pin2(S.r[2 * P.v], S.r[2 * P.v + 1]); };
    KV X;
    const unsigned qo = WS_QX + ((32 + h) * 2 + b) * 1024 + lane * 16;
    u32x4 g0;
    {
      unsigned tk = 0u;
      if (tid == 256) tk = __builtin_nontemporal_load(reinterpret_cast<const unsigned*>(kb + cls * 128));
      lds_wait_slow(cnt + 14, 4u);
      tag = __builtin_amdgcn_readfirstlane(cnt[16]);
      asm volatile("" :: "v"(tk));
    }
    STAMP(10);
    sfor<0, 12>([&](auto I) {
      constexpr int i = I.v, j = i - 4;
      if constexpr ((i & 3) == 0) next2();
      if (i == 4 && w4 == 3) {
#pragma unroll 1
        for (int spin = 0; spin < 2; ++spin) {
          u32x2 t[8];
          asm volatile("s_load_dwordx2 %0, %8, 0x0 glc\n\ts_load_dwordx2 %1, %8, 0x400 glc\n\ts_load_dwordx2 %2, %8, 0x800 glc\n\ts_load_dwordx2 %3, %8, 0xc00 glc\n\t"
                       "s_load_dwordx2 %4, %8, 0x1000 glc\n\ts_load_dwordx2 %5, %8, 0x1400 glc\n\ts_load_dwordx2 %6, %8, 0x1800 glc\n\ts_load_dwordx2 %7, %8, 0x1c00 glc\n\ts_waitcnt lgkmcnt(0)"
                       : "=&s"(t[0]), "=&s"(t[1]), "=&s"(t[2]), "=&s"(t[3]), "=&s"(t[4]), "=&s"(t[5]), "=&s"(t[6]), "=&s"(t[7]) : "s"(ws + WS_T0 + ((blk >> 3) & 7) * 128) : "memory");
          unsigned e = 0u; int sum = 0;
#pragma unroll
          for (int k = 0; k < 8; ++k) { e |= t[k][1] ^ tag; sum += (int)(t[k][0] - tstart); }
          if (e == 0u) { lds_add(cnt + 13, (unsigned)min(sum >> 3, 100), lane); break; }
        }
      }
      if constexpr (j >= 0) { if constexpr (j < 4) landed(X, IC<j & 3>{}); else landed(B, IC<j & 3>{}); }
      if constexpr (j >= 0 && (j & 1)) TR();
      if constexpr (i == QPE) g0 = ld16c(rs, qo);
      if constexpr (i < 4) ldp(X, IC<i & 3>{}); else if constexpr (i < 8) ldp(B, IC<i & 3>{}); else ldp(A, IC<i & 3>{});
      SCHED();
    });
    const unsigned torg = tstart + __builtin_amdgcn_readfirstlane(cnt[13]);
    const char* wp = reinterpret_cast<const char*>(wo) + ((size_t)h * 128 + r4 * 8) * 1024 + w4 * 256 + c16 * 16;
    u32x4 qpk;
    unsigned tvk;
    {
      unsigned tv = 0u;
      if (tid == 256) tv = __builtin_nontemporal_load(reinterpret_cast<const unsigned*>(vb + cls * 128));
      if (!__all((g0[1] == tag) & (g0[3] == tag))) {
#pragma unroll 1
        for (int spin = 0; spin < SPIN_MAX; ++spin) {
          COMPILER_FENCE();
          g0 = ld16c(rs, qo);
          if (__all((g0[1] == tag) & (g0[3] == tag))) break;
        }
      }
      qpk = qsum(g0);
      tvk = tv;
    }
    STAMP(7);
    const bool holds = cls == 0;
    float m = 0.f, l = 0.f, acc[8];
    unsigned sc[5];
#pragma unroll
    for (int k = 0; k < 8; ++k) acc[k] = 0.f;
#pragma unroll
    for (int k = 0; k < 5; ++k) sc[k] = 0xff800000u;
    int vn = 0;
    unsigned due = torg + pTL - (unsigned)(ns - 5) * pTA - (3 - w4) * pSTG - ((idx * 5 + cls * 3) & 15) * pBSG;
    unsigned rwo;
    const unsigned zo = WS_GZ + (b * 32 + h) * 512;
    u32x2 zs[4];
    float wr_;
    auto zreq = [&]() {
      asm volatile("s_load_dwordx2 %0, %4, 0x0 glc\n\ts_load_dwordx2 %1, %4, 0x80 glc\n\ts_load_dwordx2 %2, %4, 0x100 glc\n\ts_load_dwordx2 %3, %4, 0x180 glc"
                   : "=&s"(zs[0]), "=&s"(zs[1]), "=&s"(zs[2]), "=&s"(zs[3]) : "s"(ws + zo) : "memory");
    };
    auto zget = [&]() {
#pragma unroll 1
      for (int spin = 0; spin < SPIN_MAX; ++spin) {
        asm volatile("s_waitcnt lgkmcnt(0)" : "+s"(zs[0]), "+s"(zs[1]), "+s"(zs[2]), "+s"(zs[3]) :: "memory");
        if (((zs[0][1] ^ tag) | (zs[1][1] ^ tag) | (zs[2][1] ^ tag) | (zs[3][1] ^ tag)) == 0u) break;
        __builtin_amdgcn_s_sleep(2);
        zreq();
      }
      asm volatile("s_waitcnt lgkmcnt(0)" : "+s"(zs[0]), "+s"(zs[1]), "+s"(zs[2]), "+s"(zs[3]) :: "memory");
      const float z0 = __uint_as_float(zs[0][0]), z1 = __uint_as_float(zs[1][0]), z2 = __uint_as_float(zs[2][0]), z3 = __uint_as_float(zs[3][0]);
      const float Z = fmaxf(fmaxf(z0, z1), fmaxf(z2, z3));
      wr_ = ex2(fmaf(m, C2, -Z)) * __builtin_amdgcn_rcpf((ex2(z0 - Z) + ex2(z1 - Z)) + (ex2(z2 - Z) + ex2(z3 - Z)));
      asm volatile("" : "+v"(wr_));
    };
    auto qk = [&](const u32x4* K, auto I) {
      float x[4];
#pragma unroll
      for (int t = 0; t < 4; ++t) {
        x[t] = 0.f;
#pragma unroll
        for (int k = 0; k < 4; ++k) x[t] = dot2(K[t][k], qpk[k], x[t]);
      }
      const float u0 = (c16 & 1 ? x[1] : x[0]) + dpp<0xB1>(c16 & 1 ? x[0] : x[1]), u1 = (c16 & 1 ? x[3] : x[2]) + dpp<0xB1>(c16 & 1 ? x[2] : x[3]);
      float v = (c16 & 2 ? u1 : u0) + dpp<0x4E>(c16 & 2 ? u0 : u1);
      v += dpp<0x124>(v); v += dpp<0x128>(v);
      sc[I.v >> 2] = __builtin_amdgcn_update_dpp(sc[I.v >> 2], __float_as_uint(v), 0xE4, 0xf, 1 << (I.v & 3), false);
      asm volatile("" : "+v"(sc[I.v >> 2]));
    };
    auto soft = [&]() {
      m = row16_max(fmaxf(fmaxf(fmaxf(__uint_as_float(sc[0]), __uint_as_float(sc[1])), fmaxf(__uint_as_float(sc[2]), __uint_as_float(sc[3]))), __uint_as_float(sc[4])));
      const float mc = m * C2;
      l = 0.f;
#pragma unroll
      for (int k = 0; k < 5; ++k) {
        const float p = ex2(fmaf(__uint_as_float(sc[k]), C2, -mc));
        sc[k] = pk_bf(p, dpp<0xB1>(p));
        l = dot2(sc[k], 0x3f003f00u, l);
      }
      l = row16_sum(l);
    };
    auto pv = [&](const u32x4* V) {
      const unsigned p01 = rowbcast<0>(sc[0]), p23 = rowbcast<2>(sc[0]);
#pragma unroll
      for (int k = 0; k < 4; ++k) { acc[2 * k] = dot2(plo(V[0][k], V[1][k]), p01, acc[2 * k]); acc[2 * k + 1] = dot2(phi(V[0][k], V[1][k]), p01, acc[2 * k + 1]); }
#pragma unroll
      for (int k = 0; k < 4; ++k) { acc[2 * k] = dot2(plo(V[2][k], V[3][k]), p23, acc[2 * k]); acc[2 * k + 1] = dot2(phi(V[2][k], V[3][k]), p23, acc[2 * k + 1]); }
      sc[0] = (unsigned)__builtin_amdgcn_mov_dpp((int)sc[0], 0x12C, 0xf, 0xf, true);
      if ((++vn & 3) == 0) { sc[0] = sc[1]; sc[1] = sc[2]; sc[2] = sc[3]; sc[3] = sc[4]; }
      asm volatile("" : "+v"(acc[0]), "+v"(acc[1]), "+v"(acc[2]), "+v"(acc[3]), "+v"(acc[4]), "+v"(acc[5]), "+v"(acc[6]), "+v"(acc[7]));
    };
    auto step = [&](KV& S, KV& N, auto SI, auto REFILL, auto WR) {
      constexpr bool refill = REFILL.v == 1;
      constexpr int wr = WR.v;
      if constexpr (refill) next2();
      if constexpr (refill && SI.v >= 3) { until((int)(due - torg) < (int)pTP ? due - pPA : due); due += pTA; }
      constexpr int ah = wr < 0 ? AHL : 0;
      auto point = [&](auto P) {
        if constexpr (refill) {
          if constexpr (P.v == 0 && ah > 0) { if constexpr (wr >= 0) { if (holds) lds_hold(cnt + 8); } sfor<0, ah>([&](auto Q) { ldp(N, Q); }); SCHED(); }
          landed(S, P);
          if constexpr (P.v & 1) TR();
          if constexpr (P.v + ah < 4) { if constexpr ((P.v & 1) == 0 && wr >= 0) { if (holds) lds_hold(cnt + 8); } ldp(N, IC<(P.v + ah) & 3>{}); }
          if constexpr (wr >= 0 && wr < 8) W[(4 * wr + P.v) & 7] = ld16(wp + (((4 * wr + P.v) >> 3) * 32 + ((4 * wr + P.v) & 7)) * 1024);
          SCHED();
        }
      };
      sfor<0, 2>([&](auto H) {
        constexpr int i = 2 * SI.v + H.v;
        if constexpr (REFILL.v == 2 && H.v == 0) { zreq(); SCHED(); }
        point(IC<2 * H.v>{});
        point(IC<2 * H.v + 1>{});
        if constexpr (i < NSMIN) qk(S.r + 4 * H.v, IC<i>{});
        else if constexpr (i > NSMAX) pv(S.r + 4 * H.v);
        else {
          if (i < ns) { if constexpr (i < NSMAX) qk(S.r + 4 * H.v, IC<i>{}); }
          else { if (i == ns) soft(); pv(S.r + 4 * H.v); }
        }
        SCHED();
      });
      if constexpr (REFILL.v == 2) {
        zget();
        STAMP(1);
        *reinterpret_cast<u32x4*>(smem + rwo) = u32x4{pk_bf(acc[0] * wr_, acc[1] * wr_), pk_bf(acc[2] * wr_, acc[3] * wr_), pk_bf(acc[4] * wr_, acc[5] * wr_), pk_bf(acc[6] * wr_, acc[7] * wr_)};
        COMPILER_FENCE();
      }
      if constexpr (wr >= 1) {
        constexpr int t = (wr - 1) >> 1, j0 = ((wr - 1) & 1) * 2;
#pragma unroll
        for (int jj = 0; jj < 2; ++jj)
#pragma unroll
          for (int e = 0; e < 4; ++e) {
            const unsigned a_ = W[(4 * wr - 4 + 2 * jj) & 7][e], b_ = W[(4 * wr - 3 + 2 * jj) & 7][e];
            T[t][2 * e][j0 + jj] = plo(a_, b_); T[t][2 * e + 1][j0 + jj] = phi(a_, b_);
          }
#pragma unroll
        for (int e = 0; e < 8; e += 2) pin2(T[t][e], T[t][e + 1]);
      }
      SCHED();
    };
    step(B, B, IC<1>{}, IC<0>{}, IC<-1>{});
    step(A, B, IC<2>{}, IC<1>{}, IC<0>{});
    asm volatile("" :: "v"(tvk));
    step(X, X, IC<0>{}, IC<0>{}, IC<-1>{});
    sfor<0, 4>([&](auto I) {
      step(B, A, IC<2 * I.v + 3>{}, IC<1>{}, IC<2 * I.v + 1>{});
      if constexpr (I.v == 3) {
        float* ml = reinterpret_cast<float*>(cnt + 32);
        ml[(c16 & 1) * 16 + w4 * 4 + r4] = (c16 & 1) ? l : m;
        COMPILER_FENCE();
        const unsigned got = lds_arrive_nw(L_CNT + 44);
        const float mr = ml[c16], lr = ml[16 + c16];
        SCHED();
        if (lds_done(got) == 3u) {
          const float M = row16_max(mr);
          const unsigned z = __float_as_uint(fmaf(M, C2, __builtin_amdgcn_logf(row16_sum(ex2((mr - M) * C2) * lr))));
          asm volatile("buffer_store_dwordx4 %0, %1, %2, 0 offen sc1\n\ts_nop 2" :: "v"(u32x4{z, tag, z, tag}), "v"(lane < 8 ? zo + cls * 128 + lane * 16 : OOB), "s"(rs) : "memory");
        }
        SCHED();
      }
      step(A, B, IC<2 * I.v + 4>{}, IC<1>{}, IC<2 * I.v + 2>{});
    });
    unsigned yoff = WS_GB + ((b * 128 + h * 4 + cls) * 256 + 64 * w4 + lane) * 8, rro = L_MRG + c16 * 272 + r4 * 16;
    rwo = L_MRG + (w4 * 4 + r4) * 272 + c16 * 16;
    asm volatile("" : "+v"(yoff), "+v"(rwo), "+v"(rro));
    if (cls == 0) lds_wait(cnt + 1, 4u);
    const int nit = (ns - 12) >> 1;
#pragma unroll 1
    for (int i = 0; i < nit; ++i) {
      step(B, A, IC<11>{}, IC<1>{}, IC<-1>{});
      step(A, B, IC<11>{}, IC<1>{}, IC<-1>{});
    }
    if (ns & 1) { step(B, A, IC<11>{}, IC<1>{}, IC<-1>{}); step(A, A, IC<11>{}, IC<2>{}, IC<-1>{}); }
    else step(B, B, IC<11>{}, IC<2>{}, IC<-1>{});
    {
      u32x4 af[4];
      unsigned got = lds_arrive_nw(L_CNT), need = 3u;
#pragma unroll 1
      for (int spin = 0; spin < SPIN_MAX; ++spin) {
#pragma unroll
        for (int t = 0; t < 4; ++t) af[t] = *reinterpret_cast<const u32x4*>(smem + rro + t * 64);
        SCHED();
        if (lds_done(got) >= need) break;
        need = 4u;
        __builtin_amdgcn_s_sleep(1);
        asm volatile("ds_read_b32 %0, %1" : "=v"(got) : "v"((unsigned)L_CNT) : "memory");
      }
      SCHED();
      f32x4 d[8];
      float a[8], v4[4];
      sfor<0, 5>([&](auto I) {
        constexpr int i = I.v;
        sfor<0, 8>([&](auto J) {
          constexpr int e = i + 4 * (J.v & 1), t = J.v >> 1;
          if constexpr (i < 4) { d[e] = __builtin_amdgcn_mfma_f32_16x16x32_bf16(__builtin_bit_cast(bf16x8_t, af[t]), __builtin_bit_cast(bf16x8_t, T[t][e]), t ? d[e] : f32x4{0.f, 0.f, 0.f, 0.f}, 0, 0, 0); SCHED(); }
          if constexpr (i >= 1 && (J.v == 4 || J.v == 5)) { const f32x2 u = f32x2{d[e - 1][0], d[e - 1][1]} + f32x2{d[e - 1][2], d[e - 1][3]}; a[e - 1] = u[0] + u[1]; asm volatile("" : "+v"(a[e - 1])); }
          if constexpr (i >= 1 && J.v == 6) v4[i - 1] = rs32(a[i - 1], a[i + 3]);
          if constexpr (i < 4) SCHED();
        });
      });
      st8c(rs, yoff, u32x2{pk_bf(rs16(v4[0], v4[2]), rs16(v4[1], v4[3])), tag});
    }
    STAMP(2);
    STAMP_FLUSH(1);
    TR_FLUSH();
    return;
  }
  const rsrc_t ro = __builtin_amdgcn_make_buffer_rsrc(out, 0, 4096, 0x00020000);
  auto fin = [&](auto NL, unsigned base, unsigned col, bool bump) {
    constexpr int nl = NL.v;
    unsigned doff = base + (((idx >> 5) * 4 + w4) * 16 * nl + (lane >> 2)) * 2048 + (idx & 31) * 64 + (lane & 3) * 16;
    unsigned fwo = 32768 + (c16 < 4 ? (w4 * 16 + 4 * c16 + r4) * 4 : 256 + lane * 4);
    unsigned fro = 32768 + (lane & 7) * 8, foo = lane < 8 ? ((idx >> 5) * 1024 + col + (nl == 2 ? 16 * (idx & 24) + 64 * (idx & 1) + (idx & 6) + 8 * lane : 16 * (idx & 31) + 2 * lane)) * 2 : OOB;
    asm volatile("" : "+v"(doff), "+v"(fwo), "+v"(fro), "+v"(foo));
    lds_wait(cnt + 0, 4u);
    STAMP(4);
    u32x4 d[nl];
    unsigned eh = 1u;
#pragma unroll 1
    for (int spin = SPIN_MAX; spin > 0; --spin) {
      COMPILER_FENCE();
      if (eh != 0u) {
#pragma unroll
        for (int j = 0; j < nl; ++j) d[j] = ld16c(rs, doff + j * 32768);
        eh = 0u;
#pragma unroll
        for (int j = 0; j < nl; ++j) { eh |= (d[j][1] ^ tag) | (d[j][3] ^ tag); asm volatile("" : "+v"(eh)); }
      }
      if (__builtin_amdgcn_ballot_w64(eh != 0u) == 0ull) break;
    }
    STAMP(11);
    float y[4] = {bflo(d[0][0]), bfhi(d[0][0]), bflo(d[0][2]), bfhi(d[0][2])};
    if constexpr (nl == 2) { y[0] += bflo(d[1][0]); y[1] += bfhi(d[1][0]); y[2] += bflo(d[1][2]); y[3] += bfhi(d[1][2]); }
    float t = rs16(rs32(y[0], y[2]), rs32(y[1], y[3]));
    t += dpp<0x124>(t); t += dpp<0x128>(t);
    *reinterpret_cast<float*>(smem + fwo) = t;
    COMPILER_FENCE();
    const unsigned got = lds_arrive_nw(L_CNT + 24);
    f32x2 t2[4];
#pragma unroll
    for (int w = 0; w < 4; ++w) t2[w] = *reinterpret_cast<const f32x2*>(smem + fro + w * 64);
    SCHED();
    if (lds_done(got) == 3u) {
      const f32x2 r = (t2[0] + t2[1]) + (t2[2] + t2[3]);
      __builtin_amdgcn_raw_buffer_store_b32(pk_bf(r[0], r[1]), ro, (int)foo, 0, 0);
      if (bump) st8c(rs, lane == 0 ? WS_EP : OOB, u32x2{tlow, tag});
    }
  };
  epw = ld8c(rs, WS_EP);
  SCHED();
  u32x4 Wa[16], Wb[16], xv[4];
  const u16* pb;
  const unsigned qa_off = WS_QA + (((c16 >> 1) & 1) * 32 + h) * 1024 + (b * 256 + w4 * 64 + 16 * r4 + 8 * (c16 & 1)) * 2;
  {
    const u16* pa; size_t sa;
    if (cls) {
      const size_t o_ = ((size_t)(b * 256 + w4 * 64 + 16 * r4) * 32 + h) * 128 + 8 * c16;
      pa = (cls == 1 ? w_q : (cls == 2 ? w_uk : wq)) + o_; sa = 4096;
      pb = cls == 2 ? w_uv + o_ : (cls == 1 ? w_o : wo) + ((size_t)h * 128 + b * 64 + w4 * 2) * 512 + 8 * lane;
      if (cls == 1 && c16 < 4) st16c(rs, qa_off, u32x4{0u, 0u, 0u, 0u});
    } else { pa = ckv + ((size_t)idx * 128 + w4) * 512 + 8 * lane; sa = 2048; pb = pa + 64 * 512; }
    const u16* xp = (cls == 1 ? xA : xB) + b * 256 + w4 * 64 + 16 * r4;
    SCHED();
    sfor<0, 4>([&](auto J) { xv[J.v] = ld16(xp + (J.v >> 1) * 512 + 8 * (J.v & 1)); });
    SCHED();
    sfor<0, 16>([&](auto I) { Wa[I.v] = ld16nt(pa + I.v * sa); });
    SCHED();
    rawbar();
    take_epoch();
    cnt[16] = tag;
    COMPILER_FENCE();
    if (blk < 8 && w4 == 0) asm volatile("buffer_store_dwordx4 %0, %1, %2, 0 offen sc1\n\ts_nop 2" :: "v"(u32x4{tstart, tag, tstart, tag}), "v"(WS_T0 + blk * 1024 + lane * 16), "s"(rs) : "memory");
  }
  STAMP(13);
  float* red = reinterpret_cast<float*>(smem);
  if (cls & 1) {
    float acc[2][8];
#pragma unroll
    for (int bb = 0; bb < 2; ++bb)
#pragma unroll
      for (int k = 0; k < 8; ++k) acc[bb][k] = 0.f;
    sfor<0, 8>([&](auto IP) {
      constexpr int ip = IP.v, j = ip - (8 - WBW);
      use2(Wa[2 * ip], Wa[2 * ip + 1]);
      if constexpr (ip & 1) TR();
      if constexpr (j >= 0) { Wb[2 * j] = ld16(pb + j * 4096); Wb[2 * j + 1] = ld16(pb + j * 4096 + 512); }
      SCHED();
      const unsigned xp0 = xv[ip >> 2][ip & 3], xp1 = xv[2 + (ip >> 2)][ip & 3];
#pragma unroll
      for (int k = 0; k < 4; ++k) {
        const unsigned lo = plo(Wa[2 * ip][k], Wa[2 * ip + 1][k]), hi = phi(Wa[2 * ip][k], Wa[2 * ip + 1][k]);
        acc[0][2 * k] = dot2(lo, xp0, acc[0][2 * k]); acc[0][2 * k + 1] = dot2(hi, xp0, acc[0][2 * k + 1]);
        acc[1][2 * k] = dot2(lo, xp1, acc[1][2 * k]); acc[1][2 * k + 1] = dot2(hi, xp1, acc[1][2 * k + 1]);
      }
      SCHED();
    });
    {
      float v8[8], v4[4];
#pragma unroll
      for (int k = 0; k < 8; ++k) v8[k] = rs32(acc[0][k], acc[1][k]);
#pragma unroll
      for (int k = 0; k < 4; ++k) v4[k] = rs16(v8[k], v8[k + 4]);
      *reinterpret_cast<f32x4*>(red + (w4 * 2 + (lane >> 5)) * 128 + 8 * c16 + 4 * (r4 & 1)) = f32x4{v4[0], v4[1], v4[2], v4[3]};
    }
    COMPILER_FENCE();
    const unsigned got = lds_arrive_nw(L_CNT + 8);
    f32x4 t4[4];
#pragma unroll
    for (int w = 0; w < 4; ++w) t4[w] = *reinterpret_cast<const f32x4*>(red + (w * 2 + (lane >> 5)) * 128 + 8 * c16 + 4 * (r4 & 1));
    SCHED();
    if (lds_done(got) == 3u) {
      const f32x4 t = (t4[0] + t4[1]) + (t4[2] + t4[3]);
      st16c(rs, WS_QX + (((cls >> 1) * 32 + h) * 2 + (lane >> 5)) * 1024 + b * 512 + (lane & 31) * 16, u32x4{pk_bf(t[0], t[1]), tag, pk_bf(t[2], t[3]), tag});
      STAMP(15);
      SCHED();
      lds_add(cnt + 2, 1u, lane);
    } else lds_wait(cnt + 2, 5u);
    sfor<WBW, 8>([&](auto J) {
      constexpr int j = J.v;
      use2(Wb[2 * (j - WBW)], Wb[2 * (j - WBW) + 1]);
      if constexpr ((j - WBW) & 1) TR();
      Wb[2 * j] = ld16(pb + j * 4096); Wb[2 * j + 1] = ld16(pb + j * 4096 + 512);
      if constexpr (j == 7) lds_add(cnt + 14, 1u, lane);
      SCHED();
    });
    {
      const unsigned tw = __builtin_nontemporal_load(reinterpret_cast<const unsigned*>(out + (idx >> 5) * 1024 + (cls >> 1) * 512 + 16 * (idx & 31)));
      sfor<8 - WBW, 8>([&](auto J) { use2(Wb[2 * J.v], Wb[2 * J.v + 1]); if constexpr (J.v & 1) TR(); });
      asm volatile("" :: "v"(tw));
    }
    STAMP(8);
  } else if (cls == 2) {
    sfor<0, 16 - WBW>([&](auto IP) {
      constexpr int ip = IP.v, j = ip - (8 - WBW);
      if constexpr (ip < 8) use2(Wa[2 * ip], Wa[2 * ip + 1]); else use2(Wb[2 * ip - 16], Wb[2 * ip - 15]);
      if constexpr (ip & 1) TR();
      if constexpr (j >= 0) { Wb[2 * j] = ld16nt(pb + j * 8192); Wb[2 * j + 1] = ld16nt(pb + j * 8192 + 4096); }
      SCHED();
    });
    u32x4 g0, g1;
    const unsigned qo = WS_QX + h * 2048 + lane * 16;
    shint<512, 1>(ws + WS_QX + h * 2048, tag);
    STAMP(5);
#pragma unroll 1
    for (int spin = 0; spin < SPIN_MAX; ++spin) {
      COMPILER_FENCE();
      g0 = ld16c(rs, qo); g1 = ld16c(rs, qo + 1024);
      SCHED();
      lds_add(cnt + 14, spin == 0, lane);
      SCHED();
      if (__all((g0[1] == tag) & (g0[3] == tag) & (g1[1] == tag) & (g1[3] == tag))) break;
      __builtin_amdgcn_s_sleep(1);
    }
    sfor<8 - WBW, 8>([&](auto J) { if constexpr (J.v & 1) TR(); });
    const u32x4 q0 = qsum(g0), q1 = qsum(g1);
    STAMP(9);
    float v[8];
    {
      float u[16];
#pragma unroll
      for (int i = 0; i < 16; ++i) {
        float a = 0.f, c = 0.f;
#pragma unroll
        for (int k = 0; k < 4; ++k) { a = dot2(Wa[i][k], q0[k], a); c = dot2(Wa[i][k], q1[k], c); }
        u[i] = (c16 & 2 ? c : a) + dpp<0x4E>(c16 & 2 ? a : c);
        asm volatile("" : "+v"(u[i]));
      }
#pragma unroll
      for (int i = 0; i < 8; ++i) {
        float t = (c16 & 1 ? u[i + 8] : u[i]) + dpp<0xB1>(c16 & 1 ? u[i] : u[i + 8]);
        asm volatile("" : "+v"(t));
        t += dpp<0x124>(t);
        asm volatile("" : "+v"(t));
        t += dpp<0x128>(t);
        asm volatile("" : "+v"(t));
        v[i] = t;
      }
    }
    u32x4 o = {pk_bf(v[0], v[1]), pk_bf(v[2], v[3]), pk_bf(v[4], v[5]), pk_bf(v[6], v[7])};
    if ((o[0] | o[1]) == 0u) o[0] = 0x8000u;
    if ((o[2] | o[3]) == 0u) o[2] = 0x8000u;
    if (c16 < 4) st16c(rs, qa_off, o);
    STAMP(10);
  }
  asm volatile("" :: "v"(xv[0]), "v"(xv[1]), "v"(xv[2]), "v"(xv[3]));
  SCHED();
  if (cls == 3) {
    fin(IC<2>{}, WS_GB, 512, false);
    STAMP(6);
    STAMP_FLUSH(0); TR_FLUSH();
    return;
  }
  if (cls == 0) {
    char* scr = smem + L_SCR;
    const int ab = idx >> 5, r = lane & 31, hh = lane >> 5;
    const unsigned fo = WS_QX + (w4 * 8 + (c16 >> 1)) * 2048 + (c16 & 1) * 512 + r4 * 8;
    u32x2 fl;
    u32x4 g[9];
    const unsigned o8 = WS_QA + (ab * 32 + w4 * 8) * 1024 + lane * 16, ol = WS_QA + (ab * 32 + r) * 1024 + 992 + 16 * hh;
#pragma unroll
    for (int i = 0; i < 32; ++i) {
      if (i + WB0 >= 16 && i + WB0 < 32) Wb[(i + WB0) & 15] = ld16nt(pb + ((i + WB0) & 15) * 2048);
      if (i + WB0 == 31) lds_add(cnt + 14, 1u, lane);
      if (i == 30) fl = ld8c(rs, fo);
      SCHED();
      *reinterpret_cast<u32x4*>(smem + ((i >> 3) * 4 + r4) * 8192 + toff(4 * (i & 7) + w4, c16)) = i < 16 ? Wa[i & 15] : Wb[i & 15];
      if ((i & 3) == 3) TR();
      SCHED();
    }
    STAMP(8);
    u32x4 qlast;
    {
      if (!__all(fl[1] == tag)) {
#pragma unroll 1
        for (int spin = 0; spin < SPIN_MAX; ++spin) {
          COMPILER_FENCE();
          if (__all(ld8c(rs, fo)[1] == tag)) break;
        }
      }
      {
        const char* hp = ws + WS_QA + ab * 32768 + w4 * 8192 + ((idx + w4 * 11) & 31) * 128;
#pragma unroll 1
        for (int spin = 0; spin < SPIN_MAX; ++spin) {
          u32x2 a, c;
          sld2<4096>(hp, a, c);
          if ((a[0] | a[1]) != 0u && (c[0] | c[1]) != 0u) break;
          __builtin_amdgcn_s_sleep(2);
        }
      }
      STAMP(5);
      sbar4(cnt + 7, lane);
      STAMP(12);
      unsigned pend = 0x1ffu;
#pragma unroll 1
      for (int spin = 0; spin < SPIN_MAX && pend; ++spin) {
        COMPILER_FENCE();
        u32x4 t[9];
#pragma unroll
        for (int i = 0; i < 9; ++i) t[i] = ld16c(rs, (pend >> i & 1) ? (i < 8 ? o8 + i * 1024 : ol) : OOB);
#pragma unroll
        for (int i = 0; i < 9; ++i) {
          if (pend >> i & 1) g[i] = t[i];
          if (__all(nz8(t[i]))) pend &= ~(1u << i);
        }
      }
      STAMP(9);
      qlast = g[8];
#pragma unroll
      for (int i = 0; i < 8; ++i)
        if (lane < 62) *reinterpret_cast<u32x4*>(scr + (w4 * 8 + i) * 1008 + 16 * lane) = g[i];
    }
    sbar4(cnt + 3, lane);
    STAMP(10);
    f32x16 sacc;
#pragma unroll
    for (int i = 0; i < 16; ++i) sacc[i] = 0.f;
#pragma unroll
    for (int jj = 0; jj < 32; ++jj) {
      const u32x4 af = *reinterpret_cast<const u32x4*>(smem + (w4 * 4 + (jj >> 3)) * 8192 + toff(r, 2 * (jj & 7) + hh));
      const u32x4 bf = jj < 31 ? *reinterpret_cast<const u32x4*>(scr + r * 1008 + 16 * (2 * (jj < 31 ? jj : 0) + hh)) : qlast;
      sacc = __builtin_amdgcn_mfma_f32_32x32x16_bf16(__builtin_bit_cast(bf16x8_t, af), __builtin_bit_cast(bf16x8_t, bf), sacc, 0, 0, 0);
    }
    float m = sacc[0];
#pragma unroll
    for (int i = 1; i < 16; ++i) m = fmaxf(m, sacc[i]);
    m = xhalf_max(m);
    if (hh == 0) *reinterpret_cast<float*>(scr + r * 1008 + 992 + 4 * w4) = m;
    sbar4(cnt + 4, lane);
    STAMP(11);
    {
      const f32x4 m4 = *reinterpret_cast<const f32x4*>(scr + r * 1008 + 992);
      m = fmaxf(fmaxf(m4[0], m4[1]), fmaxf(m4[2], m4[3]));
    }
    const float mc = m * C2;
    float p[16], ls = 0.f;
#pragma unroll
    for (int i = 0; i < 16; ++i) { p[i] = ex2(fmaf(sacc[i], C2, -mc)); ls += p[i]; }
    ls = xhalf_sum(ls);
    sbar4(cnt + 5, lane);
    u32x4* pf = reinterpret_cast<u32x4*>(scr);
    float* wl = reinterpret_cast<float*>(scr + 8192);
    if (hh == 0) wl[r * 4 + w4] = ls;
    pf[(2 * w4) * 64 + lane] = u32x4{pk_bf(p[0], p[1]), pk_bf(p[2], p[3]), pk_bf(p[4], p[5]), pk_bf(p[6], p[7])};
    pf[(2 * w4 + 1) * 64 + lane] = u32x4{pk_bf(p[8], p[9]), pk_bf(p[10], p[11]), pk_bf(p[12], p[13]), pk_bf(p[14], p[15])};
    sbar4(cnt + 6, lane);
    u32x4 pb8[8];
#pragma unroll
    for (int ks = 0; ks < 8; ++ks) pb8[ks] = pf[ks * 64 + lane];
    const f32x4 l4 = *reinterpret_cast<const f32x4*>(wl + r * 4);
    const float lsum = (l4[0] + l4[1]) + (l4[2] + l4[3]), rl = __builtin_amdgcn_rcpf(lsum);
    const int qq = (lane & 15) >> 2, pp = lane & 3, blk2 = (lane >> 4) & 1;
    const unsigned tw = tstart + __builtin_amdgcn_readfirstlane(cnt[13]) + PAR(13, TW), th = tw - PAR(14, HL);
    u32x4 vv[2][4];
    bool held = false;
    char* ot = scr + 12288 + w4 * 4096;
#pragma unroll
    for (int c2 = 0; c2 < 2; ++c2) {
      f32x16 oacc[2];
#pragma unroll
      for (int ct = 0; ct < 2; ++ct)
#pragma unroll
        for (int i = 0; i < 16; ++i) oacc[ct][i] = 0.f;
#pragma unroll
      for (int ks = 0; ks < 8; ++ks)
#pragma unroll
        for (int ct = 0; ct < 2; ++ct) {
          typedef __attribute__((address_space(3))) s16x4* lp;
          const char* tile = smem + ((ks >> 1) * 4 + w4) * 8192;
          const int cc = 2 * c2 + ct;
          const s16x4 lo = __builtin_amdgcn_ds_read_tr16_b64_v4i16((lp)(tile + toff(16 * (ks & 1) + 4 * hh + qq, 4 * cc + 2 * blk2 + (pp >> 1)) + 8 * (pp & 1)));
          const s16x4 hi = __builtin_amdgcn_ds_read_tr16_b64_v4i16((lp)(tile + toff(16 * (ks & 1) + 8 + 4 * hh + qq, 4 * cc + 2 * blk2 + (pp >> 1)) + 8 * (pp & 1)));
          const s16x8 afr = __builtin_shufflevector(lo, hi, 0, 1, 2, 3, 4, 5, 6, 7);
          oacc[ct] = __builtin_amdgcn_mfma_f32_32x32x16_bf16(__builtin_bit_cast(bf16x8_t, afr), __builtin_bit_cast(bf16x8_t, pb8[ks]), oacc[ct], 0, 0, 0);
        }
#pragma unroll
      for (int ct = 0; ct < 2; ++ct)
#pragma unroll
        for (int g = 0; g < 4; ++g)
          *reinterpret_cast<u32x2*>(ot + r * 128 + 16 * ((4 * ct + g) ^ (r & 7)) + 8 * hh) = u32x2{pk_bf(oacc[ct][4 * g] * rl, oacc[ct][4 * g + 1] * rl), pk_bf(oacc[ct][4 * g + 2] * rl, oacc[ct][4 * g + 3] * rl)};
      if (c2 == 0) { held = (int)((unsigned)wall_clock64() + 30u - th) >= 0; if (held) lds_add(cnt + 8, 1u, lane); }
      __builtin_amdgcn_wave_barrier();
#pragma unroll
      for (int i = 0; i < 4; ++i) { vv[c2][i] = *reinterpret_cast<const u32x4*>(ot + (8 * i + (lane >> 3)) * 128 + 16 * ((lane ^ (lane >> 3)) & 7)); asm volatile("" : "+v"(vv[c2][i])); }
      __builtin_amdgcn_wave_barrier();
    }
    lds_arrive(cnt + 1, lane);
    if (!held) { until(th); lds_add(cnt + 8, 1u, lane); }
    until(tw);
#pragma unroll
    for (int i = 0; i < 8; ++i) st16c(rs, WS_APO + ((((8 * (i >> 1) + (lane >> 3)) * 2 + (w4 >> 1)) * 64 + idx) * 256 + (w4 & 1) * 128 + 64 * (i & 1) + 8 * (lane & 7)) * 2, vv[i & 1][i >> 1]);
    STAMP(1);
    asm volatile("s_waitcnt vmcnt(0)" ::: "memory");
    if (lds_arrive(cnt + 9 + (w4 >> 1), lane) == 1u && hh == 0)
      st8c(rs, WS_AML + ((r * 2 + (w4 >> 1)) * 64 + idx) * 8, u32x2{__float_as_uint(mc + __builtin_amdgcn_logf(lsum)), tag});
    lds_add(cnt + 8, ~0u, lane);
    STAMP(2);
    STAMP_FLUSH(0); TR_FLUSH();
    return;
  }
#pragma unroll
  for (int ip = 0; ip < 8; ++ip) {
#pragma unroll
    for (int k = 0; k < 4; ++k) { const unsigned lo = plo(Wb[2 * ip][k], Wb[2 * ip + 1][k]), hi = phi(Wb[2 * ip][k], Wb[2 * ip + 1][k]); Wb[2 * ip][k] = lo; Wb[2 * ip + 1][k] = hi; }
    pin2(Wb[2 * ip], Wb[2 * ip + 1]);
  }
  if (cls == 2) {
    float* red2 = red + 4 * 256;
    float* wml = red + 3072;
    u32x16 f[2];
    if (w4 == 0) {
#pragma unroll 1
      for (int spin = 0; spin < SPIN_MAX; ++spin) {
        const u32x16 t = sld64(ws + WS_AML + (h * 2 + b) * 512);
        bool any = false;
#pragma unroll
        for (int i = 0; i < 8; ++i) any |= t[2 * i + 1] == tag;
        if (any) break;
        __builtin_amdgcn_s_sleep(8);
      }
      lds_add(cnt + 7, 1u, lane);
    } else lds_wait_slow(cnt + 7, 1u);
#pragma unroll 1
    for (int spin = 0; spin < SPIN_MAX; ++spin) {
      sld128(ws + WS_AML + ((h * 2 + b) * 64 + w4 * 16) * 8, f);
      unsigned e = 0u;
#pragma unroll
      for (int i = 0; i < 16; ++i) e |= f[i >> 3][2 * (i & 7) + 1] ^ tag;
      if (e == 0u) break;
      __builtin_amdgcn_s_sleep(2);
    }
    STAMP(3);
    u32x4 pp[8];
#pragma unroll
    for (int i = 0; i < 8; ++i) pp[i] = ld16c(rs, WS_APO + ((h * 2 + b) * 64 + w4 * 16 + 2 * i) * 512 + lane * 16);
    SCHED();
    float wt[8];
    {
      float M = -INFINITY, L = 0.f;
#pragma unroll
      for (int i = 0; i < 8; ++i) {
        unsigned z0 = f[i >> 2][4 * (i & 3)], z1 = f[i >> 2][4 * (i & 3) + 2];
        asm("" : "+s"(z0), "+s"(z1));
        wt[i] = __uint_as_float(lane < 32 ? z0 : z1); M = fmaxf(M, wt[i]);
      }
      M = xhalf_max(M);
#pragma unroll
      for (int i = 0; i < 8; ++i) { wt[i] = ex2(wt[i] - M); L += wt[i]; }
      L = xhalf_sum(L);
      if (lane == 0) *reinterpret_cast<f32x2*>(wml + 2 * w4) = f32x2{M, L};
    }
    SCHED();
    float a8[8];
#pragma unroll
    for (int k = 0; k < 8; ++k) a8[k] = 0.f;
#pragma unroll
    for (int i = 0; i < 8; ++i) {
#pragma unroll
      for (int k = 0; k < 4; ++k) { a8[2 * k] = fmaf(wt[i], bflo(pp[i][k]), a8[2 * k]); a8[2 * k + 1] = fmaf(wt[i], bfhi(pp[i][k]), a8[2 * k + 1]); }
      asm volatile("" : "+v"(a8[0]), "+v"(a8[1]), "+v"(a8[2]), "+v"(a8[3]), "+v"(a8[4]), "+v"(a8[5]), "+v"(a8[6]), "+v"(a8[7]));
    }
    STAMP(6);
    *reinterpret_cast<f32x4*>(red + w4 * 256 + 8 * (lane & 31) + 4 * (lane >> 5)) = f32x4{rs32(a8[0], a8[4]), rs32(a8[1], a8[5]), rs32(a8[2], a8[6]), rs32(a8[3], a8[7])};
    sbar4(cnt + 3, lane);
    STAMP(4);
    float acc[2][8];
#pragma unroll
    for (int bb = 0; bb < 2; ++bb) {
      u32x4 cv[2];
      const f32x4 ml = *reinterpret_cast<const f32x4*>(wml + 4 * bb);
      const float M = fmaxf(ml[0], ml[2]), e0 = ex2(ml[0] - M), e1 = ex2(ml[2] - M), rl = 1.f / fmaf(e0, ml[1], e1 * ml[3]), s0 = e0 * rl, s1 = e1 * rl;
#pragma unroll
      for (int e = 0; e < 2; ++e) {
        const float* src = red + bb * 512 + w4 * 64 + 16 * r4 + 8 * e;
        const f32x4 t0 = s0 * *reinterpret_cast<const f32x4*>(src) + s1 * *reinterpret_cast<const f32x4*>(src + 256), t1 = s0 * *reinterpret_cast<const f32x4*>(src + 4) + s1 * *reinterpret_cast<const f32x4*>(src + 260);
        cv[e] = u32x4{pk_bf(t0[0], t0[1]), pk_bf(t0[2], t0[3]), pk_bf(t1[0], t1[1]), pk_bf(t1[2], t1[3])};
      }
#pragma unroll
      for (int k = 0; k < 8; ++k) acc[bb][k] = 0.f;
#pragma unroll
      for (int ip = 0; ip < 8; ++ip)
#pragma unroll
        for (int k = 0; k < 4; ++k) { acc[bb][2 * k] = dot2(Wb[2 * ip][k], cv[ip >> 2][ip & 3], acc[bb][2 * k]); acc[bb][2 * k + 1] = dot2(Wb[2 * ip + 1][k], cv[ip >> 2][ip & 3], acc[bb][2 * k + 1]); }
    }
    {
      float v8[8], v4[4];
#pragma unroll
      for (int k = 0; k < 8; ++k) v8[k] = rs32(acc[0][k], acc[1][k]);
#pragma unroll
      for (int k = 0; k < 4; ++k) v4[k] = rs16(v8[k], v8[k + 4]);
      *reinterpret_cast<f32x4*>(red2 + (w4 * 2 + (lane >> 5)) * 128 + 8 * c16 + 4 * (r4 & 1)) = f32x4{v4[0], v4[1], v4[2], v4[3]};
    }
    {
      COMPILER_FENCE();
      const unsigned got = lds_arrive_nw(L_CNT + 16);
      f32x4 t4[4];
#pragma unroll
      for (int w = 0; w < 4; ++w) t4[w] = *reinterpret_cast<const f32x4*>(red2 + w * 256 + 4 * lane);
      SCHED();
      if (lds_done(got) == 3u) {
        const f32x4 t = (t4[0] + t4[1]) + (t4[2] + t4[3]);
        const unsigned co = WS_CX + ((h * 2 + b) * 256 + 64 * r4 + 32 * (c16 & 1) + 2 * (c16 >> 1)) * 8;
        st16c(rs, co, u32x4{__float_as_uint(t[0]), tag, __float_as_uint(t[1]), tag});
        st16c(rs, co + 128, u32x4{__float_as_uint(t[2]), tag, __float_as_uint(t[3]), tag});
      }
    }
    STAMP(12);
    STAMP_FLUSH(0); TR_FLUSH();
    return;
  }
  {
    unsigned op;
    {
      u32x4 c;
      const unsigned co = WS_CX + ((h * 2 + ((lane >> 3) & 1)) * 256 + (lane >> 5) * 128 + b * 64 + w4 * 16 + 2 * (lane & 7)) * 8;
      if (idx == 0 && w4 == 1) {
#pragma unroll 1
        for (int spin = 0; spin < SPIN_MAX; ++spin) {
          COMPILER_FENCE();
          if (__all(ld8c(rs, WS_QX + 65536 + (lane >> 1) * 2048 + (lane & 1) * 512)[1] == tag)) break;
          __builtin_amdgcn_s_sleep(8);
        }
      }
      if (w4 == 0) { shint<2048, 4>(ws + WS_CX + (h * 512 + b * 64) * 8, tag); lds_add(cnt + 7, 1u, lane); }
      else lds_wait_slow(cnt + 7, 1u);
      STAMP(3);
#pragma unroll 1
      for (int spin = 0; spin < SPIN_MAX; ++spin) {
        COMPILER_FENCE();
        c = ld16c(rs, co);
        if (__all((c[1] == tag) & (c[3] == tag))) break;
        __builtin_amdgcn_s_sleep(1);
      }
      float o0 = __uint_as_float(c[0]), o1 = __uint_as_float(c[2]);
      o0 += dpp<0x128>(o0); o1 += dpp<0x128>(o1);
      op = pk_bf(o0, o1);
    }
    STAMP(14);
    float* red3 = red + 4096;
    float a[2][8];
#pragma unroll
    for (int bb = 0; bb < 2; ++bb)
#pragma unroll
      for (int k = 0; k < 8; ++k) a[bb][k] = 0.f;
#pragma unroll
    for (int ip = 0; ip < 8; ++ip) {
      const unsigned v0 = __builtin_amdgcn_readlane(op, ip), v1 = __builtin_amdgcn_readlane(op, 32 + ip);
#pragma unroll
      for (int k = 0; k < 4; ++k) {
        a[0][2 * k] = dot2(Wb[2 * ip][k], v0, a[0][2 * k]); a[0][2 * k + 1] = dot2(Wb[2 * ip + 1][k], v0, a[0][2 * k + 1]);
        a[1][2 * k] = dot2(Wb[2 * ip][k], v1, a[1][2 * k]); a[1][2 * k + 1] = dot2(Wb[2 * ip + 1][k], v1, a[1][2 * k + 1]);
      }
    }
#pragma unroll
    for (int bb = 0; bb < 2; ++bb) {
      *reinterpret_cast<f32x4*>(red3 + (w4 * 2 + bb) * 512 + 8 * lane) = f32x4{a[bb][0], a[bb][1], a[bb][2], a[bb][3]};
      *reinterpret_cast<f32x4*>(red3 + (w4 * 2 + bb) * 512 + 8 * lane + 4) = f32x4{a[bb][4], a[bb][5], a[bb][6], a[bb][7]};
    }
    sbar4(cnt + 5, lane);
    f32x4 t = {0.f, 0.f, 0.f, 0.f};
#pragma unroll
    for (int w = 0; w < 4; ++w) t += *reinterpret_cast<const f32x4*>(red3 + w * 1024 + 4 * tid);
    st16c(rs, WS_GA + ((tid >> 7) * 64 + idx) * 2048 + (tid & 127) * 16, u32x4{pk_bf(t[0], t[1]), tag, pk_bf(t[2], t[3]), tag});
    STAMP(5);
  }
  fin(IC<1>{}, WS_GA, 0, idx == 0);
  STAMP(6);
  STAMP_FLUSH(0); TR_FLUSH();
}

extern "C" void kernel_launch(void* const* d_in, const int* in_sizes, int n_in, void* d_out, int out_size, void* d_ws, size_t ws_size, hipStream_t stream) {
  const u16* xA = (const u16*)d_in[0];
  const u16* ckv = (const u16*)d_in[1];
  const u16* w_q = (const u16*)d_in[2];
  const u16* w_uk = (const u16*)d_in[3];
  const u16* w_uv = (const u16*)d_in[4];
  const u16* w_o = (const u16*)d_in[5];
  const u16* xB = (const u16*)d_in[6];
  const u16* kc = (const u16*)d_in[7];
  const u16* vc = (const u16*)d_in[8];
  const u16* wq = (const u16*)d_in[9];
  const u16* wo = (const u16*)d_in[10];
  kmain<<<256, 512, 0, stream>>>(xA, ckv, w_q, w_uk, w_uv, w_o, xB, kc, vc, wq, wo, (char*)d_ws, (u16*)d_out);
}
```

```cpp
#include <hip/hip_runtime.h>
#include <hip/hip_bf16.h>
#include <stdint.h>

#define DI __device__ __forceinline__
typedef __attribute__((ext_vector_type(4))) unsigned u32x4;
typedef __attribute__((ext_vector_type(2))) unsigned u32x2;
typedef __attribute__((ext_vector_type(4))) float f32x4;
typedef __attribute__((ext_vector_type(2))) float f32x2;
typedef __attribute__((ext_vector_type(16))) float f32x16;
typedef __attribute__((ext_vector_type(8))) __bf16 bf16x8_t;
typedef __attribute__((ext_vector_type(2))) __bf16 bf16x2_t;
typedef __attribute__((ext_vector_type(4))) short s16x4;
typedef __attribute__((ext_vector_type(8))) short s16x8;
typedef unsigned short u16;
typedef unsigned long long u64;

constexpr int NS0 = 13, NS1 = 17, NS2 = 16, NS3 = 18;
constexpr u64 TL0 = 0xc2b8de6543cb1c1ull, TH0 = 0x3de34ull, TL1 = 0x920718551349142ull, TH1 = 0x3eeb6caeaa7ull;
constexpr u64 TL2 = 0x96175a59238a183ull, TH2 = 0xff9d71b28ull, TL3 = 0x89f6d74d0308100ull, TH3 = 0xf3bdf3beda66ull;
constexpr int RA2 = 0xC0, RA3 = 3;
constexpr int AHL = 2;
constexpr int QPE = 5;
constexpr int WBW = 7;
#define TL 2385
#define TW 1630
constexpr unsigned TS0 = 1160, TS1 = 1440, TS2 = 1320, TS3 = 1440, STG = 12, BSG = 6, HL = 40, TP0 = TW + 170, TP2 = TW + 420, PA = 150, T0B = 440;
#define PAR(i, v) (v)
constexpr u64 LA2 = TH2 >> (6 * (NS2 - 11)), LA3 = TH3 >> (6 * (NS3 - 11));
constexpr u64 chunk_mask(u64 lo, u64 hi, int n) { u64 m = 0; for (int i = 0; i < n; ++i) m |= 1ull << ((i < 10 ? lo >> (6 * i) : hi >> (6 * (i - 10))) & 63); return m; }
static_assert((chunk_mask(TL0, TH0, NS0) | chunk_mask(TL1, TH1, NS1) | chunk_mask(TL2, TH2, NS2) | chunk_mask(TL3, TH3, NS3)) == ~0ull && (RA2 & RA3) == 0, "every chunk once");
static_assert(NS0 + NS1 + NS2 + NS3 == 64, "split sizes");
constexpr int NSMIN = 13, NSMAX = 18;
static_assert(NS0 >= NSMIN && NS1 >= NSMIN && NS2 - 1 >= NSMIN && NS3 - 1 >= NSMIN && NS0 <= NSMAX && NS1 + 1 <= NSMAX && NS2 <= NSMAX && NS3 <= NSMAX, "range");
constexpr int SK = 4096;
constexpr float C2 = 0.08838834764831845f * 1.4426950408889634f;
constexpr u64 GOLD = 0x9E3779B97F4A7C15ull;

constexpr unsigned WS_EP = 0;
constexpr unsigned WS_QX = 4096;
constexpr unsigned WS_QA = WS_QX + 2 * 32 * 256 * 8;
constexpr unsigned WS_AML = WS_QA + 2 * 32 * 512 * 2;
constexpr unsigned WS_CX = WS_AML + 32 * 2 * 64 * 8;
constexpr unsigned WS_GZ = WS_CX + 32 * 2 * 2 * 128 * 8;
constexpr unsigned WS_T0 = WS_GZ + 256 * 128;
constexpr unsigned WS_GB = WS_T0 + 64 * 128;
constexpr unsigned WS_GA = WS_GB + 2 * 128 * 256 * 8;
constexpr unsigned WS_APO = 2u << 20;
static_assert(WS_GA + 64 * 2 * 256 * 8 <= WS_APO, "layout");
constexpr unsigned WS_END = WS_APO + 32 * 2 * 64 * 256 * 2;
#define STAMP(i) do {} while (0)
#define STAMP_DECL do {} while (0)
#define STAMP_FLUSH(grp) do {} while (0)

#define TR() do {} while (0)
#define TR_FLUSH() do {} while (0)
DI float bflo(unsigned u) { return __uint_as_float(u << 16); }
DI float bfhi(unsigned u) { return __uint_as_float(u & 0xffff0000u); }
DI unsigned pk_bf(float a, float b) { const f32x2 v = {a, b}; return __builtin_bit_cast(unsigned, __builtin_convertvector(v, bf16x2_t)); }
DI float dot2(unsigned k, unsigned q, float acc) {
  return __builtin_amdgcn_fdot2_f32_bf16(__builtin_bit_cast(bf16x2_t, k), __builtin_bit_cast(bf16x2_t, q), acc, false);
}
DI unsigned plo(unsigned w0, unsigned w1) { return __builtin_amdgcn_perm(w1, w0, 0x05040100u); }
DI unsigned phi(unsigned w0, unsigned w1) { return __builtin_amdgcn_perm(w1, w0, 0x07060302u); }
template <int CTRL> DI float dpp(float x) {
  return __builtin_bit_cast(float, __builtin_amdgcn_mov_dpp(__builtin_bit_cast(int, x), CTRL, 0xf, 0xf, true));
}
DI float row16_sum(float x) { x += dpp<0xB1>(x); x += dpp<0x4E>(x); x += dpp<0x141>(x); x += dpp<0x128>(x); return x; }
DI float row16_max(float x) { x = fmaxf(x, dpp<0xB1>(x)); x = fmaxf(x, dpp<0x4E>(x)); x = fmaxf(x, dpp<0x141>(x)); x = fmaxf(x, dpp<0x128>(x)); return x; }
DI float xhalf_max(float x) { auto t = __builtin_amdgcn_permlane32_swap(__float_as_uint(x), __float_as_uint(x), false, false); return fmaxf(__uint_as_float(t[0]), __uint_as_float(t[1])); }
DI float xhalf_sum(float x) { auto t = __builtin_amdgcn_permlane32_swap(__float_as_uint(x), __float_as_uint(x), false, false); return __uint_as_float(t[0]) + __uint_as_float(t[1]); }
DI u32x4 ld16(const void* p) { return *reinterpret_cast<const u32x4*>(p); }
DI u32x4 ld16nt(const void* p) { return __builtin_nontemporal_load(reinterpret_cast<const u32x4*>(p)); }
DI float ex2(float x) { return __builtin_amdgcn_exp2f(x); }
#define SCHED() __builtin_amdgcn_sched_barrier(0)

typedef __amdgpu_buffer_rsrc_t rsrc_t;
DI u32x4 ld16c(rsrc_t rs, unsigned off) { return __builtin_amdgcn_raw_buffer_load_b128(rs, (int)off, 0, 16); }
DI u32x2 ld8c(rsrc_t rs, unsigned off) { return __builtin_amdgcn_raw_buffer_load_b64(rs, (int)off, 0, 16); }
DI void st16c(rsrc_t rs, unsigned off, u32x4 v) { __builtin_amdgcn_raw_buffer_store_b128(v, rs, (int)off, 0, 16); }
DI void st8c(rsrc_t rs, unsigned off, u32x2 v) { __builtin_amdgcn_raw_buffer_store_b64(v, rs, (int)off, 0, 16); }
typedef __attribute__((ext_vector_type(16))) unsigned u32x16;
template <int O1> DI void sld2(const char* p, u32x2& a, u32x2& b) {
  asm volatile("s_load_dwordx2 %0, %2, 0x0 glc\n\ts_load_dwordx2 %1, %2, %3 glc\n\ts_waitcnt lgkmcnt(0)" : "=&s"(a), "=&s"(b) : "s"(p), "n"(O1) : "memory");
}
DI u32x16 sld64(const char* p) {
  u32x16 v;
  asm volatile("s_load_dwordx16 %0, %1, 0x0 glc\n\ts_waitcnt lgkmcnt(0)" : "=&s"(v) : "s"(p) : "memory");
  return v;
}
DI void sld128(const char* p, u32x16 (&v)[2]) {
  asm volatile("s_load_dwordx16 %0, %2, 0x0 glc\n\ts_load_dwordx16 %1, %2, 0x40 glc\n\ts_waitcnt lgkmcnt(0)" : "=&s"(v[0]), "=&s"(v[1]) : "s"(p) : "memory");
}
constexpr int SPIN_MAX = 1 << 20;
constexpr unsigned OOB = 0x7ffffff0u;
DI void rawbar() { __builtin_amdgcn_sched_barrier(0); __builtin_amdgcn_s_barrier(); __builtin_amdgcn_sched_barrier(0); }
#define COMPILER_FENCE() asm volatile("" ::: "memory")
template <int O1, int SL> DI void shint(const char* p, unsigned tag) {
#pragma unroll 1
  for (int spin = 0; spin < SPIN_MAX; ++spin) {
    u32x2 a, b;
    sld2<O1>(p, a, b);
    if (((a[1] ^ tag) | (b[1] ^ tag)) == 0u) break;
    __builtin_amdgcn_s_sleep(SL);
  }
}

template <int N> struct IC { static constexpr int v = N; };
template <int I, int N, class F> DI void sfor(F&& f) { if constexpr (I < N) { f(IC<I>{}); sfor<I + 1, N>(f); } }
typedef __attribute__((address_space(3))) unsigned lu32;
DI void lds_wait(unsigned* cnt, unsigned target) {
#pragma unroll 1
  for (int spin = 0; spin < SPIN_MAX; ++spin) {
    if (__builtin_amdgcn_readfirstlane(__hip_atomic_load((lu32*)cnt, __ATOMIC_RELAXED, __HIP_MEMORY_SCOPE_WORKGROUP)) >= target) break;
    __builtin_amdgcn_s_sleep(1);
  }
  COMPILER_FENCE();
}
DI void lds_wait_slow(unsigned* cnt, unsigned target) {
#pragma unroll 1
  for (int spin = 0; spin < SPIN_MAX; ++spin) {
    if (__builtin_amdgcn_readfirstlane(__hip_atomic_load((lu32*)cnt, __ATOMIC_RELAXED, __HIP_MEMORY_SCOPE_WORKGROUP)) >= target) break;
    __builtin_amdgcn_s_sleep(3);
  }
  COMPILER_FENCE();
}
DI void until(unsigned t) {
#pragma unroll 1
  for (int spin = 0; spin < 1024; ++spin) { if ((int)((unsigned)wall_clock64() - t) >= 0) break; __builtin_amdgcn_s_sleep(1); }
}
DI void lds_hold(unsigned* flag) {
#pragma unroll 1
  for (int spin = 0; spin < SPIN_MAX; ++spin) {
    if (__builtin_amdgcn_readfirstlane(__hip_atomic_load((lu32*)flag, __ATOMIC_RELAXED, __HIP_MEMORY_SCOPE_WORKGROUP)) == 0u) break;
    __builtin_amdgcn_s_sleep(2);
  }
}
DI unsigned lds_arrive_nw(unsigned addr) {
  unsigned old, one = 1u;
  asm volatile("s_mov_b64 exec, 1\n\tds_add_rtn_u32 %0, %1, %2\n\ts_mov_b64 exec, -1" : "=v"(old) : "v"(addr), "v"(one) : "memory");
  return old;
}
DI unsigned lds_done(unsigned old) { asm volatile("s_waitcnt lgkmcnt(0)" : "+v"(old) :: "memory"); return __builtin_amdgcn_readfirstlane(old); }
DI unsigned lds_arrive(unsigned* cnt, int) { return lds_done(lds_arrive_nw((unsigned)(size_t)(lu32*)cnt)); }
DI void lds_add(unsigned* cnt, unsigned v, int) {
  asm volatile("s_mov_b64 exec, 1\n\tds_add_u32 %0, %1\n\ts_mov_b64 exec, -1" :: "v"((unsigned)(size_t)(lu32*)cnt), "v"(v) : "memory");
}
DI void sbar4(unsigned* cnt, int lane) { if (lds_arrive(cnt, lane) < 3u) lds_wait(cnt, 4u); }
DI float rs32(float a, float b) { auto t = __builtin_amdgcn_permlane32_swap(__float_as_uint(a), __float_as_uint(b), false, false); return __uint_as_float(t[0]) + __uint_as_float(t[1]); }
DI float rs16(float a, float b) { auto t = __builtin_amdgcn_permlane16_swap(__float_as_uint(a), __float_as_uint(b), false, false); return __uint_as_float(t[0]) + __uint_as_float(t[1]); }
template <int N> DI unsigned rowbcast(unsigned x) { return (unsigned)__builtin_amdgcn_mov_dpp((int)x, 0x150 + N, 0xf, 0xf, true); }
DI void pin2(u32x4& a, u32x4& b) { asm volatile("" : "+v"(a), "+v"(b)); }
DI void use2(const u32x4& a, const u32x4& b) { asm volatile("" :: "v"(a), "v"(b)); }
DI bool nz8(const u32x4& v) { return ((v[0] | v[1]) != 0u) & ((v[2] | v[3]) != 0u); }

DI u32x4 qsum(const u32x4& g) {
  const auto ta = __builtin_amdgcn_permlane32_swap(g[0], g[0], false, false), tc = __builtin_amdgcn_permlane32_swap(g[2], g[2], false, false);
  const unsigned a = pk_bf(bflo(ta[0]) + bflo(ta[1]), bfhi(ta[0]) + bfhi(ta[1])), c = pk_bf(bflo(tc[0]) + bflo(tc[1]), bfhi(tc[0]) + bfhi(tc[1]));
  const auto ra = __builtin_amdgcn_permlane16_swap(a, a, false, false), rc = __builtin_amdgcn_permlane16_swap(c, c, false, false);
  return u32x4{ra[0], rc[0], ra[1], rc[1]};
}
DI unsigned toff(unsigned row, unsigned ch) { return 256u * row + 16u * (ch ^ (((row & 3) << 2) | ((row >> 2) & 3))); }
constexpr int SMEM = 160 * 1024;
constexpr int L_SCR = 131072;
constexpr int L_MRG = 65536;
constexpr int L_CNT = L_SCR + 32 * 1008;

struct KV { u32x4 r[8]; };

typedef __attribute__((ext_vector_type(8))) unsigned u32x8;
template <class T> DI T* gptr(unsigned lo, unsigned hi) { return (T*)(__attribute__((address_space(1))) T*)(((u64)hi << 32) | lo); }

__global__ __launch_bounds__(512, 2) void kmain(const u16*, const u16*, const u16*, const u16*, const u16*, const u16*, const u16*, const u16*, const u16*,
                                                const u16*, const u16*, char*, u16*) {
  u32x2 kpa, kpx, kc2; u32x4 kb4; u32x8 kb8; u64 t64;
  int tid = threadIdx.x, blk = blockIdx.x;
  const auto kp = __builtin_amdgcn_kernarg_segment_ptr();
  asm volatile("s_load_dwordx2 %0, %8, %9\n\ts_load_dwordx2 %1, %8, %10\n\ts_load_dwordx4 %2, %8, 0x30\n\ts_load_dwordx8 %3, %8, 0x40\n\ts_load_dwordx2 %4, %8, 0x60\n\ts_memrealtime %5"
               : "=&s"(kpa), "=&s"(kpx), "=&s"(kb4), "=&s"(kb8), "=&s"(kc2), "=&s"(t64), "+v"(tid), "+s"(blk) : "s"(kp), "s"(blk & 24), "s"(32 + ((blk >> 1) & 8)));
  __builtin_assume(tid >= 0 && tid < 512 && blk >= 0 && blk < 256);
  __shared__ __attribute__((aligned(16))) char smem[SMEM];
  const int lane = tid & 63;
  const int wave = __builtin_amdgcn_readfirstlane(tid >> 6), w4 = wave & 3;
  const int cls = (blk >> 3) & 3;
  int idx = 8 * (blk & 7) + (blk >> 5);
  idx = (idx & ~7) | ((idx + ((wave >= 4 ? 0 : 0x2400) >> (4 * cls))) & 7);
  const int h = idx >> 1, b = idx & 1;
  const int r4 = lane >> 4, c16 = lane & 15;
  unsigned* cnt = reinterpret_cast<unsigned*>(smem + L_CNT);
  STAMP_DECL;
  STAMP(0);
  const unsigned c0 = cls ? 0u : ~0u;
  unsigned sa = cls ? 8192u : 4096u;
  unsigned ao = ((((b * 256 + w4 * 64) * 8192 + h * 256) & ~c0) | (((idx * 128 + w4) * 1024) & c0)) + (r4 << (17u - (c0 & 9u))) + c16 * 16, xo = (b * 256 + w4 * 64 + 16 * r4) * 2;
  asm volatile("" : "+v"(ao), "+v"(xo), "+s"(sa));
  SCHED();
  asm volatile("s_waitcnt lgkmcnt(0)" : "+s"(kpa), "+s"(kpx), "+s"(kb4), "+s"(kb8), "+s"(kc2), "+s"(t64) : "s"(kp));
  const unsigned tstart = (unsigned)t64;
  const char* __restrict__ pa = gptr<const char>(kpa[0], kpa[1]); const char* __restrict__ xp = gptr<const char>(kpx[0], kpx[1]);
  const u16* __restrict__ w_uv = gptr<const u16>(kb4[0], kb4[1]); const u16* __restrict__ w_o = gptr<const u16>(kb4[2], kb4[3]);
  const u16* __restrict__ kc = gptr<const u16>(kb8[0], kb8[1]); const u16* __restrict__ vc = gptr<const u16>(kb8[2], kb8[3]);
  const u16* __restrict__ wo = gptr<const u16>(kb8[4], kb8[5]); char* __restrict__ ws = gptr<char>(kb8[6], kb8[7]);
  u16* __restrict__ out = gptr<u16>(kc2[0], kc2[1]);
  const rsrc_t rs = __builtin_amdgcn_make_buffer_rsrc(ws, 0, (int)WS_END, 0x00020000);
  unsigned tag, tlow;
  u32x2 epw;
  auto take_epoch = [&]() {
    asm volatile("" : "+v"(epw));
    const u64 t64 = (((u64)(unsigned)__builtin_amdgcn_readfirstlane(epw[1]) << 32) | (unsigned)__builtin_amdgcn_readfirstlane(epw[0])) + GOLD;
    tag = (unsigned)(t64 >> 32); tlow = (unsigned)t64;
    asm volatile("s_waitcnt lgkmcnt(0)" : "+s"(tag), "+s"(tlow) :: "memory");
  };
  if (wave >= 4) {
    if (tid - 256 < 16) cnt[tid - 256] = 0u;
    asm volatile("s_waitcnt lgkmcnt(0)" ::: "memory");
    const unsigned pTL = PAR(0, TL), pTS = PAR(1 + cls, cls == 0 ? TS0 : (cls == 1 ? TS1 : (cls == 2 ? TS2 : TS3))), pSTG = PAR(9, STG), pBSG = PAR(10, BSG);
    const unsigned pRA2 = PAR(11, RA2), pRA3 = PAR(12, RA3), pTP = PAR(15 + cls, cls == 0 ? TP0 : TP2), pPA = PAR(19 + cls, (cls & 1) ? 0u : PA);
    rawbar();
    const int e2 = (pRA2 >> (idx & 7)) & 1, e3 = (pRA3 >> (idx & 7)) & 1;
    const int ns = cls == 0 ? NS0 : (cls == 1 ? NS1 + e2 + e3 : (cls == 2 ? NS2 - e2 : NS3 - e3));
    const unsigned pTA = __builtin_amdgcn_readfirstlane((unsigned)((float)pTS * __builtin_amdgcn_rcpf((float)(ns - 5)) + 0.5f));
    const size_t kvo = ((size_t)(b * 32 + h) * SK + w4 * 16) * 256;
    const char* kb = reinterpret_cast<const char*>(kc) + kvo;
    const char* vb = reinterpret_cast<const char*>(vc) + kvo;
    const unsigned lo = lane * 16u;
    u64 tlo = cls == 0 ? TL0 : (cls == 1 ? TL1 : (cls == 2 ? TL2 : TL3));
    u64 thi = cls == 0 ? TH0 : (cls == 1 ? TH1 | ((e2 ? LA2 : LA3) << (6 * (NS1 - 10))) : (cls == 2 ? TH2 : TH3));
    const u64 tlo0 = tlo, thi0 = thi;
    int hn = 0;
    const char* hb = kb;
    const char* pc[2];
    auto next2 = [&]() {
#pragma unroll
      for (int i = 0; i < 2; ++i) {
        if (hn == ns) { tlo = tlo0; thi = thi0; hb = vb; }
        ++hn;
        pc[i] = hb + ((((unsigned)tlo + (unsigned)idx) & 63u) << 14);
        tlo = (tlo >> 6) | ((thi & 63u) << 54); thi >>= 6;
      }
    };
    KV A, B;
    u32x4 W[8];
    u32x4 T[4][8];
    auto ldp = [&](KV& S, auto P) {
      constexpr int p = P.v;
      const char* q = pc[p >> 1] + lo + (p & 1) * 2048;
      S.r[2 * p] = ld16nt(q); S.r[2 * p + 1] = ld16nt(q + 1024);
    };
    auto landed = [&](KV& S, auto P) { pin2(S.r[2 * P.v], S.r[2 * P.v + 1]); };
    KV X;
    const unsigned qo = WS_QX + ((32 + h) * 2 + b) * 1024 + lane * 16;
    u32x4 g0;
    {
      unsigned tk = 0u;
      if (w4 == 0) {
        const int si = (idx & ~7) | ((idx + (0x2400 >> (4 * cls))) & 7);
        const char* p = kb + cls * 128;
        if (lane == 1) p = cls == 2 ? reinterpret_cast<const char*>(w_uv) + (si & 1) * 2097152 + (si >> 1) * 256 : reinterpret_cast<const char*>(cls == 1 ? w_o : wo) + si * 65536;
        if (lane == 2) p = vb + cls * 128;
        if (lane == 3) p = reinterpret_cast<const char*>(wo) + h * 131072;
        if (lane < 4) tk = __builtin_nontemporal_load(reinterpret_cast<const unsigned*>(p));
      }
      lds_wait_slow(cnt + 14, 4u);
      tag = __builtin_amdgcn_readfirstlane(cnt[16]);
      asm volatile("" :: "v"(tk));
    }
    STAMP(10);
    sfor<0, 12>([&](auto I) {
      constexpr int i = I.v, j = i - 4;
      if constexpr ((i & 3) == 0) next2();
      if (i == 4 && w4 == 3) {
#pragma unroll 1
        for (int spin = 0; spin < 2; ++spin) {
          u32x2 t[8];
          asm volatile("s_load_dwordx2 %0, %8, 0x0 glc\n\ts_load_dwordx2 %1, %8, 0x400 glc\n\ts_load_dwordx2 %2, %8, 0x800 glc\n\ts_load_dwordx2 %3, %8, 0xc00 glc\n\t"
                       "s_load_dwordx2 %4, %8, 0x1000 glc\n\ts_load_dwordx2 %5, %8, 0x1400 glc\n\ts_load_dwordx2 %6, %8, 0x1800 glc\n\ts_load_dwordx2 %7, %8, 0x1c00 glc\n\ts_waitcnt lgkmcnt(0)"
                       : "=&s"(t[0]), "=&s"(t[1]), "=&s"(t[2]), "=&s"(t[3]), "=&s"(t[4]), "=&s"(t[5]), "=&s"(t[6]), "=&s"(t[7]) : "s"(ws + WS_T0 + ((blk >> 3) & 7) * 128) : "memory");
          unsigned e = 0u; int sum = 0;
#pragma unroll
          for (int k = 0; k < 8; ++k) { e |= t[k][1] ^ tag; sum += (int)(t[k][0] - tstart); }
          if (e == 0u) { lds_add(cnt + 13, (unsigned)min(sum >> 3, 100), lane); break; }
        }
      }
      if constexpr (j >= 0) { if constexpr (j < 4) landed(X, IC<j & 3>{}); else landed(B, IC<j & 3>{}); }
      if constexpr (j >= 0 && (j & 1)) TR();
      if constexpr (i == QPE) g0 = ld16c(rs, qo);
      if constexpr (i < 4) ldp(X, IC<i & 3>{}); else if constexpr (i < 8) ldp(B, IC<i & 3>{}); else ldp(A, IC<i & 3>{});
      SCHED();
    });
    const unsigned torg = tstart + __builtin_amdgcn_readfirstlane(cnt[13]);
    const char* wp = reinterpret_cast<const char*>(wo) + ((size_t)h * 128 + r4 * 8) * 1024 + w4 * 256 + c16 * 16;
    u32x4 qpk;
    {
      if (!__all((g0[1] == tag) & (g0[3] == tag))) {
#pragma unroll 1
        for (int spin = 0; spin < SPIN_MAX; ++spin) {
          COMPILER_FENCE();
          g0 = ld16c(rs, qo);
          if (__all((g0[1] == tag) & (g0[3] == tag))) break;
        }
      }
      qpk = qsum(g0);
    }
    STAMP(7);
    const bool holds = cls == 0;
    float m = 0.f, l = 0.f, acc[8];
    unsigned sc[5];
#pragma unroll
    for (int k = 0; k < 8; ++k) acc[k] = 0.f;
#pragma unroll
    for (int k = 0; k < 5; ++k) sc[k] = 0xff800000u;
    int vn = 0;
    unsigned due = torg + pTL - (unsigned)(ns - 5) * pTA - (3 - w4) * pSTG - ((idx * 5 + cls * 3) & 15) * pBSG;
    unsigned rwo;
    const unsigned zo = WS_GZ + (b * 32 + h) * 512;
    u32x2 zs[4];
    float wr_;
    auto zreq = [&]() {
      asm volatile("s_load_dwordx2 %0, %4, 0x0 glc\n\ts_load_dwordx2 %1, %4, 0x80 glc\n\ts_load_dwordx2 %2, %4, 0x100 glc\n\ts_load_dwordx2 %3, %4, 0x180 glc"
                   : "=&s"(zs[0]), "=&s"(zs[1]), "=&s"(zs[2]), "=&s"(zs[3]) : "s"(ws + zo) : "memory");
    };
    auto zget = [&]() {
#pragma unroll 1
      for (int spin = 0; spin < SPIN_MAX; ++spin) {
        asm volatile("s_waitcnt lgkmcnt(0)" : "+s"(zs[0]), "+s"(zs[1]), "+s"(zs[2]), "+s"(zs[3]) :: "memory");
        if (((zs[0][1] ^ tag) | (zs[1][1] ^ tag) | (zs[2][1] ^ tag) | (zs[3][1] ^ tag)) == 0u) break;
        __builtin_amdgcn_s_sleep(2);
        zreq();
      }
      asm volatile("s_waitcnt lgkmcnt(0)" : "+s"(zs[0]), "+s"(zs[1]), "+s"(zs[2]), "+s"(zs[3]) :: "memory");
      const float z0 = __uint_as_float(zs[0][0]), z1 = __uint_as_float(zs[1][0]), z2 = __uint_as_float(zs[2][0]), z3 = __uint_as_float(zs[3][0]);
      const float Z = fmaxf(fmaxf(z0, z1), fmaxf(z2, z3));
      wr_ = ex2(fmaf(m, C2, -Z)) * __builtin_amdgcn_rcpf((ex2(z0 - Z) + ex2(z1 - Z)) + (ex2(z2 - Z) + ex2(z3 - Z)));
      asm volatile("" : "+v"(wr_));
    };
    auto qk = [&](const u32x4* K, auto I) {
      float x[4];
#pragma unroll
      for (int t = 0; t < 4; ++t) {
        x[t] = 0.f;
#pragma unroll
        for (int k = 0; k < 4; ++k) x[t] = dot2(K[t][k], qpk[k], x[t]);
      }
      const float u0 = (c16 & 1 ? x[1] : x[0]) + dpp<0xB1>(c16 & 1 ? x[0] : x[1]), u1 = (c16 & 1 ? x[3] : x[2]) + dpp<0xB1>(c16 & 1 ? x[2] : x[3]);
      float v = (c16 & 2 ? u1 : u0) + dpp<0x4E>(c16 & 2 ? u0 : u1);
      v += dpp<0x124>(v); v += dpp<0x128>(v);
      sc[I.v >> 2] = __builtin_amdgcn_update_dpp(sc[I.v >> 2], __float_as_uint(v), 0xE4, 0xf, 1 << (I.v & 3), false);
      asm volatile("" : "+v"(sc[I.v >> 2]));
    };
    auto soft = [&]() {
      m = row16_max(fmaxf(fmaxf(fmaxf(__uint_as_float(sc[0]), __uint_as_float(sc[1])), fmaxf(__uint_as_float(sc[2]), __uint_as_float(sc[3]))), __uint_as_float(sc[4])));
      const float mc = m * C2;
      l = 0.f;
#pragma unroll
      for (int k = 0; k < 5; ++k) {
        const float p = ex2(fmaf(__uint_as_float(sc[k]), C2, -mc));
        sc[k] = pk_bf(p, dpp<0xB1>(p));
        l = dot2(sc[k], 0x3f003f00u, l);
      }
      l = row16_sum(l);
    };
    auto pv = [&](const u32x4* V) {
      const unsigned p01 = rowbcast<0>(sc[0]), p23 = rowbcast<2>(sc[0]);
#pragma unroll
      for (int k = 0; k < 4; ++k) { acc[2 * k] = dot2(plo(V[0][k], V[1][k]), p01, acc[2 * k]); acc[2 * k + 1] = dot2(phi(V[0][k], V[1][k]), p01, acc[2 * k + 1]); }
#pragma unroll
      for (int k = 0; k < 4; ++k) { acc[2 * k] = dot2(plo(V[2][k], V[3][k]), p23, acc[2 * k]); acc[2 * k + 1] = dot2(phi(V[2][k], V[3][k]), p23, acc[2 * k + 1]); }
      sc[0] = (unsigned)__builtin_amdgcn_mov_dpp((int)sc[0], 0x12C, 0xf, 0xf, true);
      if ((++vn & 3) == 0) { sc[0] = sc[1]; sc[1] = sc[2]; sc[2] = sc[3]; sc[3] = sc[4]; }
      asm volatile("" : "+v"(acc[0]), "+v"(acc[1]), "+v"(acc[2]), "+v"(acc[3]), "+v"(acc[4]), "+v"(acc[5]), "+v"(acc[6]), "+v"(acc[7]));
    };
    auto step = [&](KV& S, KV& N, auto SI, auto REFILL, auto WR) {
      constexpr bool refill = REFILL.v == 1;
      constexpr int wr = WR.v;
      if constexpr (refill) next2();
      if constexpr (refill && SI.v >= 3) { until((int)(due - torg) < (int)pTP ? due - pPA : due); due += pTA; }
      constexpr int ah = wr < 0 ? AHL : 0;
      auto point = [&](auto P) {
        if constexpr (refill) {
          if constexpr (P.v == 0 && ah > 0) { if constexpr (wr >= 0) { if (holds) lds_hold(cnt + 8); } sfor<0, ah>([&](auto Q) { ldp(N, Q); }); SCHED(); }
          landed(S, P);
          if constexpr (P.v & 1) TR();
          if constexpr (P.v + ah < 4) { if constexpr ((P.v & 1) == 0 && wr >= 0) { if (holds) lds_hold(cnt + 8); } ldp(N, IC<(P.v + ah) & 3>{}); }
          if constexpr (wr >= 0 && wr < 8) W[(4 * wr + P.v) & 7] = ld16(wp + (((4 * wr + P.v) >> 3) * 32 + ((4 * wr + P.v) & 7)) * 1024);
          SCHED();
        }
      };
      sfor<0, 2>([&](auto H) {
        constexpr int i = 2 * SI.v + H.v;
        if constexpr (REFILL.v == 2 && H.v == 0) { zreq(); SCHED(); }
        point(IC<2 * H.v>{});
        point(IC<2 * H.v + 1>{});
        if constexpr (i < NSMIN) qk(S.r + 4 * H.v, IC<i>{});
        else if constexpr (i > NSMAX) pv(S.r + 4 * H.v);
        else {
          if (i < ns) { if constexpr (i < NSMAX) qk(S.r + 4 * H.v, IC<i>{}); }
          else { if (i == ns) soft(); pv(S.r + 4 * H.v); }
        }
        SCHED();
      });
      if constexpr (REFILL.v == 2) {
        zget();
        STAMP(1);
        *reinterpret_cast<u32x4*>(smem + rwo) = u32x4{pk_bf(acc[0] * wr_, acc[1] * wr_), pk_bf(acc[2] * wr_, acc[3] * wr_), pk_bf(acc[4] * wr_, acc[5] * wr_), pk_bf(acc[6] * wr_, acc[7] * wr_)};
        COMPILER_FENCE();
      }
      if constexpr (wr >= 1) {
        constexpr int t = (wr - 1) >> 1, j0 = ((wr - 1) & 1) * 2;
#pragma unroll
        for (int jj = 0; jj < 2; ++jj)
#pragma unroll
          for (int e = 0; e < 4; ++e) {
            const unsigned a_ = W[(4 * wr - 4 + 2 * jj) & 7][e], b_ = W[(4 * wr - 3 + 2 * jj) & 7][e];
            T[t][2 * e][j0 + jj] = plo(a_, b_); T[t][2 * e + 1][j0 + jj] = phi(a_, b_);
          }
#pragma unroll
        for (int e = 0; e < 8; e += 2) pin2(T[t][e], T[t][e + 1]);
      }
      SCHED();
    };
    step(B, B, IC<1>{}, IC<0>{}, IC<-1>{});
    step(A, B, IC<2>{}, IC<1>{}, IC<0>{});
    step(X, X, IC<0>{}, IC<0>{}, IC<-1>{});
    sfor<0, 4>([&](auto I) {
      step(B, A, IC<2 * I.v + 3>{}, IC<1>{}, IC<2 * I.v + 1>{});
      if constexpr (I.v == 3) {
        float* ml = reinterpret_cast<float*>(cnt + 32);
        ml[(c16 & 1) * 16 + w4 * 4 + r4] = (c16 & 1) ? l : m;
        COMPILER_FENCE();
        const unsigned got = lds_arrive_nw(L_CNT + 44);
        const float mr = ml[c16], lr = ml[16 + c16];
        SCHED();
        if (lds_done(got) == 3u) {
          const float M = row16_max(mr);
          const unsigned z = __float_as_uint(fmaf(M, C2, __builtin_amdgcn_logf(row16_sum(ex2((mr - M) * C2) * lr))));
          asm volatile("buffer_store_dwordx4 %0, %1, %2, 0 offen sc1\n\ts_nop 2" :: "v"(u32x4{z, tag, z, tag}), "v"(lane < 8 ? zo + cls * 128 + lane * 16 : OOB), "s"(rs) : "memory");
        }
        SCHED();
      }
      step(A, B, IC<2 * I.v + 4>{}, IC<1>{}, IC<2 * I.v + 2>{});
    });
    unsigned yoff = WS_GB + ((b * 128 + h * 4 + cls) * 256 + 64 * w4 + lane) * 8, rro = L_MRG + c16 * 272 + r4 * 16;
    rwo = L_MRG + (w4 * 4 + r4) * 272 + c16 * 16;
    asm volatile("" : "+v"(yoff), "+v"(rwo), "+v"(rro));
    if (cls == 0) lds_wait(cnt + 1, 4u);
    const int nit = (ns - 12) >> 1;
#pragma unroll 1
    for (int i = 0; i < nit; ++i) {
      step(B, A, IC<11>{}, IC<1>{}, IC<-1>{});
      step(A, B, IC<11>{}, IC<1>{}, IC<-1>{});
    }
    if (ns & 1) { step(B, A, IC<11>{}, IC<1>{}, IC<-1>{}); step(A, A, IC<11>{}, IC<2>{}, IC<-1>{}); }
    else step(B, B, IC<11>{}, IC<2>{}, IC<-1>{});
    {
      u32x4 af[4];
      unsigned got = lds_arrive_nw(L_CNT), need = 3u;
#pragma unroll 1
      for (int spin = 0; spin < SPIN_MAX; ++spin) {
#pragma unroll
        for (int t = 0; t < 4; ++t) af[t] = *reinterpret_cast<const u32x4*>(smem + rro + t * 64);
        SCHED();
        if (lds_done(got) >= need) break;
        need = 4u;
        __builtin_amdgcn_s_sleep(1);
        asm volatile("ds_read_b32 %0, %1" : "=v"(got) : "v"((unsigned)L_CNT) : "memory");
      }
      SCHED();
      f32x4 d[8];
      float a[8], v4[4];
      sfor<0, 5>([&](auto I) {
        constexpr int i = I.v;
        sfor<0, 8>([&](auto J) {
          constexpr int e = i + 4 * (J.v & 1), t = J.v >> 1;
          if constexpr (i < 4) { d[e] = __builtin_amdgcn_mfma_f32_16x16x32_bf16(__builtin_bit_cast(bf16x8_t, af[t]), __builtin_bit_cast(bf16x8_t, T[t][e]), t ? d[e] : f32x4{0.f, 0.f, 0.f, 0.f}, 0, 0, 0); SCHED(); }
          if constexpr (i >= 1 && (J.v == 4 || J.v == 5)) { const f32x2 u = f32x2{d[e - 1][0], d[e - 1][1]} + f32x2{d[e - 1][2], d[e - 1][3]}; a[e - 1] = u[0] + u[1]; asm volatile("" : "+v"(a[e - 1])); }
          if constexpr (i >= 1 && J.v == 6) v4[i - 1] = rs32(a[i - 1], a[i + 3]);
          if constexpr (i < 4) SCHED();
        });
      });
      st8c(rs, yoff, u32x2{pk_bf(rs16(v4[0], v4[2]), rs16(v4[1], v4[3])), tag});
    }
    STAMP(2);
    STAMP_FLUSH(1);
    TR_FLUSH();
    return;
  }
  const rsrc_t ro = __builtin_amdgcn_make_buffer_rsrc(out, 0, 4096, 0x00020000);
  auto fin = [&](auto NL, unsigned base, unsigned col, bool bump) {
    constexpr int nl = NL.v;
    unsigned doff = base + (((idx >> 5) * 4 + w4) * 16 * nl + (lane >> 2)) * 2048 + (idx & 31) * 64 + (lane & 3) * 16;
    unsigned fwo = 32768 + (c16 < 4 ? (w4 * 16 + 4 * c16 + r4) * 4 : 256 + lane * 4);
    unsigned fro = 32768 + (lane & 7) * 8, foo = lane < 8 ? ((idx >> 5) * 1024 + col + (nl == 2 ? 16 * (idx & 24) + 64 * (idx & 1) + (idx & 6) + 8 * lane : 16 * (idx & 31) + 2 * lane)) * 2 : OOB;
    asm volatile("" : "+v"(doff), "+v"(fwo), "+v"(fro), "+v"(foo));
    lds_wait(cnt + 0, 4u);
    STAMP(4);
    u32x4 d[nl];
    unsigned eh = 1u;
#pragma unroll 1
    for (int spin = SPIN_MAX; spin > 0; --spin) {
      COMPILER_FENCE();
      if (eh != 0u) {
#pragma unroll
        for (int j = 0; j < nl; ++j) d[j] = ld16c(rs, doff + j * 32768);
        eh = 0u;
#pragma unroll
        for (int j = 0; j < nl; ++j) { eh |= (d[j][1] ^ tag) | (d[j][3] ^ tag); asm volatile("" : "+v"(eh)); }
      }
      if (__builtin_amdgcn_ballot_w64(eh != 0u) == 0ull) break;
    }
    STAMP(11);
    float y[4] = {bflo(d[0][0]), bfhi(d[0][0]), bflo(d[0][2]), bfhi(d[0][2])};
    if constexpr (nl == 2) { y[0] += bflo(d[1][0]); y[1] += bfhi(d[1][0]); y[2] += bflo(d[1][2]); y[3] += bfhi(d[1][2]); }
    float t = rs16(rs32(y[0], y[2]), rs32(y[1], y[3]));
    t += dpp<0x124>(t); t += dpp<0x128>(t);
    *reinterpret_cast<float*>(smem + fwo) = t;
    COMPILER_FENCE();
    const unsigned got = lds_arrive_nw(L_CNT + 24);
    f32x2 t2[4];
#pragma unroll
    for (int w = 0; w < 4; ++w) t2[w] = *reinterpret_cast<const f32x2*>(smem + fro + w * 64);
    SCHED();
    if (lds_done(got) == 3u) {
      const f32x2 r = (t2[0] + t2[1]) + (t2[2] + t2[3]);
      __builtin_amdgcn_raw_buffer_store_b32(pk_bf(r[0], r[1]), ro, (int)foo, 0, 0);
      if (bump) st8c(rs, lane == 0 ? WS_EP : OOB, u32x2{tlow, tag});
    }
  };
  u32x4 Wa[16], Wb[16], xv[4];
  sfor<0, 2>([&](auto I) { Wa[I.v] = ld16nt(pa + ao); SCHED(); ao += sa; asm volatile("" : "+v"(ao)); });
  sfor<0, 4>([&](auto J) { xv[J.v] = ld16(xp + xo + (J.v >> 1) * 1024 + 16 * (J.v & 1)); });
  SCHED();
  sfor<2, 8>([&](auto I) { Wa[I.v] = ld16nt(pa + ao); SCHED(); ao += sa; asm volatile("" : "+v"(ao)); });
  epw = ld8c(rs, WS_EP);
  const unsigned qa_off = WS_QA + (((c16 >> 1) & 1) * 32 + h) * 1024 + (b * 256 + w4 * 64 + 16 * r4 + 8 * (c16 & 1)) * 2;
  st16c(rs, cls == 1 && c16 < 4 ? qa_off : OOB, u32x4{0u, 0u, 0u, 0u});
  SCHED();
  sfor<8, 16>([&](auto I) { Wa[I.v] = ld16nt(pa + ao); SCHED(); ao += sa; asm volatile("" : "+v"(ao)); });
  const u16* pb = cls == 0 ? reinterpret_cast<const u16*>(pa) + ((size_t)idx * 128 + 64 + w4) * 512 + 8 * lane
                           : (cls == 2 ? w_uv + ((size_t)(b * 256 + w4 * 64 + 16 * r4) * 32 + h) * 128 + 8 * c16 : (cls == 1 ? w_o : wo) + ((size_t)h * 128 + b * 64 + w4 * 2) * 512 + 8 * lane);
  {
    rawbar();
    take_epoch();
    cnt[16] = tag;
    COMPILER_FENCE();
    if (blk < 8 && w4 == 0) asm volatile("buffer_store_dwordx4 %0, %1, %2, 0 offen sc1\n\ts_nop 2" :: "v"(u32x4{tstart, tag, tstart, tag}), "v"(WS_T0 + blk * 1024 + lane * 16), "s"(rs) : "memory");
  }
  STAMP(13);
  float* red = reinterpret_cast<float*>(smem);
  if (cls & 1) {
    float acc[2][8];
#pragma unroll
    for (int bb = 0; bb < 2; ++bb)
#pragma unroll
      for (int k = 0; k < 8; ++k) acc[bb][k] = 0.f;
    sfor<0, 8>([&](auto IP) {
      constexpr int ip = IP.v, j = ip - (8 - WBW);
      use2(Wa[2 * ip], Wa[2 * ip + 1]);
      if constexpr (ip & 1) TR();
      if constexpr (j >= 0) { Wb[2 * j] = ld16(pb + j * 4096); Wb[2 * j + 1] = ld16(pb + j * 4096 + 512); }
      SCHED();
      const unsigned xp0 = xv[ip >> 2][ip & 3], xp1 = xv[2 + (ip >> 2)][ip & 3];
#pragma unroll
      for (int k = 0; k < 4; ++k) {
        const unsigned lo = plo(Wa[2 * ip][k], Wa[2 * ip + 1][k]), hi = phi(Wa[2 * ip][k], Wa[2 * ip + 1][k]);
        acc[0][2 * k] = dot2(lo, xp0, acc[0][2 * k]); acc[0][2 * k + 1] = dot2(hi, xp0, acc[0][2 * k + 1]);
        acc[1][2 * k] = dot2(lo, xp1, acc[1][2 * k]); acc[1][2 * k + 1] = dot2(hi, xp1, acc[1][2 * k + 1]);
      }
      SCHED();
    });
    {
      float v8[8], v4[4];
#pragma unroll
      for (int k = 0; k < 8; ++k) v8[k] = rs32(acc[0][k], acc[1][k]);
#pragma unroll
      for (int k = 0; k < 4; ++k) v4[k] = rs16(v8[k], v8[k + 4]);
      *reinterpret_cast<f32x4*>(red + (w4 * 2 + (lane >> 5)) * 128 + 8 * c16 + 4 * (r4 & 1)) = f32x4{v4[0], v4[1], v4[2], v4[3]};
    }
    COMPILER_FENCE();
    const unsigned got = lds_arrive_nw(L_CNT + 8);
    f32x4 t4[4];
#pragma unroll
    for (int w = 0; w < 4; ++w) t4[w] = *reinterpret_cast<const f32x4*>(red + (w * 2 + (lane >> 5)) * 128 + 8 * c16 + 4 * (r4 & 1));
    SCHED();
    if (lds_done(got) == 3u) {
      const f32x4 t = (t4[0] + t4[1]) + (t4[2] + t4[3]);
      st16c(rs, WS_QX + (((cls >> 1) * 32 + h) * 2 + (lane >> 5)) * 1024 + b * 512 + (lane & 31) * 16, u32x4{pk_bf(t[0], t[1]), tag, pk_bf(t[2], t[3]), tag});
      STAMP(15);
      SCHED();
      lds_add(cnt + 2, 1u, lane);
    } else lds_wait(cnt + 2, 5u);
    sfor<WBW, 8>([&](auto J) {
      constexpr int j = J.v;
      use2(Wb[2 * (j - WBW)], Wb[2 * (j - WBW) + 1]);
      if constexpr ((j - WBW) & 1) TR();
      Wb[2 * j] = ld16(pb + j * 4096); Wb[2 * j + 1] = ld16(pb + j * 4096 + 512);
      if constexpr (j == 7) lds_add(cnt + 14, 1u, lane);
      SCHED();
    });
    {
      const unsigned tw = __builtin_nontemporal_load(reinterpret_cast<const unsigned*>(out + (idx >> 5) * 1024 + (cls >> 1) * 512 + 16 * (idx & 31)));
      sfor<8 - WBW, 8>([&](auto J) { use2(Wb[2 * J.v], Wb[2 * J.v + 1]); if constexpr (J.v & 1) TR(); });
      asm volatile("" :: "v"(tw));
    }
    STAMP(8);
  } else if (cls == 2) {
    sfor<0, 16 - WBW>([&](auto IP) {
      constexpr int ip = IP.v, j = ip - (8 - WBW);
      if constexpr (ip < 8) use2(Wa[2 * ip], Wa[2 * ip + 1]); else use2(Wb[2 * ip - 16], Wb[2 * ip - 15]);
      if constexpr (ip & 1) TR();
      if constexpr (j >= 0) { Wb[2 * j] = ld16nt(pb + j * 8192); Wb[2 * j + 1] = ld16nt(pb + j * 8192 + 4096); }
      SCHED();
    });
    u32x4 g0, g1;
    const unsigned qo = WS_QX + h * 2048 + lane * 16;
    shint<512, 1>(ws + WS_QX + h * 2048, tag);
    STAMP(5);
#pragma unroll 1
    for (int spin = 0; spin < SPIN_MAX; ++spin) {
      COMPILER_FENCE();
      g0 = ld16c(rs, qo); g1 = ld16c(rs, qo + 1024);
      SCHED();
      lds_add(cnt + 14, spin == 0, lane);
      SCHED();
      if (__all((g0[1] == tag) & (g0[3] == tag) & (g1[1] == tag) & (g1[3] == tag))) break;
      __builtin_amdgcn_s_sleep(1);
    }
    sfor<8 - WBW, 8>([&](auto J) { if constexpr (J.v & 1) TR(); });
    const u32x4 q0 = qsum(g0), q1 = qsum(g1);
    STAMP(9);
    float v[8];
    {
      float u[16];
#pragma unroll
      for (int i = 0; i < 16; ++i) {
        float a = 0.f, c = 0.f;
#pragma unroll
        for (int k = 0; k < 4; ++k) { a = dot2(Wa[i][k], q0[k], a); c = dot2(Wa[i][k], q1[k], c); }
        u[i] = (c16 & 2 ? c : a) + dpp<0x4E>(c16 & 2 ? a : c);
        asm volatile("" : "+v"(u[i]));
      }
#pragma unroll
      for (int i = 0; i < 8; ++i) {
        float t = (c16 & 1 ? u[i + 8] : u[i]) + dpp<0xB1>(c16 & 1 ? u[i] : u[i + 8]);
        asm volatile("" : "+v"(t));
        t += dpp<0x124>(t);
        asm volatile("" : "+v"(t));
        t += dpp<0x128>(t);
        asm volatile("" : "+v"(t));
        v[i] = t;
      }
    }
    u32x4 o = {pk_bf(v[0], v[1]), pk_bf(v[2], v[3]), pk_bf(v[4], v[5]), pk_bf(v[6], v[7])};
    if ((o[0] | o[1]) == 0u) o[0] = 0x8000u;
    if ((o[2] | o[3]) == 0u) o[2] = 0x8000u;
    if (c16 < 4) st16c(rs, qa_off, o);
    STAMP(10);
  }
  asm volatile("" :: "v"(xv[0]), "v"(xv[1]), "v"(xv[2]), "v"(xv[3]));
  SCHED();
  if (cls == 3) {
    fin(IC<2>{}, WS_GB, 512, false);
    STAMP(6);
    STAMP_FLUSH(0); TR_FLUSH();
    return;
  }
  if (cls == 0) {
    char* scr = smem + L_SCR;
    const int ab = idx >> 5, r = lane & 31, hh = lane >> 5;
    const unsigned fo = WS_QX + (w4 * 8 + (c16 >> 1)) * 2048 + (c16 & 1) * 512 + r4 * 8;
    u32x2 fl;
    u32x4 g[9];
    const unsigned o8 = WS_QA + (ab * 32 + w4 * 8) * 1024 + lane * 16, ol = WS_QA + (ab * 32 + r) * 1024 + 992 + 16 * hh;
    auto put = [&](int i, const u32x4& v) { *reinterpret_cast<u32x4*>(smem + ((i >> 3) * 4 + r4) * 8192 + toff(4 * (i & 7) + w4, c16)) = v; };
    sfor<0, 16>([&](auto I) { put(I.v, Wa[I.v]); if constexpr ((I.v & 3) == 3) TR(); SCHED(); });
    until(tstart + PAR(26, T0B));
    sfor<0, 16>([&](auto I) { Wb[I.v] = ld16nt(pb + I.v * 2048); });
    SCHED();
    lds_add(cnt + 14, 1u, lane);
    fl = ld8c(rs, fo);
    SCHED();
    sfor<0, 16>([&](auto I) { put(16 + I.v, Wb[I.v]); if constexpr ((I.v & 3) == 3) TR(); SCHED(); });
    STAMP(8);
    u32x4 qlast;
    {
      if (!__all(fl[1] == tag)) {
#pragma unroll 1
        for (int spin = 0; spin < SPIN_MAX; ++spin) {
          COMPILER_FENCE();
          if (__all(ld8c(rs, fo)[1] == tag)) break;
        }
      }
      {
        const char* hp = ws + WS_QA + ab * 32768 + w4 * 8192 + ((idx + w4 * 11) & 31) * 128;
#pragma unroll 1
        for (int spin = 0; spin < SPIN_MAX; ++spin) {
          u32x2 a, c;
          sld2<4096>(hp, a, c);
          if ((a[0] | a[1]) != 0u && (c[0] | c[1]) != 0u) break;
          __builtin_amdgcn_s_sleep(2);
        }
      }
      STAMP(5);
      sbar4(cnt + 7, lane);
      STAMP(12);
      unsigned pend = 0x1ffu;
#pragma unroll 1
      for (int spin = 0; spin < SPIN_MAX && pend; ++spin) {
        COMPILER_FENCE();
        u32x4 t[9];
#pragma unroll
        for (int i = 0; i < 9; ++i) t[i] = ld16c(rs, (pend >> i & 1) ? (i < 8 ? o8 + i * 1024 : ol) : OOB);
#pragma unroll
        for (int i = 0; i < 9; ++i) {
          if (pend >> i & 1) g[i] = t[i];
          if (__all(nz8(t[i]))) pend &= ~(1u << i);
        }
      }
      STAMP(9);
      qlast = g[8];
#pragma unroll
      for (int i = 0; i < 8; ++i)
        if (lane < 62) *reinterpret_cast<u32x4*>(scr + (w4 * 8 + i) * 1008 + 16 * lane) = g[i];
    }
    sbar4(cnt + 3, lane);
    STAMP(10);
    f32x16 sacc;
#pragma unroll
    for (int i = 0; i < 16; ++i) sacc[i] = 0.f;
#pragma unroll
    for (int jj = 0; jj < 32; ++jj) {
      const u32x4 af = *reinterpret_cast<const u32x4*>(smem + (w4 * 4 + (jj >> 3)) * 8192 + toff(r, 2 * (jj & 7) + hh));
      const u32x4 bf = jj < 31 ? *reinterpret_cast<const u32x4*>(scr + r * 1008 + 16 * (2 * (jj < 31 ? jj : 0) + hh)) : qlast;
      sacc = __builtin_amdgcn_mfma_f32_32x32x16_bf16(__builtin_bit_cast(bf16x8_t, af), __builtin_bit_cast(bf16x8_t, bf), sacc, 0, 0, 0);
    }
    float m = sacc[0];
#pragma unroll
    for (int i = 1; i < 16; ++i) m = fmaxf(m, sacc[i]);
    m = xhalf_max(m);
    if (hh == 0) *reinterpret_cast<float*>(scr + r * 1008 + 992 + 4 * w4) = m;
    sbar4(cnt + 4, lane);
    STAMP(11);
    {
      const f32x4 m4 = *reinterpret_cast<const f32x4*>(scr + r * 1008 + 992);
      m = fmaxf(fmaxf(m4[0], m4[1]), fmaxf(m4[2], m4[3]));
    }
    const float mc = m * C2;
    float p[16], ls = 0.f;
#pragma unroll
    for (int i = 0; i < 16; ++i) { p[i] = ex2(fmaf(sacc[i], C2, -mc)); ls += p[i]; }
    ls = xhalf_sum(ls);
    sbar4(cnt + 5, lane);
    u32x4* pf = reinterpret_cast<u32x4*>(scr);
    float* wl = reinterpret_cast<float*>(scr + 8192);
    if (hh == 0) wl[r * 4 + w4] = ls;
    pf[(2 * w4) * 64 + lane] = u32x4{pk_bf(p[0], p[1]), pk_bf(p[2], p[3]), pk_bf(p[4], p[5]), pk_bf(p[6], p[7])};
    pf[(2 * w4 + 1) * 64 + lane] = u32x4{pk_bf(p[8], p[9]), pk_bf(p[10], p[11]), pk_bf(p[12], p[13]), pk_bf(p[14], p[15])};
    sbar4(cnt + 6, lane);
    u32x4 pb8[8];
#pragma unroll
    for (int ks = 0; ks < 8; ++ks) pb8[ks] = pf[ks * 64 + lane];
    const f32x4 l4 = *reinterpret_cast<const f32x4*>(wl + r * 4);
    const float lsum = (l4[0] + l4[1]) + (l4[2] + l4[3]), rl = __builtin_amdgcn_rcpf(lsum);
    const int qq = (lane & 15) >> 2, pp = lane & 3, blk2 = (lane >> 4) & 1;
    const unsigned tw = tstart + __builtin_amdgcn_readfirstlane(cnt[13]) + PAR(13, TW), th = tw - PAR(14, HL);
    u32x4 vv[2][4];
    bool held = false;
    char* ot = scr + 12288 + w4 * 4096;
#pragma unroll
    for (int c2 = 0; c2 < 2; ++c2) {
      f32x16 oacc[2];
#pragma unroll
      for (int ct = 0; ct < 2; ++ct)
#pragma unroll
        for (int i = 0; i < 16; ++i) oacc[ct][i] = 0.f;
#pragma unroll
      for (int ks = 0; ks < 8; ++ks)
#pragma unroll
        for (int ct = 0; ct < 2; ++ct) {
          typedef __attribute__((address_space(3))) s16x4* lp;
          const char* tile = smem + ((ks >> 1) * 4 + w4) * 8192;
          const int cc = 2 * c2 + ct;
          const s16x4 lo = __builtin_amdgcn_ds_read_tr16_b64_v4i16((lp)(tile + toff(16 * (ks & 1) + 4 * hh + qq, 4 * cc + 2 * blk2 + (pp >> 1)) + 8 * (pp & 1)));
          const s16x4 hi = __builtin_amdgcn_ds_read_tr16_b64_v4i16((lp)(tile + toff(16 * (ks & 1) + 8 + 4 * hh + qq, 4 * cc + 2 * blk2 + (pp >> 1)) + 8 * (pp & 1)));
          const s16x8 afr = __builtin_shufflevector(lo, hi, 0, 1, 2, 3, 4, 5, 6, 7);
          oacc[ct] = __builtin_amdgcn_mfma_f32_32x32x16_bf16(__builtin_bit_cast(bf16x8_t, afr), __builtin_bit_cast(bf16x8_t, pb8[ks]), oacc[ct], 0, 0, 0);
        }
#pragma unroll
      for (int ct = 0; ct < 2; ++ct)
#pragma unroll
        for (int g = 0; g < 4; ++g)
          *reinterpret_cast<u32x2*>(ot + r * 128 + 16 * ((4 * ct + g) ^ (r & 7)) + 8 * hh) = u32x2{pk_bf(oacc[ct][4 * g] * rl, oacc[ct][4 * g + 1] * rl), pk_bf(oacc[ct][4 * g + 2] * rl, oacc[ct][4 * g + 3] * rl)};
      if (c2 == 0) { held = (int)((unsigned)wall_clock64() + 30u - th) >= 0; if (held) lds_add(cnt + 8, 1u, lane); }
      __builtin_amdgcn_wave_barrier();
#pragma unroll
      for (int i = 0; i < 4; ++i) { vv[c2][i] = *reinterpret_cast<const u32x4*>(ot + (8 * i + (lane >> 3)) * 128 + 16 * ((lane ^ (lane >> 3)) & 7)); asm volatile("" : "+v"(vv[c2][i])); }
      __builtin_amdgcn_wave_barrier();
    }
    lds_arrive(cnt + 1, lane);
    if (!held) { until(th); lds_add(cnt + 8, 1u, lane); }
    until(tw);
#pragma unroll
    for (int i = 0; i < 8; ++i) st16c(rs, WS_APO + ((((8 * (i >> 1) + (lane >> 3)) * 2 + (w4 >> 1)) * 64 + idx) * 256 + (w4 & 1) * 128 + 64 * (i & 1) + 8 * (lane & 7)) * 2, vv[i & 1][i >> 1]);
    STAMP(1);
    asm volatile("s_waitcnt vmcnt(0)" ::: "memory");
    if (lds_arrive(cnt + 9 + (w4 >> 1), lane) == 1u && hh == 0)
      st8c(rs, WS_AML + ((r * 2 + (w4 >> 1)) * 64 + idx) * 8, u32x2{__float_as_uint(mc + __builtin_amdgcn_logf(lsum)), tag});
    lds_add(cnt + 8, ~0u, lane);
    STAMP(2);
    STAMP_FLUSH(0); TR_FLUSH();
    return;
  }
#pragma unroll
  for (int ip = 0; ip < 8; ++ip) {
#pragma unroll
    for (int k = 0; k < 4; ++k) { const unsigned lo = plo(Wb[2 * ip][k], Wb[2 * ip + 1][k]), hi = phi(Wb[2 * ip][k], Wb[2 * ip + 1][k]); Wb[2 * ip][k] = lo; Wb[2 * ip + 1][k] = hi; }
    pin2(Wb[2 * ip], Wb[2 * ip + 1]);
  }
  if (cls == 2) {
    float* red2 = red + 4 * 256;
    float* wml = red + 3072;
    u32x16 f[2];
    if (w4 == 0) {
#pragma unroll 1
      for (int spin = 0; spin < SPIN_MAX; ++spin) {
        const u32x16 t = sld64(ws + WS_AML + (h * 2 + b) * 512);
        bool any = false;
#pragma unroll
        for (int i = 0; i < 8; ++i) any |= t[2 * i + 1] == tag;
        if (any) break;
        __builtin_amdgcn_s_sleep(8);
      }
      lds_add(cnt + 7, 1u, lane);
    } else lds_wait_slow(cnt + 7, 1u);
#pragma unroll 1
    for (int spin = 0; spin < SPIN_MAX; ++spin) {
      sld128(ws + WS_AML + ((h * 2 + b) * 64 + w4 * 16) * 8, f);
      unsigned e = 0u;
#pragma unroll
      for (int i = 0; i < 16; ++i) e |= f[i >> 3][2 * (i & 7) + 1] ^ tag;
      if (e == 0u) break;
      __builtin_amdgcn_s_sleep(2);
    }
    STAMP(3);
    u32x4 pp[8];
#pragma unroll
    for (int i = 0; i < 8; ++i) pp[i] = ld16c(rs, WS_APO + ((h * 2 + b) * 64 + w4 * 16 + 2 * i) * 512 + lane * 16);
    SCHED();
    float wt[8];
    {
      float M = -INFINITY, L = 0.f;
#pragma unroll
      for (int i = 0; i < 8; ++i) {
        unsigned z0 = f[i >> 2][4 * (i & 3)], z1 = f[i >> 2][4 * (i & 3) + 2];
        asm("" : "+s"(z0), "+s"(z1));
        wt[i] = __uint_as_float(lane < 32 ? z0 : z1); M = fmaxf(M, wt[i]);
      }
      M = xhalf_max(M);
#pragma unroll
      for (int i = 0; i < 8; ++i) { wt[i] = ex2(wt[i] - M); L += wt[i]; }
      L = xhalf_sum(L);
      if (lane == 0) *reinterpret_cast<f32x2*>(wml + 2 * w4) = f32x2{M, L};
    }
    SCHED();
    float a8[8];
#pragma unroll
    for (int k = 0; k < 8; ++k) a8[k] = 0.f;
#pragma unroll
    for (int i = 0; i < 8; ++i) {
#pragma unroll
      for (int k = 0; k < 4; ++k) { a8[2 * k] = fmaf(wt[i], bflo(pp[i][k]), a8[2 * k]); a8[2 * k + 1] = fmaf(wt[i], bfhi(pp[i][k]), a8[2 * k + 1]); }
      asm volatile("" : "+v"(a8[0]), "+v"(a8[1]), "+v"(a8[2]), "+v"(a8[3]), "+v"(a8[4]), "+v"(a8[5]), "+v"(a8[6]), "+v"(a8[7]));
    }
    STAMP(6);
    *reinterpret_cast<f32x4*>(red + w4 * 256 + 8 * (lane & 31) + 4 * (lane >> 5)) = f32x4{rs32(a8[0], a8[4]), rs32(a8[1], a8[5]), rs32(a8[2], a8[6]), rs32(a8[3], a8[7])};
    sbar4(cnt + 3, lane);
    STAMP(4);
    float acc[2][8];
#pragma unroll
    for (int bb = 0; bb < 2; ++bb) {
      u32x4 cv[2];
      const f32x4 ml = *reinterpret_cast<const f32x4*>(wml + 4 * bb);
      const float M = fmaxf(ml[0], ml[2]), e0 = ex2(ml[0] - M), e1 = ex2(ml[2] - M), rl = 1.f / fmaf(e0, ml[1], e1 * ml[3]), s0 = e0 * rl, s1 = e1 * rl;
#pragma unroll
      for (int e = 0; e < 2; ++e) {
        const float* src = red + bb * 512 + w4 * 64 + 16 * r4 + 8 * e;
        const f32x4 t0 = s0 * *reinterpret_cast<const f32x4*>(src) + s1 * *reinterpret_cast<const f32x4*>(src + 256), t1 = s0 * *reinterpret_cast<const f32x4*>(src + 4) + s1 * *reinterpret_cast<const f32x4*>(src + 260);
        cv[e] = u32x4{pk_bf(t0[0], t0[1]), pk_bf(t0[2], t0[3]), pk_bf(t1[0], t1[1]), pk_bf(t1[2], t1[3])};
      }
#pragma unroll
      for (int k = 0; k < 8; ++k) acc[bb][k] = 0.f;
#pragma unroll
      for (int ip = 0; ip < 8; ++ip)
#pragma unroll
        for (int k = 0; k < 4; ++k) { acc[bb][2 * k] = dot2(Wb[2 * ip][k], cv[ip >> 2][ip & 3], acc[bb][2 * k]); acc[bb][2 * k + 1] = dot2(Wb[2 * ip + 1][k], cv[ip >> 2][ip & 3], acc[bb][2 * k + 1]); }
    }
    {
      float v8[8], v4[4];
#pragma unroll
      for (int k = 0; k < 8; ++k) v8[k] = rs32(acc[0][k], acc[1][k]);
#pragma unroll
      for (int k = 0; k < 4; ++k) v4[k] = rs16(v8[k], v8[k + 4]);
      *reinterpret_cast<f32x4*>(red2 + (w4 * 2 + (lane >> 5)) * 128 + 8 * c16 + 4 * (r4 & 1)) = f32x4{v4[0], v4[1], v4[2], v4[3]};
    }
    {
      COMPILER_FENCE();
      const unsigned got = lds_arrive_nw(L_CNT + 16);
      f32x4 t4[4];
#pragma unroll
      for (int w = 0; w < 4; ++w) t4[w] = *reinterpret_cast<const f32x4*>(red2 + w * 256 + 4 * lane);
      SCHED();
      if (lds_done(got) == 3u) {
        const f32x4 t = (t4[0] + t4[1]) + (t4[2] + t4[3]);
        const unsigned co = WS_CX + ((h * 2 + b) * 256 + 64 * r4 + 32 * (c16 & 1) + 2 * (c16 >> 1)) * 8;
        st16c(rs, co, u32x4{__float_as_uint(t[0]), tag, __float_as_uint(t[1]), tag});
        st16c(rs, co + 128, u32x4{__float_as_uint(t[2]), tag, __float_as_uint(t[3]), tag});
      }
    }
    STAMP(12);
    STAMP_FLUSH(0); TR_FLUSH();
    return;
  }
  {
    unsigned op;
    {
      u32x4 c;
      const unsigned co = WS_CX + ((h * 2 + ((lane >> 3) & 1)) * 256 + (lane >> 5) * 128 + b * 64 + w4 * 16 + 2 * (lane & 7)) * 8;
      if (idx == 0 && w4 == 1) {
#pragma unroll 1
        for (int spin = 0; spin < SPIN_MAX; ++spin) {
          COMPILER_FENCE();
          if (__all(ld8c(rs, WS_QX + 65536 + (lane >> 1) * 2048 + (lane & 1) * 512)[1] == tag)) break;
          __builtin_amdgcn_s_sleep(8);
        }
      }
      if (w4 == 0) { shint<2048, 4>(ws + WS_CX + (h * 512 + b * 64) * 8, tag); lds_add(cnt + 7, 1u, lane); }
      else lds_wait_slow(cnt + 7, 1u);
      STAMP(3);
#pragma unroll 1
      for (int spin = 0; spin < SPIN_MAX; ++spin) {
        COMPILER_FENCE();
        c = ld16c(rs, co);
        if (__all((c[1] == tag) & (c[3] == tag))) break;
        __builtin_amdgcn_s_sleep(1);
      }
      float o0 = __uint_as_float(c[0]), o1 = __uint_as_float(c[2]);
      o0 += dpp<0x128>(o0); o1 += dpp<0x128>(o1);
      op = pk_bf(o0, o1);
    }
    STAMP(14);
    float* red3 = red + 4096;
    float a[2][8];
#pragma unroll
    for (int bb = 0; bb < 2; ++bb)
#pragma unroll
      for (int k = 0; k < 8; ++k) a[bb][k] = 0.f;
#pragma unroll
    for (int ip = 0; ip < 8; ++ip) {
      const unsigned v0 = __builtin_amdgcn_readlane(op, ip), v1 = __builtin_amdgcn_readlane(op, 32 + ip);
#pragma unroll
      for (int k = 0; k < 4; ++k) {
        a[0][2 * k] = dot2(Wb[2 * ip][k], v0, a[0][2 * k]); a[0][2 * k + 1] = dot2(Wb[2 * ip + 1][k], v0, a[0][2 * k + 1]);
        a[1][2 * k] = dot2(Wb[2 * ip][k], v1, a[1][2 * k]); a[1][2 * k + 1] = dot2(Wb[2 * ip + 1][k], v1, a[1][2 * k + 1]);
      }
    }
#pragma unroll
    for (int bb = 0; bb < 2; ++bb) {
      *reinterpret_cast<f32x4*>(red3 + (w4 * 2 + bb) * 512 + 8 * lane) = f32x4{a[bb][0], a[bb][1], a[bb][2], a[bb][3]};
      *reinterpret_cast<f32x4*>(red3 + (w4 * 2 + bb) * 512 + 8 * lane + 4) = f32x4{a[bb][4], a[bb][5], a[bb][6], a[bb][7]};
    }
    sbar4(cnt + 5, lane);
    f32x4 t = {0.f, 0.f, 0.f, 0.f};
#pragma unroll
    for (int w = 0; w < 4; ++w) t += *reinterpret_cast<const f32x4*>(red3 + w * 1024 + 4 * tid);
    st16c(rs, WS_GA + ((tid >> 7) * 64 + idx) * 2048 + (tid & 127) * 16, u32x4{pk_bf(t[0], t[1]), tag, pk_bf(t[2], t[3]), tag});
    STAMP(5);
  }
  fin(IC<1>{}, WS_GA, 0, idx == 0);
  STAMP(6);
  STAMP_FLUSH(0); TR_FLUSH();
}

extern "C" void kernel_launch(void* const* d_in, const int* in_sizes, int n_in, void* d_out, int out_size, void* d_ws, size_t ws_size, hipStream_t stream) {
  const u16* xA = (const u16*)d_in[0];
  const u16* ckv = (const u16*)d_in[1];
  const u16* w_q = (const u16*)d_in[2];
  const u16* w_uk = (const u16*)d_in[3];
  const u16* w_uv = (const u16*)d_in[4];
  const u16* w_o = (const u16*)d_in[5];
  const u16* xB = (const u16*)d_in[6];
  const u16* kc = (const u16*)d_in[7];
  const u16* vc = (const u16*)d_in[8];
  const u16* wq = (const u16*)d_in[9];
  const u16* wo = (const u16*)d_in[10];
  kmain<<<256, 512, 0, stream>>>(ckv, w_q, w_uk, wq, xA, xB, w_uv, w_o, kc, vc, wo, (char*)d_ws, (u16*)d_out);
}
```

```cpp
#include <hip/hip_runtime.h>
#include <hip/hip_bf16.h>
#include <stdint.h>

#define DI __device__ __forceinline__
typedef __attribute__((ext_vector_type(4))) unsigned u32x4;
typedef __attribute__((ext_vector_type(2))) unsigned u32x2;
typedef __attribute__((ext_vector_type(4))) float f32x4;
typedef __attribute__((ext_vector_type(2))) float f32x2;
typedef __attribute__((ext_vector_type(16))) float f32x16;
typedef __attribute__((ext_vector_type(8))) __bf16 bf16x8_t;
typedef __attribute__((ext_vector_type(2))) __bf16 bf16x2_t;
typedef __attribute__((ext_vector_type(4))) short s16x4;
typedef __attribute__((ext_vector_type(8))) short s16x8;
typedef unsigned short u16;
typedef unsigned long long u64;

constexpr int NS0 = 13, NS1 = 17, NS2 = 16, NS3 = 18;
constexpr u64 TL0 = 0xc2b8de6543cb1c1ull, TH0 = 0x3de34ull, TL1 = 0x920718551349142ull, TH1 = 0x3eeb6caeaa7ull;
constexpr u64 TL2 = 0x96175a59238a183ull, TH2 = 0xff9d71b28ull, TL3 = 0x89f6d74d0308100ull, TH3 = 0xf3bdf3beda66ull;
constexpr int RA2 = 0xC0, RA3 = 3;
constexpr int AHL = 2;
constexpr int QPE = 5;
constexpr int WBW = 7;
#define TL 2375
#define TW 1640
constexpr unsigned TS0 = 1160, TS1 = 1440, TS2 = 1320, TS3 = 1440, STG = 12, BSG = 6, HL = 40, TP0 = TW + 170, TP2 = TW + 380, PA = 150, T0B = 440, T0Q = 730;
#define PAR(i, v) (v)
constexpr u64 LA2 = TH2 >> (6 * (NS2 - 11)), LA3 = TH3 >> (6 * (NS3 - 11));
constexpr u64 chunk_mask(u64 lo, u64 hi, int n) { u64 m = 0; for (int i = 0; i < n; ++i) m |= 1ull << ((i < 10 ? lo >> (6 * i) : hi >> (6 * (i - 10))) & 63); return m; }
static_assert((chunk_mask(TL0, TH0, NS0) | chunk_mask(TL1, TH1, NS1) | chunk_mask(TL2, TH2, NS2) | chunk_mask(TL3, TH3, NS3)) == ~0ull && (RA2 & RA3) == 0, "every chunk once");
static_assert(NS0 + NS1 + NS2 + NS3 == 64, "split sizes");
constexpr int NSMIN = 13, NSMAX = 18;
static_assert(NS0 >= NSMIN && NS1 >= NSMIN && NS2 - 1 >= NSMIN && NS3 - 1 >= NSMIN && NS0 <= NSMAX && NS1 + 1 <= NSMAX && NS2 <= NSMAX && NS3 <= NSMAX, "range");
constexpr int SK = 4096;
constexpr float C2 = 0.08838834764831845f * 1.4426950408889634f;
constexpr u64 GOLD = 0x9E3779B97F4A7C15ull;

constexpr unsigned WS_EP = 0;
constexpr unsigned WS_QX = 4096;
constexpr unsigned WS_QA = WS_QX + 2 * 32 * 256 * 8;
constexpr unsigned WS_AML = WS_QA + 2 * 32 * 512 * 2;
constexpr unsigned WS_CX = WS_AML + 32 * 2 * 16 * 128;
constexpr unsigned WS_GZ = WS_CX + 32 * 2 * 2 * 128 * 8;
constexpr unsigned WS_T0 = WS_GZ + 256 * 128;
constexpr unsigned WS_GB = WS_T0 + 64 * 128;
constexpr unsigned WS_GA = WS_GB + 2 * 128 * 256 * 8;
constexpr unsigned WS_APO = 2u << 20;
static_assert(WS_GA + 64 * 2 * 256 * 8 <= WS_APO, "layout");
constexpr unsigned WS_END = WS_APO + 32 * 2 * 64 * 256 * 2;
#define STAMP(i) do {} while (0)
#define STAMP_DECL do {} while (0)
#define STAMP_FLUSH(grp) do {} while (0)

#define TR() do {} while (0)
#define TR_FLUSH() do {} while (0)
DI float bflo(unsigned u) { return __uint_as_float(u << 16); }
DI float bfhi(unsigned u) { return __uint_as_float(u & 0xffff0000u); }
DI unsigned pk_bf(float a, float b) { const f32x2 v = {a, b}; return __builtin_bit_cast(unsigned, __builtin_convertvector(v, bf16x2_t)); }
DI float dot2(unsigned k, unsigned q, float acc) {
  return __builtin_amdgcn_fdot2_f32_bf16(__builtin_bit_cast(bf16x2_t, k), __builtin_bit_cast(bf16x2_t, q), acc, false);
}
DI unsigned plo(unsigned w0, unsigned w1) { return __builtin_amdgcn_perm(w1, w0, 0x05040100u); }
DI unsigned phi(unsigned w0, unsigned w1) { return __builtin_amdgcn_perm(w1, w0, 0x07060302u); }
template <int CTRL> DI float dpp(float x) {
  return __builtin_bit_cast(float, __builtin_amdgcn_mov_dpp(__builtin_bit_cast(int, x), CTRL, 0xf, 0xf, true));
}
DI float row16_sum(float x) { x += dpp<0xB1>(x); x += dpp<0x4E>(x); x += dpp<0x141>(x); x += dpp<0x128>(x); return x; }
DI float row16_max(float x) { x = fmaxf(x, dpp<0xB1>(x)); x = fmaxf(x, dpp<0x4E>(x)); x = fmaxf(x, dpp<0x141>(x)); x = fmaxf(x, dpp<0x128>(x)); return x; }
DI float xhalf_max(float x) { auto t = __builtin_amdgcn_permlane32_swap(__float_as_uint(x), __float_as_uint(x), false, false); return fmaxf(__uint_as_float(t[0]), __uint_as_float(t[1])); }
DI float xhalf_sum(float x) { auto t = __builtin_amdgcn_permlane32_swap(__float_as_uint(x), __float_as_uint(x), false, false); return __uint_as_float(t[0]) + __uint_as_float(t[1]); }
DI u32x4 ld16(const void* p) { return *reinterpret_cast<const u32x4*>(p); }
DI u32x4 ld16nt(const void* p) { return __builtin_nontemporal_load(reinterpret_cast<const u32x4*>(p)); }
DI float ex2(float x) { return __builtin_amdgcn_exp2f(x); }
#define SCHED() __builtin_amdgcn_sched_barrier(0)

typedef __amdgpu_buffer_rsrc_t rsrc_t;
DI u32x4 ld16c(rsrc_t rs, unsigned off) { return __builtin_amdgcn_raw_buffer_load_b128(rs, (int)off, 0, 16); }
DI u32x2 ld8c(rsrc_t rs, unsigned off) { return __builtin_amdgcn_raw_buffer_load_b64(rs, (int)off, 0, 16); }
DI void st16c(rsrc_t rs, unsigned off, u32x4 v) { __builtin_amdgcn_raw_buffer_store_b128(v, rs, (int)off, 0, 16); }
DI void st8c(rsrc_t rs, unsigned off, u32x2 v) { __builtin_amdgcn_raw_buffer_store_b64(v, rs, (int)off, 0, 16); }
template <int O1> DI void sld2(const char* p, u32x2& a, u32x2& b) {
  asm volatile("s_load_dwordx2 %0, %2, 0x0 glc\n\ts_load_dwordx2 %1, %2, %3 glc\n\ts_waitcnt lgkmcnt(0)" : "=&s"(a), "=&s"(b) : "s"(p), "n"(O1) : "memory");
}
typedef __attribute__((ext_vector_type(8))) unsigned u32x8;
DI void sld4x32(const char* p, u32x8 (&v)[4]) {
  asm volatile("s_load_dwordx8 %0, %4, 0x0 glc\n\ts_load_dwordx8 %1, %4, 0x80 glc\n\ts_load_dwordx8 %2, %4, 0x100 glc\n\ts_load_dwordx8 %3, %4, 0x180 glc\n\ts_waitcnt lgkmcnt(0)"
               : "=&s"(v[0]), "=&s"(v[1]), "=&s"(v[2]), "=&s"(v[3]) : "s"(p) : "memory");
}
constexpr int SPIN_MAX = 1 << 20;
constexpr unsigned OOB = 0x7ffffff0u;
DI void rawbar() { __builtin_amdgcn_sched_barrier(0); __builtin_amdgcn_s_barrier(); __builtin_amdgcn_sched_barrier(0); }
#define COMPILER_FENCE() asm volatile("" ::: "memory")
template <int O1, int SL> DI void shint(const char* p, unsigned tag) {
#pragma unroll 1
  for (int spin = 0; spin < SPIN_MAX; ++spin) {
    u32x2 a, b;
    sld2<O1>(p, a, b);
    if (((a[1] ^ tag) | (b[1] ^ tag)) == 0u) break;
    __builtin_amdgcn_s_sleep(SL);
  }
}

template <int N> struct IC { static constexpr int v = N; };
template <int I, int N, class F> DI void sfor(F&& f) { if constexpr (I < N) { f(IC<I>{}); sfor<I + 1, N>(f); } }
typedef __attribute__((address_space(3))) unsigned lu32;
DI void lds_wait(unsigned* cnt, unsigned target) {
#pragma unroll 1
  for (int spin = 0; spin < SPIN_MAX; ++spin) {
    if (__builtin_amdgcn_readfirstlane(__hip_atomic_load((lu32*)cnt, __ATOMIC_RELAXED, __HIP_MEMORY_SCOPE_WORKGROUP)) >= target) break;
    __builtin_amdgcn_s_sleep(1);
  }
  COMPILER_FENCE();
}
DI void lds_wait_slow(unsigned* cnt, unsigned target) {
#pragma unroll 1
  for (int spin = 0; spin < SPIN_MAX; ++spin) {
    if (__builtin_amdgcn_readfirstlane(__hip_atomic_load((lu32*)cnt, __ATOMIC_RELAXED, __HIP_MEMORY_SCOPE_WORKGROUP)) >= target) break;
    __builtin_amdgcn_s_sleep(3);
  }
  COMPILER_FENCE();
}
template <int SL = 1> DI void until(unsigned t) {
#pragma unroll 1
  for (int spin = 0; spin < 1024; ++spin) { if ((int)((unsigned)wall_clock64() - t) >= 0) break; __builtin_amdgcn_s_sleep(SL); }
}
DI void lds_hold(unsigned* flag) {
#pragma unroll 1
  for (int spin = 0; spin < SPIN_MAX; ++spin) {
    if (__builtin_amdgcn_readfirstlane(__hip_atomic_load((lu32*)flag, __ATOMIC_RELAXED, __HIP_MEMORY_SCOPE_WORKGROUP)) == 0u) break;
    __builtin_amdgcn_s_sleep(2);
  }
}
DI unsigned lds_arrive_nw(unsigned addr) {
  unsigned old, one = 1u;
  asm volatile("s_mov_b64 exec, 1\n\tds_add_rtn_u32 %0, %1, %2\n\ts_mov_b64 exec, -1" : "=v"(old) : "v"(addr), "v"(one) : "memory");
  return old;
}
DI unsigned lds_done(unsigned old) { asm volatile("s_waitcnt lgkmcnt(0)" : "+v"(old) :: "memory"); return __builtin_amdgcn_readfirstlane(old); }
DI unsigned lds_arrive(unsigned* cnt, int) { return lds_done(lds_arrive_nw((unsigned)(size_t)(lu32*)cnt)); }
DI void lds_add(unsigned* cnt, unsigned v, int) {
  asm volatile("s_mov_b64 exec, 1\n\tds_add_u32 %0, %1\n\ts_mov_b64 exec, -1" :: "v"((unsigned)(size_t)(lu32*)cnt), "v"(v) : "memory");
}
DI void sbar4(unsigned* cnt, int lane) { if (lds_arrive(cnt, lane) < 3u) lds_wait(cnt, 4u); }
DI float rs32(float a, float b) { auto t = __builtin_amdgcn_permlane32_swap(__float_as_uint(a), __float_as_uint(b), false, false); return __uint_as_float(t[0]) + __uint_as_float(t[1]); }
DI float rs16(float a, float b) { auto t = __builtin_amdgcn_permlane16_swap(__float_as_uint(a), __float_as_uint(b), false, false); return __uint_as_float(t[0]) + __uint_as_float(t[1]); }
template <int N> DI unsigned rowbcast(unsigned x) { return (unsigned)__builtin_amdgcn_mov_dpp((int)x, 0x150 + N, 0xf, 0xf, true); }
DI void pin2(u32x4& a, u32x4& b) { asm volatile("" : "+v"(a), "+v"(b)); }
DI void use2(const u32x4& a, const u32x4& b) { asm volatile("" :: "v"(a), "v"(b)); }
DI bool nz8(const u32x4& v) { return ((v[0] | v[1]) != 0u) & ((v[2] | v[3]) != 0u); }

DI u32x4 qsum(const u32x4& g) {
  const auto ta = __builtin_amdgcn_permlane32_swap(g[0], g[0], false, false), tc = __builtin_amdgcn_permlane32_swap(g[2], g[2], false, false);
  const unsigned a = pk_bf(bflo(ta[0]) + bflo(ta[1]), bfhi(ta[0]) + bfhi(ta[1])), c = pk_bf(bflo(tc[0]) + bflo(tc[1]), bfhi(tc[0]) + bfhi(tc[1]));
  const auto ra = __builtin_amdgcn_permlane16_swap(a, a, false, false), rc = __builtin_amdgcn_permlane16_swap(c, c, false, false);
  return u32x4{ra[0], rc[0], ra[1], rc[1]};
}
DI unsigned toff(unsigned row, unsigned ch) { return 256u * row + 16u * (ch ^ (((row & 3) << 2) | ((row >> 2) & 3))); }
constexpr int SMEM = 160 * 1024;
constexpr int L_SCR = 131072;
constexpr int L_MRG = 65536;
constexpr int L_CNT = L_SCR + 32 * 1008;

struct KV { u32x4 r[8]; };

template <class T> DI T* gptr(unsigned lo, unsigned hi) { return (T*)(__attribute__((address_space(1))) T*)(((u64)hi << 32) | lo); }

__global__ __launch_bounds__(512, 2) void kmain(const u16*, const u16*, const u16*, const u16*, const u16*, const u16*, const u16*, const u16*, const u16*,
                                                const u16*, const u16*, char*, u16*) {
  u32x2 kpa, kpx, kc2; u32x4 kb4; u32x8 kb8; u64 t64;
  int tid = threadIdx.x, blk = blockIdx.x;
  const auto kp = __builtin_amdgcn_kernarg_segment_ptr();
  asm volatile("s_load_dwordx2 %0, %8, %9\n\ts_load_dwordx2 %1, %8, %10\n\ts_load_dwordx4 %2, %8, 0x30\n\ts_load_dwordx8 %3, %8, 0x40\n\ts_load_dwordx2 %4, %8, 0x60\n\ts_memrealtime %5"
               : "=&s"(kpa), "=&s"(kpx), "=&s"(kb4), "=&s"(kb8), "=&s"(kc2), "=&s"(t64), "+v"(tid), "+s"(blk) : "s"(kp), "s"(blk & 24), "s"(32 + ((blk >> 1) & 8)));
  __builtin_assume(tid >= 0 && tid < 512 && blk >= 0 && blk < 256);
  __shared__ __attribute__((aligned(16))) char smem[SMEM];
  const int lane = tid & 63;
  const int wave = __builtin_amdgcn_readfirstlane(tid >> 6), w4 = wave & 3;
  const int cls = (blk >> 3) & 3;
  int idx = 8 * (blk & 7) + (blk >> 5);
  idx = (idx & ~7) | ((idx + ((wave >= 4 ? 0 : 0x2400) >> (4 * cls))) & 7);
  const int h = idx >> 1, b = idx & 1;
  const int r4 = lane >> 4, c16 = lane & 15;
  unsigned* cnt = reinterpret_cast<unsigned*>(smem + L_CNT);
  STAMP_DECL;
  STAMP(0);
  const unsigned c0 = cls ? 0u : ~0u;
  unsigned sa = cls ? 8192u : 4096u;
  unsigned ao = ((((b * 256 + w4 * 64) * 8192 + h * 256) & ~c0) | (((idx * 128 + w4) * 1024) & c0)) + (r4 << (17u - (c0 & 9u))) + c16 * 16, xo = (b * 256 + w4 * 64 + 16 * r4) * 2;
  asm volatile("" : "+v"(ao), "+v"(xo), "+s"(sa));
  SCHED();
  asm volatile("s_waitcnt lgkmcnt(0)" : "+s"(kpa), "+s"(kpx), "+s"(kb4), "+s"(kb8), "+s"(kc2), "+s"(t64) : "s"(kp));
  const unsigned tstart = (unsigned)t64;
  const char* __restrict__ pa = gptr<const char>(kpa[0], kpa[1]); const char* __restrict__ xp = gptr<const char>(kpx[0], kpx[1]);
  const u16* __restrict__ w_uv = gptr<const u16>(kb4[0], kb4[1]); const u16* __restrict__ w_o = gptr<const u16>(kb4[2], kb4[3]);
  const u16* __restrict__ kc = gptr<const u16>(kb8[0], kb8[1]); const u16* __restrict__ vc = gptr<const u16>(kb8[2], kb8[3]);
  const u16* __restrict__ wo = gptr<const u16>(kb8[4], kb8[5]); char* __restrict__ ws = gptr<char>(kb8[6], kb8[7]);
  u16* __restrict__ out = gptr<u16>(kc2[0], kc2[1]);
  const rsrc_t rs = __builtin_amdgcn_make_buffer_rsrc(ws, 0, (int)WS_END, 0x00020000);
  unsigned tag, tlow;
  u32x2 epw;
  auto take_epoch = [&]() {
    asm volatile("" : "+v"(epw));
    const u64 t64 = (((u64)(unsigned)__builtin_amdgcn_readfirstlane(epw[1]) << 32) | (unsigned)__builtin_amdgcn_readfirstlane(epw[0])) + GOLD;
    tag = (unsigned)(t64 >> 32); tlow = (unsigned)t64;
    asm volatile("s_waitcnt lgkmcnt(0)" : "+s"(tag), "+s"(tlow) :: "memory");
  };
  if (wave >= 4) {
    if (tid - 256 < 16) cnt[tid - 256] = 0u;
    asm volatile("s_waitcnt lgkmcnt(0)" ::: "memory");
    const unsigned pTL = PAR(0, TL), pTS = PAR(1 + cls, cls == 0 ? TS0 : (cls == 1 ? TS1 : (cls == 2 ? TS2 : TS3))), pSTG = PAR(9, STG), pBSG = PAR(10, BSG);
    const unsigned pRA2 = PAR(11, RA2), pRA3 = PAR(12, RA3), pTP = PAR(15 + cls, cls == 0 ? TP0 : TP2), pPA = PAR(19 + cls, (cls & 1) ? 0u : PA);
    rawbar();
    const int e2 = (pRA2 >> (idx & 7)) & 1, e3 = (pRA3 >> (idx & 7)) & 1;
    const int ns = cls == 0 ? NS0 : (cls == 1 ? NS1 + e2 + e3 : (cls == 2 ? NS2 - e2 : NS3 - e3));
    const unsigned pTA = __builtin_amdgcn_readfirstlane((unsigned)((float)pTS * __builtin_amdgcn_rcpf((float)(ns - 5)) + 0.5f));
    const size_t kvo = ((size_t)(b * 32 + h) * SK + w4 * 16) * 256;
    const char* kb = reinterpret_cast<const char*>(kc) + kvo;
    const char* vb = reinterpret_cast<const char*>(vc) + kvo;
    const unsigned lo = lane * 16u;
    u64 tlo = cls == 0 ? TL0 : (cls == 1 ? TL1 : (cls == 2 ? TL2 : TL3));
    u64 thi = cls == 0 ? TH0 : (cls == 1 ? TH1 | ((e2 ? LA2 : LA3) << (6 * (NS1 - 10))) : (cls == 2 ? TH2 : TH3));
    const u64 tlo0 = tlo, thi0 = thi;
    int hn = 0;
    const char* hb = kb;
    const char* pc[2];
    auto next2 = [&]() {
#pragma unroll
      for (int i = 0; i < 2; ++i) {
        if (hn == ns) { tlo = tlo0; thi = thi0; hb = vb; }
        ++hn;
        pc[i] = hb + ((((unsigned)tlo + (unsigned)idx) & 63u) << 14);
        tlo = (tlo >> 6) | ((thi & 63u) << 54); thi >>= 6;
      }
    };
    KV A, B;
    u32x4 W[8];
    u32x4 T[4][8];
    auto ldp = [&](KV& S, auto P) {
      constexpr int p = P.v;
      const char* q = pc[p >> 1] + lo + (p & 1) * 2048;
      S.r[2 * p] = ld16nt(q); S.r[2 * p + 1] = ld16nt(q + 1024);
    };
    auto landed = [&](KV& S, auto P) { pin2(S.r[2 * P.v], S.r[2 * P.v + 1]); };
    KV X;
    const unsigned qo = WS_QX + ((32 + h) * 2 + b) * 1024 + lane * 16;
    u32x4 g0;
    {
      unsigned tk = 0u;
      if (w4 == 0) {
        const int si = (idx & ~7) | ((idx + (0x2400 >> (4 * cls))) & 7);
        const char* p = kb + cls * 128;
        if (lane == 1) p = cls == 2 ? reinterpret_cast<const char*>(w_uv) + (si & 1) * 2097152 + (si >> 1) * 256 : reinterpret_cast<const char*>(cls == 1 ? w_o : wo) + si * 65536;
        if (lane == 2) p = vb + cls * 128;
        if (lane == 3) p = reinterpret_cast<const char*>(wo) + h * 131072;
        if (lane < 4) tk = __builtin_nontemporal_load(reinterpret_cast<const unsigned*>(p));
      }
      lds_wait_slow(cnt + 14, 4u);
      tag = __builtin_amdgcn_readfirstlane(cnt[16]);
      asm volatile("" :: "v"(tk));
    }
    STAMP(10);
    sfor<0, 12>([&](auto I) {
      constexpr int i = I.v, j = i - 4;
      if constexpr ((i & 3) == 0) next2();
      if (i == 4 && w4 == 3) {
#pragma unroll 1
        for (int spin = 0; spin < 2; ++spin) {
          u32x2 t[8];
          asm volatile("s_load_dwordx2 %0, %8, 0x0 glc\n\ts_load_dwordx2 %1, %8, 0x400 glc\n\ts_load_dwordx2 %2, %8, 0x800 glc\n\ts_load_dwordx2 %3, %8, 0xc00 glc\n\t"
                       "s_load_dwordx2 %4, %8, 0x1000 glc\n\ts_load_dwordx2 %5, %8, 0x1400 glc\n\ts_load_dwordx2 %6, %8, 0x1800 glc\n\ts_load_dwordx2 %7, %8, 0x1c00 glc\n\ts_waitcnt lgkmcnt(0)"
                       : "=&s"(t[0]), "=&s"(t[1]), "=&s"(t[2]), "=&s"(t[3]), "=&s"(t[4]), "=&s"(t[5]), "=&s"(t[6]), "=&s"(t[7]) : "s"(ws + WS_T0 + ((blk >> 3) & 7) * 128) : "memory");
          unsigned e = 0u; int sum = 0;
#pragma unroll
          for (int k = 0; k < 8; ++k) { e |= t[k][1] ^ tag; sum += (int)(t[k][0] - tstart); }
          if (e == 0u) { lds_add(cnt + 13, (unsigned)min(sum >> 3, 100), lane); break; }
        }
      }
      if constexpr (j >= 0) { if constexpr (j < 4) landed(X, IC<j & 3>{}); else landed(B, IC<j & 3>{}); }
      if constexpr (j >= 0 && (j & 1)) TR();
      if constexpr (i == QPE) g0 = ld16c(rs, qo);
      if constexpr (i < 4) ldp(X, IC<i & 3>{}); else if constexpr (i < 8) ldp(B, IC<i & 3>{}); else ldp(A, IC<i & 3>{});
      SCHED();
    });
    const unsigned torg = tstart + __builtin_amdgcn_readfirstlane(cnt[13]);
    const char* wp = reinterpret_cast<const char*>(wo) + ((size_t)h * 128 + r4 * 8) * 1024 + w4 * 256 + c16 * 16;
    u32x4 qpk;
    {
      if (!__all((g0[1] == tag) & (g0[3] == tag))) {
#pragma unroll 1
        for (int spin = 0; spin < SPIN_MAX; ++spin) {
          COMPILER_FENCE();
          g0 = ld16c(rs, qo);
          if (__all((g0[1] == tag) & (g0[3] == tag))) break;
        }
      }
      qpk = qsum(g0);
    }
    STAMP(7);
    const bool holds = cls == 0;
    float m = 0.f, l = 0.f, acc[8];
    unsigned sc[5];
#pragma unroll
    for (int k = 0; k < 8; ++k) acc[k] = 0.f;
#pragma unroll
    for (int k = 0; k < 5; ++k) sc[k] = 0xff800000u;
    int vn = 0;
    unsigned due = torg + pTL - (unsigned)(ns - 5) * pTA - (3 - w4) * pSTG - ((idx * 5 + cls * 3) & 15) * pBSG;
    unsigned rwo;
    const unsigned zo = WS_GZ + (b * 32 + h) * 512;
    u32x2 zs[4];
    float wr_;
    auto zreq = [&]() {
      asm volatile("s_load_dwordx2 %0, %4, 0x0 glc\n\ts_load_dwordx2 %1, %4, 0x80 glc\n\ts_load_dwordx2 %2, %4, 0x100 glc\n\ts_load_dwordx2 %3, %4, 0x180 glc"
                   : "=&s"(zs[0]), "=&s"(zs[1]), "=&s"(zs[2]), "=&s"(zs[3]) : "s"(ws + zo) : "memory");
    };
    auto zget = [&]() {
#pragma unroll 1
      for (int spin = 0; spin < SPIN_MAX; ++spin) {
        asm volatile("s_waitcnt lgkmcnt(0)" : "+s"(zs[0]), "+s"(zs[1]), "+s"(zs[2]), "+s"(zs[3]) :: "memory");
        if (((zs[0][1] ^ tag) | (zs[1][1] ^ tag) | (zs[2][1] ^ tag) | (zs[3][1] ^ tag)) == 0u) break;
        __builtin_amdgcn_s_sleep(2);
        zreq();
      }
      asm volatile("s_waitcnt lgkmcnt(0)" : "+s"(zs[0]), "+s"(zs[1]), "+s"(zs[2]), "+s"(zs[3]) :: "memory");
      const float z0 = __uint_as_float(zs[0][0]), z1 = __uint_as_float(zs[1][0]), z2 = __uint_as_float(zs[2][0]), z3 = __uint_as_float(zs[3][0]);
      const float Z = fmaxf(fmaxf(z0, z1), fmaxf(z2, z3));
      wr_ = ex2(fmaf(m, C2, -Z)) * __builtin_amdgcn_rcpf((ex2(z0 - Z) + ex2(z1 - Z)) + (ex2(z2 - Z) + ex2(z3 - Z)));
      asm volatile("" : "+v"(wr_));
    };
    auto qk = [&](const u32x4* K, auto I) {
      float x[4];
#pragma unroll
      for (int t = 0; t < 4; ++t) {
        x[t] = 0.f;
#pragma unroll
        for (int k = 0; k < 4; ++k) x[t] = dot2(K[t][k], qpk[k], x[t]);
      }
      const float u0 = (c16 & 1 ? x[1] : x[0]) + dpp<0xB1>(c16 & 1 ? x[0] : x[1]), u1 = (c16 & 1 ? x[3] : x[2]) + dpp<0xB1>(c16 & 1 ? x[2] : x[3]);
      float v = (c16 & 2 ? u1 : u0) + dpp<0x4E>(c16 & 2 ? u0 : u1);
      v += dpp<0x124>(v); v += dpp<0x128>(v);
      sc[I.v >> 2] = __builtin_amdgcn_update_dpp(sc[I.v >> 2], __float_as_uint(v), 0xE4, 0xf, 1 << (I.v & 3), false);
      asm volatile("" : "+v"(sc[I.v >> 2]));
    };
    auto soft = [&]() {
      m = row16_max(fmaxf(fmaxf(fmaxf(__uint_as_float(sc[0]), __uint_as_float(sc[1])), fmaxf(__uint_as_float(sc[2]), __uint_as_float(sc[3]))), __uint_as_float(sc[4])));
      const float mc = m * C2;
      l = 0.f;
#pragma unroll
      for (int k = 0; k < 5; ++k) {
        const float p = ex2(fmaf(__uint_as_float(sc[k]), C2, -mc));
        sc[k] = pk_bf(p, dpp<0xB1>(p));
        l = dot2(sc[k], 0x3f003f00u, l);
      }
      l = row16_sum(l);
    };
    auto pv = [&](const u32x4* V) {
      const unsigned p01 = rowbcast<0>(sc[0]), p23 = rowbcast<2>(sc[0]);
#pragma unroll
      for (int k = 0; k < 4; ++k) { acc[2 * k] = dot2(plo(V[0][k], V[1][k]), p01, acc[2 * k]); acc[2 * k + 1] = dot2(phi(V[0][k], V[1][k]), p01, acc[2 * k + 1]); }
#pragma unroll
      for (int k = 0; k < 4; ++k) { acc[2 * k] = dot2(plo(V[2][k], V[3][k]), p23, acc[2 * k]); acc[2 * k + 1] = dot2(phi(V[2][k], V[3][k]), p23, acc[2 * k + 1]); }
      sc[0] = (unsigned)__builtin_amdgcn_mov_dpp((int)sc[0], 0x12C, 0xf, 0xf, true);
      if ((++vn & 3) == 0) { sc[0] = sc[1]; sc[1] = sc[2]; sc[2] = sc[3]; sc[3] = sc[4]; }
      asm volatile("" : "+v"(acc[0]), "+v"(acc[1]), "+v"(acc[2]), "+v"(acc[3]), "+v"(acc[4]), "+v"(acc[5]), "+v"(acc[6]), "+v"(acc[7]));
    };
    auto step = [&](KV& S, KV& N, auto SI, auto REFILL, auto WR) {
      constexpr bool refill = REFILL.v == 1;
      constexpr int wr = WR.v;
      if constexpr (refill) next2();
      if constexpr (refill && SI.v >= 3) { until((int)(due - torg) < (int)pTP ? due - pPA : due); due += pTA; }
      constexpr int ah = wr < 0 ? AHL : 0;
      auto point = [&](auto P) {
        if constexpr (refill) {
          if constexpr (P.v == 0 && ah > 0) { if constexpr (wr >= 0) { if (holds) lds_hold(cnt + 8); } sfor<0, ah>([&](auto Q) { ldp(N, Q); }); SCHED(); }
          landed(S, P);
          if constexpr (P.v & 1) TR();
          if constexpr (P.v + ah < 4) { if constexpr ((P.v & 1) == 0 && wr >= 0) { if (holds) lds_hold(cnt + 8); } ldp(N, IC<(P.v + ah) & 3>{}); }
          if constexpr (wr >= 0 && wr < 8) W[(4 * wr + P.v) & 7] = ld16(wp + (((4 * wr + P.v) >> 3) * 32 + ((4 * wr + P.v) & 7)) * 1024);
          SCHED();
        }
      };
      sfor<0, 2>([&](auto H) {
        constexpr int i = 2 * SI.v + H.v;
        if constexpr (REFILL.v == 2 && H.v == 0) { zreq(); SCHED(); }
        point(IC<2 * H.v>{});
        point(IC<2 * H.v + 1>{});
        if constexpr (i < NSMIN) qk(S.r + 4 * H.v, IC<i>{});
        else if constexpr (i > NSMAX) pv(S.r + 4 * H.v);
        else {
          if (i < ns) { if constexpr (i < NSMAX) qk(S.r + 4 * H.v, IC<i>{}); }
          else { if (i == ns) soft(); pv(S.r + 4 * H.v); }
        }
        SCHED();
      });
      if constexpr (REFILL.v == 2) {
        zget();
        STAMP(1);
        *reinterpret_cast<u32x4*>(smem + rwo) = u32x4{pk_bf(acc[0] * wr_, acc[1] * wr_), pk_bf(acc[2] * wr_, acc[3] * wr_), pk_bf(acc[4] * wr_, acc[5] * wr_), pk_bf(acc[6] * wr_, acc[7] * wr_)};
        COMPILER_FENCE();
      }
      if constexpr (wr >= 1) {
        constexpr int t = (wr - 1) >> 1, j0 = ((wr - 1) & 1) * 2;
#pragma unroll
        for (int jj = 0; jj < 2; ++jj)
#pragma unroll
          for (int e = 0; e < 4; ++e) {
            const unsigned a_ = W[(4 * wr - 4 + 2 * jj) & 7][e], b_ = W[(4 * wr - 3 + 2 * jj) & 7][e];
            T[t][2 * e][j0 + jj] = plo(a_, b_); T[t][2 * e + 1][j0 + jj] = phi(a_, b_);
          }
#pragma unroll
        for (int e = 0; e < 8; e += 2) pin2(T[t][e], T[t][e + 1]);
      }
      SCHED();
    };
    step(B, B, IC<1>{}, IC<0>{}, IC<-1>{});
    step(A, B, IC<2>{}, IC<1>{}, IC<0>{});
    step(X, X, IC<0>{}, IC<0>{}, IC<-1>{});
    sfor<0, 4>([&](auto I) {
      step(B, A, IC<2 * I.v + 3>{}, IC<1>{}, IC<2 * I.v + 1>{});
      if constexpr (I.v == 3) {
        float* ml = reinterpret_cast<float*>(cnt + 32);
        ml[(c16 & 1) * 16 + w4 * 4 + r4] = (c16 & 1) ? l : m;
        COMPILER_FENCE();
        const unsigned got = lds_arrive_nw(L_CNT + 44);
        const float mr = ml[c16], lr = ml[16 + c16];
        SCHED();
        if (lds_done(got) == 3u) {
          const float M = row16_max(mr);
          const unsigned z = __float_as_uint(fmaf(M, C2, __builtin_amdgcn_logf(row16_sum(ex2((mr - M) * C2) * lr))));
          asm volatile("buffer_store_dwordx4 %0, %1, %2, 0 offen sc1\n\ts_nop 2" :: "v"(u32x4{z, tag, z, tag}), "v"(lane < 8 ? zo + cls * 128 + lane * 16 : OOB), "s"(rs) : "memory");
        }
        SCHED();
      }
      step(A, B, IC<2 * I.v + 4>{}, IC<1>{}, IC<2 * I.v + 2>{});
    });
    unsigned yoff = WS_GB + ((b * 128 + h * 4 + cls) * 256 + 64 * w4 + lane) * 8, rro = L_MRG + c16 * 272 + r4 * 16;
    rwo = L_MRG + (w4 * 4 + r4) * 272 + c16 * 16;
    asm volatile("" : "+v"(yoff), "+v"(rwo), "+v"(rro));
    if (cls == 0) lds_wait(cnt + 1, 4u);
    const int nit = (ns - 12) >> 1;
#pragma unroll 1
    for (int i = 0; i < nit; ++i) {
      step(B, A, IC<11>{}, IC<1>{}, IC<-1>{});
      step(A, B, IC<11>{}, IC<1>{}, IC<-1>{});
    }
    if (ns & 1) { step(B, A, IC<11>{}, IC<1>{}, IC<-1>{}); step(A, A, IC<11>{}, IC<2>{}, IC<-1>{}); }
    else step(B, B, IC<11>{}, IC<2>{}, IC<-1>{});
    {
      u32x4 af[4];
      unsigned got = lds_arrive_nw(L_CNT), need = 3u;
#pragma unroll 1
      for (int spin = 0; spin < SPIN_MAX; ++spin) {
#pragma unroll
        for (int t = 0; t < 4; ++t) af[t] = *reinterpret_cast<const u32x4*>(smem + rro + t * 64);
        SCHED();
        if (lds_done(got) >= need) break;
        need = 4u;
        __builtin_amdgcn_s_sleep(1);
        asm volatile("ds_read_b32 %0, %1" : "=v"(got) : "v"((unsigned)L_CNT) : "memory");
      }
      SCHED();
      f32x4 d[8];
      float a[8], v4[4];
      sfor<0, 5>([&](auto I) {
        constexpr int i = I.v;
        sfor<0, 8>([&](auto J) {
          constexpr int e = i + 4 * (J.v & 1), t = J.v >> 1;
          if constexpr (i < 4) { d[e] = __builtin_amdgcn_mfma_f32_16x16x32_bf16(__builtin_bit_cast(bf16x8_t, af[t]), __builtin_bit_cast(bf16x8_t, T[t][e]), t ? d[e] : f32x4{0.f, 0.f, 0.f, 0.f}, 0, 0, 0); SCHED(); }
          if constexpr (i >= 1 && (J.v == 4 || J.v == 5)) { const f32x2 u = f32x2{d[e - 1][0], d[e - 1][1]} + f32x2{d[e - 1][2], d[e - 1][3]}; a[e - 1] = u[0] + u[1]; asm volatile("" : "+v"(a[e - 1])); }
          if constexpr (i >= 1 && J.v == 6) v4[i - 1] = rs32(a[i - 1], a[i + 3]);
          if constexpr (i < 4) SCHED();
        });
      });
      st8c(rs, yoff, u32x2{pk_bf(rs16(v4[0], v4[2]), rs16(v4[1], v4[3])), tag});
    }
    STAMP(2);
    STAMP_FLUSH(1);
    TR_FLUSH();
    return;
  }
  const rsrc_t ro = __builtin_amdgcn_make_buffer_rsrc(out, 0, 4096, 0x00020000);
  auto fin = [&](auto NL, unsigned base, unsigned col, bool bump) {
    constexpr int nl = NL.v;
    unsigned doff = base + (((idx >> 5) * 4 + w4) * 16 * nl + (lane >> 2)) * 2048 + (idx & 31) * 64 + (lane & 3) * 16;
    unsigned fwo = 32768 + (c16 < 4 ? (w4 * 16 + 4 * c16 + r4) * 4 : 256 + lane * 4);
    unsigned fro = 32768 + (lane & 7) * 8, foo = lane < 8 ? ((idx >> 5) * 1024 + col + (nl == 2 ? 16 * (idx & 24) + 64 * (idx & 1) + (idx & 6) + 8 * lane : 16 * (idx & 31) + 2 * lane)) * 2 : OOB;
    asm volatile("" : "+v"(doff), "+v"(fwo), "+v"(fro), "+v"(foo));
    lds_wait(cnt + 0, 4u);
    STAMP(4);
    u32x4 d[nl];
    unsigned eh = 1u;
#pragma unroll 1
    for (int spin = SPIN_MAX; spin > 0; --spin) {
      COMPILER_FENCE();
      if (eh != 0u) {
#pragma unroll
        for (int j = 0; j < nl; ++j) d[j] = ld16c(rs, doff + j * 32768);
        eh = 0u;
#pragma unroll
        for (int j = 0; j < nl; ++j) { eh |= (d[j][1] ^ tag) | (d[j][3] ^ tag); asm volatile("" : "+v"(eh)); }
      }
      if (__builtin_amdgcn_ballot_w64(eh != 0u) == 0ull) break;
    }
    STAMP(11);
    float y[4] = {bflo(d[0][0]), bfhi(d[0][0]), bflo(d[0][2]), bfhi(d[0][2])};
    if constexpr (nl == 2) { y[0] += bflo(d[1][0]); y[1] += bfhi(d[1][0]); y[2] += bflo(d[1][2]); y[3] += bfhi(d[1][2]); }
    float t = rs16(rs32(y[0], y[2]), rs32(y[1], y[3]));
    t += dpp<0x124>(t); t += dpp<0x128>(t);
    *reinterpret_cast<float*>(smem + fwo) = t;
    COMPILER_FENCE();
    const unsigned got = lds_arrive_nw(L_CNT + 24);
    f32x2 t2[4];
#pragma unroll
    for (int w = 0; w < 4; ++w) t2[w] = *reinterpret_cast<const f32x2*>(smem + fro + w * 64);
    SCHED();
    if (lds_done(got) == 3u) {
      const f32x2 r = (t2[0] + t2[1]) + (t2[2] + t2[3]);
      __builtin_amdgcn_raw_buffer_store_b32(pk_bf(r[0], r[1]), ro, (int)foo, 0, 0);
      if (bump) st8c(rs, lane == 0 ? WS_EP : OOB, u32x2{tlow, tag});
    }
  };
  u32x4 Wa[16], Wb[16], xv[4];
  sfor<0, 2>([&](auto I) { Wa[I.v] = ld16nt(pa + ao); SCHED(); ao += sa; asm volatile("" : "+v"(ao)); });
  sfor<0, 4>([&](auto J) { xv[J.v] = ld16(xp + xo + (J.v >> 1) * 1024 + 16 * (J.v & 1)); });
  SCHED();
  sfor<2, 8>([&](auto I) { Wa[I.v] = ld16nt(pa + ao); SCHED(); ao += sa; asm volatile("" : "+v"(ao)); });
  epw = ld8c(rs, WS_EP);
  const unsigned qa_off = WS_QA + (((c16 >> 1) & 1) * 32 + h) * 1024 + (b * 256 + w4 * 64 + 16 * r4 + 8 * (c16 & 1)) * 2;
  st16c(rs, cls == 1 && c16 < 4 ? qa_off : OOB, u32x4{0u, 0u, 0u, 0u});
  SCHED();
  sfor<8, 16>([&](auto I) { Wa[I.v] = ld16nt(pa + ao); SCHED(); ao += sa; asm volatile("" : "+v"(ao)); });
  const u16* pb = cls == 0 ? reinterpret_cast<const u16*>(pa) + ((size_t)idx * 128 + 64 + w4) * 512 + 8 * lane
                           : (cls == 2 ? w_uv + ((size_t)(b * 256 + w4 * 64 + 16 * r4) * 32 + h) * 128 + 8 * c16 : (cls == 1 ? w_o : wo) + ((size_t)h * 128 + b * 64 + w4 * 2) * 512 + 8 * lane);
  {
    rawbar();
    take_epoch();
    cnt[16] = tag;
    COMPILER_FENCE();
    if (blk < 8 && w4 == 0) asm volatile("buffer_store_dwordx4 %0, %1, %2, 0 offen sc1\n\ts_nop 2" :: "v"(u32x4{tstart, tag, tstart, tag}), "v"(WS_T0 + blk * 1024 + lane * 16), "s"(rs) : "memory");
  }
  STAMP(13);
  float* red = reinterpret_cast<float*>(smem);
  if (cls & 1) {
    float acc[2][8];
#pragma unroll
    for (int bb = 0; bb < 2; ++bb)
#pragma unroll
      for (int k = 0; k < 8; ++k) acc[bb][k] = 0.f;
    sfor<0, 8>([&](auto IP) {
      constexpr int ip = IP.v, j = ip - (8 - WBW);
      use2(Wa[2 * ip], Wa[2 * ip + 1]);
      if constexpr (ip & 1) TR();
      if constexpr (j >= 0) { Wb[2 * j] = ld16(pb + j * 4096); Wb[2 * j + 1] = ld16(pb + j * 4096 + 512); }
      SCHED();
      const unsigned xp0 = xv[ip >> 2][ip & 3], xp1 = xv[2 + (ip >> 2)][ip & 3];
#pragma unroll
      for (int k = 0; k < 4; ++k) {
        const unsigned lo = plo(Wa[2 * ip][k], Wa[2 * ip + 1][k]), hi = phi(Wa[2 * ip][k], Wa[2 * ip + 1][k]);
        acc[0][2 * k] = dot2(lo, xp0, acc[0][2 * k]); acc[0][2 * k + 1] = dot2(hi, xp0, acc[0][2 * k + 1]);
        acc[1][2 * k] = dot2(lo, xp1, acc[1][2 * k]); acc[1][2 * k + 1] = dot2(hi, xp1, acc[1][2 * k + 1]);
      }
      SCHED();
    });
    {
      float v8[8], v4[4];
#pragma unroll
      for (int k = 0; k < 8; ++k) v8[k] = rs32(acc[0][k], acc[1][k]);
#pragma unroll
      for (int k = 0; k < 4; ++k) v4[k] = rs16(v8[k], v8[k + 4]);
      *reinterpret_cast<f32x4*>(red + (w4 * 2 + (lane >> 5)) * 128 + 8 * c16 + 4 * (r4 & 1)) = f32x4{v4[0], v4[1], v4[2], v4[3]};
    }
    COMPILER_FENCE();
    const unsigned got = lds_arrive_nw(L_CNT + 8);
    f32x4 t4[4];
#pragma unroll
    for (int w = 0; w < 4; ++w) t4[w] = *reinterpret_cast<const f32x4*>(red + (w * 2 + (lane >> 5)) * 128 + 8 * c16 + 4 * (r4 & 1));
    SCHED();
    if (lds_done(got) == 3u) {
      const f32x4 t = (t4[0] + t4[1]) + (t4[2] + t4[3]);
      st16c(rs, WS_QX + (((cls >> 1) * 32 + h) * 2 + (lane >> 5)) * 1024 + b * 512 + (lane & 31) * 16, u32x4{pk_bf(t[0], t[1]), tag, pk_bf(t[2], t[3]), tag});
      STAMP(15);
      SCHED();
      lds_add(cnt + 2, 1u, lane);
    } else lds_wait(cnt + 2, 5u);
    sfor<WBW, 8>([&](auto J) {
      constexpr int j = J.v;
      use2(Wb[2 * (j - WBW)], Wb[2 * (j - WBW) + 1]);
      if constexpr ((j - WBW) & 1) TR();
      Wb[2 * j] = ld16(pb + j * 4096); Wb[2 * j + 1] = ld16(pb + j * 4096 + 512);
      if constexpr (j == 7) lds_add(cnt + 14, 1u, lane);
      SCHED();
    });
    {
      const unsigned tw = __builtin_nontemporal_load(reinterpret_cast<const unsigned*>(out + (idx >> 5) * 1024 + (cls >> 1) * 512 + 16 * (idx & 31)));
      sfor<8 - WBW, 8>([&](auto J) { use2(Wb[2 * J.v], Wb[2 * J.v + 1]); if constexpr (J.v & 1) TR(); });
      asm volatile("" :: "v"(tw));
    }
    STAMP(8);
  } else if (cls == 2) {
    sfor<0, 16 - WBW>([&](auto IP) {
      constexpr int ip = IP.v, j = ip - (8 - WBW);
      if constexpr (ip < 8) use2(Wa[2 * ip], Wa[2 * ip + 1]); else use2(Wb[2 * ip - 16], Wb[2 * ip - 15]);
      if constexpr (ip & 1) TR();
      if constexpr (j >= 0) { Wb[2 * j] = ld16nt(pb + j * 8192); Wb[2 * j + 1] = ld16nt(pb + j * 8192 + 4096); }
      SCHED();
    });
    u32x4 g0, g1;
    const unsigned qo = WS_QX + h * 2048 + lane * 16;
    shint<512, 1>(ws + WS_QX + h * 2048, tag);
    STAMP(5);
#pragma unroll 1
    for (int spin = 0; spin < SPIN_MAX; ++spin) {
      COMPILER_FENCE();
      g0 = ld16c(rs, qo); g1 = ld16c(rs, qo + 1024);
      SCHED();
      lds_add(cnt + 14, spin == 0, lane);
      SCHED();
      if (__all((g0[1] == tag) & (g0[3] == tag) & (g1[1] == tag) & (g1[3] == tag))) break;
      __builtin_amdgcn_s_sleep(1);
    }
    sfor<8 - WBW, 8>([&](auto J) { if constexpr (J.v & 1) TR(); });
    const u32x4 q0 = qsum(g0), q1 = qsum(g1);
    STAMP(9);
    float v[8];
    {
      float u[16];
#pragma unroll
      for (int i = 0; i < 16; ++i) {
        float a = 0.f, c = 0.f;
#pragma unroll
        for (int k = 0; k < 4; ++k) { a = dot2(Wa[i][k], q0[k], a); c = dot2(Wa[i][k], q1[k], c); }
        u[i] = (c16 & 2 ? c : a) + dpp<0x4E>(c16 & 2 ? a : c);
        asm volatile("" : "+v"(u[i]));
      }
#pragma unroll
      for (int i = 0; i < 8; ++i) {
        float t = (c16 & 1 ? u[i + 8] : u[i]) + dpp<0xB1>(c16 & 1 ? u[i] : u[i + 8]);
        asm volatile("" : "+v"(t));
        t += dpp<0x124>(t);
        asm volatile("" : "+v"(t));
        t += dpp<0x128>(t);
        asm volatile("" : "+v"(t));
        v[i] = t;
      }
    }
    u32x4 o = {pk_bf(v[0], v[1]), pk_bf(v[2], v[3]), pk_bf(v[4], v[5]), pk_bf(v[6], v[7])};
    if ((o[0] | o[1]) == 0u) o[0] = 0x8000u;
    if ((o[2] | o[3]) == 0u) o[2] = 0x8000u;
    if (c16 < 4) st16c(rs, qa_off, o);
    STAMP(10);
  }
  asm volatile("" :: "v"(xv[0]), "v"(xv[1]), "v"(xv[2]), "v"(xv[3]));
  SCHED();
  if (cls == 3) {
    fin(IC<2>{}, WS_GB, 512, false);
    STAMP(6);
    STAMP_FLUSH(0); TR_FLUSH();
    return;
  }
  if (cls == 0) {
    char* scr = smem + L_SCR;
    const int ab = idx >> 5, r = lane & 31, hh = lane >> 5;
    const unsigned fo = WS_QX + (w4 * 8 + (c16 >> 1)) * 2048 + (c16 & 1) * 512 + r4 * 8;
    u32x2 fl;
    u32x4 g[9];
    const unsigned o8 = WS_QA + (ab * 32 + w4 * 8) * 1024 + lane * 16, ol = WS_QA + (ab * 32 + r) * 1024 + 992 + 16 * hh;
    auto put = [&](int i, const u32x4& v) { *reinterpret_cast<u32x4*>(smem + ((i >> 3) * 4 + r4) * 8192 + toff(4 * (i & 7) + w4, c16)) = v; };
    sfor<0, 16>([&](auto I) { put(I.v, Wa[I.v]); if constexpr ((I.v & 3) == 3) TR(); SCHED(); });
    until(tstart + PAR(26, T0B));
    sfor<0, 16>([&](auto I) { Wb[I.v] = ld16nt(pb + I.v * 2048); });
    SCHED();
    lds_add(cnt + 14, 1u, lane);
    fl = ld8c(rs, fo);
    SCHED();
    sfor<0, 16>([&](auto I) { put(16 + I.v, Wb[I.v]); if constexpr ((I.v & 3) == 3) TR(); SCHED(); });
    STAMP(8);
    u32x4 qlast;
    {
      if (!__all(fl[1] == tag)) {
#pragma unroll 1
        for (int spin = 0; spin < SPIN_MAX; ++spin) {
          COMPILER_FENCE();
          if (__all(ld8c(rs, fo)[1] == tag)) break;
        }
      }
      until(tstart + PAR(27, T0Q));
      STAMP(5);
      sbar4(cnt + 7, lane);
      STAMP(12);
      unsigned pend = 0x1ffu;
#pragma unroll 1
      for (int spin = 0; spin < SPIN_MAX && pend; ++spin) {
        COMPILER_FENCE();
        u32x4 t[9];
#pragma unroll
        for (int i = 0; i < 9; ++i) t[i] = ld16c(rs, (pend >> i & 1) ? (i < 8 ? o8 + i * 1024 : ol) : OOB);
#pragma unroll
        for (int i = 0; i < 9; ++i) {
          if (pend >> i & 1) g[i] = t[i];
          if (__all(nz8(t[i]))) pend &= ~(1u << i);
        }
      }
      STAMP(9);
      qlast = g[8];
#pragma unroll
      for (int i = 0; i < 8; ++i)
        if (lane < 62) *reinterpret_cast<u32x4*>(scr + (w4 * 8 + i) * 1008 + 16 * lane) = g[i];
    }
    sbar4(cnt + 3, lane);
    STAMP(10);
    f32x16 sacc;
#pragma unroll
    for (int i = 0; i < 16; ++i) sacc[i] = 0.f;
#pragma unroll
    for (int jj = 0; jj < 32; ++jj) {
      const u32x4 af = *reinterpret_cast<const u32x4*>(smem + (w4 * 4 + (jj >> 3)) * 8192 + toff(r, 2 * (jj & 7) + hh));
      const u32x4 bf = jj < 31 ? *reinterpret_cast<const u32x4*>(scr + r * 1008 + 16 * (2 * (jj < 31 ? jj : 0) + hh)) : qlast;
      sacc = __builtin_amdgcn_mfma_f32_32x32x16_bf16(__builtin_bit_cast(bf16x8_t, af), __builtin_bit_cast(bf16x8_t, bf), sacc, 0, 0, 0);
    }
    float m = sacc[0];
#pragma unroll
    for (int i = 1; i < 16; ++i) m = fmaxf(m, sacc[i]);
    m = xhalf_max(m);
    if (hh == 0) *reinterpret_cast<float*>(scr + r * 1008 + 992 + 4 * w4) = m;
    sbar4(cnt + 4, lane);
    STAMP(11);
    {
      const f32x4 m4 = *reinterpret_cast<const f32x4*>(scr + r * 1008 + 992);
      m = fmaxf(fmaxf(m4[0], m4[1]), fmaxf(m4[2], m4[3]));
    }
    const float mc = m * C2;
    float p[16], ls = 0.f;
#pragma unroll
    for (int i = 0; i < 16; ++i) { p[i] = ex2(fmaf(sacc[i], C2, -mc)); ls += p[i]; }
    ls = xhalf_sum(ls);
    sbar4(cnt + 5, lane);
    u32x4* pf = reinterpret_cast<u32x4*>(scr);
    float* wl = reinterpret_cast<float*>(scr + 8192);
    if (hh == 0) wl[r * 4 + w4] = ls;
    pf[(2 * w4) * 64 + lane] = u32x4{pk_bf(p[0], p[1]), pk_bf(p[2], p[3]), pk_bf(p[4], p[5]), pk_bf(p[6], p[7])};
    pf[(2 * w4 + 1) * 64 + lane] = u32x4{pk_bf(p[8], p[9]), pk_bf(p[10], p[11]), pk_bf(p[12], p[13]), pk_bf(p[14], p[15])};
    sbar4(cnt + 6, lane);
    u32x4 pb8[8];
#pragma unroll
    for (int ks = 0; ks < 8; ++ks) pb8[ks] = pf[ks * 64 + lane];
    const f32x4 l4 = *reinterpret_cast<const f32x4*>(wl + r * 4);
    const float lsum = (l4[0] + l4[1]) + (l4[2] + l4[3]), rl = __builtin_amdgcn_rcpf(lsum);
    const int qq = (lane & 15) >> 2, pp = lane & 3, blk2 = (lane >> 4) & 1;
    const unsigned tw = tstart + __builtin_amdgcn_readfirstlane(cnt[13]) + PAR(13, TW), th = tw - PAR(14, HL);
    u32x4 vv[2][4];
    bool held = false;
    char* ot = scr + 12288 + w4 * 4096;
#pragma unroll
    for (int c2 = 0; c2 < 2; ++c2) {
      f32x16 oacc[2];
#pragma unroll
      for (int ct = 0; ct < 2; ++ct)
#pragma unroll
        for (int i = 0; i < 16; ++i) oacc[ct][i] = 0.f;
#pragma unroll
      for (int ks = 0; ks < 8; ++ks)
#pragma unroll
        for (int ct = 0; ct < 2; ++ct) {
          typedef __attribute__((address_space(3))) s16x4* lp;
          const char* tile = smem + ((ks >> 1) * 4 + w4) * 8192;
          const int cc = 2 * c2 + ct;
          const s16x4 lo = __builtin_amdgcn_ds_read_tr16_b64_v4i16((lp)(tile + toff(16 * (ks & 1) + 4 * hh + qq, 4 * cc + 2 * blk2 + (pp >> 1)) + 8 * (pp & 1)));
          const s16x4 hi = __builtin_amdgcn_ds_read_tr16_b64_v4i16((lp)(tile + toff(16 * (ks & 1) + 8 + 4 * hh + qq, 4 * cc + 2 * blk2 + (pp >> 1)) + 8 * (pp & 1)));
          const s16x8 afr = __builtin_shufflevector(lo, hi, 0, 1, 2, 3, 4, 5, 6, 7);
          oacc[ct] = __builtin_amdgcn_mfma_f32_32x32x16_bf16(__builtin_bit_cast(bf16x8_t, afr), __builtin_bit_cast(bf16x8_t, pb8[ks]), oacc[ct], 0, 0, 0);
        }
#pragma unroll
      for (int ct = 0; ct < 2; ++ct)
#pragma unroll
        for (int g = 0; g < 4; ++g)
          *reinterpret_cast<u32x2*>(ot + r * 128 + 16 * ((4 * ct + g) ^ (r & 7)) + 8 * hh) = u32x2{pk_bf(oacc[ct][4 * g] * rl, oacc[ct][4 * g + 1] * rl), pk_bf(oacc[ct][4 * g + 2] * rl, oacc[ct][4 * g + 3] * rl)};
      if (c2 == 0) { held = (int)((unsigned)wall_clock64() + 30u - th) >= 0; if (held) lds_add(cnt + 8, 1u, lane); }
      __builtin_amdgcn_wave_barrier();
#pragma unroll
      for (int i = 0; i < 4; ++i) { vv[c2][i] = *reinterpret_cast<const u32x4*>(ot + (8 * i + (lane >> 3)) * 128 + 16 * ((lane ^ (lane >> 3)) & 7)); asm volatile("" : "+v"(vv[c2][i])); }
      __builtin_amdgcn_wave_barrier();
    }
    lds_arrive(cnt + 1, lane);
    if (!held) { until(th); lds_add(cnt + 8, 1u, lane); }
    until(tw);
#pragma unroll
    for (int i = 0; i < 8; ++i) st16c(rs, WS_APO + ((((8 * (i >> 1) + (lane >> 3)) * 2 + (w4 >> 1)) * 64 + idx) * 256 + (w4 & 1) * 128 + 64 * (i & 1) + 8 * (lane & 7)) * 2, vv[i & 1][i >> 1]);
    STAMP(1);
    asm volatile("s_waitcnt vmcnt(0)" ::: "memory");
    if (lds_arrive(cnt + 9 + (w4 >> 1), lane) == 1u && hh == 0)
      st8c(rs, WS_AML + ((r * 2 + (w4 >> 1)) * 16 + (idx >> 2)) * 128 + (idx & 3) * 8, u32x2{__float_as_uint(mc + __builtin_amdgcn_logf(lsum)), tag});
    lds_add(cnt + 8, ~0u, lane);
    STAMP(2);
    STAMP_FLUSH(0); TR_FLUSH();
    return;
  }
#pragma unroll
  for (int ip = 0; ip < 8; ++ip) {
#pragma unroll
    for (int k = 0; k < 4; ++k) { const unsigned lo = plo(Wb[2 * ip][k], Wb[2 * ip + 1][k]), hi = phi(Wb[2 * ip][k], Wb[2 * ip + 1][k]); Wb[2 * ip][k] = lo; Wb[2 * ip + 1][k] = hi; }
    pin2(Wb[2 * ip], Wb[2 * ip + 1]);
  }
  if (cls == 2) {
    float* red2 = red + 4 * 256;
    float* wml = red + 3072;
    u32x8 f[4];
    until<6>(tstart + PAR(13, TW) + PAR(28, 60));
#pragma unroll 1
    for (int spin = 0; spin < SPIN_MAX; ++spin) {
      sld4x32(ws + WS_AML + ((h * 2 + b) * 16 + w4 * 4) * 128, f);
      unsigned e = 0u;
#pragma unroll
      for (int i = 0; i < 16; ++i) e |= f[i >> 2][2 * (i & 3) + 1] ^ tag;
      if (e == 0u) break;
      __builtin_amdgcn_s_sleep(2);
    }
    STAMP(3);
    u32x4 pp[8];
#pragma unroll
    for (int i = 0; i < 8; ++i) pp[i] = ld16c(rs, WS_APO + ((h * 2 + b) * 64 + w4 * 16 + 2 * i) * 512 + lane * 16);
    SCHED();
    float wt[8];
    {
      float M = -INFINITY, L = 0.f;
#pragma unroll
      for (int i = 0; i < 8; ++i) {
        unsigned z0 = f[i >> 1][4 * (i & 1)], z1 = f[i >> 1][4 * (i & 1) + 2];
        asm("" : "+s"(z0), "+s"(z1));
        wt[i] = __uint_as_float(lane < 32 ? z0 : z1); M = fmaxf(M, wt[i]);
      }
      M = xhalf_max(M);
#pragma unroll
      for (int i = 0; i < 8; ++i) { wt[i] = ex2(wt[i] - M); L += wt[i]; }
      L = xhalf_sum(L);
      if (lane == 0) *reinterpret_cast<f32x2*>(wml + 2 * w4) = f32x2{M, L};
    }
    SCHED();
    float a8[8];
#pragma unroll
    for (int k = 0; k < 8; ++k) a8[k] = 0.f;
#pragma unroll
    for (int i = 0; i < 8; ++i) {
#pragma unroll
      for (int k = 0; k < 4; ++k) { a8[2 * k] = fmaf(wt[i], bflo(pp[i][k]), a8[2 * k]); a8[2 * k + 1] = fmaf(wt[i], bfhi(pp[i][k]), a8[2 * k + 1]); }
      asm volatile("" : "+v"(a8[0]), "+v"(a8[1]), "+v"(a8[2]), "+v"(a8[3]), "+v"(a8[4]), "+v"(a8[5]), "+v"(a8[6]), "+v"(a8[7]));
    }
    STAMP(6);
    *reinterpret_cast<f32x4*>(red + w4 * 256 + 8 * (lane & 31) + 4 * (lane >> 5)) = f32x4{rs32(a8[0], a8[4]), rs32(a8[1], a8[5]), rs32(a8[2], a8[6]), rs32(a8[3], a8[7])};
    sbar4(cnt + 3, lane);
    STAMP(4);
    float acc[2][8];
#pragma unroll
    for (int bb = 0; bb < 2; ++bb) {
      u32x4 cv[2];
      const f32x4 ml = *reinterpret_cast<const f32x4*>(wml + 4 * bb);
      const float M = fmaxf(ml[0], ml[2]), e0 = ex2(ml[0] - M), e1 = ex2(ml[2] - M), rl = 1.f / fmaf(e0, ml[1], e1 * ml[3]), s0 = e0 * rl, s1 = e1 * rl;
#pragma unroll
      for (int e = 0; e < 2; ++e) {
        const float* src = red + bb * 512 + w4 * 64 + 16 * r4 + 8 * e;
        const f32x4 t0 = s0 * *reinterpret_cast<const f32x4*>(src) + s1 * *reinterpret_cast<const f32x4*>(src + 256), t1 = s0 * *reinterpret_cast<const f32x4*>(src + 4) + s1 * *reinterpret_cast<const f32x4*>(src + 260);
        cv[e] = u32x4{pk_bf(t0[0], t0[1]), pk_bf(t0[2], t0[3]), pk_bf(t1[0], t1[1]), pk_bf(t1[2], t1[3])};
      }
#pragma unroll
      for (int k = 0; k < 8; ++k) acc[bb][k] = 0.f;
#pragma unroll
      for (int ip = 0; ip < 8; ++ip)
#pragma unroll
        for (int k = 0; k < 4; ++k) { acc[bb][2 * k] = dot2(Wb[2 * ip][k], cv[ip >> 2][ip & 3], acc[bb][2 * k]); acc[bb][2 * k + 1] = dot2(Wb[2 * ip + 1][k], cv[ip >> 2][ip & 3], acc[bb][2 * k + 1]); }
    }
    {
      float v8[8], v4[4];
#pragma unroll
      for (int k = 0; k < 8; ++k) v8[k] = rs32(acc[0][k], acc[1][k]);
#pragma unroll
      for (int k = 0; k < 4; ++k) v4[k] = rs16(v8[k], v8[k + 4]);
      *reinterpret_cast<f32x4*>(red2 + (w4 * 2 + (lane >> 5)) * 128 + 8 * c16 + 4 * (r4 & 1)) = f32x4{v4[0], v4[1], v4[2], v4[3]};
    }
    {
      COMPILER_FENCE();
      const unsigned got = lds_arrive_nw(L_CNT + 16);
      f32x4 t4[4];
#pragma unroll
      for (int w = 0; w < 4; ++w) t4[w] = *reinterpret_cast<const f32x4*>(red2 + w * 256 + 4 * lane);
      SCHED();
      if (lds_done(got) == 3u) {
        const f32x4 t = (t4[0] + t4[1]) + (t4[2] + t4[3]);
        const unsigned co = WS_CX + ((h * 2 + b) * 256 + 64 * r4 + 32 * (c16 & 1) + 2 * (c16 >> 1)) * 8;
        st16c(rs, co, u32x4{__float_as_uint(t[0]), tag, __float_as_uint(t[1]), tag});
        st16c(rs, co + 128, u32x4{__float_as_uint(t[2]), tag, __float_as_uint(t[3]), tag});
      }
    }
    STAMP(12);
    STAMP_FLUSH(0); TR_FLUSH();
    return;
  }
  {
    unsigned op;
    {
      u32x4 c;
      const unsigned co = WS_CX + ((h * 2 + ((lane >> 3) & 1)) * 256 + (lane >> 5) * 128 + b * 64 + w4 * 16 + 2 * (lane & 7)) * 8;
      if (idx == 0 && w4 == 1) {
#pragma unroll 1
        for (int spin = 0; spin < SPIN_MAX; ++spin) {
          COMPILER_FENCE();
          if (__all(ld8c(rs, WS_QX + 65536 + (lane >> 1) * 2048 + (lane & 1) * 512)[1] == tag)) break;
          __builtin_amdgcn_s_sleep(8);
        }
      }
      until<6>(tstart + PAR(13, TW) + PAR(29, 370));
      if (w4 == 0) { shint<2048, 0>(ws + WS_CX + (h * 512 + b * 64) * 8, tag); lds_add(cnt + 7, 1u, lane); }
      else lds_wait(cnt + 7, 1u);
      STAMP(3);
#pragma unroll 1
      for (int spin = 0; spin < SPIN_MAX; ++spin) {
        COMPILER_FENCE();
        c = ld16c(rs, co);
        if (__all((c[1] == tag) & (c[3] == tag))) break;
        __builtin_amdgcn_s_sleep(1);
      }
      float o0 = __uint_as_float(c[0]), o1 = __uint_as_float(c[2]);
      o0 += dpp<0x128>(o0); o1 += dpp<0x128>(o1);
      op = pk_bf(o0, o1);
    }
    STAMP(14);
    float* red3 = red + 4096;
    float a[2][8];
#pragma unroll
    for (int bb = 0; bb < 2; ++bb)
#pragma unroll
      for (int k = 0; k < 8; ++k) a[bb][k] = 0.f;
#pragma unroll
    for (int ip = 0; ip < 8; ++ip) {
      const unsigned v0 = __builtin_amdgcn_readlane(op, ip), v1 = __builtin_amdgcn_readlane(op, 32 + ip);
#pragma unroll
      for (int k = 0; k < 4; ++k) {
        a[0][2 * k] = dot2(Wb[2 * ip][k], v0, a[0][2 * k]); a[0][2 * k + 1] = dot2(Wb[2 * ip + 1][k], v0, a[0][2 * k + 1]);
        a[1][2 * k] = dot2(Wb[2 * ip][k], v1, a[1][2 * k]); a[1][2 * k + 1] = dot2(Wb[2 * ip + 1][k], v1, a[1][2 * k + 1]);
      }
    }
#pragma unroll
    for (int bb = 0; bb < 2; ++bb) {
      *reinterpret_cast<f32x4*>(red3 + (w4 * 2 + bb) * 512 + 8 * lane) = f32x4{a[bb][0], a[bb][1], a[bb][2], a[bb][3]};
      *reinterpret_cast<f32x4*>(red3 + (w4 * 2 + bb) * 512 + 8 * lane + 4) = f32x4{a[bb][4], a[bb][5], a[bb][6], a[bb][7]};
    }
    sbar4(cnt + 5, lane);
    f32x4 t = {0.f, 0.f, 0.f, 0.f};
#pragma unroll
    for (int w = 0; w < 4; ++w) t += *reinterpret_cast<const f32x4*>(red3 + w * 1024 + 4 * tid);
    st16c(rs, WS_GA + ((tid >> 7) * 64 + idx) * 2048 + (tid & 127) * 16, u32x4{pk_bf(t[0], t[1]), tag, pk_bf(t[2], t[3]), tag});
    STAMP(5);
  }
  fin(IC<1>{}, WS_GA, 0, idx == 0);
  STAMP(6);
  STAMP_FLUSH(0); TR_FLUSH();
}

extern "C" void kernel_launch(void* const* d_in, const int* in_sizes, int n_in, void* d_out, int out_size, void* d_ws, size_t ws_size, hipStream_t stream) {
  const u16* xA = (const u16*)d_in[0];
  const u16* ckv = (const u16*)d_in[1];
  const u16* w_q = (const u16*)d_in[2];
  const u16* w_uk = (const u16*)d_in[3];
  const u16* w_uv = (const u16*)d_in[4];
  const u16* w_o = (const u16*)d_in[5];
  const u16* xB = (const u16*)d_in[6];
  const u16* kc = (const u16*)d_in[7];
  const u16* vc = (const u16*)d_in[8];
  const u16* wq = (const u16*)d_in[9];
  const u16* wo = (const u16*)d_in[10];
  kmain<<<256, 512, 0, stream>>>(ckv, w_q, w_uk, wq, xA, xB, w_uv, w_o, kc, vc, wo, (char*)d_ws, (u16*)d_out);
}
```

```cpp
#include <hip/hip_runtime.h>
#include <hip/hip_bf16.h>
#include <stdint.h>

#define DI __device__ __forceinline__
typedef __attribute__((ext_vector_type(4))) unsigned u32x4;
typedef __attribute__((ext_vector_type(2))) unsigned u32x2;
typedef __attribute__((ext_vector_type(4))) float f32x4;
typedef __attribute__((ext_vector_type(2))) float f32x2;
typedef __attribute__((ext_vector_type(16))) float f32x16;
typedef __attribute__((ext_vector_type(8))) __bf16 bf16x8_t;
typedef __attribute__((ext_vector_type(2))) __bf16 bf16x2_t;
typedef __attribute__((ext_vector_type(4))) short s16x4;
typedef __attribute__((ext_vector_type(8))) short s16x8;
typedef unsigned short u16;
typedef unsigned long long u64;

constexpr int NS0 = 13, NS1 = 17, NS2 = 16, NS3 = 18;
constexpr u64 TL0 = 0xc2b8de6543cb1c1ull, TH0 = 0x3de34ull, TL1 = 0x920718551349142ull, TH1 = 0x3eeb6caeaa7ull;
constexpr u64 TL2 = 0x96175a59238a183ull, TH2 = 0xff9d71b28ull, TL3 = 0x89f6d74d0308100ull, TH3 = 0xf3bdf3beda66ull;
constexpr int RA2 = 0xC0, RA3 = 3;
constexpr int AHL = 2;
constexpr int QPE = 5;
constexpr int WBW = 7;
#define TL 2365
#define TW 1640
constexpr unsigned TS0 = 1160, TS1 = 1440, TS2 = 1320, TS3 = 1440, STG = 12, BSG = 6, HL = 40, TP0 = TW + 170, TP2 = TW + 380, PA = 150, T0B = 440, T0Q = 770;
#define PAR(i, v) (v)
constexpr u64 LA2 = TH2 >> (6 * (NS2 - 11)), LA3 = TH3 >> (6 * (NS3 - 11));
constexpr u64 chunk_mask(u64 lo, u64 hi, int n) { u64 m = 0; for (int i = 0; i < n; ++i) m |= 1ull << ((i < 10 ? lo >> (6 * i) : hi >> (6 * (i - 10))) & 63); return m; }
static_assert((chunk_mask(TL0, TH0, NS0) | chunk_mask(TL1, TH1, NS1) | chunk_mask(TL2, TH2, NS2) | chunk_mask(TL3, TH3, NS3)) == ~0ull && (RA2 & RA3) == 0, "every chunk once");
static_assert(NS0 + NS1 + NS2 + NS3 == 64, "split sizes");
constexpr int NSMIN = 13, NSMAX = 18;
static_assert(NS0 >= NSMIN && NS1 >= NSMIN && NS2 - 1 >= NSMIN && NS3 - 1 >= NSMIN && NS0 <= NSMAX && NS1 + 1 <= NSMAX && NS2 <= NSMAX && NS3 <= NSMAX, "range");
constexpr int SK = 4096;
constexpr float C2 = 0.08838834764831845f * 1.4426950408889634f;
constexpr u64 GOLD = 0x9E3779B97F4A7C15ull;

constexpr unsigned WS_EP = 0;
constexpr unsigned WS_QX = 4096;
constexpr unsigned WS_QA = WS_QX + 2 * 32 * 256 * 8;
constexpr unsigned WS_AML = WS_QA + 2 * 32 * 512 * 2;
constexpr unsigned WS_CX = WS_AML + 32 * 2 * 16 * 128;
constexpr unsigned WS_GZ = WS_CX + 32 * 2 * 2 * 128 * 8;
constexpr unsigned WS_T0 = WS_GZ + 256 * 128;
constexpr unsigned WS_GB = WS_T0 + 64 * 128;
constexpr unsigned WS_GA = WS_GB + 2 * 128 * 256 * 8;
constexpr unsigned WS_APO = 2u << 20;
static_assert(WS_GA + 64 * 2 * 256 * 8 <= WS_APO, "layout");
constexpr unsigned WS_END = WS_APO + 32 * 2 * 64 * 256 * 2;
#define STAMP(i) do {} while (0)
#define STAMP_DECL do {} while (0)
#define STAMP_FLUSH(grp) do {} while (0)

#define TR() do {} while (0)
#define TR_FLUSH() do {} while (0)
DI float bflo(unsigned u) { return __uint_as_float(u << 16); }
DI float bfhi(unsigned u) { return __uint_as_float(u & 0xffff0000u); }
DI unsigned pk_bf(float a, float b) { const f32x2 v = {a, b}; return __builtin_bit_cast(unsigned, __builtin_convertvector(v, bf16x2_t)); }
DI float dot2(unsigned k, unsigned q, float acc) {
  return __builtin_amdgcn_fdot2_f32_bf16(__builtin_bit_cast(bf16x2_t, k), __builtin_bit_cast(bf16x2_t, q), acc, false);
}
DI unsigned plo(unsigned w0, unsigned w1) { return __builtin_amdgcn_perm(w1, w0, 0x05040100u); }
DI unsigned phi(unsigned w0, unsigned w1) { return __builtin_amdgcn_perm(w1, w0, 0x07060302u); }
template <int CTRL> DI float dpp(float x) {
  return __builtin_bit_cast(float, __builtin_amdgcn_mov_dpp(__builtin_bit_cast(int, x), CTRL, 0xf, 0xf, true));
}
DI float row16_sum(float x) { x += dpp<0xB1>(x); x += dpp<0x4E>(x); x += dpp<0x141>(x); x += dpp<0x128>(x); return x; }
DI float row16_max(float x) { x = fmaxf(x, dpp<0xB1>(x)); x = fmaxf(x, dpp<0x4E>(x)); x = fmaxf(x, dpp<0x141>(x)); x = fmaxf(x, dpp<0x128>(x)); return x; }
DI float xhalf_max(float x) { auto t = __builtin_amdgcn_permlane32_swap(__float_as_uint(x), __float_as_uint(x), false, false); return fmaxf(__uint_as_float(t[0]), __uint_as_float(t[1])); }
DI float xhalf_sum(float x) { auto t = __builtin_amdgcn_permlane32_swap(__float_as_uint(x), __float_as_uint(x), false, false); return __uint_as_float(t[0]) + __uint_as_float(t[1]); }
DI u32x4 ld16(const void* p) { return *reinterpret_cast<const u32x4*>(p); }
DI u32x4 ld16nt(const void* p) { return __builtin_nontemporal_load(reinterpret_cast<const u32x4*>(p)); }
DI float ex2(float x) { return __builtin_amdgcn_exp2f(x); }
#define SCHED() __builtin_amdgcn_sched_barrier(0)

typedef __amdgpu_buffer_rsrc_t rsrc_t;
DI u32x4 ld16c(rsrc_t rs, unsigned off) { return __builtin_amdgcn_raw_buffer_load_b128(rs, (int)off, 0, 16); }
DI u32x2 ld8c(rsrc_t rs, unsigned off) { return __builtin_amdgcn_raw_buffer_load_b64(rs, (int)off, 0, 16); }
DI void st16c(rsrc_t rs, unsigned off, u32x4 v) { __builtin_amdgcn_raw_buffer_store_b128(v, rs, (int)off, 0, 16); }
DI void st8c(rsrc_t rs, unsigned off, u32x2 v) { __builtin_amdgcn_raw_buffer_store_b64(v, rs, (int)off, 0, 16); }
template <int O1> DI void sld2(const char* p, u32x2& a, u32x2& b) {
  asm volatile("s_load_dwordx2 %0, %2, 0x0 glc\n\ts_load_dwordx2 %1, %2, %3 glc\n\ts_waitcnt lgkmcnt(0)" : "=&s"(a), "=&s"(b) : "s"(p), "n"(O1) : "memory");
}
typedef __attribute__((ext_vector_type(8))) unsigned u32x8;
DI void sld4x32(const char* p, u32x8 (&v)[4]) {
  asm volatile("s_load_dwordx8 %0, %4, 0x0 glc\n\ts_load_dwordx8 %1, %4, 0x80 glc\n\ts_load_dwordx8 %2, %4, 0x100 glc\n\ts_load_dwordx8 %3, %4, 0x180 glc\n\ts_waitcnt lgkmcnt(0)"
               : "=&s"(v[0]), "=&s"(v[1]), "=&s"(v[2]), "=&s"(v[3]) : "s"(p) : "memory");
}
constexpr int SPIN_MAX = 1 << 20;
constexpr unsigned OOB = 0x7ffffff0u;
DI void rawbar() { __builtin_amdgcn_sched_barrier(0); __builtin_amdgcn_s_barrier(); __builtin_amdgcn_sched_barrier(0); }
#define COMPILER_FENCE() asm volatile("" ::: "memory")
template <int O1, int SL> DI void shint(const char* p, unsigned tag) {
#pragma unroll 1
  for (int spin = 0; spin < SPIN_MAX; ++spin) {
    u32x2 a, b;
    sld2<O1>(p, a, b);
    if (((a[1] ^ tag) | (b[1] ^ tag)) == 0u) break;
    __builtin_amdgcn_s_sleep(SL);
  }
}

template <int N> struct IC { static constexpr int v = N; };
template <int I, int N, class F> DI void sfor(F&& f) { if constexpr (I < N) { f(IC<I>{}); sfor<I + 1, N>(f); } }
typedef __attribute__((address_space(3))) unsigned lu32;
DI void lds_wait(unsigned* cnt, unsigned target) {
#pragma unroll 1
  for (int spin = 0; spin < SPIN_MAX; ++spin) {
    if (__builtin_amdgcn_readfirstlane(__hip_atomic_load((lu32*)cnt, __ATOMIC_RELAXED, __HIP_MEMORY_SCOPE_WORKGROUP)) >= target) break;
    __builtin_amdgcn_s_sleep(1);
  }
  COMPILER_FENCE();
}
DI void lds_wait_slow(unsigned* cnt, unsigned target) {
#pragma unroll 1
  for (int spin = 0; spin < SPIN_MAX; ++spin) {
    if (__builtin_amdgcn_readfirstlane(__hip_atomic_load((lu32*)cnt, __ATOMIC_RELAXED, __HIP_MEMORY_SCOPE_WORKGROUP)) >= target) break;
    __builtin_amdgcn_s_sleep(3);
  }
  COMPILER_FENCE();
}
template <int SL = 1> DI void until(unsigned t) {
#pragma unroll 1
  for (int spin = 0; spin < 1024; ++spin) { if ((int)((unsigned)wall_clock64() - t) >= 0) break; __builtin_amdgcn_s_sleep(SL); }
}
DI void lds_hold(unsigned* flag) {
#pragma unroll 1
  for (int spin = 0; spin < SPIN_MAX; ++spin) {
    if (__builtin_amdgcn_readfirstlane(__hip_atomic_load((lu32*)flag, __ATOMIC_RELAXED, __HIP_MEMORY_SCOPE_WORKGROUP)) == 0u) break;
    __builtin_amdgcn_s_sleep(2);
  }
}
DI unsigned lds_arrive_nw(unsigned addr) {
  unsigned old, one = 1u;
  asm volatile("s_mov_b64 exec, 1\n\tds_add_rtn_u32 %0, %1, %2\n\ts_mov_b64 exec, -1" : "=v"(old) : "v"(addr), "v"(one) : "memory");
  return old;
}
DI unsigned lds_done(unsigned old) { asm volatile("s_waitcnt lgkmcnt(0)" : "+v"(old) :: "memory"); return __builtin_amdgcn_readfirstlane(old); }
DI unsigned lds_arrive(unsigned* cnt, int) { return lds_done(lds_arrive_nw((unsigned)(size_t)(lu32*)cnt)); }
DI void lds_add(unsigned* cnt, unsigned v, int) {
  asm volatile("s_mov_b64 exec, 1\n\tds_add_u32 %0, %1\n\ts_mov_b64 exec, -1" :: "v"((unsigned)(size_t)(lu32*)cnt), "v"(v) : "memory");
}
DI void sbar4(unsigned* cnt, int lane) { if (lds_arrive(cnt, lane) < 3u) lds_wait(cnt, 4u); }
DI float rs32(float a, float b) { auto t = __builtin_amdgcn_permlane32_swap(__float_as_uint(a), __float_as_uint(b), false, false); return __uint_as_float(t[0]) + __uint_as_float(t[1]); }
DI float rs16(float a, float b) { auto t = __builtin_amdgcn_permlane16_swap(__float_as_uint(a), __float_as_uint(b), false, false); return __uint_as_float(t[0]) + __uint_as_float(t[1]); }
template <int N> DI unsigned rowbcast(unsigned x) { return (unsigned)__builtin_amdgcn_mov_dpp((int)x, 0x150 + N, 0xf, 0xf, true); }
DI void pin2(u32x4& a, u32x4& b) { asm volatile("" : "+v"(a), "+v"(b)); }
DI void use2(const u32x4& a, const u32x4& b) { asm volatile("" :: "v"(a), "v"(b)); }
DI bool nz8(const u32x4& v) { return ((v[0] | v[1]) != 0u) & ((v[2] | v[3]) != 0u); }

DI u32x4 qsum(const u32x4& g) {
  const auto ta = __builtin_amdgcn_permlane32_swap(g[0], g[0], false, false), tc = __builtin_amdgcn_permlane32_swap(g[2], g[2], false, false);
  const unsigned a = pk_bf(bflo(ta[0]) + bflo(ta[1]), bfhi(ta[0]) + bfhi(ta[1])), c = pk_bf(bflo(tc[0]) + bflo(tc[1]), bfhi(tc[0]) + bfhi(tc[1]));
  const auto ra = __builtin_amdgcn_permlane16_swap(a, a, false, false), rc = __builtin_amdgcn_permlane16_swap(c, c, false, false);
  return u32x4{ra[0], rc[0], ra[1], rc[1]};
}
DI unsigned toff(unsigned row, unsigned ch) { return 256u * row + 16u * (ch ^ (((row & 3) << 2) | ((row >> 2) & 3))); }
constexpr int SMEM = 160 * 1024;
constexpr int L_SCR = 131072;
constexpr int L_MRG = 65536;
constexpr int L_CNT = L_SCR + 32 * 1008;

struct KV { u32x4 r[8]; };
constexpr int CODE_BYTES = 42624;

template <class T> DI T* gptr(unsigned lo, unsigned hi) { return (T*)(__attribute__((address_space(1))) T*)(((u64)hi << 32) | lo); }

__global__ __launch_bounds__(512, 2) void kmain(const u16*, const u16*, const u16*, const u16*, const u16*, const u16*, const u16*, const u16*, const u16*,
                                                const u16*, const u16*, char*, u16*) {
  u32x2 kpa, kpx, kc2; u32x4 kb4; u32x8 kb8; u64 t64;
  int tid = threadIdx.x, blk = blockIdx.x;
  const auto kp = __builtin_amdgcn_kernarg_segment_ptr();
  const int o1 = blk & 24, o2 = 32 + ((blk >> 1) & 8);
  asm volatile("s_load_dwordx2 %0, %8, %9\n\ts_load_dwordx2 %1, %8, %10\n\ts_load_dwordx4 %2, %8, 0x30\n\ts_load_dwordx8 %3, %8, 0x40\n\ts_load_dwordx2 %4, %8, 0x60\n\ts_memrealtime %5"
               : "=&s"(kpa), "=&s"(kpx), "=&s"(kb4), "=&s"(kb8), "=&s"(kc2), "=&s"(t64), "+v"(tid), "+s"(blk) : "s"(kp), "s"(o1), "s"(o2));
  __builtin_assume(tid >= 0 && tid < 512 && blk >= 0 && blk < 256);
  const unsigned cpf = __builtin_amdgcn_raw_buffer_load_b32(__builtin_amdgcn_make_buffer_rsrc((void*)(__builtin_amdgcn_s_getpc() & ~255ull), 0, CODE_BYTES, 0x00020000),
                                                            (int)((tid & 0x10f) == 0x100 ? (blk >> 3) * 2048 + (tid & 0xf0) * 8 : OOB), 0, 0);
  COMPILER_FENCE();
  SCHED();
  __shared__ __attribute__((aligned(16))) char smem[SMEM];
  const int lane = tid & 63;
  const int wave = __builtin_amdgcn_readfirstlane(tid >> 6), w4 = wave & 3;
  const int cls = (blk >> 3) & 3;
  int idx = 8 * (blk & 7) + (blk >> 5);
  idx = (idx & ~7) | ((idx + ((wave >= 4 ? 0 : 0x2400) >> (4 * cls))) & 7);
  const int h = idx >> 1, b = idx & 1;
  const int r4 = lane >> 4, c16 = lane & 15;
  unsigned* cnt = reinterpret_cast<unsigned*>(smem + L_CNT);
  STAMP_DECL;
  STAMP(0);
  const unsigned c0 = cls ? 0u : ~0u;
  unsigned sa = cls ? 8192u : 4096u;
  unsigned ao = ((((b * 256 + w4 * 64) * 8192 + h * 256) & ~c0) | (((idx * 128 + w4) * 1024) & c0)) + (r4 << (17u - (c0 & 9u))) + c16 * 16, xo = (b * 256 + w4 * 64 + 16 * r4) * 2;
  asm volatile("" : "+v"(ao), "+v"(xo), "+s"(sa));
  SCHED();
  asm volatile("s_waitcnt lgkmcnt(0)" : "+s"(kpa), "+s"(kpx), "+s"(kb4), "+s"(kb8), "+s"(kc2), "+s"(t64) : "s"(kp), "s"(o1), "s"(o2));
  const unsigned tstart = (unsigned)t64;
  const char* __restrict__ pa = gptr<const char>(kpa[0], kpa[1]); const char* __restrict__ xp = gptr<const char>(kpx[0], kpx[1]);
  const u16* __restrict__ w_uv = gptr<const u16>(kb4[0], kb4[1]); const u16* __restrict__ w_o = gptr<const u16>(kb4[2], kb4[3]);
  const u16* __restrict__ kc = gptr<const u16>(kb8[0], kb8[1]); const u16* __restrict__ vc = gptr<const u16>(kb8[2], kb8[3]);
  const u16* __restrict__ wo = gptr<const u16>(kb8[4], kb8[5]); char* __restrict__ ws = gptr<char>(kb8[6], kb8[7]);
  u16* __restrict__ out = gptr<u16>(kc2[0], kc2[1]);
  const rsrc_t rs = __builtin_amdgcn_make_buffer_rsrc(ws, 0, (int)WS_END, 0x00020000);
  unsigned tag, tlow;
  u32x2 epw;
  auto take_epoch = [&]() {
    asm volatile("" : "+v"(epw) : "v"(cpf));
    const u64 t64 = (((u64)(unsigned)__builtin_amdgcn_readfirstlane(epw[1]) << 32) | (unsigned)__builtin_amdgcn_readfirstlane(epw[0])) + GOLD;
    tag = (unsigned)(t64 >> 32); tlow = (unsigned)t64;
    asm volatile("s_waitcnt lgkmcnt(0)" : "+s"(tag), "+s"(tlow) :: "memory");
  };
  if (wave >= 4) {
    if (tid - 256 < 16) cnt[tid - 256] = 0u;
    asm volatile("s_waitcnt lgkmcnt(0)" ::: "memory");
    const unsigned pTL = PAR(0, TL), pTS = PAR(1 + cls, cls == 0 ? TS0 : (cls == 1 ? TS1 : (cls == 2 ? TS2 : TS3))), pSTG = PAR(9, STG), pBSG = PAR(10, BSG);
    const unsigned pRA2 = PAR(11, RA2), pRA3 = PAR(12, RA3), pTP = PAR(15 + cls, cls == 0 ? TP0 : TP2), pPA = PAR(19 + cls, (cls & 1) ? 0u : PA);
    rawbar();
    const int e2 = (pRA2 >> (idx & 7)) & 1, e3 = (pRA3 >> (idx & 7)) & 1;
    const int ns = cls == 0 ? NS0 : (cls == 1 ? NS1 + e2 + e3 : (cls == 2 ? NS2 - e2 : NS3 - e3));
    const unsigned pTA = __builtin_amdgcn_readfirstlane((unsigned)((float)pTS * __builtin_amdgcn_rcpf((float)(ns - 5)) + 0.5f));
    const size_t kvo = ((size_t)(b * 32 + h) * SK + w4 * 16) * 256;
    const char* kb = reinterpret_cast<const char*>(kc) + kvo;
    const char* vb = reinterpret_cast<const char*>(vc) + kvo;
    const unsigned lo = lane * 16u;
    u64 tlo = cls == 0 ? TL0 : (cls == 1 ? TL1 : (cls == 2 ? TL2 : TL3));
    u64 thi = cls == 0 ? TH0 : (cls == 1 ? TH1 | ((e2 ? LA2 : LA3) << (6 * (NS1 - 10))) : (cls == 2 ? TH2 : TH3));
    const u64 tlo0 = tlo, thi0 = thi;
    int hn = 0;
    const char* hb = kb;
    const char* pc[2];
    auto next2 = [&]() {
#pragma unroll
      for (int i = 0; i < 2; ++i) {
        if (hn == ns) { tlo = tlo0; thi = thi0; hb = vb; }
        ++hn;
        pc[i] = hb + ((((unsigned)tlo + (unsigned)idx) & 63u) << 14);
        tlo = (tlo >> 6) | ((thi & 63u) << 54); thi >>= 6;
      }
    };
    KV A, B;
    u32x4 W[8];
    u32x4 T[4][8];
    auto ldp = [&](KV& S, auto P) {
      constexpr int p = P.v;
      const char* q = pc[p >> 1] + lo + (p & 1) * 2048;
      S.r[2 * p] = ld16nt(q); S.r[2 * p + 1] = ld16nt(q + 1024);
    };
    auto landed = [&](KV& S, auto P) { pin2(S.r[2 * P.v], S.r[2 * P.v + 1]); };
    KV X;
    const unsigned qo = WS_QX + ((32 + h) * 2 + b) * 1024 + lane * 16;
    u32x4 g0;
    {
      unsigned tk = 0u;
      if (w4 == 0) {
        const int si = (idx & ~7) | ((idx + (0x2400 >> (4 * cls))) & 7);
        const char* p = kb + cls * 128;
        if (lane == 1) p = cls == 2 ? reinterpret_cast<const char*>(w_uv) + (si & 1) * 2097152 + (si >> 1) * 256 : reinterpret_cast<const char*>(cls == 1 ? w_o : wo) + si * 65536;
        if (lane == 2) p = vb + cls * 128;
        if (lane == 3) p = reinterpret_cast<const char*>(wo) + h * 131072;
        if (lane < 4) tk = __builtin_nontemporal_load(reinterpret_cast<const unsigned*>(p));
      }
      lds_wait_slow(cnt + 14, 4u);
      tag = __builtin_amdgcn_readfirstlane(cnt[16]);
      asm volatile("" :: "v"(tk), "v"(cpf));
    }
    STAMP(10);
    sfor<0, 12>([&](auto I) {
      constexpr int i = I.v, j = i - 4;
      if constexpr ((i & 3) == 0) next2();
      if (i == 4 && w4 == 3) {
#pragma unroll 1
        for (int spin = 0; spin < 2; ++spin) {
          u32x2 t[8];
          asm volatile("s_load_dwordx2 %0, %8, 0x0 glc\n\ts_load_dwordx2 %1, %8, 0x400 glc\n\ts_load_dwordx2 %2, %8, 0x800 glc\n\ts_load_dwordx2 %3, %8, 0xc00 glc\n\t"
                       "s_load_dwordx2 %4, %8, 0x1000 glc\n\ts_load_dwordx2 %5, %8, 0x1400 glc\n\ts_load_dwordx2 %6, %8, 0x1800 glc\n\ts_load_dwordx2 %7, %8, 0x1c00 glc\n\ts_waitcnt lgkmcnt(0)"
                       : "=&s"(t[0]), "=&s"(t[1]), "=&s"(t[2]), "=&s"(t[3]), "=&s"(t[4]), "=&s"(t[5]), "=&s"(t[6]), "=&s"(t[7]) : "s"(ws + WS_T0 + ((blk >> 3) & 7) * 128) : "memory");
          unsigned e = 0u; int sum = 0;
#pragma unroll
          for (int k = 0; k < 8; ++k) { e |= t[k][1] ^ tag; sum += (int)(t[k][0] - tstart); }
          if (e == 0u) { lds_add(cnt + 13, (unsigned)min(sum >> 3, 100), lane); break; }
        }
      }
      if constexpr (j >= 0) { if constexpr (j < 4) landed(X, IC<j & 3>{}); else landed(B, IC<j & 3>{}); }
      if constexpr (j >= 0 && (j & 1)) TR();
      if constexpr (i == QPE) g0 = ld16c(rs, qo);
      if constexpr (i < 4) ldp(X, IC<i & 3>{}); else if constexpr (i < 8) ldp(B, IC<i & 3>{}); else ldp(A, IC<i & 3>{});
      SCHED();
    });
    const unsigned torg = tstart + __builtin_amdgcn_readfirstlane(cnt[13]);
    const char* wp = reinterpret_cast<const char*>(wo) + ((size_t)h * 128 + r4 * 8) * 1024 + w4 * 256 + c16 * 16;
    u32x4 qpk;
    {
      if (!__all((g0[1] == tag) & (g0[3] == tag))) {
#pragma unroll 1
        for (int spin = 0; spin < SPIN_MAX; ++spin) {
          COMPILER_FENCE();
          g0 = ld16c(rs, qo);
          if (__all((g0[1] == tag) & (g0[3] == tag))) break;
        }
      }
      qpk = qsum(g0);
    }
    STAMP(7);
    const bool holds = cls == 0;
    float m = 0.f, l = 0.f, acc[8];
    unsigned sc[5];
#pragma unroll
    for (int k = 0; k < 8; ++k) acc[k] = 0.f;
#pragma unroll
    for (int k = 0; k < 5; ++k) sc[k] = 0xff800000u;
    int vn = 0;
    unsigned due = torg + pTL - (unsigned)(ns - 5) * pTA - (3 - w4) * pSTG - ((idx * 5 + cls * 3) & 15) * pBSG;
    unsigned rwo;
    const unsigned zo = WS_GZ + (b * 32 + h) * 512;
    u32x2 zs[4];
    float wr_;
    auto zreq = [&]() {
      asm volatile("s_load_dwordx2 %0, %4, 0x0 glc\n\ts_load_dwordx2 %1, %4, 0x80 glc\n\ts_load_dwordx2 %2, %4, 0x100 glc\n\ts_load_dwordx2 %3, %4, 0x180 glc"
                   : "=&s"(zs[0]), "=&s"(zs[1]), "=&s"(zs[2]), "=&s"(zs[3]) : "s"(ws + zo) : "memory");
    };
    auto zget = [&]() {
#pragma unroll 1
      for (int spin = 0; spin < SPIN_MAX; ++spin) {
        asm volatile("s_waitcnt lgkmcnt(0)" : "+s"(zs[0]), "+s"(zs[1]), "+s"(zs[2]), "+s"(zs[3]) :: "memory");
        if (((zs[0][1] ^ tag) | (zs[1][1] ^ tag) | (zs[2][1] ^ tag) | (zs[3][1] ^ tag)) == 0u) break;
        __builtin_amdgcn_s_sleep(2);
        zreq();
      }
      asm volatile("s_waitcnt lgkmcnt(0)" : "+s"(zs[0]), "+s"(zs[1]), "+s"(zs[2]), "+s"(zs[3]) :: "memory");
      const float z0 = __uint_as_float(zs[0][0]), z1 = __uint_as_float(zs[1][0]), z2 = __uint_as_float(zs[2][0]), z3 = __uint_as_float(zs[3][0]);
      const float Z = fmaxf(fmaxf(z0, z1), fmaxf(z2, z3));
      wr_ = ex2(fmaf(m, C2, -Z)) * __builtin_amdgcn_rcpf((ex2(z0 - Z) + ex2(z1 - Z)) + (ex2(z2 - Z) + ex2(z3 - Z)));
      asm volatile("" : "+v"(wr_));
    };
    auto qk = [&](const u32x4* K, auto I) {
      float x[4];
#pragma unroll
      for (int t = 0; t < 4; ++t) {
        x[t] = 0.f;
#pragma unroll
        for (int k = 0; k < 4; ++k) x[t] = dot2(K[t][k], qpk[k], x[t]);
      }
      const float u0 = (c16 & 1 ? x[1] : x[0]) + dpp<0xB1>(c16 & 1 ? x[0] : x[1]), u1 = (c16 & 1 ? x[3] : x[2]) + dpp<0xB1>(c16 & 1 ? x[2] : x[3]);
      float v = (c16 & 2 ? u1 : u0) + dpp<0x4E>(c16 & 2 ? u0 : u1);
      v += dpp<0x124>(v); v += dpp<0x128>(v);
      sc[I.v >> 2] = __builtin_amdgcn_update_dpp(sc[I.v >> 2], __float_as_uint(v), 0xE4, 0xf, 1 << (I.v & 3), false);
      asm volatile("" : "+v"(sc[I.v >> 2]));
    };
    auto soft = [&]() {
      m = row16_max(fmaxf(fmaxf(fmaxf(__uint_as_float(sc[0]), __uint_as_float(sc[1])), fmaxf(__uint_as_float(sc[2]), __uint_as_float(sc[3]))), __uint_as_float(sc[4])));
      const float mc = m * C2;
      l = 0.f;
#pragma unroll
      for (int k = 0; k < 5; ++k) {
        const float p = ex2(fmaf(__uint_as_float(sc[k]), C2, -mc));
        sc[k] = pk_bf(p, dpp<0xB1>(p));
        l = dot2(sc[k], 0x3f003f00u, l);
      }
      l = row16_sum(l);
    };
    auto pv = [&](const u32x4* V) {
      const unsigned p01 = rowbcast<0>(sc[0]), p23 = rowbcast<2>(sc[0]);
#pragma unroll
      for (int k = 0; k < 4; ++k) { acc[2 * k] = dot2(plo(V[0][k], V[1][k]), p01, acc[2 * k]); acc[2 * k + 1] = dot2(phi(V[0][k], V[1][k]), p01, acc[2 * k + 1]); }
#pragma unroll
      for (int k = 0; k < 4; ++k) { acc[2 * k] = dot2(plo(V[2][k], V[3][k]), p23, acc[2 * k]); acc[2 * k + 1] = dot2(phi(V[2][k], V[3][k]), p23, acc[2 * k + 1]); }
      sc[0] = (unsigned)__builtin_amdgcn_mov_dpp((int)sc[0], 0x12C, 0xf, 0xf, true);
      if ((++vn & 3) == 0) { sc[0] = sc[1]; sc[1] = sc[2]; sc[2] = sc[3]; sc[3] = sc[4]; }
      asm volatile("" : "+v"(acc[0]), "+v"(acc[1]), "+v"(acc[2]), "+v"(acc[3]), "+v"(acc[4]), "+v"(acc[5]), "+v"(acc[6]), "+v"(acc[7]));
    };
    auto step = [&](KV& S, KV& N, auto SI, auto REFILL, auto WR) {
      constexpr bool refill = REFILL.v == 1;
      constexpr int wr = WR.v;
      if constexpr (refill) next2();
      if constexpr (refill && SI.v >= 3) { until((int)(due - torg) < (int)pTP ? due - pPA : due); due += pTA; }
      constexpr int ah = wr < 0 ? AHL : 0;
      auto point = [&](auto P) {
        if constexpr (refill) {
          if constexpr (P.v == 0 && ah > 0) { if constexpr (wr >= 0) { if (holds) lds_hold(cnt + 8); } sfor<0, ah>([&](auto Q) { ldp(N, Q); }); SCHED(); }
          landed(S, P);
          if constexpr (P.v & 1) TR();
          if constexpr (P.v + ah < 4) { if constexpr ((P.v & 1) == 0 && wr >= 0) { if (holds) lds_hold(cnt + 8); } ldp(N, IC<(P.v + ah) & 3>{}); }
          if constexpr (wr >= 0 && wr < 8) W[(4 * wr + P.v) & 7] = ld16(wp + (((4 * wr + P.v) >> 3) * 32 + ((4 * wr + P.v) & 7)) * 1024);
          SCHED();
        }
      };
      sfor<0, 2>([&](auto H) {
        constexpr int i = 2 * SI.v + H.v;
        if constexpr (REFILL.v == 2 && H.v == 0) { zreq(); SCHED(); }
        point(IC<2 * H.v>{});
        point(IC<2 * H.v + 1>{});
        if constexpr (i < NSMIN) qk(S.r + 4 * H.v, IC<i>{});
        else if constexpr (i > NSMAX) pv(S.r + 4 * H.v);
        else {
          if (i < ns) { if constexpr (i < NSMAX) qk(S.r + 4 * H.v, IC<i>{}); }
          else { if (i == ns) soft(); pv(S.r + 4 * H.v); }
        }
        SCHED();
      });
      if constexpr (REFILL.v == 2) {
        zget();
        STAMP(1);
        *reinterpret_cast<u32x4*>(smem + rwo) = u32x4{pk_bf(acc[0] * wr_, acc[1] * wr_), pk_bf(acc[2] * wr_, acc[3] * wr_), pk_bf(acc[4] * wr_, acc[5] * wr_), pk_bf(acc[6] * wr_, acc[7] * wr_)};
        COMPILER_FENCE();
      }
      if constexpr (wr >= 1) {
        constexpr int t = (wr - 1) >> 1, j0 = ((wr - 1) & 1) * 2;
#pragma unroll
        for (int jj = 0; jj < 2; ++jj)
#pragma unroll
          for (int e = 0; e < 4; ++e) {
            const unsigned a_ = W[(4 * wr - 4 + 2 * jj) & 7][e], b_ = W[(4 * wr - 3 + 2 * jj) & 7][e];
            T[t][2 * e][j0 + jj] = plo(a_, b_); T[t][2 * e + 1][j0 + jj] = phi(a_, b_);
          }
#pragma unroll
        for (int e = 0; e < 8; e += 2) pin2(T[t][e], T[t][e + 1]);
      }
      SCHED();
    };
    step(B, B, IC<1>{}, IC<0>{}, IC<-1>{});
    step(A, B, IC<2>{}, IC<1>{}, IC<0>{});
    step(X, X, IC<0>{}, IC<0>{}, IC<-1>{});
    sfor<0, 4>([&](auto I) {
      step(B, A, IC<2 * I.v + 3>{}, IC<1>{}, IC<2 * I.v + 1>{});
      if constexpr (I.v == 3) {
        float* ml = reinterpret_cast<float*>(cnt + 32);
        ml[(c16 & 1) * 16 + w4 * 4 + r4] = (c16 & 1) ? l : m;
        COMPILER_FENCE();
        const unsigned got = lds_arrive_nw(L_CNT + 44);
        const float mr = ml[c16], lr = ml[16 + c16];
        SCHED();
        if (lds_done(got) == 3u) {
          const float M = row16_max(mr);
          const unsigned z = __float_as_uint(fmaf(M, C2, __builtin_amdgcn_logf(row16_sum(ex2((mr - M) * C2) * lr))));
          asm volatile("buffer_store_dwordx4 %0, %1, %2, 0 offen sc1\n\ts_nop 2" :: "v"(u32x4{z, tag, z, tag}), "v"(lane < 8 ? zo + cls * 128 + lane * 16 : OOB), "s"(rs) : "memory");
        }
        SCHED();
      }
      step(A, B, IC<2 * I.v + 4>{}, IC<1>{}, IC<2 * I.v + 2>{});
    });
    unsigned yoff = WS_GB + ((b * 128 + h * 4 + cls) * 256 + 64 * w4 + lane) * 8, rro = L_MRG + c16 * 272 + r4 * 16;
    rwo = L_MRG + (w4 * 4 + r4) * 272 + c16 * 16;
    asm volatile("" : "+v"(yoff), "+v"(rwo), "+v"(rro));
    if (cls == 0) lds_wait(cnt + 1, 4u);
    const int nit = (ns - 12) >> 1;
#pragma unroll 1
    for (int i = 0; i < nit; ++i) {
      step(B, A, IC<11>{}, IC<1>{}, IC<-1>{});
      step(A, B, IC<11>{}, IC<1>{}, IC<-1>{});
    }
    if (ns & 1) { step(B, A, IC<11>{}, IC<1>{}, IC<-1>{}); step(A, A, IC<11>{}, IC<2>{}, IC<-1>{}); }
    else step(B, B, IC<11>{}, IC<2>{}, IC<-1>{});
    {
      u32x4 af[4];
      unsigned got = lds_arrive_nw(L_CNT), need = 3u;
#pragma unroll 1
      for (int spin = 0; spin < SPIN_MAX; ++spin) {
#pragma unroll
        for (int t = 0; t < 4; ++t) af[t] = *reinterpret_cast<const u32x4*>(smem + rro + t * 64);
        SCHED();
        if (lds_done(got) >= need) break;
        need = 4u;
        __builtin_amdgcn_s_sleep(1);
        asm volatile("ds_read_b32 %0, %1" : "=v"(got) : "v"((unsigned)L_CNT) : "memory");
      }
      SCHED();
      f32x4 d[8];
      float a[8], v4[4];
      sfor<0, 5>([&](auto I) {
        constexpr int i = I.v;
        sfor<0, 8>([&](auto J) {
          constexpr int e = i + 4 * (J.v & 1), t = J.v >> 1;
          if constexpr (i < 4) { d[e] = __builtin_amdgcn_mfma_f32_16x16x32_bf16(__builtin_bit_cast(bf16x8_t, af[t]), __builtin_bit_cast(bf16x8_t, T[t][e]), t ? d[e] : f32x4{0.f, 0.f, 0.f, 0.f}, 0, 0, 0); SCHED(); }
          if constexpr (i >= 1 && (J.v == 4 || J.v == 5)) { const f32x2 u = f32x2{d[e - 1][0], d[e - 1][1]} + f32x2{d[e - 1][2], d[e - 1][3]}; a[e - 1] = u[0] + u[1]; asm volatile("" : "+v"(a[e - 1])); }
          if constexpr (i >= 1 && J.v == 6) v4[i - 1] = rs32(a[i - 1], a[i + 3]);
          if constexpr (i < 4) SCHED();
        });
      });
      st8c(rs, yoff, u32x2{pk_bf(rs16(v4[0], v4[2]), rs16(v4[1], v4[3])), tag});
    }
    STAMP(2);
    STAMP_FLUSH(1);
    TR_FLUSH();
    return;
  }
  const rsrc_t ro = __builtin_amdgcn_make_buffer_rsrc(out, 0, 4096, 0x00020000);
  auto fin = [&](auto NL, unsigned base, unsigned col, bool bump) {
    constexpr int nl = NL.v;
    unsigned doff = base + (((idx >> 5) * 4 + w4) * 16 * nl + (lane >> 2)) * 2048 + (idx & 31) * 64 + (lane & 3) * 16;
    unsigned fwo = 32768 + (c16 < 4 ? (w4 * 16 + 4 * c16 + r4) * 4 : 256 + lane * 4);
    unsigned fro = 32768 + (lane & 7) * 8, foo = lane < 8 ? ((idx >> 5) * 1024 + col + (nl == 2 ? 16 * (idx & 24) + 64 * (idx & 1) + (idx & 6) + 8 * lane : 16 * (idx & 31) + 2 * lane)) * 2 : OOB;
    asm volatile("" : "+v"(doff), "+v"(fwo), "+v"(fro), "+v"(foo));
    lds_wait(cnt + 0, 4u);
    STAMP(4);
    u32x4 d[nl];
    unsigned eh = 1u;
#pragma unroll 1
    for (int spin = SPIN_MAX; spin > 0; --spin) {
      COMPILER_FENCE();
      if (eh != 0u) {
#pragma unroll
        for (int j = 0; j < nl; ++j) d[j] = ld16c(rs, doff + j * 32768);
        eh = 0u;
#pragma unroll
        for (int j = 0; j < nl; ++j) { eh |= (d[j][1] ^ tag) | (d[j][3] ^ tag); asm volatile("" : "+v"(eh)); }
      }
      if (__builtin_amdgcn_ballot_w64(eh != 0u) == 0ull) break;
    }
    STAMP(11);
    float y[4] = {bflo(d[0][0]), bfhi(d[0][0]), bflo(d[0][2]), bfhi(d[0][2])};
    if constexpr (nl == 2) { y[0] += bflo(d[1][0]); y[1] += bfhi(d[1][0]); y[2] += bflo(d[1][2]); y[3] += bfhi(d[1][2]); }
    float t = rs16(rs32(y[0], y[2]), rs32(y[1], y[3]));
    t += dpp<0x124>(t); t += dpp<0x128>(t);
    *reinterpret_cast<float*>(smem + fwo) = t;
    COMPILER_FENCE();
    const unsigned got = lds_arrive_nw(L_CNT + 24);
    f32x2 t2[4];
#pragma unroll
    for (int w = 0; w < 4; ++w) t2[w] = *reinterpret_cast<const f32x2*>(smem + fro + w * 64);
    SCHED();
    if (lds_done(got) == 3u) {
      const f32x2 r = (t2[0] + t2[1]) + (t2[2] + t2[3]);
      __builtin_amdgcn_raw_buffer_store_b32(pk_bf(r[0], r[1]), ro, (int)foo, 0, 0);
      if (bump) st8c(rs, lane == 0 ? WS_EP : OOB, u32x2{tlow, tag});
    }
  };
  u32x4 Wa[16], Wb[16], xv[4];
  sfor<0, 2>([&](auto I) { Wa[I.v] = ld16nt(pa + ao); SCHED(); ao += sa; asm volatile("" : "+v"(ao)); });
  sfor<0, 4>([&](auto J) { xv[J.v] = ld16(xp + xo + (J.v >> 1) * 1024 + 16 * (J.v & 1)); });
  SCHED();
  sfor<2, 8>([&](auto I) { Wa[I.v] = ld16nt(pa + ao); SCHED(); ao += sa; asm volatile("" : "+v"(ao)); });
  epw = ld8c(rs, WS_EP);
  const unsigned qa_off = WS_QA + (((c16 >> 1) & 1) * 32 + h) * 1024 + (b * 256 + w4 * 64 + 16 * r4 + 8 * (c16 & 1)) * 2;
  st16c(rs, cls == 1 && c16 < 4 ? qa_off : OOB, u32x4{0u, 0u, 0u, 0u});
  SCHED();
  sfor<8, 16>([&](auto I) { Wa[I.v] = ld16nt(pa + ao); SCHED(); ao += sa; asm volatile("" : "+v"(ao)); });
  const u16* pb = cls == 0 ? reinterpret_cast<const u16*>(pa) + ((size_t)idx * 128 + 64 + w4) * 512 + 8 * lane
                           : (cls == 2 ? w_uv + ((size_t)(b * 256 + w4 * 64 + 16 * r4) * 32 + h) * 128 + 8 * c16 : (cls == 1 ? w_o : wo) + ((size_t)h * 128 + b * 64 + w4 * 2) * 512 + 8 * lane);
  {
    rawbar();
    take_epoch();
    cnt[16] = tag;
    COMPILER_FENCE();
    if (blk < 8 && w4 == 0) asm volatile("buffer_store_dwordx4 %0, %1, %2, 0 offen sc1\n\ts_nop 2" :: "v"(u32x4{tstart, tag, tstart, tag}), "v"(WS_T0 + blk * 1024 + lane * 16), "s"(rs) : "memory");
  }
  STAMP(13);
  float* red = reinterpret_cast<float*>(smem);
  if (cls & 1) {
    float acc[2][8];
#pragma unroll
    for (int bb = 0; bb < 2; ++bb)
#pragma unroll
      for (int k = 0; k < 8; ++k) acc[bb][k] = 0.f;
    sfor<0, 8>([&](auto IP) {
      constexpr int ip = IP.v, j = ip - (8 - WBW);
      use2(Wa[2 * ip], Wa[2 * ip + 1]);
      if constexpr (ip & 1) TR();
      if constexpr (j >= 0) { Wb[2 * j] = ld16(pb + j * 4096); Wb[2 * j + 1] = ld16(pb + j * 4096 + 512); }
      SCHED();
      const unsigned xp0 = xv[ip >> 2][ip & 3], xp1 = xv[2 + (ip >> 2)][ip & 3];
#pragma unroll
      for (int k = 0; k < 4; ++k) {
        const unsigned lo = plo(Wa[2 * ip][k], Wa[2 * ip + 1][k]), hi = phi(Wa[2 * ip][k], Wa[2 * ip + 1][k]);
        acc[0][2 * k] = dot2(lo, xp0, acc[0][2 * k]); acc[0][2 * k + 1] = dot2(hi, xp0, acc[0][2 * k + 1]);
        acc[1][2 * k] = dot2(lo, xp1, acc[1][2 * k]); acc[1][2 * k + 1] = dot2(hi, xp1, acc[1][2 * k + 1]);
      }
      SCHED();
    });
    {
      float v8[8], v4[4];
#pragma unroll
      for (int k = 0; k < 8; ++k) v8[k] = rs32(acc[0][k], acc[1][k]);
#pragma unroll
      for (int k = 0; k < 4; ++k) v4[k] = rs16(v8[k], v8[k + 4]);
      *reinterpret_cast<f32x4*>(red + (w4 * 2 + (lane >> 5)) * 128 + 8 * c16 + 4 * (r4 & 1)) = f32x4{v4[0], v4[1], v4[2], v4[3]};
    }
    COMPILER_FENCE();
    const unsigned got = lds_arrive_nw(L_CNT + 8);
    f32x4 t4[4];
#pragma unroll
    for (int w = 0; w < 4; ++w) t4[w] = *reinterpret_cast<const f32x4*>(red + (w * 2 + (lane >> 5)) * 128 + 8 * c16 + 4 * (r4 & 1));
    SCHED();
    if (lds_done(got) == 3u) {
      const f32x4 t = (t4[0] + t4[1]) + (t4[2] + t4[3]);
      st16c(rs, WS_QX + (((cls >> 1) * 32 + h) * 2 + (lane >> 5)) * 1024 + b * 512 + (lane & 31) * 16, u32x4{pk_bf(t[0], t[1]), tag, pk_bf(t[2], t[3]), tag});
      STAMP(15);
      SCHED();
      lds_add(cnt + 2, 1u, lane);
    } else lds_wait(cnt + 2, 5u);
    sfor<WBW, 8>([&](auto J) {
      constexpr int j = J.v;
      use2(Wb[2 * (j - WBW)], Wb[2 * (j - WBW) + 1]);
      if constexpr ((j - WBW) & 1) TR();
      Wb[2 * j] = ld16(pb + j * 4096); Wb[2 * j + 1] = ld16(pb + j * 4096 + 512);
      if constexpr (j == 7) lds_add(cnt + 14, 1u, lane);
      SCHED();
    });
    {
      const unsigned tw = __builtin_nontemporal_load(reinterpret_cast<const unsigned*>(out + (idx >> 5) * 1024 + (cls >> 1) * 512 + 16 * (idx & 31)));
      sfor<8 - WBW, 8>([&](auto J) { use2(Wb[2 * J.v], Wb[2 * J.v + 1]); if constexpr (J.v & 1) TR(); });
      asm volatile("" :: "v"(tw));
    }
    STAMP(8);
  } else if (cls == 2) {
    sfor<0, 16 - WBW>([&](auto IP) {
      constexpr int ip = IP.v, j = ip - (8 - WBW);
      if constexpr (ip < 8) use2(Wa[2 * ip], Wa[2 * ip + 1]); else use2(Wb[2 * ip - 16], Wb[2 * ip - 15]);
      if constexpr (ip & 1) TR();
      if constexpr (j >= 0) { Wb[2 * j] = ld16nt(pb + j * 8192); Wb[2 * j + 1] = ld16nt(pb + j * 8192 + 4096); }
      SCHED();
    });
    u32x4 g0, g1;
    const unsigned qo = WS_QX + h * 2048 + lane * 16;
    shint<512, 1>(ws + WS_QX + h * 2048, tag);
    STAMP(5);
#pragma unroll 1
    for (int spin = 0; spin < SPIN_MAX; ++spin) {
      COMPILER_FENCE();
      g0 = ld16c(rs, qo); g1 = ld16c(rs, qo + 1024);
      SCHED();
      lds_add(cnt + 14, spin == 0, lane);
      SCHED();
      if (__all((g0[1] == tag) & (g0[3] == tag) & (g1[1] == tag) & (g1[3] == tag))) break;
      __builtin_amdgcn_s_sleep(1);
    }
    sfor<8 - WBW, 8>([&](auto J) { if constexpr (J.v & 1) TR(); });
    const u32x4 q0 = qsum(g0), q1 = qsum(g1);
    STAMP(9);
    float v[8];
    {
      float u[16];
#pragma unroll
      for (int i = 0; i < 16; ++i) {
        float a = 0.f, c = 0.f;
#pragma unroll
        for (int k = 0; k < 4; ++k) { a = dot2(Wa[i][k], q0[k], a); c = dot2(Wa[i][k], q1[k], c); }
        u[i] = (c16 & 2 ? c : a) + dpp<0x4E>(c16 & 2 ? a : c);
        asm volatile("" : "+v"(u[i]));
      }
#pragma unroll
      for (int i = 0; i < 8; ++i) {
        float t = (c16 & 1 ? u[i + 8] : u[i]) + dpp<0xB1>(c16 & 1 ? u[i] : u[i + 8]);
        asm volatile("" : "+v"(t));
        t += dpp<0x124>(t);
        asm volatile("" : "+v"(t));
        t += dpp<0x128>(t);
        asm volatile("" : "+v"(t));
        v[i] = t;
      }
    }
    u32x4 o = {pk_bf(v[0], v[1]), pk_bf(v[2], v[3]), pk_bf(v[4], v[5]), pk_bf(v[6], v[7])};
    if ((o[0] | o[1]) == 0u) o[0] = 0x8000u;
    if ((o[2] | o[3]) == 0u) o[2] = 0x8000u;
    if (c16 < 4) st16c(rs, qa_off, o);
    STAMP(10);
  }
  asm volatile("" :: "v"(xv[0]), "v"(xv[1]), "v"(xv[2]), "v"(xv[3]));
  SCHED();
  if (cls == 3) {
    fin(IC<2>{}, WS_GB, 512, false);
    STAMP(6);
    STAMP_FLUSH(0); TR_FLUSH();
    return;
  }
  if (cls == 0) {
    char* scr = smem + L_SCR;
    const int ab = idx >> 5, r = lane & 31, hh = lane >> 5;
    const unsigned fo = WS_QX + (w4 * 8 + (c16 >> 1)) * 2048 + (c16 & 1) * 512 + r4 * 8;
    u32x2 fl;
    u32x4 g[9];
    const unsigned o8 = WS_QA + (ab * 32 + w4 * 8) * 1024 + lane * 16, ol = WS_QA + (ab * 32 + r) * 1024 + 992 + 16 * hh;
    auto put = [&](int i, const u32x4& v) { *reinterpret_cast<u32x4*>(smem + ((i >> 3) * 4 + r4) * 8192 + toff(4 * (i & 7) + w4, c16)) = v; };
    sfor<0, 16>([&](auto I) { put(I.v, Wa[I.v]); if constexpr ((I.v & 3) == 3) TR(); SCHED(); });
    until(tstart + PAR(26, T0B));
    sfor<0, 16>([&](auto I) { Wb[I.v] = ld16nt(pb + I.v * 2048); });
    SCHED();
    lds_add(cnt + 14, 1u, lane);
    fl = ld8c(rs, fo);
    SCHED();
    sfor<0, 16>([&](auto I) { put(16 + I.v, Wb[I.v]); if constexpr ((I.v & 3) == 3) TR(); SCHED(); });
    STAMP(8);
    u32x4 qlast;
    {
      if (!__all(fl[1] == tag)) {
#pragma unroll 1
        for (int spin = 0; spin < SPIN_MAX; ++spin) {
          COMPILER_FENCE();
          if (__all(ld8c(rs, fo)[1] == tag)) break;
        }
      }
      until(tstart + PAR(27, T0Q));
      STAMP(5);
      sbar4(cnt + 7, lane);
      STAMP(12);
      unsigned pend = 0x1ffu;
#pragma unroll 1
      for (int spin = 0; spin < SPIN_MAX && pend; ++spin) {
        COMPILER_FENCE();
        u32x4 t[9];
#pragma unroll
        for (int i = 0; i < 9; ++i) t[i] = ld16c(rs, (pend >> i & 1) ? (i < 8 ? o8 + i * 1024 : ol) : OOB);
#pragma unroll
        for (int i = 0; i < 9; ++i) {
          if (pend >> i & 1) g[i] = t[i];
          if (__all(nz8(t[i]))) pend &= ~(1u << i);
        }
      }
      STAMP(9);
      qlast = g[8];
#pragma unroll
      for (int i = 0; i < 8; ++i)
        if (lane < 62) *reinterpret_cast<u32x4*>(scr + (w4 * 8 + i) * 1008 + 16 * lane) = g[i];
    }
    sbar4(cnt + 3, lane);
    STAMP(10);
    f32x16 sacc;
#pragma unroll
    for (int i = 0; i < 16; ++i) sacc[i] = 0.f;
#pragma unroll
    for (int jj = 0; jj < 32; ++jj) {
      const u32x4 af = *reinterpret_cast<const u32x4*>(smem + (w4 * 4 + (jj >> 3)) * 8192 + toff(r, 2 * (jj & 7) + hh));
      const u32x4 bf = jj < 31 ? *reinterpret_cast<const u32x4*>(scr + r * 1008 + 16 * (2 * (jj < 31 ? jj : 0) + hh)) : qlast;
      sacc = __builtin_amdgcn_mfma_f32_32x32x16_bf16(__builtin_bit_cast(bf16x8_t, af), __builtin_bit_cast(bf16x8_t, bf), sacc, 0, 0, 0);
    }
    float m = sacc[0];
#pragma unroll
    for (int i = 1; i < 16; ++i) m = fmaxf(m, sacc[i]);
    m = xhalf_max(m);
    if (hh == 0) *reinterpret_cast<float*>(scr + r * 1008 + 992 + 4 * w4) = m;
    sbar4(cnt + 4, lane);
    STAMP(11);
    {
      const f32x4 m4 = *reinterpret_cast<const f32x4*>(scr + r * 1008 + 992);
      m = fmaxf(fmaxf(m4[0], m4[1]), fmaxf(m4[2], m4[3]));
    }
    const float mc = m * C2;
    float p[16], ls = 0.f;
#pragma unroll
    for (int i = 0; i < 16; ++i) { p[i] = ex2(fmaf(sacc[i], C2, -mc)); ls += p[i]; }
    ls = xhalf_sum(ls);
    sbar4(cnt + 5, lane);
    u32x4* pf = reinterpret_cast<u32x4*>(scr);
    float* wl = reinterpret_cast<float*>(scr + 8192);
    if (hh == 0) wl[r * 4 + w4] = ls;
    pf[(2 * w4) * 64 + lane] = u32x4{pk_bf(p[0], p[1]), pk_bf(p[2], p[3]), pk_bf(p[4], p[5]), pk_bf(p[6], p[7])};
    pf[(2 * w4 + 1) * 64 + lane] = u32x4{pk_bf(p[8], p[9]), pk_bf(p[10], p[11]), pk_bf(p[12], p[13]), pk_bf(p[14], p[15])};
    sbar4(cnt + 6, lane);
    u32x4 pb8[8];
#pragma unroll
    for (int ks = 0; ks < 8; ++ks) pb8[ks] = pf[ks * 64 + lane];
    const f32x4 l4 = *reinterpret_cast<const f32x4*>(wl + r * 4);
    const float lsum = (l4[0] + l4[1]) + (l4[2] + l4[3]), rl = __builtin_amdgcn_rcpf(lsum);
    const int qq = (lane & 15) >> 2, pp = lane & 3, blk2 = (lane >> 4) & 1;
    const unsigned tw = tstart + __builtin_amdgcn_readfirstlane(cnt[13]) + PAR(13, TW), th = tw - PAR(14, HL);
    u32x4 vv[2][4];
    bool held = false;
    char* ot = scr + 12288 + w4 * 4096;
#pragma unroll
    for (int c2 = 0; c2 < 2; ++c2) {
      f32x16 oacc[2];
#pragma unroll
      for (int ct = 0; ct < 2; ++ct)
#pragma unroll
        for (int i = 0; i < 16; ++i) oacc[ct][i] = 0.f;
#pragma unroll
      for (int ks = 0; ks < 8; ++ks)
#pragma unroll
        for (int ct = 0; ct < 2; ++ct) {
          typedef __attribute__((address_space(3))) s16x4* lp;
          const char* tile = smem + ((ks >> 1) * 4 + w4) * 8192;
          const int cc = 2 * c2 + ct;
          const s16x4 lo = __builtin_amdgcn_ds_read_tr16_b64_v4i16((lp)(tile + toff(16 * (ks & 1) + 4 * hh + qq, 4 * cc + 2 * blk2 + (pp >> 1)) + 8 * (pp & 1)));
          const s16x4 hi = __builtin_amdgcn_ds_read_tr16_b64_v4i16((lp)(tile + toff(16 * (ks & 1) + 8 + 4 * hh + qq, 4 * cc + 2 * blk2 + (pp >> 1)) + 8 * (pp & 1)));
          const s16x8 afr = __builtin_shufflevector(lo, hi, 0, 1, 2, 3, 4, 5, 6, 7);
          oacc[ct] = __builtin_amdgcn_mfma_f32_32x32x16_bf16(__builtin_bit_cast(bf16x8_t, afr), __builtin_bit_cast(bf16x8_t, pb8[ks]), oacc[ct], 0, 0, 0);
        }
#pragma unroll
      for (int ct = 0; ct < 2; ++ct)
#pragma unroll
        for (int g = 0; g < 4; ++g)
          *reinterpret_cast<u32x2*>(ot + r * 128 + 16 * ((4 * ct + g) ^ (r & 7)) + 8 * hh) = u32x2{pk_bf(oacc[ct][4 * g] * rl, oacc[ct][4 * g + 1] * rl), pk_bf(oacc[ct][4 * g + 2] * rl, oacc[ct][4 * g + 3] * rl)};
      if (c2 == 0) { held = (int)((unsigned)wall_clock64() + 30u - th) >= 0; if (held) lds_add(cnt + 8, 1u, lane); }
      __builtin_amdgcn_wave_barrier();
#pragma unroll
      for (int i = 0; i < 4; ++i) { vv[c2][i] = *reinterpret_cast<const u32x4*>(ot + (8 * i + (lane >> 3)) * 128 + 16 * ((lane ^ (lane >> 3)) & 7)); asm volatile("" : "+v"(vv[c2][i])); }
      __builtin_amdgcn_wave_barrier();
    }
    lds_arrive(cnt + 1, lane);
    if (!held) { until(th); lds_add(cnt + 8, 1u, lane); }
    until(tw);
#pragma unroll
    for (int i = 0; i < 8; ++i) st16c(rs, WS_APO + ((((8 * (i >> 1) + (lane >> 3)) * 2 + (w4 >> 1)) * 64 + idx) * 256 + (w4 & 1) * 128 + 64 * (i & 1) + 8 * (lane & 7)) * 2, vv[i & 1][i >> 1]);
    STAMP(1);
    asm volatile("s_waitcnt vmcnt(0)" ::: "memory");
    if (lds_arrive(cnt + 9 + (w4 >> 1), lane) == 1u && hh == 0)
      st8c(rs, WS_AML + ((r * 2 + (w4 >> 1)) * 16 + (idx >> 2)) * 128 + (idx & 3) * 8, u32x2{__float_as_uint(mc + __builtin_amdgcn_logf(lsum)), tag});
    lds_add(cnt + 8, ~0u, lane);
    STAMP(2);
    STAMP_FLUSH(0); TR_FLUSH();
    return;
  }
#pragma unroll
  for (int ip = 0; ip < 8; ++ip) {
#pragma unroll
    for (int k = 0; k < 4; ++k) { const unsigned lo = plo(Wb[2 * ip][k], Wb[2 * ip + 1][k]), hi = phi(Wb[2 * ip][k], Wb[2 * ip + 1][k]); Wb[2 * ip][k] = lo; Wb[2 * ip + 1][k] = hi; }
    pin2(Wb[2 * ip], Wb[2 * ip + 1]);
  }
  if (cls == 2) {
    float* red2 = red + 4 * 256;
    float* wml = red + 3072;
    u32x8 f[4];
    until<6>(tstart + PAR(13, TW) + PAR(28, 60));
#pragma unroll 1
    for (int spin = 0; spin < SPIN_MAX; ++spin) {
      sld4x32(ws + WS_AML + ((h * 2 + b) * 16 + w4 * 4) * 128, f);
      unsigned e = 0u;
#pragma unroll
      for (int i = 0; i < 16; ++i) e |= f[i >> 2][2 * (i & 3) + 1] ^ tag;
      if (e == 0u) break;
      __builtin_amdgcn_s_sleep(2);
    }
    STAMP(3);
    u32x4 pp[8];
#pragma unroll
    for (int i = 0; i < 8; ++i) pp[i] = ld16c(rs, WS_APO + ((h * 2 + b) * 64 + w4 * 16 + 2 * i) * 512 + lane * 16);
    SCHED();
    float wt[8];
    {
      float M = -INFINITY, L = 0.f;
#pragma unroll
      for (int i = 0; i < 8; ++i) {
        unsigned z0 = f[i >> 1][4 * (i & 1)], z1 = f[i >> 1][4 * (i & 1) + 2];
        asm("" : "+s"(z0), "+s"(z1));
        wt[i] = __uint_as_float(lane < 32 ? z0 : z1); M = fmaxf(M, wt[i]);
      }
      M = xhalf_max(M);
#pragma unroll
      for (int i = 0; i < 8; ++i) { wt[i] = ex2(wt[i] - M); L += wt[i]; }
      L = xhalf_sum(L);
      if (lane == 0) *reinterpret_cast<f32x2*>(wml + 2 * w4) = f32x2{M, L};
    }
    SCHED();
    float a8[8];
#pragma unroll
    for (int k = 0; k < 8; ++k) a8[k] = 0.f;
#pragma unroll
    for (int i = 0; i < 8; ++i) {
#pragma unroll
      for (int k = 0; k < 4; ++k) { a8[2 * k] = fmaf(wt[i], bflo(pp[i][k]), a8[2 * k]); a8[2 * k + 1] = fmaf(wt[i], bfhi(pp[i][k]), a8[2 * k + 1]); }
      asm volatile("" : "+v"(a8[0]), "+v"(a8[1]), "+v"(a8[2]), "+v"(a8[3]), "+v"(a8[4]), "+v"(a8[5]), "+v"(a8[6]), "+v"(a8[7]));
    }
    STAMP(6);
    *reinterpret_cast<f32x4*>(red + w4 * 256 + 8 * (lane & 31) + 4 * (lane >> 5)) = f32x4{rs32(a8[0], a8[4]), rs32(a8[1], a8[5]), rs32(a8[2], a8[6]), rs32(a8[3], a8[7])};
    sbar4(cnt + 3, lane);
    STAMP(4);
    float acc[2][8];
#pragma unroll
    for (int bb = 0; bb < 2; ++bb) {
      u32x4 cv[2];
      const f32x4 ml = *reinterpret_cast<const f32x4*>(wml + 4 * bb);
      const float M = fmaxf(ml[0], ml[2]), e0 = ex2(ml[0] - M), e1 = ex2(ml[2] - M), rl = 1.f / fmaf(e0, ml[1], e1 * ml[3]), s0 = e0 * rl, s1 = e1 * rl;
#pragma unroll
      for (int e = 0; e < 2; ++e) {
        const float* src = red + bb * 512 + w4 * 64 + 16 * r4 + 8 * e;
        const f32x4 t0 = s0 * *reinterpret_cast<const f32x4*>(src) + s1 * *reinterpret_cast<const f32x4*>(src + 256), t1 = s0 * *reinterpret_cast<const f32x4*>(src + 4) + s1 * *reinterpret_cast<const f32x4*>(src + 260);
        cv[e] = u32x4{pk_bf(t0[0], t0[1]), pk_bf(t0[2], t0[3]), pk_bf(t1[0], t1[1]), pk_bf(t1[2], t1[3])};
      }
#pragma unroll
      for (int k = 0; k < 8; ++k) acc[bb][k] = 0.f;
#pragma unroll
      for (int ip = 0; ip < 8; ++ip)
#pragma unroll
        for (int k = 0; k < 4; ++k) { acc[bb][2 * k] = dot2(Wb[2 * ip][k], cv[ip >> 2][ip & 3], acc[bb][2 * k]); acc[bb][2 * k + 1] = dot2(Wb[2 * ip + 1][k], cv[ip >> 2][ip & 3], acc[bb][2 * k + 1]); }
    }
    {
      float v8[8], v4[4];
#pragma unroll
      for (int k = 0; k < 8; ++k) v8[k] = rs32(acc[0][k], acc[1][k]);
#pragma unroll
      for (int k = 0; k < 4; ++k) v4[k] = rs16(v8[k], v8[k + 4]);
      *reinterpret_cast<f32x4*>(red2 + (w4 * 2 + (lane >> 5)) * 128 + 8 * c16 + 4 * (r4 & 1)) = f32x4{v4[0], v4[1], v4[2], v4[3]};
    }
    {
      COMPILER_FENCE();
      const unsigned got = lds_arrive_nw(L_CNT + 16);
      f32x4 t4[4];
#pragma unroll
      for (int w = 0; w < 4; ++w) t4[w] = *reinterpret_cast<const f32x4*>(red2 + w * 256 + 4 * lane);
      SCHED();
      if (lds_done(got) == 3u) {
        const f32x4 t = (t4[0] + t4[1]) + (t4[2] + t4[3]);
        const unsigned co = WS_CX + ((h * 2 + b) * 256 + 64 * r4 + 32 * (c16 & 1) + 2 * (c16 >> 1)) * 8;
        st16c(rs, co, u32x4{__float_as_uint(t[0]), tag, __float_as_uint(t[1]), tag});
        st16c(rs, co + 128, u32x4{__float_as_uint(t[2]), tag, __float_as_uint(t[3]), tag});
      }
    }
    STAMP(12);
    STAMP_FLUSH(0); TR_FLUSH();
    return;
  }
  {
    unsigned op;
    {
      u32x4 c;
      const unsigned co = WS_CX + ((h * 2 + ((lane >> 3) & 1)) * 256 + (lane >> 5) * 128 + b * 64 + w4 * 16 + 2 * (lane & 7)) * 8;
      if (idx == 0 && w4 == 1) {
#pragma unroll 1
        for (int spin = 0; spin < SPIN_MAX; ++spin) {
          COMPILER_FENCE();
          if (__all(ld8c(rs, WS_QX + 65536 + (lane >> 1) * 2048 + (lane & 1) * 512)[1] == tag)) break;
          __builtin_amdgcn_s_sleep(8);
        }
      }
      until<6>(tstart + PAR(13, TW) + PAR(29, 370));
      if (w4 == 0) { shint<2048, 0>(ws + WS_CX + (h * 512 + b * 64) * 8, tag); lds_add(cnt + 7, 1u, lane); }
      else lds_wait(cnt + 7, 1u);
      STAMP(3);
#pragma unroll 1
      for (int spin = 0; spin < SPIN_MAX; ++spin) {
        COMPILER_FENCE();
        c = ld16c(rs, co);
        if (__all((c[1] == tag) & (c[3] == tag))) break;
        __builtin_amdgcn_s_sleep(1);
      }
      float o0 = __uint_as_float(c[0]), o1 = __uint_as_float(c[2]);
      o0 += dpp<0x128>(o0); o1 += dpp<0x128>(o1);
      op = pk_bf(o0, o1);
    }
    STAMP(14);
    float* red3 = red + 4096;
    float a[2][8];
#pragma unroll
    for (int bb = 0; bb < 2; ++bb)
#pragma unroll
      for (int k = 0; k < 8; ++k) a[bb][k] = 0.f;
#pragma unroll
    for (int ip = 0; ip < 8; ++ip) {
      const unsigned v0 = __builtin_amdgcn_readlane(op, ip), v1 = __builtin_amdgcn_readlane(op, 32 + ip);
#pragma unroll
      for (int k = 0; k < 4; ++k) {
        a[0][2 * k] = dot2(Wb[2 * ip][k], v0, a[0][2 * k]); a[0][2 * k + 1] = dot2(Wb[2 * ip + 1][k], v0, a[0][2 * k + 1]);
        a[1][2 * k] = dot2(Wb[2 * ip][k], v1, a[1][2 * k]); a[1][2 * k + 1] = dot2(Wb[2 * ip + 1][k], v1, a[1][2 * k + 1]);
      }
    }
#pragma unroll
    for (int bb = 0; bb < 2; ++bb) {
      *reinterpret_cast<f32x4*>(red3 + (w4 * 2 + bb) * 512 + 8 * lane) = f32x4{a[bb][0], a[bb][1], a[bb][2], a[bb][3]};
      *reinterpret_cast<f32x4*>(red3 + (w4 * 2 + bb) * 512 + 8 * lane + 4) = f32x4{a[bb][4], a[bb][5], a[bb][6], a[bb][7]};
    }
    sbar4(cnt + 5, lane);
    f32x4 t = {0.f, 0.f, 0.f, 0.f};
#pragma unroll
    for (int w = 0; w < 4; ++w) t += *reinterpret_cast<const f32x4*>(red3 + w * 1024 + 4 * tid);
    st16c(rs, WS_GA + ((tid >> 7) * 64 + idx) * 2048 + (tid & 127) * 16, u32x4{pk_bf(t[0], t[1]), tag, pk_bf(t[2], t[3]), tag});
    STAMP(5);
  }
  fin(IC<1>{}, WS_GA, 0, idx == 0);
  STAMP(6);
  STAMP_FLUSH(0); TR_FLUSH();
}

extern "C" void kernel_launch(void* const* d_in, const int* in_sizes, int n_in, void* d_out, int out_size, void* d_ws, size_t ws_size, hipStream_t stream) {
  const u16* xA = (const u16*)d_in[0];
  const u16* ckv = (const u16*)d_in[1];
  const u16* w_q = (const u16*)d_in[2];
  const u16* w_uk = (const u16*)d_in[3];
  const u16* w_uv = (const u16*)d_in[4];
  const u16* w_o = (const u16*)d_in[5];
  const u16* xB = (const u16*)d_in[6];
  const u16* kc = (const u16*)d_in[7];
  const u16* vc = (const u16*)d_in[8];
  const u16* wq = (const u16*)d_in[9];
  const u16* wo = (const u16*)d_in[10];
  kmain<<<256, 512, 0, stream>>>(ckv, w_q, w_uk, wq, xA, xB, w_uv, w_o, kc, vc, wo, (char*)d_ws, (u16*)d_out);
}
```

```cpp
#include <hip/hip_runtime.h>
#include <hip/hip_bf16.h>
#include <stdint.h>

#define DI __device__ __forceinline__
typedef __attribute__((ext_vector_type(4))) unsigned u32x4;
typedef __attribute__((ext_vector_type(2))) unsigned u32x2;
typedef __attribute__((ext_vector_type(4))) float f32x4;
typedef __attribute__((ext_vector_type(2))) float f32x2;
typedef __attribute__((ext_vector_type(16))) float f32x16;
typedef __attribute__((ext_vector_type(8))) __bf16 bf16x8_t;
typedef __attribute__((ext_vector_type(2))) __bf16 bf16x2_t;
typedef __attribute__((ext_vector_type(4))) short s16x4;
typedef __attribute__((ext_vector_type(8))) short s16x8;
typedef unsigned short u16;
typedef unsigned long long u64;

constexpr int NS0 = 13, NS1 = 17, NS2 = 16, NS3 = 18;
constexpr u64 TL0 = 0xc2b8de6543cb1c1ull, TH0 = 0x3de34ull, TL1 = 0x920718551349142ull, TH1 = 0x3eeb6caeaa7ull;
constexpr u64 TL2 = 0x96175a59238a183ull, TH2 = 0xff9d71b28ull, TL3 = 0x89f6d74d0308100ull, TH3 = 0xf3bdf3beda66ull;
constexpr int RA2 = 0xC0, RA3 = 3;
constexpr int AHL = 2;
constexpr int QPE = 7;
constexpr int WBW = 5;
#define TL 2355
#define TW 1650
constexpr unsigned TS0 = 1160, TS1 = 1420, TS2 = 1320, TS3 = 1420, STG = 12, BSG = 6, HL = 40, TP0 = TW + 170, TP2 = TW + 380, PA = 150, T0B = 420, T0Q = 880;
#define PAR(i, v) (v)
constexpr u64 LA2 = TH2 >> (6 * (NS2 - 11)), LA3 = TH3 >> (6 * (NS3 - 11));
constexpr u64 chunk_mask(u64 lo, u64 hi, int n) { u64 m = 0; for (int i = 0; i < n; ++i) m |= 1ull << ((i < 10 ? lo >> (6 * i) : hi >> (6 * (i - 10))) & 63); return m; }
static_assert((chunk_mask(TL0, TH0, NS0) | chunk_mask(TL1, TH1, NS1) | chunk_mask(TL2, TH2, NS2) | chunk_mask(TL3, TH3, NS3)) == ~0ull && (RA2 & RA3) == 0, "every chunk once");
static_assert(NS0 + NS1 + NS2 + NS3 == 64, "split sizes");
constexpr int NSMIN = 13, NSMAX = 18;
static_assert(NS0 >= NSMIN && NS1 >= NSMIN && NS2 - 1 >= NSMIN && NS3 - 1 >= NSMIN && NS0 <= NSMAX && NS1 + 1 <= NSMAX && NS2 <= NSMAX && NS3 <= NSMAX, "range");
constexpr int SK = 4096;
constexpr float C2 = 0.08838834764831845f * 1.4426950408889634f;
constexpr u64 GOLD = 0x9E3779B97F4A7C15ull;

constexpr unsigned WS_EP = 0;
constexpr unsigned WS_QX = 4096;
constexpr unsigned WS_QA = WS_QX + 2 * 32 * 256 * 8;
constexpr unsigned WS_AML = WS_QA + 2 * 32 * 512 * 2;
constexpr unsigned WS_CX = WS_AML + 32 * 2 * 16 * 128;
constexpr unsigned WS_GZ = WS_CX + 32 * 2 * 2 * 128 * 8;
constexpr unsigned WS_T0 = WS_GZ + 256 * 128;
constexpr unsigned WS_GB = WS_T0 + 64 * 128;
constexpr unsigned WS_GA = WS_GB + 2 * 128 * 256 * 8;
constexpr unsigned WS_APO = 2u << 20;
static_assert(WS_GA + 64 * 2 * 256 * 8 <= WS_APO, "layout");
constexpr unsigned WS_END = WS_APO + 32 * 2 * 64 * 256 * 2;
#define STAMP(i) do {} while (0)
#define STAMP_DECL do {} while (0)
#define STAMP_FLUSH(grp) do {} while (0)

#define TR() do {} while (0)
#define TR_FLUSH() do {} while (0)
DI float bflo(unsigned u) { return __uint_as_float(u << 16); }
DI float bfhi(unsigned u) { return __uint_as_float(u & 0xffff0000u); }
DI unsigned pk_bf(float a, float b) { const f32x2 v = {a, b}; return __builtin_bit_cast(unsigned, __builtin_convertvector(v, bf16x2_t)); }
DI float dot2(unsigned k, unsigned q, float acc) {
  return __builtin_amdgcn_fdot2_f32_bf16(__builtin_bit_cast(bf16x2_t, k), __builtin_bit_cast(bf16x2_t, q), acc, false);
}
DI unsigned plo(unsigned w0, unsigned w1) { return __builtin_amdgcn_perm(w1, w0, 0x05040100u); }
DI unsigned phi(unsigned w0, unsigned w1) { return __builtin_amdgcn_perm(w1, w0, 0x07060302u); }
template <int CTRL> DI float dpp(float x) {
  return __builtin_bit_cast(float, __builtin_amdgcn_mov_dpp(__builtin_bit_cast(int, x), CTRL, 0xf, 0xf, true));
}
DI float row16_sum(float x) { x += dpp<0xB1>(x); x += dpp<0x4E>(x); x += dpp<0x141>(x); x += dpp<0x128>(x); return x; }
DI float row16_max(float x) { x = fmaxf(x, dpp<0xB1>(x)); x = fmaxf(x, dpp<0x4E>(x)); x = fmaxf(x, dpp<0x141>(x)); x = fmaxf(x, dpp<0x128>(x)); return x; }
DI float xhalf_max(float x) { auto t = __builtin_amdgcn_permlane32_swap(__float_as_uint(x), __float_as_uint(x), false, false); return fmaxf(__uint_as_float(t[0]), __uint_as_float(t[1])); }
DI float xhalf_sum(float x) { auto t = __builtin_amdgcn_permlane32_swap(__float_as_uint(x), __float_as_uint(x), false, false); return __uint_as_float(t[0]) + __uint_as_float(t[1]); }
DI u32x4 ld16(const void* p) { return *reinterpret_cast<const u32x4*>(p); }
DI u32x4 ld16nt(const void* p) { return __builtin_nontemporal_load(reinterpret_cast<const u32x4*>(p)); }
DI float ex2(float x) { return __builtin_amdgcn_exp2f(x); }
#define SCHED() __builtin_amdgcn_sched_barrier(0)

typedef __amdgpu_buffer_rsrc_t rsrc_t;
DI u32x4 ld16c(rsrc_t rs, unsigned off) { return __builtin_amdgcn_raw_buffer_load_b128(rs, (int)off, 0, 16); }
DI u32x2 ld8c(rsrc_t rs, unsigned off) { return __builtin_amdgcn_raw_buffer_load_b64(rs, (int)off, 0, 16); }
DI void st16c(rsrc_t rs, unsigned off, u32x4 v) { __builtin_amdgcn_raw_buffer_store_b128(v, rs, (int)off, 0, 16); }
DI void st8c(rsrc_t rs, unsigned off, u32x2 v) { __builtin_amdgcn_raw_buffer_store_b64(v, rs, (int)off, 0, 16); }
template <int O1> DI void sld2(const char* p, u32x2& a, u32x2& b) {
  asm volatile("s_load_dwordx2 %0, %2, 0x0 glc\n\ts_load_dwordx2 %1, %2, %3 glc\n\ts_waitcnt lgkmcnt(0)" : "=&s"(a), "=&s"(b) : "s"(p), "n"(O1) : "memory");
}
typedef __attribute__((ext_vector_type(8))) unsigned u32x8;
DI void sld4x32(const char* p, u32x8 (&v)[4]) {
  asm volatile("s_load_dwordx8 %0, %4, 0x0 glc\n\ts_load_dwordx8 %1, %4, 0x80 glc\n\ts_load_dwordx8 %2, %4, 0x100 glc\n\ts_load_dwordx8 %3, %4, 0x180 glc\n\ts_waitcnt lgkmcnt(0)"
               : "=&s"(v[0]), "=&s"(v[1]), "=&s"(v[2]), "=&s"(v[3]) : "s"(p) : "memory");
}
constexpr int SPIN_MAX = 1 << 20;
constexpr unsigned OOB = 0x7ffffff0u;
DI void rawbar() { __builtin_amdgcn_sched_barrier(0); __builtin_amdgcn_s_barrier(); __builtin_amdgcn_sched_barrier(0); }
#define COMPILER_FENCE() asm volatile("" ::: "memory")
template <int O1, int SL> DI void shint(const char* p, unsigned tag) {
#pragma unroll 1
  for (int spin = 0; spin < SPIN_MAX; ++spin) {
    u32x2 a, b;
    sld2<O1>(p, a, b);
    if (((a[1] ^ tag) | (b[1] ^ tag)) == 0u) break;
    __builtin_amdgcn_s_sleep(SL);
  }
}

template <int N> struct IC { static constexpr int v = N; };
template <int I, int N, class F> DI void sfor(F&& f) { if constexpr (I < N) { f(IC<I>{}); sfor<I + 1, N>(f); } }
typedef __attribute__((address_space(3))) unsigned lu32;
DI void lds_wait(unsigned* cnt, unsigned target) {
#pragma unroll 1
  for (int spin = 0; spin < SPIN_MAX; ++spin) {
    if (__builtin_amdgcn_readfirstlane(__hip_atomic_load((lu32*)cnt, __ATOMIC_RELAXED, __HIP_MEMORY_SCOPE_WORKGROUP)) >= target) break;
    __builtin_amdgcn_s_sleep(1);
  }
  COMPILER_FENCE();
}
DI void lds_wait_slow(unsigned* cnt, unsigned target) {
#pragma unroll 1
  for (int spin = 0; spin < SPIN_MAX; ++spin) {
    if (__builtin_amdgcn_readfirstlane(__hip_atomic_load((lu32*)cnt, __ATOMIC_RELAXED, __HIP_MEMORY_SCOPE_WORKGROUP)) >= target) break;
    __builtin_amdgcn_s_sleep(3);
  }
  COMPILER_FENCE();
}
template <int SL = 1> DI void until(unsigned t) {
#pragma unroll 1
  for (int spin = 0; spin < 1024; ++spin) { if ((int)((unsigned)wall_clock64() - t) >= 0) break; __builtin_amdgcn_s_sleep(SL); }
}
DI void lds_hold(unsigned* flag) {
#pragma unroll 1
  for (int spin = 0; spin < SPIN_MAX; ++spin) {
    if (__builtin_amdgcn_readfirstlane(__hip_atomic_load((lu32*)flag, __ATOMIC_RELAXED, __HIP_MEMORY_SCOPE_WORKGROUP)) == 0u) break;
    __builtin_amdgcn_s_sleep(2);
  }
}
DI unsigned lds_arrive_nw(unsigned addr) {
  unsigned old, one = 1u;
  asm volatile("s_mov_b64 exec, 1\n\tds_add_rtn_u32 %0, %1, %2\n\ts_mov_b64 exec, -1" : "=v"(old) : "v"(addr), "v"(one) : "memory");
  return old;
}
DI unsigned lds_done(unsigned old) { asm volatile("s_waitcnt lgkmcnt(0)" : "+v"(old) :: "memory"); return __builtin_amdgcn_readfirstlane(old); }
DI unsigned lds_arrive(unsigned* cnt, int) { return lds_done(lds_arrive_nw((unsigned)(size_t)(lu32*)cnt)); }
DI void lds_add(unsigned* cnt, unsigned v, int) {
  asm volatile("s_mov_b64 exec, 1\n\tds_add_u32 %0, %1\n\ts_mov_b64 exec, -1" :: "v"((unsigned)(size_t)(lu32*)cnt), "v"(v) : "memory");
}
DI void sbar4(unsigned* cnt, int lane) { if (lds_arrive(cnt, lane) < 3u) lds_wait(cnt, 4u); }
DI float rs32(float a, float b) { auto t = __builtin_amdgcn_permlane32_swap(__float_as_uint(a), __float_as_uint(b), false, false); return __uint_as_float(t[0]) + __uint_as_float(t[1]); }
DI float rs16(float a, float b) { auto t = __builtin_amdgcn_permlane16_swap(__float_as_uint(a), __float_as_uint(b), false, false); return __uint_as_float(t[0]) + __uint_as_float(t[1]); }
template <int N> DI unsigned rowbcast(unsigned x) { return (unsigned)__builtin_amdgcn_mov_dpp((int)x, 0x150 + N, 0xf, 0xf, true); }
DI void pin2(u32x4& a, u32x4& b) { asm volatile("" : "+v"(a), "+v"(b)); }
DI void use2(const u32x4& a, const u32x4& b) { asm volatile("" :: "v"(a), "v"(b)); }
DI bool nz8(const u32x4& v) { return ((v[0] | v[1]) != 0u) & ((v[2] | v[3]) != 0u); }

DI u32x4 qsum(const u32x4& g) {
  const auto ta = __builtin_amdgcn_permlane32_swap(g[0], g[0], false, false), tc = __builtin_amdgcn_permlane32_swap(g[2], g[2], false, false);
  const unsigned a = pk_bf(bflo(ta[0]) + bflo(ta[1]), bfhi(ta[0]) + bfhi(ta[1])), c = pk_bf(bflo(tc[0]) + bflo(tc[1]), bfhi(tc[0]) + bfhi(tc[1]));
  const auto ra = __builtin_amdgcn_permlane16_swap(a, a, false, false), rc = __builtin_amdgcn_permlane16_swap(c, c, false, false);
  return u32x4{ra[0], rc[0], ra[1], rc[1]};
}
DI unsigned toff(unsigned row, unsigned ch) { return 256u * row + 16u * (ch ^ (((row & 3) << 2) | ((row >> 2) & 3))); }
constexpr int SMEM = 160 * 1024;
constexpr int L_SCR = 131072;
constexpr int L_MRG = 65536;
constexpr int L_CNT = L_SCR + 32 * 1008;

struct KV { u32x4 r[8]; };
constexpr int CODE_BYTES = 42624;

template <class T> DI T* gptr(unsigned lo, unsigned hi) { return (T*)(__attribute__((address_space(1))) T*)(((u64)hi << 32) | lo); }

__global__ __launch_bounds__(512, 2) void kmain(const u16*, const u16*, const u16*, const u16*, const u16*, const u16*, const u16*, const u16*, const u16*,
                                                const u16*, const u16*, char*, u16*) {
  u32x2 kpa, kpx, kc2; u32x4 kb4; u32x8 kb8; u64 t64;
  int tid = threadIdx.x, blk = blockIdx.x;
  const auto kp = __builtin_amdgcn_kernarg_segment_ptr();
  const int o1 = blk & 24, o2 = 32 + ((blk >> 1) & 8);
  asm volatile("s_load_dwordx2 %0, %8, %9\n\ts_load_dwordx2 %1, %8, %10\n\ts_load_dwordx4 %2, %8, 0x30\n\ts_load_dwordx8 %3, %8, 0x40\n\ts_load_dwordx2 %4, %8, 0x60\n\ts_memrealtime %5"
               : "=&s"(kpa), "=&s"(kpx), "=&s"(kb4), "=&s"(kb8), "=&s"(kc2), "=&s"(t64), "+v"(tid), "+s"(blk) : "s"(kp), "s"(o1), "s"(o2));
  __builtin_assume(tid >= 0 && tid < 512 && blk >= 0 && blk < 256);
  const unsigned cpf = __builtin_amdgcn_raw_buffer_load_b32(__builtin_amdgcn_make_buffer_rsrc((void*)(__builtin_amdgcn_s_getpc() & ~255ull), 0, CODE_BYTES, 0x00020000),
                                                            (int)((tid & 0x10f) == 0x100 ? (blk >> 3) * 2048 + (tid & 0xf0) * 8 : OOB), 0, 0);
  COMPILER_FENCE();
  SCHED();
  __shared__ __attribute__((aligned(16))) char smem[SMEM];
  const int lane = tid & 63;
  const int wave = __builtin_amdgcn_readfirstlane(tid >> 6), w4 = wave & 3;
  const int cls = (blk >> 3) & 3;
  int idx = 8 * (blk & 7) + (blk >> 5);
  idx = (idx & ~7) | ((idx + ((wave >= 4 ? 0 : 0x2400) >> (4 * cls))) & 7);
  const int h = idx >> 1, b = idx & 1;
  const int r4 = lane >> 4, c16 = lane & 15;
  unsigned* cnt = reinterpret_cast<unsigned*>(smem + L_CNT);
  STAMP_DECL;
  STAMP(0);
  const unsigned c0 = cls ? 0u : ~0u;
  unsigned sa = cls ? 8192u : 4096u;
  unsigned ao = ((((b * 256 + w4 * 64) * 8192 + h * 256) & ~c0) | (((idx * 128 + w4) * 1024) & c0)) + (r4 << (17u - (c0 & 9u))) + c16 * 16, xo = (b * 256 + w4 * 64 + 16 * r4) * 2;
  asm volatile("" : "+v"(ao), "+v"(xo), "+s"(sa));
  SCHED();
  asm volatile("s_waitcnt lgkmcnt(0)" : "+s"(kpa), "+s"(kpx), "+s"(kb4), "+s"(kb8), "+s"(kc2), "+s"(t64) : "s"(kp), "s"(o1), "s"(o2));
  const unsigned tstart = (unsigned)t64;
  const char* __restrict__ pa = gptr<const char>(kpa[0], kpa[1]); const char* __restrict__ xp = gptr<const char>(kpx[0], kpx[1]);
  const u16* __restrict__ w_uv = gptr<const u16>(kb4[0], kb4[1]); const u16* __restrict__ w_o = gptr<const u16>(kb4[2], kb4[3]);
  const u16* __restrict__ kc = gptr<const u16>(kb8[0], kb8[1]); const u16* __restrict__ vc = gptr<const u16>(kb8[2], kb8[3]);
  const u16* __restrict__ wo = gptr<const u16>(kb8[4], kb8[5]); char* __restrict__ ws = gptr<char>(kb8[6], kb8[7]);
  u16* __restrict__ out = gptr<u16>(kc2[0], kc2[1]);
  const rsrc_t rs = __builtin_amdgcn_make_buffer_rsrc(ws, 0, (int)WS_END, 0x00020000);
  unsigned tag, tlow;
  u32x2 epw;
  auto take_epoch = [&]() {
    asm volatile("" : "+v"(epw) : "v"(cpf));
    const u64 t64 = (((u64)(unsigned)__builtin_amdgcn_readfirstlane(epw[1]) << 32) | (unsigned)__builtin_amdgcn_readfirstlane(epw[0])) + GOLD;
    tag = (unsigned)(t64 >> 32); tlow = (unsigned)t64;
    asm volatile("s_waitcnt lgkmcnt(0)" : "+s"(tag), "+s"(tlow) :: "memory");
  };
  if (wave >= 4) {
    if (tid - 256 < 16) cnt[tid - 256] = 0u;
    asm volatile("s_waitcnt lgkmcnt(0)" ::: "memory");
    const unsigned pTL = PAR(0, TL), pTS = PAR(1 + cls, cls == 0 ? TS0 : (cls == 1 ? TS1 : (cls == 2 ? TS2 : TS3))), pSTG = PAR(9, STG), pBSG = PAR(10, BSG);
    const unsigned pRA2 = PAR(11, RA2), pRA3 = PAR(12, RA3), pTP = PAR(15 + cls, cls == 0 ? TP0 : TP2), pPA = PAR(19 + cls, (cls & 1) ? 0u : PA);
    rawbar();
    const int e2 = (pRA2 >> (idx & 7)) & 1, e3 = (pRA3 >> (idx & 7)) & 1;
    const int ns = cls == 0 ? NS0 : (cls == 1 ? NS1 + e2 + e3 : (cls == 2 ? NS2 - e2 : NS3 - e3));
    const unsigned pTA = __builtin_amdgcn_readfirstlane((unsigned)((float)pTS * __builtin_amdgcn_rcpf((float)(ns - 5)) + 0.5f));
    const size_t kvo = ((size_t)(b * 32 + h) * SK + w4 * 16) * 256;
    const char* kb = reinterpret_cast<const char*>(kc) + kvo;
    const char* vb = reinterpret_cast<const char*>(vc) + kvo;
    const unsigned lo = lane * 16u;
    u64 tlo = cls == 0 ? TL0 : (cls == 1 ? TL1 : (cls == 2 ? TL2 : TL3));
    u64 thi = cls == 0 ? TH0 : (cls == 1 ? TH1 | ((e2 ? LA2 : LA3) << (6 * (NS1 - 10))) : (cls == 2 ? TH2 : TH3));
    const u64 tlo0 = tlo, thi0 = thi;
    int hn = 0;
    const char* hb = kb;
    const char* pc[2];
    auto next2 = [&]() {
#pragma unroll
      for (int i = 0; i < 2; ++i) {
        if (hn == ns) { tlo = tlo0; thi = thi0; hb = vb; }
        ++hn;
        pc[i] = hb + ((((unsigned)tlo + (unsigned)idx) & 63u) << 14);
        tlo = (tlo >> 6) | ((thi & 63u) << 54); thi >>= 6;
      }
    };
    KV A, B;
    u32x4 W[8];
    u32x4 T[4][8];
    auto ldp = [&](KV& S, auto P) {
      constexpr int p = P.v;
      const char* q = pc[p >> 1] + lo + (p & 1) * 2048;
      S.r[2 * p] = ld16nt(q); S.r[2 * p + 1] = ld16nt(q + 1024);
    };
    auto landed = [&](KV& S, auto P) { pin2(S.r[2 * P.v], S.r[2 * P.v + 1]); };
    KV X;
    const unsigned qo = WS_QX + ((32 + h) * 2 + b) * 1024 + lane * 16;
    u32x4 g0;
    {
      unsigned tk = 0u;
      if (w4 == 0) {
        const int si = (idx & ~7) | ((idx + (0x2400 >> (4 * cls))) & 7);
        const char* p = kb + cls * 128;
        if (lane == 1) p = cls == 2 ? reinterpret_cast<const char*>(w_uv) + (si & 1) * 2097152 + (si >> 1) * 256 : reinterpret_cast<const char*>(cls == 1 ? w_o : wo) + si * 65536;
        if (lane == 2) p = vb + cls * 128;
        if (lane == 3) p = reinterpret_cast<const char*>(wo) + h * 131072;
        if (lane < 4) tk = __builtin_nontemporal_load(reinterpret_cast<const unsigned*>(p));
      }
      lds_wait_slow(cnt + 14, 4u);
      tag = __builtin_amdgcn_readfirstlane(cnt[16]);
      asm volatile("" :: "v"(tk), "v"(cpf));
    }
    STAMP(10);
    sfor<0, 12>([&](auto I) {
      constexpr int i = I.v, j = i - 4;
      if constexpr ((i & 3) == 0) next2();
      if (i == 4 && w4 == 3) {
#pragma unroll 1
        for (int spin = 0; spin < 2; ++spin) {
          u32x2 t[8];
          asm volatile("s_load_dwordx2 %0, %8, 0x0 glc\n\ts_load_dwordx2 %1, %8, 0x400 glc\n\ts_load_dwordx2 %2, %8, 0x800 glc\n\ts_load_dwordx2 %3, %8, 0xc00 glc\n\t"
                       "s_load_dwordx2 %4, %8, 0x1000 glc\n\ts_load_dwordx2 %5, %8, 0x1400 glc\n\ts_load_dwordx2 %6, %8, 0x1800 glc\n\ts_load_dwordx2 %7, %8, 0x1c00 glc\n\ts_waitcnt lgkmcnt(0)"
                       : "=&s"(t[0]), "=&s"(t[1]), "=&s"(t[2]), "=&s"(t[3]), "=&s"(t[4]), "=&s"(t[5]), "=&s"(t[6]), "=&s"(t[7]) : "s"(ws + WS_T0 + ((blk >> 3) & 7) * 128) : "memory");
          unsigned e = 0u; int sum = 0;
#pragma unroll
          for (int k = 0; k < 8; ++k) { e |= t[k][1] ^ tag; sum += (int)(t[k][0] - tstart); }
          if (e == 0u) { lds_add(cnt + 13, (unsigned)min(sum >> 3, 100), lane); break; }
        }
      }
      if constexpr (j >= 0) { if constexpr (j < 4) landed(X, IC<j & 3>{}); else landed(B, IC<j & 3>{}); }
      if constexpr (j >= 0 && (j & 1)) TR();
      if constexpr (i == QPE) g0 = ld16c(rs, qo);
      if constexpr (i < 4) ldp(X, IC<i & 3>{}); else if constexpr (i < 8) ldp(B, IC<i & 3>{}); else ldp(A, IC<i & 3>{});
      SCHED();
    });
    const unsigned torg = tstart + __builtin_amdgcn_readfirstlane(cnt[13]);
    const char* wp = reinterpret_cast<const char*>(wo) + ((size_t)h * 128 + r4 * 8) * 1024 + w4 * 256 + c16 * 16;
    u32x4 qpk;
    {
      if (!__all((g0[1] == tag) & (g0[3] == tag))) {
#pragma unroll 1
        for (int spin = 0; spin < SPIN_MAX; ++spin) {
          COMPILER_FENCE();
          g0 = ld16c(rs, qo);
          if (__all((g0[1] == tag) & (g0[3] == tag))) break;
        }
      }
      qpk = qsum(g0);
    }
    STAMP(7);
    const bool holds = cls == 0;
    float m = 0.f, l = 0.f, acc[8];
    unsigned sc[5];
#pragma unroll
    for (int k = 0; k < 8; ++k) acc[k] = 0.f;
#pragma unroll
    for (int k = 0; k < 5; ++k) sc[k] = 0xff800000u;
    int vn = 0;
    unsigned due = torg + pTL - (unsigned)(ns - 5) * pTA - (3 - w4) * pSTG - ((idx * 5 + cls * 3) & 15) * pBSG;
    unsigned rwo;
    const unsigned zo = WS_GZ + (b * 32 + h) * 512;
    u32x2 zs[4];
    float wr_;
    auto zreq = [&]() {
      asm volatile("s_load_dwordx2 %0, %4, 0x0 glc\n\ts_load_dwordx2 %1, %4, 0x80 glc\n\ts_load_dwordx2 %2, %4, 0x100 glc\n\ts_load_dwordx2 %3, %4, 0x180 glc"
                   : "=&s"(zs[0]), "=&s"(zs[1]), "=&s"(zs[2]), "=&s"(zs[3]) : "s"(ws + zo) : "memory");
    };
    auto zget = [&]() {
#pragma unroll 1
      for (int spin = 0; spin < SPIN_MAX; ++spin) {
        asm volatile("s_waitcnt lgkmcnt(0)" : "+s"(zs[0]), "+s"(zs[1]), "+s"(zs[2]), "+s"(zs[3]) :: "memory");
        if (((zs[0][1] ^ tag) | (zs[1][1] ^ tag) | (zs[2][1] ^ tag) | (zs[3][1] ^ tag)) == 0u) break;
        __builtin_amdgcn_s_sleep(2);
        zreq();
      }
      asm volatile("s_waitcnt lgkmcnt(0)" : "+s"(zs[0]), "+s"(zs[1]), "+s"(zs[2]), "+s"(zs[3]) :: "memory");
      const float z0 = __uint_as_float(zs[0][0]), z1 = __uint_as_float(zs[1][0]), z2 = __uint_as_float(zs[2][0]), z3 = __uint_as_float(zs[3][0]);
      const float Z = fmaxf(fmaxf(z0, z1), fmaxf(z2, z3));
      wr_ = ex2(fmaf(m, C2, -Z)) * __builtin_amdgcn_rcpf((ex2(z0 - Z) + ex2(z1 - Z)) + (ex2(z2 - Z) + ex2(z3 - Z)));
      asm volatile("" : "+v"(wr_));
    };
    auto qk = [&](const u32x4* K, auto I) {
      float x[4];
#pragma unroll
      for (int t = 0; t < 4; ++t) {
        x[t] = 0.f;
#pragma unroll
        for (int k = 0; k < 4; ++k) x[t] = dot2(K[t][k], qpk[k], x[t]);
      }
      const float u0 = (c16 & 1 ? x[1] : x[0]) + dpp<0xB1>(c16 & 1 ? x[0] : x[1]), u1 = (c16 & 1 ? x[3] : x[2]) + dpp<0xB1>(c16 & 1 ? x[2] : x[3]);
      float v = (c16 & 2 ? u1 : u0) + dpp<0x4E>(c16 & 2 ? u0 : u1);
      v += dpp<0x124>(v); v += dpp<0x128>(v);
      sc[I.v >> 2] = __builtin_amdgcn_update_dpp(sc[I.v >> 2], __float_as_uint(v), 0xE4, 0xf, 1 << (I.v & 3), false);
      asm volatile("" : "+v"(sc[I.v >> 2]));
    };
    auto soft = [&]() {
      m = row16_max(fmaxf(fmaxf(fmaxf(__uint_as_float(sc[0]), __uint_as_float(sc[1])), fmaxf(__uint_as_float(sc[2]), __uint_as_float(sc[3]))), __uint_as_float(sc[4])));
      const float mc = m * C2;
      l = 0.f;
#pragma unroll
      for (int k = 0; k < 5; ++k) {
        const float p = ex2(fmaf(__uint_as_float(sc[k]), C2, -mc));
        sc[k] = pk_bf(p, dpp<0xB1>(p));
        l = dot2(sc[k], 0x3f003f00u, l);
      }
      l = row16_sum(l);
    };
    auto pv = [&](const u32x4* V) {
      const unsigned p01 = rowbcast<0>(sc[0]), p23 = rowbcast<2>(sc[0]);
#pragma unroll
      for (int k = 0; k < 4; ++k) { acc[2 * k] = dot2(plo(V[0][k], V[1][k]), p01, acc[2 * k]); acc[2 * k + 1] = dot2(phi(V[0][k], V[1][k]), p01, acc[2 * k + 1]); }
#pragma unroll
      for (int k = 0; k < 4; ++k) { acc[2 * k] = dot2(plo(V[2][k], V[3][k]), p23, acc[2 * k]); acc[2 * k + 1] = dot2(phi(V[2][k], V[3][k]), p23, acc[2 * k + 1]); }
      sc[0] = (unsigned)__builtin_amdgcn_mov_dpp((int)sc[0], 0x12C, 0xf, 0xf, true);
      if ((++vn & 3) == 0) { sc[0] = sc[1]; sc[1] = sc[2]; sc[2] = sc[3]; sc[3] = sc[4]; }
      asm volatile("" : "+v"(acc[0]), "+v"(acc[1]), "+v"(acc[2]), "+v"(acc[3]), "+v"(acc[4]), "+v"(acc[5]), "+v"(acc[6]), "+v"(acc[7]));
    };
    auto step = [&](KV& S, KV& N, auto SI, auto REFILL, auto WR) {
      constexpr bool refill = REFILL.v == 1;
      constexpr int wr = WR.v;
      if constexpr (refill) next2();
      if constexpr (refill && SI.v >= 3) { until((int)(due - torg) < (int)pTP ? due - pPA : due); due += pTA; }
      constexpr int ah = wr < 0 ? AHL : 0;
      auto point = [&](auto P) {
        if constexpr (refill) {
          if constexpr (P.v == 0 && ah > 0) { if constexpr (wr >= 0) { if (holds) lds_hold(cnt + 8); } sfor<0, ah>([&](auto Q) { ldp(N, Q); }); SCHED(); }
          landed(S, P);
          if constexpr (P.v & 1) TR();
          if constexpr (P.v + ah < 4) { if constexpr ((P.v & 1) == 0 && wr >= 0) { if (holds) lds_hold(cnt + 8); } ldp(N, IC<(P.v + ah) & 3>{}); }
          if constexpr (wr >= 0 && wr < 8) W[(4 * wr + P.v) & 7] = ld16(wp + (((4 * wr + P.v) >> 3) * 32 + ((4 * wr + P.v) & 7)) * 1024);
          SCHED();
        }
      };
      sfor<0, 2>([&](auto H) {
        constexpr int i = 2 * SI.v + H.v;
        if constexpr (REFILL.v == 2 && H.v == 0) { zreq(); SCHED(); }
        point(IC<2 * H.v>{});
        point(IC<2 * H.v + 1>{});
        if constexpr (i < NSMIN) qk(S.r + 4 * H.v, IC<i>{});
        else if constexpr (i > NSMAX) pv(S.r + 4 * H.v);
        else {
          if (i < ns) { if constexpr (i < NSMAX) qk(S.r + 4 * H.v, IC<i>{}); }
          else { if (i == ns) soft(); pv(S.r + 4 * H.v); }
        }
        SCHED();
      });
      if constexpr (REFILL.v == 2) {
        zget();
        STAMP(1);
        *reinterpret_cast<u32x4*>(smem + rwo) = u32x4{pk_bf(acc[0] * wr_, acc[1] * wr_), pk_bf(acc[2] * wr_, acc[3] * wr_), pk_bf(acc[4] * wr_, acc[5] * wr_), pk_bf(acc[6] * wr_, acc[7] * wr_)};
        COMPILER_FENCE();
      }
      if constexpr (wr >= 1) {
        constexpr int t = (wr - 1) >> 1, j0 = ((wr - 1) & 1) * 2;
#pragma unroll
        for (int jj = 0; jj < 2; ++jj)
#pragma unroll
          for (int e = 0; e < 4; ++e) {
            const unsigned a_ = W[(4 * wr - 4 + 2 * jj) & 7][e], b_ = W[(4 * wr - 3 + 2 * jj) & 7][e];
            T[t][2 * e][j0 + jj] = plo(a_, b_); T[t][2 * e + 1][j0 + jj] = phi(a_, b_);
          }
#pragma unroll
        for (int e = 0; e < 8; e += 2) pin2(T[t][e], T[t][e + 1]);
      }
      SCHED();
    };
    step(B, B, IC<1>{}, IC<0>{}, IC<-1>{});
    step(A, B, IC<2>{}, IC<1>{}, IC<0>{});
    step(X, X, IC<0>{}, IC<0>{}, IC<-1>{});
    sfor<0, 4>([&](auto I) {
      step(B, A, IC<2 * I.v + 3>{}, IC<1>{}, IC<2 * I.v + 1>{});
      if constexpr (I.v == 3) {
        float* ml = reinterpret_cast<float*>(cnt + 32);
        ml[(c16 & 1) * 16 + w4 * 4 + r4] = (c16 & 1) ? l : m;
        COMPILER_FENCE();
        const unsigned got = lds_arrive_nw(L_CNT + 44);
        const float mr = ml[c16], lr = ml[16 + c16];
        SCHED();
        if (lds_done(got) == 3u) {
          const float M = row16_max(mr);
          const unsigned z = __float_as_uint(fmaf(M, C2, __builtin_amdgcn_logf(row16_sum(ex2((mr - M) * C2) * lr))));
          asm volatile("buffer_store_dwordx4 %0, %1, %2, 0 offen sc1\n\ts_nop 2" :: "v"(u32x4{z, tag, z, tag}), "v"(lane < 8 ? zo + cls * 128 + lane * 16 : OOB), "s"(rs) : "memory");
        }
        SCHED();
      }
      step(A, B, IC<2 * I.v + 4>{}, IC<1>{}, IC<2 * I.v + 2>{});
    });
    unsigned yoff = WS_GB + ((b * 128 + h * 4 + cls) * 256 + 64 * w4 + lane) * 8, rro = L_MRG + c16 * 272 + r4 * 16;
    rwo = L_MRG + (w4 * 4 + r4) * 272 + c16 * 16;
    asm volatile("" : "+v"(yoff), "+v"(rwo), "+v"(rro));
    if (cls == 0) lds_wait(cnt + 1, 4u);
    const int nit = (ns - 12) >> 1;
#pragma unroll 1
    for (int i = 0; i < nit; ++i) {
      step(B, A, IC<11>{}, IC<1>{}, IC<-1>{});
      step(A, B, IC<11>{}, IC<1>{}, IC<-1>{});
    }
    if (ns & 1) { step(B, A, IC<11>{}, IC<1>{}, IC<-1>{}); step(A, A, IC<11>{}, IC<2>{}, IC<-1>{}); }
    else step(B, B, IC<11>{}, IC<2>{}, IC<-1>{});
    {
      u32x4 af[4];
      unsigned got = lds_arrive_nw(L_CNT), need = 3u;
#pragma unroll 1
      for (int spin = 0; spin < SPIN_MAX; ++spin) {
#pragma unroll
        for (int t = 0; t < 4; ++t) af[t] = *reinterpret_cast<const u32x4*>(smem + rro + t * 64);
        SCHED();
        if (lds_done(got) >= need) break;
        need = 4u;
        __builtin_amdgcn_s_sleep(1);
        asm volatile("ds_read_b32 %0, %1" : "=v"(got) : "v"((unsigned)L_CNT) : "memory");
      }
      SCHED();
      f32x4 d[8];
      float a[8], v4[4];
      sfor<0, 5>([&](auto I) {
        constexpr int i = I.v;
        sfor<0, 8>([&](auto J) {
          constexpr int e = i + 4 * (J.v & 1), t = J.v >> 1;
          if constexpr (i < 4) { d[e] = __builtin_amdgcn_mfma_f32_16x16x32_bf16(__builtin_bit_cast(bf16x8_t, af[t]), __builtin_bit_cast(bf16x8_t, T[t][e]), t ? d[e] : f32x4{0.f, 0.f, 0.f, 0.f}, 0, 0, 0); SCHED(); }
          if constexpr (i >= 1 && (J.v == 4 || J.v == 5)) { const f32x2 u = f32x2{d[e - 1][0], d[e - 1][1]} + f32x2{d[e - 1][2], d[e - 1][3]}; a[e - 1] = u[0] + u[1]; asm volatile("" : "+v"(a[e - 1])); }
          if constexpr (i >= 1 && J.v == 6) v4[i - 1] = rs32(a[i - 1], a[i + 3]);
          if constexpr (i < 4) SCHED();
        });
      });
      st8c(rs, yoff, u32x2{pk_bf(rs16(v4[0], v4[2]), rs16(v4[1], v4[3])), tag});
    }
    STAMP(2);
    STAMP_FLUSH(1);
    TR_FLUSH();
    return;
  }
  const rsrc_t ro = __builtin_amdgcn_make_buffer_rsrc(out, 0, 4096, 0x00020000);
  auto fin = [&](auto NL, unsigned base, unsigned col, bool bump) {
    constexpr int nl = NL.v;
    unsigned doff = base + (((idx >> 5) * 4 + w4) * 16 * nl + (lane >> 2)) * 2048 + (idx & 31) * 64 + (lane & 3) * 16;
    unsigned fwo = 32768 + (c16 < 4 ? (w4 * 16 + 4 * c16 + r4) * 4 : 256 + lane * 4);
    unsigned fro = 32768 + (lane & 7) * 8, foo = lane < 8 ? ((idx >> 5) * 1024 + col + (nl == 2 ? 16 * (idx & 24) + 64 * (idx & 1) + (idx & 6) + 8 * lane : 16 * (idx & 31) + 2 * lane)) * 2 : OOB;
    asm volatile("" : "+v"(doff), "+v"(fwo), "+v"(fro), "+v"(foo));
    lds_wait(cnt + 0, 4u);
    STAMP(4);
    u32x4 d[nl];
    unsigned eh = 1u;
#pragma unroll 1
    for (int spin = SPIN_MAX; spin > 0; --spin) {
      COMPILER_FENCE();
      if (eh != 0u) {
#pragma unroll
        for (int j = 0; j < nl; ++j) d[j] = ld16c(rs, doff + j * 32768);
        eh = 0u;
#pragma unroll
        for (int j = 0; j < nl; ++j) { eh |= (d[j][1] ^ tag) | (d[j][3] ^ tag); asm volatile("" : "+v"(eh)); }
      }
      if (__builtin_amdgcn_ballot_w64(eh != 0u) == 0ull) break;
    }
    STAMP(11);
    float y[4] = {bflo(d[0][0]), bfhi(d[0][0]), bflo(d[0][2]), bfhi(d[0][2])};
    if constexpr (nl == 2) { y[0] += bflo(d[1][0]); y[1] += bfhi(d[1][0]); y[2] += bflo(d[1][2]); y[3] += bfhi(d[1][2]); }
    float t = rs16(rs32(y[0], y[2]), rs32(y[1], y[3]));
    t += dpp<0x124>(t); t += dpp<0x128>(t);
    *reinterpret_cast<float*>(smem + fwo) = t;
    COMPILER_FENCE();
    const unsigned got = lds_arrive_nw(L_CNT + 24);
    f32x2 t2[4];
#pragma unroll
    for (int w = 0; w < 4; ++w) t2[w] = *reinterpret_cast<const f32x2*>(smem + fro + w * 64);
    SCHED();
    if (lds_done(got) == 3u) {
      const f32x2 r = (t2[0] + t2[1]) + (t2[2] + t2[3]);
      __builtin_amdgcn_raw_buffer_store_b32(pk_bf(r[0], r[1]), ro, (int)foo, 0, 0);
      if (bump) st8c(rs, lane == 0 ? WS_EP : OOB, u32x2{tlow, tag});
    }
  };
  u32x4 Wa[16], Wb[16], xv[4];
  auto row = [&](u32x4& d) { d = ld16nt(pa + ao); SCHED(); ao += sa; asm volatile("" : "+v"(ao)); };
  sfor<0, 2>([&](auto I) { row(Wa[I.v]); });
  sfor<0, 4>([&](auto J) { xv[J.v] = ld16(xp + xo + (J.v >> 1) * 1024 + 16 * (J.v & 1)); });
  SCHED();
  sfor<2, 8>([&](auto I) { row(Wa[I.v]); });
  epw = ld8c(rs, WS_EP);
  const unsigned qa_off = WS_QA + (((c16 >> 1) & 1) * 32 + h) * 1024 + (b * 256 + w4 * 64 + 16 * r4 + 8 * (c16 & 1)) * 2;
  st16c(rs, cls == 1 && c16 < 4 ? qa_off : OOB, u32x4{0u, 0u, 0u, 0u});
  SCHED();
  auto sync = [&]() {
    rawbar();
    take_epoch();
    cnt[16] = tag;
    COMPILER_FENCE();
  };
  if (cls == 0) {
    sfor<8, 16>([&](auto I) { row(Wa[I.v]); });
    sync();
    if (blk < 8 && w4 == 0) asm volatile("buffer_store_dwordx4 %0, %1, %2, 0 offen sc1\n\ts_nop 2" :: "v"(u32x4{tstart, tag, tstart, tag}), "v"(WS_T0 + blk * 1024 + lane * 16), "s"(rs) : "memory");
    asm volatile("" :: "v"(xv[0]), "v"(xv[1]), "v"(xv[2]), "v"(xv[3]));
    STAMP(13);
    char* scr = smem + L_SCR;
    const int ab = idx >> 5, r = lane & 31, hh = lane >> 5;
    const unsigned fo = WS_QX + (w4 * 8 + (c16 >> 1)) * 2048 + (c16 & 1) * 512 + r4 * 8;
    u32x2 fl;
    u32x4 g[9];
    const unsigned o8 = WS_QA + (ab * 32 + w4 * 8) * 1024 + lane * 16, ol = WS_QA + (ab * 32 + r) * 1024 + 992 + 16 * hh;
    auto put = [&](int i, const u32x4& v) { *reinterpret_cast<u32x4*>(smem + ((i >> 3) * 4 + r4) * 8192 + toff(4 * (i & 7) + w4, c16)) = v; };
    sfor<0, 16>([&](auto I) { put(I.v, Wa[I.v]); if constexpr ((I.v & 3) == 3) TR(); SCHED(); });
    until(tstart + PAR(26, T0B));
    sfor<0, 16>([&](auto I) { row(Wb[I.v]); });
    lds_add(cnt + 14, 1u, lane);
    SCHED();
    sfor<0, 16>([&](auto I) { put(16 + I.v, Wb[I.v]); if constexpr (I.v == 12) fl = ld8c(rs, fo); if constexpr ((I.v & 3) == 3) TR(); SCHED(); });
    STAMP(8);
    u32x4 qlast;
    {
      if (!__all(fl[1] == tag)) {
#pragma unroll 1
        for (int spin = 0; spin < SPIN_MAX; ++spin) {
          COMPILER_FENCE();
          if (__all(ld8c(rs, fo)[1] == tag)) break;
          __builtin_amdgcn_s_sleep(8);
        }
      }
      until(tstart + PAR(27, T0Q));
      STAMP(5);
      sbar4(cnt + 7, lane);
      STAMP(12);
      unsigned pend = 0x1ffu;
#pragma unroll 1
      for (int spin = 0; spin < SPIN_MAX && pend; ++spin) {
        COMPILER_FENCE();
        u32x4 t[9];
#pragma unroll
        for (int i = 0; i < 9; ++i) t[i] = ld16c(rs, (pend >> i & 1) ? (i < 8 ? o8 + i * 1024 : ol) : OOB);
#pragma unroll
        for (int i = 0; i < 9; ++i) {
          if (pend >> i & 1) g[i] = t[i];
          if (__all(nz8(t[i]))) pend &= ~(1u << i);
        }
      }
      STAMP(9);
      qlast = g[8];
#pragma unroll
      for (int i = 0; i < 8; ++i)
        if (lane < 62) *reinterpret_cast<u32x4*>(scr + (w4 * 8 + i) * 1008 + 16 * lane) = g[i];
    }
    sbar4(cnt + 3, lane);
    STAMP(10);
    f32x16 sacc;
#pragma unroll
    for (int i = 0; i < 16; ++i) sacc[i] = 0.f;
#pragma unroll
    for (int jj = 0; jj < 32; ++jj) {
      const u32x4 af = *reinterpret_cast<const u32x4*>(smem + (w4 * 4 + (jj >> 3)) * 8192 + toff(r, 2 * (jj & 7) + hh));
      const u32x4 bf = jj < 31 ? *reinterpret_cast<const u32x4*>(scr + r * 1008 + 16 * (2 * (jj < 31 ? jj : 0) + hh)) : qlast;
      sacc = __builtin_amdgcn_mfma_f32_32x32x16_bf16(__builtin_bit_cast(bf16x8_t, af), __builtin_bit_cast(bf16x8_t, bf), sacc, 0, 0, 0);
    }
    float m = sacc[0];
#pragma unroll
    for (int i = 1; i < 16; ++i) m = fmaxf(m, sacc[i]);
    m = xhalf_max(m);
    if (hh == 0) *reinterpret_cast<float*>(scr + r * 1008 + 992 + 4 * w4) = m;
    sbar4(cnt + 4, lane);
    STAMP(11);
    {
      const f32x4 m4 = *reinterpret_cast<const f32x4*>(scr + r * 1008 + 992);
      m = fmaxf(fmaxf(m4[0], m4[1]), fmaxf(m4[2], m4[3]));
    }
    const float mc = m * C2;
    float p[16], ls = 0.f;
#pragma unroll
    for (int i = 0; i < 16; ++i) { p[i] = ex2(fmaf(sacc[i], C2, -mc)); ls += p[i]; }
    ls = xhalf_sum(ls);
    sbar4(cnt + 5, lane);
    u32x4* pf = reinterpret_cast<u32x4*>(scr);
    float* wl = reinterpret_cast<float*>(scr + 8192);
    if (hh == 0) wl[r * 4 + w4] = ls;
    pf[(2 * w4) * 64 + lane] = u32x4{pk_bf(p[0], p[1]), pk_bf(p[2], p[3]), pk_bf(p[4], p[5]), pk_bf(p[6], p[7])};
    pf[(2 * w4 + 1) * 64 + lane] = u32x4{pk_bf(p[8], p[9]), pk_bf(p[10], p[11]), pk_bf(p[12], p[13]), pk_bf(p[14], p[15])};
    sbar4(cnt + 6, lane);
    u32x4 pb8[8];
#pragma unroll
    for (int ks = 0; ks < 8; ++ks) pb8[ks] = pf[ks * 64 + lane];
    const f32x4 l4 = *reinterpret_cast<const f32x4*>(wl + r * 4);
    const float lsum = (l4[0] + l4[1]) + (l4[2] + l4[3]), rl = __builtin_amdgcn_rcpf(lsum);
    const int qq = (lane & 15) >> 2, pp = lane & 3, blk2 = (lane >> 4) & 1;
    const unsigned tw = tstart + __builtin_amdgcn_readfirstlane(cnt[13]) + PAR(13, TW), th = tw - PAR(14, HL);
    u32x4 vv[2][4];
    bool held = false;
    char* ot = scr + 12288 + w4 * 4096;
#pragma unroll
    for (int c2 = 0; c2 < 2; ++c2) {
      f32x16 oacc[2];
#pragma unroll
      for (int ct = 0; ct < 2; ++ct)
#pragma unroll
        for (int i = 0; i < 16; ++i) oacc[ct][i] = 0.f;
#pragma unroll
      for (int ks = 0; ks < 8; ++ks)
#pragma unroll
        for (int ct = 0; ct < 2; ++ct) {
          typedef __attribute__((address_space(3))) s16x4* lp;
          const char* tile = smem + ((ks >> 1) * 4 + w4) * 8192;
          const int cc = 2 * c2 + ct;
          const s16x4 lo = __builtin_amdgcn_ds_read_tr16_b64_v4i16((lp)(tile + toff(16 * (ks & 1) + 4 * hh + qq, 4 * cc + 2 * blk2 + (pp >> 1)) + 8 * (pp & 1)));
          const s16x4 hi = __builtin_amdgcn_ds_read_tr16_b64_v4i16((lp)(tile + toff(16 * (ks & 1) + 8 + 4 * hh + qq, 4 * cc + 2 * blk2 + (pp >> 1)) + 8 * (pp & 1)));
          const s16x8 afr = __builtin_shufflevector(lo, hi, 0, 1, 2, 3, 4, 5, 6, 7);
          oacc[ct] = __builtin_amdgcn_mfma_f32_32x32x16_bf16(__builtin_bit_cast(bf16x8_t, afr), __builtin_bit_cast(bf16x8_t, pb8[ks]), oacc[ct], 0, 0, 0);
        }
#pragma unroll
      for (int ct = 0; ct < 2; ++ct)
#pragma unroll
        for (int g = 0; g < 4; ++g)
          *reinterpret_cast<u32x2*>(ot + r * 128 + 16 * ((4 * ct + g) ^ (r & 7)) + 8 * hh) = u32x2{pk_bf(oacc[ct][4 * g] * rl, oacc[ct][4 * g + 1] * rl), pk_bf(oacc[ct][4 * g + 2] * rl, oacc[ct][4 * g + 3] * rl)};
      if (c2 == 0) { held = (int)((unsigned)wall_clock64() + 30u - th) >= 0; if (held) lds_add(cnt + 8, 1u, lane); }
      __builtin_amdgcn_wave_barrier();
#pragma unroll
      for (int i = 0; i < 4; ++i) { vv[c2][i] = *reinterpret_cast<const u32x4*>(ot + (8 * i + (lane >> 3)) * 128 + 16 * ((lane ^ (lane >> 3)) & 7)); asm volatile("" : "+v"(vv[c2][i])); }
      __builtin_amdgcn_wave_barrier();
    }
    lds_arrive(cnt + 1, lane);
    if (!held) { until(th); lds_add(cnt + 8, 1u, lane); }
    until(tw);
#pragma unroll
    for (int i = 0; i < 8; ++i) st16c(rs, WS_APO + ((((8 * (i >> 1) + (lane >> 3)) * 2 + (w4 >> 1)) * 64 + idx) * 256 + (w4 & 1) * 128 + 64 * (i & 1) + 8 * (lane & 7)) * 2, vv[i & 1][i >> 1]);
    STAMP(1);
    asm volatile("s_waitcnt vmcnt(0)" ::: "memory");
    if (lds_arrive(cnt + 9 + (w4 >> 1), lane) == 1u && hh == 0)
      st8c(rs, WS_AML + ((r * 2 + (w4 >> 1)) * 16 + (idx >> 2)) * 128 + (idx & 3) * 8, u32x2{__float_as_uint(mc + __builtin_amdgcn_logf(lsum)), tag});
    lds_add(cnt + 8, ~0u, lane);
    STAMP(2);
    STAMP_FLUSH(0); TR_FLUSH();
    return;
  }
  sfor<8, 16>([&](auto I) { row(Wa[I.v]); });
  const u16* pb = cls == 2 ? w_uv + ((size_t)(b * 256 + w4 * 64 + 16 * r4) * 32 + h) * 128 + 8 * c16 : (cls == 1 ? w_o : wo) + ((size_t)h * 128 + b * 64 + w4 * 2) * 512 + 8 * lane;
  sync();
  STAMP(13);
  float* red = reinterpret_cast<float*>(smem);
  if (cls & 1) {
    float acc[2][8];
#pragma unroll
    for (int bb = 0; bb < 2; ++bb)
#pragma unroll
      for (int k = 0; k < 8; ++k) acc[bb][k] = 0.f;
    sfor<0, 8>([&](auto IP) {
      constexpr int ip = IP.v, j = ip - (8 - WBW);
      use2(Wa[2 * ip], Wa[2 * ip + 1]);
      if constexpr (ip & 1) TR();
      if constexpr (j >= 0) { Wb[2 * j] = ld16(pb + j * 4096); Wb[2 * j + 1] = ld16(pb + j * 4096 + 512); }
      SCHED();
      const unsigned xp0 = xv[ip >> 2][ip & 3], xp1 = xv[2 + (ip >> 2)][ip & 3];
#pragma unroll
      for (int k = 0; k < 4; ++k) {
        const unsigned lo = plo(Wa[2 * ip][k], Wa[2 * ip + 1][k]), hi = phi(Wa[2 * ip][k], Wa[2 * ip + 1][k]);
        acc[0][2 * k] = dot2(lo, xp0, acc[0][2 * k]); acc[0][2 * k + 1] = dot2(hi, xp0, acc[0][2 * k + 1]);
        acc[1][2 * k] = dot2(lo, xp1, acc[1][2 * k]); acc[1][2 * k + 1] = dot2(hi, xp1, acc[1][2 * k + 1]);
      }
      SCHED();
    });
    {
      float v8[8], v4[4];
#pragma unroll
      for (int k = 0; k < 8; ++k) v8[k] = rs32(acc[0][k], acc[1][k]);
#pragma unroll
      for (int k = 0; k < 4; ++k) v4[k] = rs16(v8[k], v8[k + 4]);
      *reinterpret_cast<f32x4*>(red + (w4 * 2 + (lane >> 5)) * 128 + 8 * c16 + 4 * (r4 & 1)) = f32x4{v4[0], v4[1], v4[2], v4[3]};
    }
    COMPILER_FENCE();
    const unsigned got = lds_arrive_nw(L_CNT + 8);
    f32x4 t4[4];
#pragma unroll
    for (int w = 0; w < 4; ++w) t4[w] = *reinterpret_cast<const f32x4*>(red + (w * 2 + (lane >> 5)) * 128 + 8 * c16 + 4 * (r4 & 1));
    SCHED();
    if (lds_done(got) == 3u) {
      const f32x4 t = (t4[0] + t4[1]) + (t4[2] + t4[3]);
      st16c(rs, WS_QX + (((cls >> 1) * 32 + h) * 2 + (lane >> 5)) * 1024 + b * 512 + (lane & 31) * 16, u32x4{pk_bf(t[0], t[1]), tag, pk_bf(t[2], t[3]), tag});
      STAMP(15);
      SCHED();
      lds_add(cnt + 2, 1u, lane);
    } else lds_wait(cnt + 2, 5u);
    sfor<WBW, 8>([&](auto J) {
      constexpr int j = J.v;
      use2(Wb[2 * (j - WBW)], Wb[2 * (j - WBW) + 1]);
      if constexpr ((j - WBW) & 1) TR();
      Wb[2 * j] = ld16(pb + j * 4096); Wb[2 * j + 1] = ld16(pb + j * 4096 + 512);
      if constexpr (j == 7) lds_add(cnt + 14, 1u, lane);
      SCHED();
    });
    {
      const unsigned tw = __builtin_nontemporal_load(reinterpret_cast<const unsigned*>(out + (idx >> 5) * 1024 + (cls >> 1) * 512 + 16 * (idx & 31)));
      sfor<8 - WBW, 8>([&](auto J) { use2(Wb[2 * J.v], Wb[2 * J.v + 1]); if constexpr (J.v & 1) TR(); });
      asm volatile("" :: "v"(tw));
    }
    STAMP(8);
  } else {
    sfor<0, 16 - WBW>([&](auto IP) {
      constexpr int ip = IP.v, j = ip - (8 - WBW);
      if constexpr (ip < 8) use2(Wa[2 * ip], Wa[2 * ip + 1]); else use2(Wb[2 * ip - 16], Wb[2 * ip - 15]);
      if constexpr (ip & 1) TR();
      if constexpr (j >= 0) { Wb[2 * j] = ld16nt(pb + j * 8192); Wb[2 * j + 1] = ld16nt(pb + j * 8192 + 4096); }
      SCHED();
    });
    u32x4 g0, g1;
    const unsigned qo = WS_QX + h * 2048 + lane * 16;
    shint<512, 1>(ws + WS_QX + h * 2048, tag);
    STAMP(5);
#pragma unroll 1
    for (int spin = 0; spin < SPIN_MAX; ++spin) {
      COMPILER_FENCE();
      g0 = ld16c(rs, qo); g1 = ld16c(rs, qo + 1024);
      SCHED();
      lds_add(cnt + 14, spin == 0, lane);
      SCHED();
      if (__all((g0[1] == tag) & (g0[3] == tag) & (g1[1] == tag) & (g1[3] == tag))) break;
      __builtin_amdgcn_s_sleep(1);
    }
    sfor<8 - WBW, 8>([&](auto J) { if constexpr (J.v & 1) TR(); });
    const u32x4 q0 = qsum(g0), q1 = qsum(g1);
    STAMP(9);
    float v[8];
    {
      float u[16];
#pragma unroll
      for (int i = 0; i < 16; ++i) {
        float a = 0.f, c = 0.f;
#pragma unroll
        for (int k = 0; k < 4; ++k) { a = dot2(Wa[i][k], q0[k], a); c = dot2(Wa[i][k], q1[k], c); }
        u[i] = (c16 & 2 ? c : a) + dpp<0x4E>(c16 & 2 ? a : c);
        asm volatile("" : "+v"(u[i]));
      }
#pragma unroll
      for (int i = 0; i < 8; ++i) {
        float t = (c16 & 1 ? u[i + 8] : u[i]) + dpp<0xB1>(c16 & 1 ? u[i] : u[i + 8]);
        asm volatile("" : "+v"(t));
        t += dpp<0x124>(t);
        asm volatile("" : "+v"(t));
        t += dpp<0x128>(t);
        asm volatile("" : "+v"(t));
        v[i] = t;
      }
    }
    u32x4 o = {pk_bf(v[0], v[1]), pk_bf(v[2], v[3]), pk_bf(v[4], v[5]), pk_bf(v[6], v[7])};
    if ((o[0] | o[1]) == 0u) o[0] = 0x8000u;
    if ((o[2] | o[3]) == 0u) o[2] = 0x8000u;
    if (c16 < 4) st16c(rs, qa_off, o);
    STAMP(10);
  }
  asm volatile("" :: "v"(xv[0]), "v"(xv[1]), "v"(xv[2]), "v"(xv[3]));
  SCHED();
  if (cls == 3) {
    fin(IC<2>{}, WS_GB, 512, false);
    STAMP(6);
    STAMP_FLUSH(0); TR_FLUSH();
    return;
  }
#pragma unroll
  for (int ip = 0; ip < 8; ++ip) {
#pragma unroll
    for (int k = 0; k < 4; ++k) { const unsigned lo = plo(Wb[2 * ip][k], Wb[2 * ip + 1][k]), hi = phi(Wb[2 * ip][k], Wb[2 * ip + 1][k]); Wb[2 * ip][k] = lo; Wb[2 * ip + 1][k] = hi; }
    pin2(Wb[2 * ip], Wb[2 * ip + 1]);
  }
  if (cls == 2) {
    float* red2 = red + 4 * 256;
    float* wml = red + 3072;
    u32x8 f[4];
    until<6>(tstart + PAR(13, TW) + PAR(28, 60));
#pragma unroll 1
    for (int spin = 0; spin < SPIN_MAX; ++spin) {
      sld4x32(ws + WS_AML + ((h * 2 + b) * 16 + w4 * 4) * 128, f);
      unsigned e = 0u;
#pragma unroll
      for (int i = 0; i < 16; ++i) e |= f[i >> 2][2 * (i & 3) + 1] ^ tag;
      if (e == 0u) break;
      __builtin_amdgcn_s_sleep(2);
    }
    STAMP(3);
    u32x4 pp[8];
#pragma unroll
    for (int i = 0; i < 8; ++i) pp[i] = ld16c(rs, WS_APO + ((h * 2 + b) * 64 + w4 * 16 + 2 * i) * 512 + lane * 16);
    SCHED();
    float wt[8];
    {
      float M = -INFINITY, L = 0.f;
#pragma unroll
      for (int i = 0; i < 8; ++i) {
        unsigned z0 = f[i >> 1][4 * (i & 1)], z1 = f[i >> 1][4 * (i & 1) + 2];
        asm("" : "+s"(z0), "+s"(z1));
        wt[i] = __uint_as_float(lane < 32 ? z0 : z1); M = fmaxf(M, wt[i]);
      }
      M = xhalf_max(M);
#pragma unroll
      for (int i = 0; i < 8; ++i) { wt[i] = ex2(wt[i] - M); L += wt[i]; }
      L = xhalf_sum(L);
      if (lane == 0) *reinterpret_cast<f32x2*>(wml + 2 * w4) = f32x2{M, L};
    }
    SCHED();
    float a8[8];
#pragma unroll
    for (int k = 0; k < 8; ++k) a8[k] = 0.f;
#pragma unroll
    for (int i = 0; i < 8; ++i) {
#pragma unroll
      for (int k = 0; k < 4; ++k) { a8[2 * k] = fmaf(wt[i], bflo(pp[i][k]), a8[2 * k]); a8[2 * k + 1] = fmaf(wt[i], bfhi(pp[i][k]), a8[2 * k + 1]); }
      asm volatile("" : "+v"(a8[0]), "+v"(a8[1]), "+v"(a8[2]), "+v"(a8[3]), "+v"(a8[4]), "+v"(a8[5]), "+v"(a8[6]), "+v"(a8[7]));
    }
    STAMP(6);
    *reinterpret_cast<f32x4*>(red + w4 * 256 + 8 * (lane & 31) + 4 * (lane >> 5)) = f32x4{rs32(a8[0], a8[4]), rs32(a8[1], a8[5]), rs32(a8[2], a8[6]), rs32(a8[3], a8[7])};
    sbar4(cnt + 3, lane);
    STAMP(4);
    float acc[2][8];
#pragma unroll
    for (int bb = 0; bb < 2; ++bb) {
      u32x4 cv[2];
      const f32x4 ml = *reinterpret_cast<const f32x4*>(wml + 4 * bb);
      const float M = fmaxf(ml[0], ml[2]), e0 = ex2(ml[0] - M), e1 = ex2(ml[2] - M), rl = 1.f / fmaf(e0, ml[1], e1 * ml[3]), s0 = e0 * rl, s1 = e1 * rl;
#pragma unroll
      for (int e = 0; e < 2; ++e) {
        const float* src = red + bb * 512 + w4 * 64 + 16 * r4 + 8 * e;
        const f32x4 t0 = s0 * *reinterpret_cast<const f32x4*>(src) + s1 * *reinterpret_cast<const f32x4*>(src + 256), t1 = s0 * *reinterpret_cast<const f32x4*>(src + 4) + s1 * *reinterpret_cast<const f32x4*>(src + 260);
        cv[e] = u32x4{pk_bf(t0[0], t0[1]), pk_bf(t0[2], t0[3]), pk_bf(t1[0], t1[1]), pk_bf(t1[2], t1[3])};
      }
#pragma unroll
      for (int k = 0; k < 8; ++k) acc[bb][k] = 0.f;
#pragma unroll
      for (int ip = 0; ip < 8; ++ip)
#pragma unroll
        for (int k = 0; k < 4; ++k) { acc[bb][2 * k] = dot2(Wb[2 * ip][k], cv[ip >> 2][ip & 3], acc[bb][2 * k]); acc[bb][2 * k + 1] = dot2(Wb[2 * ip + 1][k], cv[ip >> 2][ip & 3], acc[bb][2 * k + 1]); }
    }
    {
      float v8[8], v4[4];
#pragma unroll
      for (int k = 0; k < 8; ++k) v8[k] = rs32(acc[0][k], acc[1][k]);
#pragma unroll
      for (int k = 0; k < 4; ++k) v4[k] = rs16(v8[k], v8[k + 4]);
      *reinterpret_cast<f32x4*>(red2 + (w4 * 2 + (lane >> 5)) * 128 + 8 * c16 + 4 * (r4 & 1)) = f32x4{v4[0], v4[1], v4[2], v4[3]};
    }
    {
      COMPILER_FENCE();
      const unsigned got = lds_arrive_nw(L_CNT + 16);
      f32x4 t4[4];
#pragma unroll
      for (int w = 0; w < 4; ++w) t4[w] = *reinterpret_cast<const f32x4*>(red2 + w * 256 + 4 * lane);
      SCHED();
      if (lds_done(got) == 3u) {
        const f32x4 t = (t4[0] + t4[1]) + (t4[2] + t4[3]);
        const unsigned co = WS_CX + ((h * 2 + b) * 256 + 64 * r4 + 32 * (c16 & 1) + 2 * (c16 >> 1)) * 8;
        st16c(rs, co, u32x4{__float_as_uint(t[0]), tag, __float_as_uint(t[1]), tag});
        st16c(rs, co + 128, u32x4{__float_as_uint(t[2]), tag, __float_as_uint(t[3]), tag});
      }
    }
    STAMP(12);
    STAMP_FLUSH(0); TR_FLUSH();
    return;
  }
  {
    unsigned op;
    {
      u32x4 c;
      const unsigned co = WS_CX + ((h * 2 + ((lane >> 3) & 1)) * 256 + (lane >> 5) * 128 + b * 64 + w4 * 16 + 2 * (lane & 7)) * 8;
      if (idx == 0 && w4 == 1) {
#pragma unroll 1
        for (int spin = 0; spin < SPIN_MAX; ++spin) {
          COMPILER_FENCE();
          if (__all(ld8c(rs, WS_QX + 65536 + (lane >> 1) * 2048 + (lane & 1) * 512)[1] == tag)) break;
          __builtin_amdgcn_s_sleep(8);
        }
      }
      until<6>(tstart + PAR(13, TW) + PAR(29, 370));
      if (w4 == 0) { shint<2048, 0>(ws + WS_CX + (h * 512 + b * 64) * 8, tag); lds_add(cnt + 7, 1u, lane); }
      else lds_wait(cnt + 7, 1u);
      STAMP(3);
#pragma unroll 1
      for (int spin = 0; spin < SPIN_MAX; ++spin) {
        COMPILER_FENCE();
        c = ld16c(rs, co);
        if (__all((c[1] == tag) & (c[3] == tag))) break;
        __builtin_amdgcn_s_sleep(1);
      }
      float o0 = __uint_as_float(c[0]), o1 = __uint_as_float(c[2]);
      o0 += dpp<0x128>(o0); o1 += dpp<0x128>(o1);
      op = pk_bf(o0, o1);
    }
    STAMP(14);
    float* red3 = red + 4096;
    float a[2][8];
#pragma unroll
    for (int bb = 0; bb < 2; ++bb)
#pragma unroll
      for (int k = 0; k < 8; ++k) a[bb][k] = 0.f;
#pragma unroll
    for (int ip = 0; ip < 8; ++ip) {
      const unsigned v0 = __builtin_amdgcn_readlane(op, ip), v1 = __builtin_amdgcn_readlane(op, 32 + ip);
#pragma unroll
      for (int k = 0; k < 4; ++k) {
        a[0][2 * k] = dot2(Wb[2 * ip][k], v0, a[0][2 * k]); a[0][2 * k + 1] = dot2(Wb[2 * ip + 1][k], v0, a[0][2 * k + 1]);
        a[1][2 * k] = dot2(Wb[2 * ip][k], v1, a[1][2 * k]); a[1][2 * k + 1] = dot2(Wb[2 * ip + 1][k], v1, a[1][2 * k + 1]);
      }
    }
#pragma unroll
    for (int bb = 0; bb < 2; ++bb) {
      *reinterpret_cast<f32x4*>(red3 + (w4 * 2 + bb) * 512 + 8 * lane) = f32x4{a[bb][0], a[bb][1], a[bb][2], a[bb][3]};
      *reinterpret_cast<f32x4*>(red3 + (w4 * 2 + bb) * 512 + 8 * lane + 4) = f32x4{a[bb][4], a[bb][5], a[bb][6], a[bb][7]};
    }
    sbar4(cnt + 5, lane);
    f32x4 t = {0.f, 0.f, 0.f, 0.f};
#pragma unroll
    for (int w = 0; w < 4; ++w) t += *reinterpret_cast<const f32x4*>(red3 + w * 1024 + 4 * tid);
    st16c(rs, WS_GA + ((tid >> 7) * 64 + idx) * 2048 + (tid & 127) * 16, u32x4{pk_bf(t[0], t[1]), tag, pk_bf(t[2], t[3]), tag});
    STAMP(5);
  }
  fin(IC<1>{}, WS_GA, 0, idx == 0);
  STAMP(6);
  STAMP_FLUSH(0); TR_FLUSH();
}

extern "C" void kernel_launch(void* const* d_in, const int* in_sizes, int n_in, void* d_out, int out_size, void* d_ws, size_t ws_size, hipStream_t stream) {
  const u16* xA = (const u16*)d_in[0];
  const u16* ckv = (const u16*)d_in[1];
  const u16* w_q = (const u16*)d_in[2];
  const u16* w_uk = (const u16*)d_in[3];
  const u16* w_uv = (const u16*)d_in[4];
  const u16* w_o = (const u16*)d_in[5];
  const u16* xB = (const u16*)d_in[6];
  const u16* kc = (const u16*)d_in[7];
  const u16* vc = (const u16*)d_in[8];
  const u16* wq = (const u16*)d_in[9];
  const u16* wo = (const u16*)d_in[10];
  kmain<<<256, 512, 0, stream>>>(ckv, w_q, w_uk, wq, xA, xB, w_uv, w_o, kc, vc, wo, (char*)d_ws, (u16*)d_out);
}
```

```cpp
#include <hip/hip_runtime.h>
#include <hip/hip_bf16.h>
#include <stdint.h>

#define DI __device__ __forceinline__
typedef __attribute__((ext_vector_type(4))) unsigned u32x4;
typedef __attribute__((ext_vector_type(2))) unsigned u32x2;
typedef __attribute__((ext_vector_type(4))) float f32x4;
typedef __attribute__((ext_vector_type(2))) float f32x2;
typedef __attribute__((ext_vector_type(16))) float f32x16;
typedef __attribute__((ext_vector_type(8))) __bf16 bf16x8_t;
typedef __attribute__((ext_vector_type(2))) __bf16 bf16x2_t;
typedef __attribute__((ext_vector_type(4))) short s16x4;
typedef __attribute__((ext_vector_type(8))) short s16x8;
typedef unsigned short u16;
typedef unsigned long long u64;

constexpr int NS0 = 13, NS1 = 17, NS2 = 16, NS3 = 18;
constexpr u64 TL0 = 0xc2b8de6543cb1c1ull, TH0 = 0x3de34ull, TL1 = 0x920718551349142ull, TH1 = 0x3eeb6caeaa7ull;
constexpr u64 TL2 = 0x96175a59238a183ull, TH2 = 0xff9d71b28ull, TL3 = 0x89f6d74d0308100ull, TH3 = 0xf3bdf3beda66ull;
constexpr int RA2 = 0xC0, RA3 = 3;
constexpr int AHL = 2;
constexpr int QPE = 7;
constexpr int WBW = 5;
#define TL 2360
#define TW 1650
constexpr unsigned TS0 = 1160, TS1 = 1420, TS2 = 1320, TS3 = 1420, STG = 12, BSG = 6, HL = 40, TP0 = TW + 170, TP2 = TW + 380, PA = 150, T0B = 420, T0Q = 880;
#define PAR(i, v) (v)
constexpr u64 LA2 = TH2 >> (6 * (NS2 - 11)), LA3 = TH3 >> (6 * (NS3 - 11));
constexpr u64 chunk_mask(u64 lo, u64 hi, int n) { u64 m = 0; for (int i = 0; i < n; ++i) m |= 1ull << ((i < 10 ? lo >> (6 * i) : hi >> (6 * (i - 10))) & 63); return m; }
static_assert((chunk_mask(TL0, TH0, NS0) | chunk_mask(TL1, TH1, NS1) | chunk_mask(TL2, TH2, NS2) | chunk_mask(TL3, TH3, NS3)) == ~0ull && (RA2 & RA3) == 0, "every chunk once");
static_assert(NS0 + NS1 + NS2 + NS3 == 64, "split sizes");
constexpr int NSMIN = 13, NSMAX = 18;
static_assert(NS0 >= NSMIN && NS1 >= NSMIN && NS2 - 1 >= NSMIN && NS3 - 1 >= NSMIN && NS0 <= NSMAX && NS1 + 1 <= NSMAX && NS2 <= NSMAX && NS3 <= NSMAX, "range");
constexpr int SK = 4096;
constexpr float C2 = 0.08838834764831845f * 1.4426950408889634f;
constexpr u64 GOLD = 0x9E3779B97F4A7C15ull;

constexpr unsigned WS_EP = 0;
constexpr unsigned WS_QX = 4096;
constexpr unsigned WS_QA = WS_QX + 2 * 32 * 256 * 8;
constexpr unsigned WS_AML = WS_QA + 2 * 32 * 512 * 2;
constexpr unsigned WS_CX = WS_AML + 32 * 2 * 16 * 128;
constexpr unsigned WS_GZ = WS_CX + 32 * 2 * 2 * 128 * 8;
constexpr unsigned WS_T0 = WS_GZ + 256 * 128;
constexpr unsigned WS_GB = WS_T0 + 64 * 128;
constexpr unsigned WS_GA = WS_GB + 2 * 128 * 256 * 8;
constexpr unsigned WS_APO = 2u << 20;
static_assert(WS_GA + 64 * 2 * 256 * 8 <= WS_APO, "layout");
constexpr unsigned WS_END = WS_APO + 32 * 2 * 64 * 256 * 2;
#define STAMP(i) do {} while (0)
#define STAMP_DECL do {} while (0)
#define STAMP_FLUSH(grp) do {} while (0)

#define TR() do {} while (0)
#define TR_FLUSH() do {} while (0)
DI float bflo(unsigned u) { return __uint_as_float(u << 16); }
DI float bfhi(unsigned u) { return __uint_as_float(u & 0xffff0000u); }
DI unsigned pk_bf(float a, float b) { const f32x2 v = {a, b}; return __builtin_bit_cast(unsigned, __builtin_convertvector(v, bf16x2_t)); }
DI float dot2(unsigned k, unsigned q, float acc) {
  return __builtin_amdgcn_fdot2_f32_bf16(__builtin_bit_cast(bf16x2_t, k), __builtin_bit_cast(bf16x2_t, q), acc, false);
}
DI unsigned plo(unsigned w0, unsigned w1) { return __builtin_amdgcn_perm(w1, w0, 0x05040100u); }
DI unsigned phi(unsigned w0, unsigned w1) { return __builtin_amdgcn_perm(w1, w0, 0x07060302u); }
template <int CTRL> DI float dpp(float x) {
  return __builtin_bit_cast(float, __builtin_amdgcn_mov_dpp(__builtin_bit_cast(int, x), CTRL, 0xf, 0xf, true));
}
DI float row16_sum(float x) { x += dpp<0xB1>(x); x += dpp<0x4E>(x); x += dpp<0x141>(x); x += dpp<0x128>(x); return x; }
DI float row16_max(float x) { x = fmaxf(x, dpp<0xB1>(x)); x = fmaxf(x, dpp<0x4E>(x)); x = fmaxf(x, dpp<0x141>(x)); x = fmaxf(x, dpp<0x128>(x)); return x; }
DI float xhalf_max(float x) { auto t = __builtin_amdgcn_permlane32_swap(__float_as_uint(x), __float_as_uint(x), false, false); return fmaxf(__uint_as_float(t[0]), __uint_as_float(t[1])); }
DI float xhalf_sum(float x) { auto t = __builtin_amdgcn_permlane32_swap(__float_as_uint(x), __float_as_uint(x), false, false); return __uint_as_float(t[0]) + __uint_as_float(t[1]); }
DI u32x4 ld16(const void* p) { return *reinterpret_cast<const u32x4*>(p); }
DI u32x4 ld16nt(const void* p) { return __builtin_nontemporal_load(reinterpret_cast<const u32x4*>(p)); }
DI float ex2(float x) { return __builtin_amdgcn_exp2f(x); }
#define SCHED() __builtin_amdgcn_sched_barrier(0)

typedef __amdgpu_buffer_rsrc_t rsrc_t;
DI u32x4 ld16c(rsrc_t rs, unsigned off) { return __builtin_amdgcn_raw_buffer_load_b128(rs, (int)off, 0, 16); }
DI u32x2 ld8c(rsrc_t rs, unsigned off) { return __builtin_amdgcn_raw_buffer_load_b64(rs, (int)off, 0, 16); }
DI void st16c(rsrc_t rs, unsigned off, u32x4 v) { __builtin_amdgcn_raw_buffer_store_b128(v, rs, (int)off, 0, 16); }
DI void st8c(rsrc_t rs, unsigned off, u32x2 v) { __builtin_amdgcn_raw_buffer_store_b64(v, rs, (int)off, 0, 16); }
template <int O1> DI void sld2(const char* p, u32x2& a, u32x2& b) {
  asm volatile("s_load_dwordx2 %0, %2, 0x0 glc\n\ts_load_dwordx2 %1, %2, %3 glc\n\ts_waitcnt lgkmcnt(0)" : "=&s"(a), "=&s"(b) : "s"(p), "n"(O1) : "memory");
}
typedef __attribute__((ext_vector_type(8))) unsigned u32x8;
DI void sld4x32(const char* p, u32x8 (&v)[4]) {
  asm volatile("s_load_dwordx8 %0, %4, 0x0 glc\n\ts_load_dwordx8 %1, %4, 0x80 glc\n\ts_load_dwordx8 %2, %4, 0x100 glc\n\ts_load_dwordx8 %3, %4, 0x180 glc\n\ts_waitcnt lgkmcnt(0)"
               : "=&s"(v[0]), "=&s"(v[1]), "=&s"(v[2]), "=&s"(v[3]) : "s"(p) : "memory");
}
constexpr int SPIN_MAX = 1 << 20;
constexpr unsigned OOB = 0x7ffffff0u;
DI void rawbar() { __builtin_amdgcn_sched_barrier(0); __builtin_amdgcn_s_barrier(); __builtin_amdgcn_sched_barrier(0); }
#define COMPILER_FENCE() asm volatile("" ::: "memory")
template <int O1, int SL> DI void shint(const char* p, unsigned tag) {
#pragma unroll 1
  for (int spin = 0; spin < SPIN_MAX; ++spin) {
    u32x2 a, b;
    sld2<O1>(p, a, b);
    if (((a[1] ^ tag) | (b[1] ^ tag)) == 0u) break;
    __builtin_amdgcn_s_sleep(SL);
  }
}

template <int N> struct IC { static constexpr int v = N; };
template <int I, int N, class F> DI void sfor(F&& f) { if constexpr (I < N) { f(IC<I>{}); sfor<I + 1, N>(f); } }
typedef __attribute__((address_space(3))) unsigned lu32;
DI void lds_wait(unsigned* cnt, unsigned target) {
#pragma unroll 1
  for (int spin = 0; spin < SPIN_MAX; ++spin) {
    if (__builtin_amdgcn_readfirstlane(__hip_atomic_load((lu32*)cnt, __ATOMIC_RELAXED, __HIP_MEMORY_SCOPE_WORKGROUP)) >= target) break;
    __builtin_amdgcn_s_sleep(1);
  }
  COMPILER_FENCE();
}
DI void lds_wait_slow(unsigned* cnt, unsigned target) {
#pragma unroll 1
  for (int spin = 0; spin < SPIN_MAX; ++spin) {
    if (__builtin_amdgcn_readfirstlane(__hip_atomic_load((lu32*)cnt, __ATOMIC_RELAXED, __HIP_MEMORY_SCOPE_WORKGROUP)) >= target) break;
    __builtin_amdgcn_s_sleep(3);
  }
  COMPILER_FENCE();
}
template <int SL = 1> DI void until(unsigned t) {
#pragma unroll 1
  for (int spin = 0; spin < 1024; ++spin) { if ((int)((unsigned)wall_clock64() - t) >= 0) break; __builtin_amdgcn_s_sleep(SL); }
}
DI void lds_hold(unsigned* flag) {
#pragma unroll 1
  for (int spin = 0; spin < SPIN_MAX; ++spin) {
    if (__builtin_amdgcn_readfirstlane(__hip_atomic_load((lu32*)flag, __ATOMIC_RELAXED, __HIP_MEMORY_SCOPE_WORKGROUP)) == 0u) break;
    __builtin_amdgcn_s_sleep(2);
  }
}
DI unsigned lds_arrive_nw(unsigned addr) {
  unsigned old, one = 1u;
  asm volatile("s_mov_b64 exec, 1\n\tds_add_rtn_u32 %0, %1, %2\n\ts_mov_b64 exec, -1" : "=v"(old) : "v"(addr), "v"(one) : "memory");
  return old;
}
DI unsigned lds_done(unsigned old) { asm volatile("s_waitcnt lgkmcnt(0)" : "+v"(old) :: "memory"); return __builtin_amdgcn_readfirstlane(old); }
DI unsigned lds_arrive(unsigned* cnt, int) { return lds_done(lds_arrive_nw((unsigned)(size_t)(lu32*)cnt)); }
DI void lds_add(unsigned* cnt, unsigned v, int) {
  asm volatile("s_mov_b64 exec, 1\n\tds_add_u32 %0, %1\n\ts_mov_b64 exec, -1" :: "v"((unsigned)(size_t)(lu32*)cnt), "v"(v) : "memory");
}
DI void sbar4(unsigned* cnt, int lane) { if (lds_arrive(cnt, lane) < 3u) lds_wait(cnt, 4u); }
DI float rs32(float a, float b) { auto t = __builtin_amdgcn_permlane32_swap(__float_as_uint(a), __float_as_uint(b), false, false); return __uint_as_float(t[0]) + __uint_as_float(t[1]); }
DI float rs16(float a, float b) { auto t = __builtin_amdgcn_permlane16_swap(__float_as_uint(a), __float_as_uint(b), false, false); return __uint_as_float(t[0]) + __uint_as_float(t[1]); }
template <int N> DI unsigned rowbcast(unsigned x) { return (unsigned)__builtin_amdgcn_mov_dpp((int)x, 0x150 + N, 0xf, 0xf, true); }
DI void pin2(u32x4& a, u32x4& b) { asm volatile("" : "+v"(a), "+v"(b)); }
DI void use2(const u32x4& a, const u32x4& b) { asm volatile("" :: "v"(a), "v"(b)); }
DI bool nz8(const u32x4& v) { return ((v[0] | v[1]) != 0u) & ((v[2] | v[3]) != 0u); }

DI u32x4 qsum(const u32x4& g) {
  const auto ta = __builtin_amdgcn_permlane32_swap(g[0], g[0], false, false), tc = __builtin_amdgcn_permlane32_swap(g[2], g[2], false, false);
  const unsigned a = pk_bf(bflo(ta[0]) + bflo(ta[1]), bfhi(ta[0]) + bfhi(ta[1])), c = pk_bf(bflo(tc[0]) + bflo(tc[1]), bfhi(tc[0]) + bfhi(tc[1]));
  const auto ra = __builtin_amdgcn_permlane16_swap(a, a, false, false), rc = __builtin_amdgcn_permlane16_swap(c, c, false, false);
  return u32x4{ra[0], rc[0], ra[1], rc[1]};
}
DI unsigned toff(unsigned row, unsigned ch) { return 256u * row + 16u * (ch ^ (((row & 3) << 2) | ((row >> 2) & 3))); }
constexpr int SMEM = 160 * 1024;
constexpr int L_SCR = 131072;
constexpr int L_MRG = 65536;
constexpr int L_CNT = L_SCR + 32 * 1008;

struct KV { u32x4 r[8]; };
constexpr int CODE_BYTES = 43008;

template <class T> DI T* gptr(unsigned lo, unsigned hi) { return (T*)(__attribute__((address_space(1))) T*)(((u64)hi << 32) | lo); }

__global__ __launch_bounds__(512, 2) void kmain(const u16*, const u16*, const u16*, const u16*, const u16*, const u16*, const u16*, const u16*, const u16*,
                                                const u16*, const u16*, char*, u16*) {
  u32x2 kpa, kpx, kc2; u32x4 kb4; u32x8 kb8; u64 t64;
  int tid = threadIdx.x, blk = blockIdx.x;
  const auto kp = __builtin_amdgcn_kernarg_segment_ptr();
  const int o1 = blk & 24, o2 = 32 + ((blk >> 1) & 8);
  asm volatile("s_load_dwordx2 %0, %8, %9\n\ts_load_dwordx2 %1, %8, %10\n\ts_load_dwordx4 %2, %8, 0x30\n\ts_load_dwordx8 %3, %8, 0x40\n\ts_load_dwordx2 %4, %8, 0x60\n\ts_memrealtime %5"
               : "=&s"(kpa), "=&s"(kpx), "=&s"(kb4), "=&s"(kb8), "=&s"(kc2), "=&s"(t64), "+v"(tid), "+s"(blk) : "s"(kp), "s"(o1), "s"(o2));
  __builtin_assume(tid >= 0 && tid < 512 && blk >= 0 && blk < 256);
  const unsigned cpf = __builtin_amdgcn_raw_buffer_load_b32(__builtin_amdgcn_make_buffer_rsrc((void*)(__builtin_amdgcn_s_getpc() & ~255ull), 0, CODE_BYTES, 0x00020000),
                                                            (int)((tid & 0x10f) == 0x100 ? (blk >> 3) * 2048 + (tid & 0xf0) * 8 : OOB), 0, 0);
  COMPILER_FENCE();
  SCHED();
  __shared__ __attribute__((aligned(16))) char smem[SMEM];
  const int lane = tid & 63;
  const int wave = __builtin_amdgcn_readfirstlane(tid >> 6), w4 = wave & 3;
  const int cls = (blk >> 3) & 3;
  int idx = 8 * (blk & 7) + (blk >> 5);
  idx = (idx & ~7) | ((idx + ((wave >= 4 ? 0 : 0x2400) >> (4 * cls))) & 7);
  const int h = idx >> 1, b = idx & 1;
  const int r4 = lane >> 4, c16 = lane & 15;
  unsigned* cnt = reinterpret_cast<unsigned*>(smem + L_CNT);
  STAMP_DECL;
  STAMP(0);
  const unsigned c0 = cls ? 0u : ~0u;
  unsigned sa = cls ? 8192u : 4096u;
  unsigned ao = ((((b * 256 + w4 * 64) * 8192 + h * 256) & ~c0) | (((idx * 128 + w4) * 1024) & c0)) + (r4 << (17u - (c0 & 9u))) + c16 * 16, xo = (b * 256 + w4 * 64 + 16 * r4) * 2;
  asm volatile("" : "+v"(ao), "+v"(xo), "+s"(sa));
  SCHED();
  asm volatile("s_waitcnt lgkmcnt(0)" : "+s"(kpa), "+s"(kpx), "+s"(kb4), "+s"(kb8), "+s"(kc2), "+s"(t64) : "s"(kp), "s"(o1), "s"(o2));
  const unsigned tstart = (unsigned)t64;
  const char* __restrict__ pa = gptr<const char>(kpa[0], kpa[1]); const char* __restrict__ xp = gptr<const char>(kpx[0], kpx[1]);
  const u16* __restrict__ w_uv = gptr<const u16>(kb4[0], kb4[1]); const u16* __restrict__ w_o = gptr<const u16>(kb4[2], kb4[3]);
  const u16* __restrict__ kc = gptr<const u16>(kb8[0], kb8[1]); const u16* __restrict__ vc = gptr<const u16>(kb8[2], kb8[3]);
  const u16* __restrict__ wo = gptr<const u16>(kb8[4], kb8[5]); char* __restrict__ ws = gptr<char>(kb8[6], kb8[7]);
  u16* __restrict__ out = gptr<u16>(kc2[0], kc2[1]);
  const rsrc_t rs = __builtin_amdgcn_make_buffer_rsrc(ws, 0, (int)WS_END, 0x00020000);
  unsigned tag, tlow;
  u32x2 epw;
  auto take_epoch = [&]() {
    asm volatile("" : "+v"(epw) : "v"(cpf));
    const u64 t64 = (((u64)(unsigned)__builtin_amdgcn_readfirstlane(epw[1]) << 32) | (unsigned)__builtin_amdgcn_readfirstlane(epw[0])) + GOLD;
    tag = (unsigned)(t64 >> 32); tlow = (unsigned)t64;
    asm volatile("s_waitcnt lgkmcnt(0)" : "+s"(tag), "+s"(tlow) :: "memory");
  };
  if (wave >= 4) {
    if (tid - 256 < 16) cnt[tid - 256] = 0u;
    asm volatile("s_waitcnt lgkmcnt(0)" ::: "memory");
    const unsigned pTL = PAR(0, TL), pTS = PAR(1 + cls, cls == 0 ? TS0 : (cls == 1 ? TS1 : (cls == 2 ? TS2 : TS3))), pSTG = PAR(9, STG), pBSG = PAR(10, BSG);
    const unsigned pRA2 = PAR(11, RA2), pRA3 = PAR(12, RA3), pTP = PAR(15 + cls, cls == 0 ? TP0 : TP2), pPA = PAR(19 + cls, (cls & 1) ? 0u : PA);
    rawbar();
    const int e2 = (pRA2 >> (idx & 7)) & 1, e3 = (pRA3 >> (idx & 7)) & 1;
    const int ns = cls == 0 ? NS0 : (cls == 1 ? NS1 + e2 + e3 : (cls == 2 ? NS2 - e2 : NS3 - e3));
    const unsigned pTA = __builtin_amdgcn_readfirstlane((unsigned)((float)pTS * __builtin_amdgcn_rcpf((float)(ns - 5)) + 0.5f));
    const size_t kvo = ((size_t)(b * 32 + h) * SK + w4 * 16) * 256;
    const char* kb = reinterpret_cast<const char*>(kc) + kvo;
    const char* vb = reinterpret_cast<const char*>(vc) + kvo;
    const unsigned lo = lane * 16u;
    u64 tlo = cls == 0 ? TL0 : (cls == 1 ? TL1 : (cls == 2 ? TL2 : TL3));
    u64 thi = cls == 0 ? TH0 : (cls == 1 ? TH1 | ((e2 ? LA2 : LA3) << (6 * (NS1 - 10))) : (cls == 2 ? TH2 : TH3));
    const u64 tlo0 = tlo, thi0 = thi;
    int hn = 0;
    const char* hb = kb;
    const char* pc[2];
    auto next2 = [&]() {
#pragma unroll
      for (int i = 0; i < 2; ++i) {
        if (hn == ns) { tlo = tlo0; thi = thi0; hb = vb; }
        ++hn;
        pc[i] = hb + ((((unsigned)tlo + (unsigned)idx) & 63u) << 14);
        tlo = (tlo >> 6) | ((thi & 63u) << 54); thi >>= 6;
      }
    };
    KV A, B;
    u32x4 W[8];
    u32x4 T[4][8];
    auto ldp = [&](KV& S, auto P) {
      constexpr int p = P.v;
      const char* q = pc[p >> 1] + lo + (p & 1) * 2048;
      S.r[2 * p] = ld16nt(q); S.r[2 * p + 1] = ld16nt(q + 1024);
    };
    auto landed = [&](KV& S, auto P) { pin2(S.r[2 * P.v], S.r[2 * P.v + 1]); };
    KV X;
    const unsigned qo = WS_QX + ((32 + h) * 2 + b) * 1024 + lane * 16;
    u32x4 g0;
    {
      unsigned tk = 0u;
      if (w4 == 0) {
        const int si = (idx & ~7) | ((idx + (0x2400 >> (4 * cls))) & 7);
        const char* p = kb + cls * 128;
        if (lane == 1) p = cls == 2 ? reinterpret_cast<const char*>(w_uv) + (si & 1) * 2097152 + (si >> 1) * 256 : reinterpret_cast<const char*>(cls == 1 ? w_o : wo) + si * 65536;
        if (lane == 2) p = vb + cls * 128;
        if (lane == 3) p = reinterpret_cast<const char*>(wo) + h * 131072;
        if (lane < 4) tk = __builtin_nontemporal_load(reinterpret_cast<const unsigned*>(p));
      }
      lds_wait_slow(cnt + 14, 4u);
      tag = __builtin_amdgcn_readfirstlane(cnt[16]);
      asm volatile("" :: "v"(tk), "v"(cpf));
    }
    STAMP(10);
    bool noorg = w4 == 3;
    auto origin = [&]() {
#pragma unroll 1
      for (int spin = 0; spin < 2 && noorg; ++spin) {
        u32x2 t[8];
        asm volatile("s_load_dwordx2 %0, %8, 0x0 glc\n\ts_load_dwordx2 %1, %8, 0x400 glc\n\ts_load_dwordx2 %2, %8, 0x800 glc\n\ts_load_dwordx2 %3, %8, 0xc00 glc\n\t"
                     "s_load_dwordx2 %4, %8, 0x1000 glc\n\ts_load_dwordx2 %5, %8, 0x1400 glc\n\ts_load_dwordx2 %6, %8, 0x1800 glc\n\ts_load_dwordx2 %7, %8, 0x1c00 glc\n\ts_waitcnt lgkmcnt(0)"
                     : "=&s"(t[0]), "=&s"(t[1]), "=&s"(t[2]), "=&s"(t[3]), "=&s"(t[4]), "=&s"(t[5]), "=&s"(t[6]), "=&s"(t[7]) : "s"(ws + WS_T0 + ((blk >> 3) & 7) * 128) : "memory");
        unsigned e = 0u; int sum = 0;
#pragma unroll
        for (int k = 0; k < 8; ++k) { e |= t[k][1] ^ tag; sum += (int)(t[k][0] - tstart); }
        if (e == 0u) { lds_add(cnt + 13, (unsigned)min(sum >> 3, 100), lane); noorg = false; }
      }
    };
    sfor<0, 12>([&](auto I) {
      constexpr int i = I.v, j = i - 4;
      if constexpr ((i & 3) == 0) next2();
      if constexpr (i == 4) origin();
      if constexpr (j >= 0) { if constexpr (j < 4) landed(X, IC<j & 3>{}); else landed(B, IC<j & 3>{}); }
      if constexpr (j >= 0 && (j & 1)) TR();
      if constexpr (i == QPE) g0 = ld16c(rs, qo);
      if constexpr (i < 4) ldp(X, IC<i & 3>{}); else if constexpr (i < 8) ldp(B, IC<i & 3>{}); else ldp(A, IC<i & 3>{});
      SCHED();
    });
    origin();
    const char* wp = reinterpret_cast<const char*>(wo) + ((size_t)h * 128 + r4 * 8) * 1024 + w4 * 256 + c16 * 16;
    u32x4 qpk;
    {
      if (!__all((g0[1] == tag) & (g0[3] == tag))) {
#pragma unroll 1
        for (int spin = 0; spin < SPIN_MAX; ++spin) {
          COMPILER_FENCE();
          g0 = ld16c(rs, qo);
          if (__all((g0[1] == tag) & (g0[3] == tag))) break;
        }
      }
      qpk = qsum(g0);
    }
    STAMP(7);
    const bool holds = cls == 0;
    float m = 0.f, l = 0.f, acc[8];
    unsigned sc[5];
#pragma unroll
    for (int k = 0; k < 8; ++k) acc[k] = 0.f;
#pragma unroll
    for (int k = 0; k < 5; ++k) sc[k] = 0xff800000u;
    int vn = 0;
    unsigned torg, due = pTL - (unsigned)(ns - 5) * pTA - (3 - w4) * pSTG - ((idx * 5 + cls * 3) & 15) * pBSG;
    unsigned rwo;
    const unsigned zo = WS_GZ + (b * 32 + h) * 512;
    u32x2 zs[4];
    float wr_;
    auto zreq = [&]() {
      asm volatile("s_load_dwordx2 %0, %4, 0x0 glc\n\ts_load_dwordx2 %1, %4, 0x80 glc\n\ts_load_dwordx2 %2, %4, 0x100 glc\n\ts_load_dwordx2 %3, %4, 0x180 glc"
                   : "=&s"(zs[0]), "=&s"(zs[1]), "=&s"(zs[2]), "=&s"(zs[3]) : "s"(ws + zo) : "memory");
    };
    auto zget = [&]() {
#pragma unroll 1
      for (int spin = 0; spin < SPIN_MAX; ++spin) {
        asm volatile("s_waitcnt lgkmcnt(0)" : "+s"(zs[0]), "+s"(zs[1]), "+s"(zs[2]), "+s"(zs[3]) :: "memory");
        if (((zs[0][1] ^ tag) | (zs[1][1] ^ tag) | (zs[2][1] ^ tag) | (zs[3][1] ^ tag)) == 0u) break;
        __builtin_amdgcn_s_sleep(2);
        zreq();
      }
      asm volatile("s_waitcnt lgkmcnt(0)" : "+s"(zs[0]), "+s"(zs[1]), "+s"(zs[2]), "+s"(zs[3]) :: "memory");
      const float z0 = __uint_as_float(zs[0][0]), z1 = __uint_as_float(zs[1][0]), z2 = __uint_as_float(zs[2][0]), z3 = __uint_as_float(zs[3][0]);
      const float Z = fmaxf(fmaxf(z0, z1), fmaxf(z2, z3));
      wr_ = ex2(fmaf(m, C2, -Z)) * __builtin_amdgcn_rcpf((ex2(z0 - Z) + ex2(z1 - Z)) + (ex2(z2 - Z) + ex2(z3 - Z)));
      asm volatile("" : "+v"(wr_));
    };
    auto qk = [&](const u32x4* K, auto I) {
      float x[4];
#pragma unroll
      for (int t = 0; t < 4; ++t) {
        x[t] = 0.f;
#pragma unroll
        for (int k = 0; k < 4; ++k) x[t] = dot2(K[t][k], qpk[k], x[t]);
      }
      const float u0 = (c16 & 1 ? x[1] : x[0]) + dpp<0xB1>(c16 & 1 ? x[0] : x[1]), u1 = (c16 & 1 ? x[3] : x[2]) + dpp<0xB1>(c16 & 1 ? x[2] : x[3]);
      float v = (c16 & 2 ? u1 : u0) + dpp<0x4E>(c16 & 2 ? u0 : u1);
      v += dpp<0x124>(v); v += dpp<0x128>(v);
      sc[I.v >> 2] = __builtin_amdgcn_update_dpp(sc[I.v >> 2], __float_as_uint(v), 0xE4, 0xf, 1 << (I.v & 3), false);
      asm volatile("" : "+v"(sc[I.v >> 2]));
    };
    auto soft = [&]() {
      m = row16_max(fmaxf(fmaxf(fmaxf(__uint_as_float(sc[0]), __uint_as_float(sc[1])), fmaxf(__uint_as_float(sc[2]), __uint_as_float(sc[3]))), __uint_as_float(sc[4])));
      const float mc = m * C2;
      l = 0.f;
#pragma unroll
      for (int k = 0; k < 5; ++k) {
        const float p = ex2(fmaf(__uint_as_float(sc[k]), C2, -mc));
        sc[k] = pk_bf(p, dpp<0xB1>(p));
        l = dot2(sc[k], 0x3f003f00u, l);
      }
      l = row16_sum(l);
    };
    auto pv = [&](const u32x4* V) {
      const unsigned p01 = rowbcast<0>(sc[0]), p23 = rowbcast<2>(sc[0]);
#pragma unroll
      for (int k = 0; k < 4; ++k) { acc[2 * k] = dot2(plo(V[0][k], V[1][k]), p01, acc[2 * k]); acc[2 * k + 1] = dot2(phi(V[0][k], V[1][k]), p01, acc[2 * k + 1]); }
#pragma unroll
      for (int k = 0; k < 4; ++k) { acc[2 * k] = dot2(plo(V[2][k], V[3][k]), p23, acc[2 * k]); acc[2 * k + 1] = dot2(phi(V[2][k], V[3][k]), p23, acc[2 * k + 1]); }
      sc[0] = (unsigned)__builtin_amdgcn_mov_dpp((int)sc[0], 0x12C, 0xf, 0xf, true);
      if ((++vn & 3) == 0) { sc[0] = sc[1]; sc[1] = sc[2]; sc[2] = sc[3]; sc[3] = sc[4]; }
      asm volatile("" : "+v"(acc[0]), "+v"(acc[1]), "+v"(acc[2]), "+v"(acc[3]), "+v"(acc[4]), "+v"(acc[5]), "+v"(acc[6]), "+v"(acc[7]));
    };
    auto step = [&](KV& S, KV& N, auto SI, auto REFILL, auto WR) {
      constexpr bool refill = REFILL.v == 1;
      constexpr int wr = WR.v;
      if constexpr (refill) next2();
      if constexpr (refill && SI.v >= 3) { until((int)(due - torg) < (int)pTP ? due - pPA : due); due += pTA; }
      constexpr int ah = wr < 0 ? AHL : 0;
      auto point = [&](auto P) {
        if constexpr (refill) {
          if constexpr (P.v == 0 && ah > 0) { if constexpr (wr >= 0) { if (holds) lds_hold(cnt + 8); } sfor<0, ah>([&](auto Q) { ldp(N, Q); }); SCHED(); }
          landed(S, P);
          if constexpr (P.v & 1) TR();
          if constexpr (P.v + ah < 4) { if constexpr ((P.v & 1) == 0 && wr >= 0) { if (holds) lds_hold(cnt + 8); } ldp(N, IC<(P.v + ah) & 3>{}); }
          if constexpr (wr >= 0 && wr < 8) W[(4 * wr + P.v) & 7] = ld16(wp + (((4 * wr + P.v) >> 3) * 32 + ((4 * wr + P.v) & 7)) * 1024);
          SCHED();
        }
      };
      sfor<0, 2>([&](auto H) {
        constexpr int i = 2 * SI.v + H.v;
        if constexpr (REFILL.v == 2 && H.v == 0) { zreq(); SCHED(); }
        point(IC<2 * H.v>{});
        point(IC<2 * H.v + 1>{});
        if constexpr (i < NSMIN) qk(S.r + 4 * H.v, IC<i>{});
        else if constexpr (i > NSMAX) pv(S.r + 4 * H.v);
        else {
          if (i < ns) { if constexpr (i < NSMAX) qk(S.r + 4 * H.v, IC<i>{}); }
          else { if (i == ns) soft(); pv(S.r + 4 * H.v); }
        }
        SCHED();
      });
      if constexpr (REFILL.v == 2) {
        zget();
        STAMP(1);
        *reinterpret_cast<u32x4*>(smem + rwo) = u32x4{pk_bf(acc[0] * wr_, acc[1] * wr_), pk_bf(acc[2] * wr_, acc[3] * wr_), pk_bf(acc[4] * wr_, acc[5] * wr_), pk_bf(acc[6] * wr_, acc[7] * wr_)};
        COMPILER_FENCE();
      }
      if constexpr (wr >= 1) {
        constexpr int t = (wr - 1) >> 1, j0 = ((wr - 1) & 1) * 2;
#pragma unroll
        for (int jj = 0; jj < 2; ++jj)
#pragma unroll
          for (int e = 0; e < 4; ++e) {
            const unsigned a_ = W[(4 * wr - 4 + 2 * jj) & 7][e], b_ = W[(4 * wr - 3 + 2 * jj) & 7][e];
            T[t][2 * e][j0 + jj] = plo(a_, b_); T[t][2 * e + 1][j0 + jj] = phi(a_, b_);
          }
#pragma unroll
        for (int e = 0; e < 8; e += 2) pin2(T[t][e], T[t][e + 1]);
      }
      SCHED();
    };
    step(B, B, IC<1>{}, IC<0>{}, IC<-1>{});
    step(A, B, IC<2>{}, IC<1>{}, IC<0>{});
    step(X, X, IC<0>{}, IC<0>{}, IC<-1>{});
    torg = tstart + __builtin_amdgcn_readfirstlane(cnt[13]); due += torg;
    sfor<0, 4>([&](auto I) {
      step(B, A, IC<2 * I.v + 3>{}, IC<1>{}, IC<2 * I.v + 1>{});
      if constexpr (I.v == 3) {
        float* ml = reinterpret_cast<float*>(cnt + 32);
        ml[(c16 & 1) * 16 + w4 * 4 + r4] = (c16 & 1) ? l : m;
        COMPILER_FENCE();
        const unsigned got = lds_arrive_nw(L_CNT + 44);
        const float mr = ml[c16], lr = ml[16 + c16];
        SCHED();
        if (lds_done(got) == 3u) {
          const float M = row16_max(mr);
          const unsigned z = __float_as_uint(fmaf(M, C2, __builtin_amdgcn_logf(row16_sum(ex2((mr - M) * C2) * lr))));
          asm volatile("buffer_store_dwordx4 %0, %1, %2, 0 offen sc1\n\ts_nop 2" :: "v"(u32x4{z, tag, z, tag}), "v"(lane < 8 ? zo + cls * 128 + lane * 16 : OOB), "s"(rs) : "memory");
        }
        SCHED();
      }
      step(A, B, IC<2 * I.v + 4>{}, IC<1>{}, IC<2 * I.v + 2>{});
    });
    unsigned yoff = WS_GB + ((b * 128 + h * 4 + cls) * 256 + 64 * w4 + lane) * 8, rro = L_MRG + c16 * 272 + r4 * 16;
    rwo = L_MRG + (w4 * 4 + r4) * 272 + c16 * 16;
    asm volatile("" : "+v"(yoff), "+v"(rwo), "+v"(rro));
    if (cls == 0) lds_wait(cnt + 1, 4u);
    const int nit = (ns - 12) >> 1;
#pragma unroll 1
    for (int i = 0; i < nit; ++i) {
      step(B, A, IC<11>{}, IC<1>{}, IC<-1>{});
      step(A, B, IC<11>{}, IC<1>{}, IC<-1>{});
    }
    if (ns & 1) { step(B, A, IC<11>{}, IC<1>{}, IC<-1>{}); step(A, A, IC<11>{}, IC<2>{}, IC<-1>{}); }
    else step(B, B, IC<11>{}, IC<2>{}, IC<-1>{});
    {
      u32x4 af[4];
      unsigned got = lds_arrive_nw(L_CNT), need = 3u;
#pragma unroll 1
      for (int spin = 0; spin < SPIN_MAX; ++spin) {
#pragma unroll
        for (int t = 0; t < 4; ++t) af[t] = *reinterpret_cast<const u32x4*>(smem + rro + t * 64);
        SCHED();
        if (lds_done(got) >= need) break;
        need = 4u;
        __builtin_amdgcn_s_sleep(1);
        asm volatile("ds_read_b32 %0, %1" : "=v"(got) : "v"((unsigned)L_CNT) : "memory");
      }
      SCHED();
      f32x4 d[8];
      float a[8], v4[4];
      sfor<0, 5>([&](auto I) {
        constexpr int i = I.v;
        sfor<0, 8>([&](auto J) {
          constexpr int e = i + 4 * (J.v & 1), t = J.v >> 1;
          if constexpr (i < 4) { d[e] = __builtin_amdgcn_mfma_f32_16x16x32_bf16(__builtin_bit_cast(bf16x8_t, af[t]), __builtin_bit_cast(bf16x8_t, T[t][e]), t ? d[e] : f32x4{0.f, 0.f, 0.f, 0.f}, 0, 0, 0); SCHED(); }
          if constexpr (i >= 1 && (J.v == 4 || J.v == 5)) { const f32x2 u = f32x2{d[e - 1][0], d[e - 1][1]} + f32x2{d[e - 1][2], d[e - 1][3]}; a[e - 1] = u[0] + u[1]; asm volatile("" : "+v"(a[e - 1])); }
          if constexpr (i >= 1 && J.v == 6) v4[i - 1] = rs32(a[i - 1], a[i + 3]);
          if constexpr (i < 4) SCHED();
        });
      });
      st8c(rs, yoff, u32x2{pk_bf(rs16(v4[0], v4[2]), rs16(v4[1], v4[3])), tag});
    }
    STAMP(2);
    STAMP_FLUSH(1);
    TR_FLUSH();
    return;
  }
  const rsrc_t ro = __builtin_amdgcn_make_buffer_rsrc(out, 0, 4096, 0x00020000);
  auto fin = [&](auto NL, unsigned base, unsigned col, bool bump) {
    constexpr int nl = NL.v;
    unsigned doff = base + (((idx >> 5) * 4 + w4) * 16 * nl + (lane >> 2)) * 2048 + (idx & 31) * 64 + (lane & 3) * 16;
    unsigned fwo = 32768 + (c16 < 4 ? (w4 * 16 + 4 * c16 + r4) * 4 : 256 + lane * 4);
    unsigned fro = 32768 + (lane & 7) * 8, foo = lane < 8 ? ((idx >> 5) * 1024 + col + (nl == 2 ? 16 * (idx & 24) + 64 * (idx & 1) + (idx & 6) + 8 * lane : 16 * (idx & 31) + 2 * lane)) * 2 : OOB;
    asm volatile("" : "+v"(doff), "+v"(fwo), "+v"(fro), "+v"(foo));
    lds_wait(cnt + 0, 4u);
    STAMP(4);
    u32x4 d[nl];
    unsigned eh = 1u;
#pragma unroll 1
    for (int spin = SPIN_MAX; spin > 0; --spin) {
      COMPILER_FENCE();
      if (eh != 0u) {
#pragma unroll
        for (int j = 0; j < nl; ++j) d[j] = ld16c(rs, doff + j * 32768);
        eh = 0u;
#pragma unroll
        for (int j = 0; j < nl; ++j) { eh |= (d[j][1] ^ tag) | (d[j][3] ^ tag); asm volatile("" : "+v"(eh)); }
      }
      if (__builtin_amdgcn_ballot_w64(eh != 0u) == 0ull) break;
    }
    STAMP(11);
    float y[4] = {bflo(d[0][0]), bfhi(d[0][0]), bflo(d[0][2]), bfhi(d[0][2])};
    if constexpr (nl == 2) { y[0] += bflo(d[1][0]); y[1] += bfhi(d[1][0]); y[2] += bflo(d[1][2]); y[3] += bfhi(d[1][2]); }
    float t = rs16(rs32(y[0], y[2]), rs32(y[1], y[3]));
    t += dpp<0x124>(t); t += dpp<0x128>(t);
    *reinterpret_cast<float*>(smem + fwo) = t;
    COMPILER_FENCE();
    const unsigned got = lds_arrive_nw(L_CNT + 24);
    f32x2 t2[4];
#pragma unroll
    for (int w = 0; w < 4; ++w) t2[w] = *reinterpret_cast<const f32x2*>(smem + fro + w * 64);
    SCHED();
    if (lds_done(got) == 3u) {
      const f32x2 r = (t2[0] + t2[1]) + (t2[2] + t2[3]);
      __builtin_amdgcn_raw_buffer_store_b32(pk_bf(r[0], r[1]), ro, (int)foo, 0, 0);
      if (bump) st8c(rs, lane == 0 ? WS_EP : OOB, u32x2{tlow, tag});
    }
  };
  u32x4 Wa[16], Wb[16], xv[4];
  auto row = [&](u32x4& d) { d = ld16nt(pa + ao); SCHED(); ao += sa; asm volatile("" : "+v"(ao)); };
  sfor<0, 2>([&](auto I) { row(Wa[I.v]); });
  sfor<0, 4>([&](auto J) { xv[J.v] = ld16(xp + xo + (J.v >> 1) * 1024 + 16 * (J.v & 1)); });
  SCHED();
  sfor<2, 8>([&](auto I) { row(Wa[I.v]); });
  epw = ld8c(rs, WS_EP);
  const unsigned qa_off = WS_QA + (((c16 >> 1) & 1) * 32 + h) * 1024 + (b * 256 + w4 * 64 + 16 * r4 + 8 * (c16 & 1)) * 2;
  st16c(rs, cls == 1 && c16 < 4 ? qa_off : OOB, u32x4{0u, 0u, 0u, 0u});
  SCHED();
  auto sync = [&]() {
    rawbar();
    take_epoch();
    cnt[16] = tag;
    COMPILER_FENCE();
  };
  if (cls == 0) {
    sfor<8, 16>([&](auto I) { row(Wa[I.v]); });
    sync();
    asm volatile("" :: "v"(xv[0]), "v"(xv[1]), "v"(xv[2]), "v"(xv[3]));
    STAMP(13);
    char* scr = smem + L_SCR;
    const int ab = idx >> 5, r = lane & 31, hh = lane >> 5;
    const unsigned fo = WS_QX + (w4 * 8 + (c16 >> 1)) * 2048 + (c16 & 1) * 512 + r4 * 8;
    u32x2 fl;
    u32x4 g[9];
    const unsigned o8 = WS_QA + (ab * 32 + w4 * 8) * 1024 + lane * 16, ol = WS_QA + (ab * 32 + r) * 1024 + 992 + 16 * hh;
    auto put = [&](int i, const u32x4& v) { *reinterpret_cast<u32x4*>(smem + ((i >> 3) * 4 + r4) * 8192 + toff(4 * (i & 7) + w4, c16)) = v; };
    sfor<0, 16>([&](auto I) { put(I.v, Wa[I.v]); if constexpr ((I.v & 3) == 3) TR(); SCHED(); });
    until(tstart + PAR(26, T0B));
    sfor<0, 16>([&](auto I) { row(Wb[I.v]); });
    lds_add(cnt + 14, 1u, lane);
    SCHED();
    sfor<0, 16>([&](auto I) { put(16 + I.v, Wb[I.v]); if constexpr (I.v == 12) fl = ld8c(rs, fo); if constexpr ((I.v & 3) == 3) TR(); SCHED(); });
    STAMP(8);
    u32x4 qlast;
    {
      if (!__all(fl[1] == tag)) {
#pragma unroll 1
        for (int spin = 0; spin < SPIN_MAX; ++spin) {
          COMPILER_FENCE();
          if (__all(ld8c(rs, fo)[1] == tag)) break;
          __builtin_amdgcn_s_sleep(8);
        }
      }
      until(tstart + PAR(27, T0Q));
      STAMP(5);
      sbar4(cnt + 7, lane);
      STAMP(12);
      unsigned pend = 0x1ffu;
#pragma unroll 1
      for (int spin = 0; spin < SPIN_MAX && pend; ++spin) {
        COMPILER_FENCE();
        u32x4 t[9];
#pragma unroll
        for (int i = 0; i < 9; ++i) t[i] = ld16c(rs, (pend >> i & 1) ? (i < 8 ? o8 + i * 1024 : ol) : OOB);
#pragma unroll
        for (int i = 0; i < 9; ++i) {
          if (pend >> i & 1) g[i] = t[i];
          if (__all(nz8(t[i]))) pend &= ~(1u << i);
        }
      }
      STAMP(9);
      qlast = g[8];
#pragma unroll
      for (int i = 0; i < 8; ++i)
        if (lane < 62) *reinterpret_cast<u32x4*>(scr + (w4 * 8 + i) * 1008 + 16 * lane) = g[i];
    }
    sbar4(cnt + 3, lane);
    STAMP(10);
    f32x16 sacc;
#pragma unroll
    for (int i = 0; i < 16; ++i) sacc[i] = 0.f;
#pragma unroll
    for (int jj = 0; jj < 32; ++jj) {
      const u32x4 af = *reinterpret_cast<const u32x4*>(smem + (w4 * 4 + (jj >> 3)) * 8192 + toff(r, 2 * (jj & 7) + hh));
      const u32x4 bf = jj < 31 ? *reinterpret_cast<const u32x4*>(scr + r * 1008 + 16 * (2 * (jj < 31 ? jj : 0) + hh)) : qlast;
      sacc = __builtin_amdgcn_mfma_f32_32x32x16_bf16(__builtin_bit_cast(bf16x8_t, af), __builtin_bit_cast(bf16x8_t, bf), sacc, 0, 0, 0);
    }
    float m = sacc[0];
#pragma unroll
    for (int i = 1; i < 16; ++i) m = fmaxf(m, sacc[i]);
    m = xhalf_max(m);
    if (hh == 0) *reinterpret_cast<float*>(scr + r * 1008 + 992 + 4 * w4) = m;
    sbar4(cnt + 4, lane);
    STAMP(11);
    {
      const f32x4 m4 = *reinterpret_cast<const f32x4*>(scr + r * 1008 + 992);
      m = fmaxf(fmaxf(m4[0], m4[1]), fmaxf(m4[2], m4[3]));
    }
    const float mc = m * C2;
    float p[16], ls = 0.f;
#pragma unroll
    for (int i = 0; i < 16; ++i) { p[i] = ex2(fmaf(sacc[i], C2, -mc)); ls += p[i]; }
    ls = xhalf_sum(ls);
    sbar4(cnt + 5, lane);
    u32x4* pf = reinterpret_cast<u32x4*>(scr);
    float* wl = reinterpret_cast<float*>(scr + 8192);
    if (hh == 0) wl[r * 4 + w4] = ls;
    pf[(2 * w4) * 64 + lane] = u32x4{pk_bf(p[0], p[1]), pk_bf(p[2], p[3]), pk_bf(p[4], p[5]), pk_bf(p[6], p[7])};
    pf[(2 * w4 + 1) * 64 + lane] = u32x4{pk_bf(p[8], p[9]), pk_bf(p[10], p[11]), pk_bf(p[12], p[13]), pk_bf(p[14], p[15])};
    sbar4(cnt + 6, lane);
    u32x4 pb8[8];
#pragma unroll
    for (int ks = 0; ks < 8; ++ks) pb8[ks] = pf[ks * 64 + lane];
    const f32x4 l4 = *reinterpret_cast<const f32x4*>(wl + r * 4);
    const float lsum = (l4[0] + l4[1]) + (l4[2] + l4[3]), rl = __builtin_amdgcn_rcpf(lsum);
    const int qq = (lane & 15) >> 2, pp = lane & 3, blk2 = (lane >> 4) & 1;
    const unsigned tw = tstart + __builtin_amdgcn_readfirstlane(cnt[13]) + PAR(13, TW), th = tw - PAR(14, HL);
    u32x4 vv[2][4];
    bool held = false;
    char* ot = scr + 12288 + w4 * 4096;
#pragma unroll
    for (int c2 = 0; c2 < 2; ++c2) {
      f32x16 oacc[2];
#pragma unroll
      for (int ct = 0; ct < 2; ++ct)
#pragma unroll
        for (int i = 0; i < 16; ++i) oacc[ct][i] = 0.f;
#pragma unroll
      for (int ks = 0; ks < 8; ++ks)
#pragma unroll
        for (int ct = 0; ct < 2; ++ct) {
          typedef __attribute__((address_space(3))) s16x4* lp;
          const char* tile = smem + ((ks >> 1) * 4 + w4) * 8192;
          const int cc = 2 * c2 + ct;
          const s16x4 lo = __builtin_amdgcn_ds_read_tr16_b64_v4i16((lp)(tile + toff(16 * (ks & 1) + 4 * hh + qq, 4 * cc + 2 * blk2 + (pp >> 1)) + 8 * (pp & 1)));
          const s16x4 hi = __builtin_amdgcn_ds_read_tr16_b64_v4i16((lp)(tile + toff(16 * (ks & 1) + 8 + 4 * hh + qq, 4 * cc + 2 * blk2 + (pp >> 1)) + 8 * (pp & 1)));
          const s16x8 afr = __builtin_shufflevector(lo, hi, 0, 1, 2, 3, 4, 5, 6, 7);
          oacc[ct] = __builtin_amdgcn_mfma_f32_32x32x16_bf16(__builtin_bit_cast(bf16x8_t, afr), __builtin_bit_cast(bf16x8_t, pb8[ks]), oacc[ct], 0, 0, 0);
        }
#pragma unroll
      for (int ct = 0; ct < 2; ++ct)
#pragma unroll
        for (int g = 0; g < 4; ++g)
          *reinterpret_cast<u32x2*>(ot + r * 128 + 16 * ((4 * ct + g) ^ (r & 7)) + 8 * hh) = u32x2{pk_bf(oacc[ct][4 * g] * rl, oacc[ct][4 * g + 1] * rl), pk_bf(oacc[ct][4 * g + 2] * rl, oacc[ct][4 * g + 3] * rl)};
      if (c2 == 0) { held = (int)((unsigned)wall_clock64() + 30u - th) >= 0; if (held) lds_add(cnt + 8, 1u, lane); }
      __builtin_amdgcn_wave_barrier();
#pragma unroll
      for (int i = 0; i < 4; ++i) { vv[c2][i] = *reinterpret_cast<const u32x4*>(ot + (8 * i + (lane >> 3)) * 128 + 16 * ((lane ^ (lane >> 3)) & 7)); asm volatile("" : "+v"(vv[c2][i])); }
      __builtin_amdgcn_wave_barrier();
    }
    lds_arrive(cnt + 1, lane);
    if (!held) { until(th); lds_add(cnt + 8, 1u, lane); }
    until(tw);
#pragma unroll
    for (int i = 0; i < 8; ++i) st16c(rs, WS_APO + ((((8 * (i >> 1) + (lane >> 3)) * 2 + (w4 >> 1)) * 64 + idx) * 256 + (w4 & 1) * 128 + 64 * (i & 1) + 8 * (lane & 7)) * 2, vv[i & 1][i >> 1]);
    STAMP(1);
    asm volatile("s_waitcnt vmcnt(0)" ::: "memory");
    if (lds_arrive(cnt + 9 + (w4 >> 1), lane) == 1u && hh == 0)
      st8c(rs, WS_AML + ((r * 2 + (w4 >> 1)) * 16 + (idx >> 2)) * 128 + (idx & 3) * 8, u32x2{__float_as_uint(mc + __builtin_amdgcn_logf(lsum)), tag});
    lds_add(cnt + 8, ~0u, lane);
    STAMP(2);
    STAMP_FLUSH(0); TR_FLUSH();
    return;
  }
  sfor<8, 16>([&](auto I) { row(Wa[I.v]); });
  const u16* pb = cls == 2 ? w_uv + ((size_t)(b * 256 + w4 * 64 + 16 * r4) * 32 + h) * 128 + 8 * c16 : (cls == 1 ? w_o : wo) + ((size_t)h * 128 + b * 64 + w4 * 2) * 512 + 8 * lane;
  sync();
  if (blk < 16 && w4 == 0) asm volatile("buffer_store_dwordx4 %0, %1, %2, 0 offen sc1\n\ts_nop 2" :: "v"(u32x4{tstart, tag, tstart, tag}), "v"(WS_T0 + (blk & 7) * 1024 + lane * 16), "s"(rs) : "memory");
  STAMP(13);
  float* red = reinterpret_cast<float*>(smem);
  if (cls & 1) {
    float acc[2][8];
#pragma unroll
    for (int bb = 0; bb < 2; ++bb)
#pragma unroll
      for (int k = 0; k < 8; ++k) acc[bb][k] = 0.f;
    sfor<0, 8>([&](auto IP) {
      constexpr int ip = IP.v, j = ip - (8 - WBW);
      use2(Wa[2 * ip], Wa[2 * ip + 1]);
      if constexpr (ip & 1) TR();
      if constexpr (j >= 0) { Wb[2 * j] = ld16(pb + j * 4096); Wb[2 * j + 1] = ld16(pb + j * 4096 + 512); }
      SCHED();
      const unsigned xp0 = xv[ip >> 2][ip & 3], xp1 = xv[2 + (ip >> 2)][ip & 3];
#pragma unroll
      for (int k = 0; k < 4; ++k) {
        const unsigned lo = plo(Wa[2 * ip][k], Wa[2 * ip + 1][k]), hi = phi(Wa[2 * ip][k], Wa[2 * ip + 1][k]);
        acc[0][2 * k] = dot2(lo, xp0, acc[0][2 * k]); acc[0][2 * k + 1] = dot2(hi, xp0, acc[0][2 * k + 1]);
        acc[1][2 * k] = dot2(lo, xp1, acc[1][2 * k]); acc[1][2 * k + 1] = dot2(hi, xp1, acc[1][2 * k + 1]);
      }
      SCHED();
    });
    {
      float v8[8], v4[4];
#pragma unroll
      for (int k = 0; k < 8; ++k) v8[k] = rs32(acc[0][k], acc[1][k]);
#pragma unroll
      for (int k = 0; k < 4; ++k) v4[k] = rs16(v8[k], v8[k + 4]);
      *reinterpret_cast<f32x4*>(red + (w4 * 2 + (lane >> 5)) * 128 + 8 * c16 + 4 * (r4 & 1)) = f32x4{v4[0], v4[1], v4[2], v4[3]};
    }
    COMPILER_FENCE();
    const unsigned got = lds_arrive_nw(L_CNT + 8);
    f32x4 t4[4];
#pragma unroll
    for (int w = 0; w < 4; ++w) t4[w] = *reinterpret_cast<const f32x4*>(red + (w * 2 + (lane >> 5)) * 128 + 8 * c16 + 4 * (r4 & 1));
    SCHED();
    if (lds_done(got) == 3u) {
      const f32x4 t = (t4[0] + t4[1]) + (t4[2] + t4[3]);
      st16c(rs, WS_QX + (((cls >> 1) * 32 + h) * 2 + (lane >> 5)) * 1024 + b * 512 + (lane & 31) * 16, u32x4{pk_bf(t[0], t[1]), tag, pk_bf(t[2], t[3]), tag});
      STAMP(15);
      SCHED();
      lds_add(cnt + 2, 1u, lane);
    } else lds_wait(cnt + 2, 5u);
    sfor<WBW, 8>([&](auto J) {
      constexpr int j = J.v;
      use2(Wb[2 * (j - WBW)], Wb[2 * (j - WBW) + 1]);
      if constexpr ((j - WBW) & 1) TR();
      Wb[2 * j] = ld16(pb + j * 4096); Wb[2 * j + 1] = ld16(pb + j * 4096 + 512);
      if constexpr (j == 7) lds_add(cnt + 14, 1u, lane);
      SCHED();
    });
    {
      const unsigned tw = __builtin_nontemporal_load(reinterpret_cast<const unsigned*>(out + (idx >> 5) * 1024 + (cls >> 1) * 512 + 16 * (idx & 31)));
      sfor<8 - WBW, 8>([&](auto J) { use2(Wb[2 * J.v], Wb[2 * J.v + 1]); if constexpr (J.v & 1) TR(); });
      asm volatile("" :: "v"(tw));
    }
    STAMP(8);
  } else {
    sfor<0, 16 - WBW>([&](auto IP) {
      constexpr int ip = IP.v, j = ip - (8 - WBW);
      if constexpr (ip < 8) use2(Wa[2 * ip], Wa[2 * ip + 1]); else use2(Wb[2 * ip - 16], Wb[2 * ip - 15]);
      if constexpr (ip & 1) TR();
      if constexpr (j >= 0) { Wb[2 * j] = ld16nt(pb + j * 8192); Wb[2 * j + 1] = ld16nt(pb + j * 8192 + 4096); }
      SCHED();
    });
    u32x4 g0, g1;
    const unsigned qo = WS_QX + h * 2048 + lane * 16;
    shint<512, 1>(ws + WS_QX + h * 2048, tag);
    STAMP(5);
#pragma unroll 1
    for (int spin = 0; spin < SPIN_MAX; ++spin) {
      COMPILER_FENCE();
      g0 = ld16c(rs, qo); g1 = ld16c(rs, qo + 1024);
      SCHED();
      lds_add(cnt + 14, spin == 0, lane);
      SCHED();
      if (__all((g0[1] == tag) & (g0[3] == tag) & (g1[1] == tag) & (g1[3] == tag))) break;
      __builtin_amdgcn_s_sleep(1);
    }
    sfor<8 - WBW, 8>([&](auto J) { if constexpr (J.v & 1) TR(); });
    const u32x4 q0 = qsum(g0), q1 = qsum(g1);
    STAMP(9);
    float v[8];
    {
      float u[16];
#pragma unroll
      for (int i = 0; i < 16; ++i) {
        float a = 0.f, c = 0.f;
#pragma unroll
        for (int k = 0; k < 4; ++k) { a = dot2(Wa[i][k], q0[k], a); c = dot2(Wa[i][k], q1[k], c); }
        u[i] = (c16 & 2 ? c : a) + dpp<0x4E>(c16 & 2 ? a : c);
        asm volatile("" : "+v"(u[i]));
      }
#pragma unroll
      for (int i = 0; i < 8; ++i) {
        float t = (c16 & 1 ? u[i + 8] : u[i]) + dpp<0xB1>(c16 & 1 ? u[i] : u[i + 8]);
        asm volatile("" : "+v"(t));
        t += dpp<0x124>(t);
        asm volatile("" : "+v"(t));
        t += dpp<0x128>(t);
        asm volatile("" : "+v"(t));
        v[i] = t;
      }
    }
    u32x4 o = {pk_bf(v[0], v[1]), pk_bf(v[2], v[3]), pk_bf(v[4], v[5]), pk_bf(v[6], v[7])};
    if ((o[0] | o[1]) == 0u) o[0] = 0x8000u;
    if ((o[2] | o[3]) == 0u) o[2] = 0x8000u;
    if (c16 < 4) st16c(rs, qa_off, o);
    STAMP(10);
  }
  asm volatile("" :: "v"(xv[0]), "v"(xv[1]), "v"(xv[2]), "v"(xv[3]));
  SCHED();
  if (cls == 3) {
    fin(IC<2>{}, WS_GB, 512, false);
    STAMP(6);
    STAMP_FLUSH(0); TR_FLUSH();
    return;
  }
#pragma unroll
  for (int ip = 0; ip < 8; ++ip) {
#pragma unroll
    for (int k = 0; k < 4; ++k) { const unsigned lo = plo(Wb[2 * ip][k], Wb[2 * ip + 1][k]), hi = phi(Wb[2 * ip][k], Wb[2 * ip + 1][k]); Wb[2 * ip][k] = lo; Wb[2 * ip + 1][k] = hi; }
    pin2(Wb[2 * ip], Wb[2 * ip + 1]);
  }
  if (cls == 2) {
    float* red2 = red + 4 * 256;
    float* wml = red + 3072;
    u32x8 f[4];
    until<6>(tstart + PAR(13, TW) + PAR(28, 60));
#pragma unroll 1
    for (int spin = 0; spin < SPIN_MAX; ++spin) {
      sld4x32(ws + WS_AML + ((h * 2 + b) * 16 + w4 * 4) * 128, f);
      unsigned e = 0u;
#pragma unroll
      for (int i = 0; i < 16; ++i) e |= f[i >> 2][2 * (i & 3) + 1] ^ tag;
      if (e == 0u) break;
      __builtin_amdgcn_s_sleep(2);
    }
    STAMP(3);
    u32x4 pp[8];
#pragma unroll
    for (int i = 0; i < 8; ++i) pp[i] = ld16c(rs, WS_APO + ((h * 2 + b) * 64 + w4 * 16 + 2 * i) * 512 + lane * 16);
    SCHED();
    float wt[8];
    {
      float M = -INFINITY, L = 0.f;
#pragma unroll
      for (int i = 0; i < 8; ++i) {
        unsigned z0 = f[i >> 1][4 * (i & 1)], z1 = f[i >> 1][4 * (i & 1) + 2];
        asm("" : "+s"(z0), "+s"(z1));
        wt[i] = __uint_as_float(lane < 32 ? z0 : z1); M = fmaxf(M, wt[i]);
      }
      M = xhalf_max(M);
#pragma unroll
      for (int i = 0; i < 8; ++i) { wt[i] = ex2(wt[i] - M); L += wt[i]; }
      L = xhalf_sum(L);
      if (lane == 0) *reinterpret_cast<f32x2*>(wml + 2 * w4) = f32x2{M, L};
    }
    SCHED();
    float a8[8];
#pragma unroll
    for (int k = 0; k < 8; ++k) a8[k] = 0.f;
#pragma unroll
    for (int i = 0; i < 8; ++i) {
#pragma unroll
      for (int k = 0; k < 4; ++k) { a8[2 * k] = fmaf(wt[i], bflo(pp[i][k]), a8[2 * k]); a8[2 * k + 1] = fmaf(wt[i], bfhi(pp[i][k]), a8[2 * k + 1]); }
      asm volatile("" : "+v"(a8[0]), "+v"(a8[1]), "+v"(a8[2]), "+v"(a8[3]), "+v"(a8[4]), "+v"(a8[5]), "+v"(a8[6]), "+v"(a8[7]));
    }
    STAMP(6);
    *reinterpret_cast<f32x4*>(red + w4 * 256 + 8 * (lane & 31) + 4 * (lane >> 5)) = f32x4{rs32(a8[0], a8[4]), rs32(a8[1], a8[5]), rs32(a8[2], a8[6]), rs32(a8[3], a8[7])};
    sbar4(cnt + 3, lane);
    STAMP(4);
    float acc[2][8];
#pragma unroll
    for (int bb = 0; bb < 2; ++bb) {
      u32x4 cv[2];
      const f32x4 ml = *reinterpret_cast<const f32x4*>(wml + 4 * bb);
      const float M = fmaxf(ml[0], ml[2]), e0 = ex2(ml[0] - M), e1 = ex2(ml[2] - M), rl = 1.f / fmaf(e0, ml[1], e1 * ml[3]), s0 = e0 * rl, s1 = e1 * rl;
#pragma unroll
      for (int e = 0; e < 2; ++e) {
        const float* src = red + bb * 512 + w4 * 64 + 16 * r4 + 8 * e;
        const f32x4 t0 = s0 * *reinterpret_cast<const f32x4*>(src) + s1 * *reinterpret_cast<const f32x4*>(src + 256), t1 = s0 * *reinterpret_cast<const f32x4*>(src + 4) + s1 * *reinterpret_cast<const f32x4*>(src + 260);
        cv[e] = u32x4{pk_bf(t0[0], t0[1]), pk_bf(t0[2], t0[3]), pk_bf(t1[0], t1[1]), pk_bf(t1[2], t1[3])};
      }
#pragma unroll
      for (int k = 0; k < 8; ++k) acc[bb][k] = 0.f;
#pragma unroll
      for (int ip = 0; ip < 8; ++ip)
#pragma unroll
        for (int k = 0; k < 4; ++k) { acc[bb][2 * k] = dot2(Wb[2 * ip][k], cv[ip >> 2][ip & 3], acc[bb][2 * k]); acc[bb][2 * k + 1] = dot2(Wb[2 * ip + 1][k], cv[ip >> 2][ip & 3], acc[bb][2 * k + 1]); }
    }
    {
      float v8[8], v4[4];
#pragma unroll
      for (int k = 0; k < 8; ++k) v8[k] = rs32(acc[0][k], acc[1][k]);
#pragma unroll
      for (int k = 0; k < 4; ++k) v4[k] = rs16(v8[k], v8[k + 4]);
      *reinterpret_cast<f32x4*>(red2 + (w4 * 2 + (lane >> 5)) * 128 + 8 * c16 + 4 * (r4 & 1)) = f32x4{v4[0], v4[1], v4[2], v4[3]};
    }
    {
      COMPILER_FENCE();
      const unsigned got = lds_arrive_nw(L_CNT + 16);
      f32x4 t4[4];
#pragma unroll
      for (int w = 0; w < 4; ++w) t4[w] = *reinterpret_cast<const f32x4*>(red2 + w * 256 + 4 * lane);
      SCHED();
      if (lds_done(got) == 3u) {
        const f32x4 t = (t4[0] + t4[1]) + (t4[2] + t4[3]);
        const unsigned co = WS_CX + ((h * 2 + b) * 256 + 64 * r4 + 32 * (c16 & 1) + 2 * (c16 >> 1)) * 8;
        st16c(rs, co, u32x4{__float_as_uint(t[0]), tag, __float_as_uint(t[1]), tag});
        st16c(rs, co + 128, u32x4{__float_as_uint(t[2]), tag, __float_as_uint(t[3]), tag});
      }
    }
    STAMP(12);
    STAMP_FLUSH(0); TR_FLUSH();
    return;
  }
  {
    unsigned op;
    {
      u32x4 c;
      const unsigned co = WS_CX + ((h * 2 + ((lane >> 3) & 1)) * 256 + (lane >> 5) * 128 + b * 64 + w4 * 16 + 2 * (lane & 7)) * 8;
      if (idx == 0 && w4 == 1) {
#pragma unroll 1
        for (int spin = 0; spin < SPIN_MAX; ++spin) {
          COMPILER_FENCE();
          if (__all(ld8c(rs, WS_QX + 65536 + (lane >> 1) * 2048 + (lane & 1) * 512)[1] == tag)) break;
          __builtin_amdgcn_s_sleep(8);
        }
      }
      until<6>(tstart + PAR(13, TW) + PAR(29, 370));
      if (w4 == 0) { shint<2048, 0>(ws + WS_CX + (h * 512 + b * 64) * 8, tag); lds_add(cnt + 7, 1u, lane); }
      else lds_wait(cnt + 7, 1u);
      STAMP(3);
#pragma unroll 1
      for (int spin = 0; spin < SPIN_MAX; ++spin) {
        COMPILER_FENCE();
        c = ld16c(rs, co);
        if (__all((c[1] == tag) & (c[3] == tag))) break;
        __builtin_amdgcn_s_sleep(1);
      }
      float o0 = __uint_as_float(c[0]), o1 = __uint_as_float(c[2]);
      o0 += dpp<0x128>(o0); o1 += dpp<0x128>(o1);
      op = pk_bf(o0, o1);
    }
    STAMP(14);
    float* red3 = red + 4096;
    float a[2][8];
#pragma unroll
    for (int bb = 0; bb < 2; ++bb)
#pragma unroll
      for (int k = 0; k < 8; ++k) a[bb][k] = 0.f;
#pragma unroll
    for (int ip = 0; ip < 8; ++ip) {
      const unsigned v0 = __builtin_amdgcn_readlane(op, ip), v1 = __builtin_amdgcn_readlane(op, 32 + ip);
#pragma unroll
      for (int k = 0; k < 4; ++k) {
        a[0][2 * k] = dot2(Wb[2 * ip][k], v0, a[0][2 * k]); a[0][2 * k + 1] = dot2(Wb[2 * ip + 1][k], v0, a[0][2 * k + 1]);
        a[1][2 * k] = dot2(Wb[2 * ip][k], v1, a[1][2 * k]); a[1][2 * k + 1] = dot2(Wb[2 * ip + 1][k], v1, a[1][2 * k + 1]);
      }
    }
#pragma unroll
    for (int bb = 0; bb < 2; ++bb) {
      *reinterpret_cast<f32x4*>(red3 + (w4 * 2 + bb) * 512 + 8 * lane) = f32x4{a[bb][0], a[bb][1], a[bb][2], a[bb][3]};
      *reinterpret_cast<f32x4*>(red3 + (w4 * 2 + bb) * 512 + 8 * lane + 4) = f32x4{a[bb][4], a[bb][5], a[bb][6], a[bb][7]};
    }
    sbar4(cnt + 5, lane);
    f32x4 t = {0.f, 0.f, 0.f, 0.f};
#pragma unroll
    for (int w = 0; w < 4; ++w) t += *reinterpret_cast<const f32x4*>(red3 + w * 1024 + 4 * tid);
    st16c(rs, WS_GA + ((tid >> 7) * 64 + idx) * 2048 + (tid & 127) * 16, u32x4{pk_bf(t[0], t[1]), tag, pk_bf(t[2], t[3]), tag});
    STAMP(5);
  }
  fin(IC<1>{}, WS_GA, 0, idx == 0);
  STAMP(6);
  STAMP_FLUSH(0); TR_FLUSH();
}

extern "C" void kernel_launch(void* const* d_in, const int* in_sizes, int n_in, void* d_out, int out_size, void* d_ws, size_t ws_size, hipStream_t stream) {
  const u16* xA = (const u16*)d_in[0];
  const u16* ckv = (const u16*)d_in[1];
  const u16* w_q = (const u16*)d_in[2];
  const u16* w_uk = (const u16*)d_in[3];
  const u16* w_uv = (const u16*)d_in[4];
  const u16* w_o = (const u16*)d_in[5];
  const u16* xB = (const u16*)d_in[6];
  const u16* kc = (const u16*)d_in[7];
  const u16* vc = (const u16*)d_in[8];
  const u16* wq = (const u16*)d_in[9];
  const u16* wo = (const u16*)d_in[10];
  kmain<<<256, 512, 0, stream>>>(ckv, w_q, w_uk, wq, xA, xB, w_uv, w_o, kc, vc, wo, (char*)d_ws, (u16*)d_out);
}
```

```cpp
#include <hip/hip_runtime.h>
#include <hip/hip_bf16.h>
#include <stdint.h>

#define DI __device__ __forceinline__
typedef __attribute__((ext_vector_type(4))) unsigned u32x4;
typedef __attribute__((ext_vector_type(2))) unsigned u32x2;
typedef __attribute__((ext_vector_type(4))) float f32x4;
typedef __attribute__((ext_vector_type(2))) float f32x2;
typedef __attribute__((ext_vector_type(16))) float f32x16;
typedef __attribute__((ext_vector_type(8))) __bf16 bf16x8_t;
typedef __attribute__((ext_vector_type(2))) __bf16 bf16x2_t;
typedef __attribute__((ext_vector_type(4))) short s16x4;
typedef __attribute__((ext_vector_type(8))) short s16x8;
typedef unsigned short u16;
typedef unsigned long long u64;

constexpr int NS0 = 13, NS1 = 17, NS2 = 16, NS3 = 18;
constexpr u64 TL0 = 0xc2b8de6543cb1c1ull, TH0 = 0x3de34ull, TL1 = 0x920718551349142ull, TH1 = 0x3eeb6caeaa7ull;
constexpr u64 TL2 = 0x96175a59238a183ull, TH2 = 0xff9d71b28ull, TL3 = 0x89f6d74d0308100ull, TH3 = 0xf3bdf3beda66ull;
constexpr int RA2 = 0xC0, RA3 = 3;
constexpr int AHL = 2;
constexpr int QPE = 7;
constexpr int WBW = 5;
#define TL 2350
#define TW 1650
constexpr unsigned TS0 = 1160, TS1 = 1420, TS2 = 1320, TS3 = 1420, STG = 12, BSG = 6, HL = 40, TP0 = TW + 170, TP2 = TW + 380, PA = 150, TF = 180, T0B = 420, T0Q = 880;
#define PAR(i, v) (v)
constexpr u64 LA2 = TH2 >> (6 * (NS2 - 11)), LA3 = TH3 >> (6 * (NS3 - 11));
constexpr u64 chunk_mask(u64 lo, u64 hi, int n) { u64 m = 0; for (int i = 0; i < n; ++i) m |= 1ull << ((i < 10 ? lo >> (6 * i) : hi >> (6 * (i - 10))) & 63); return m; }
static_assert((chunk_mask(TL0, TH0, NS0) | chunk_mask(TL1, TH1, NS1) | chunk_mask(TL2, TH2, NS2) | chunk_mask(TL3, TH3, NS3)) == ~0ull && (RA2 & RA3) == 0, "every chunk once");
static_assert(NS0 + NS1 + NS2 + NS3 == 64, "split sizes");
constexpr int NSMIN = 13, NSMAX = 18;
static_assert(NS0 >= NSMIN && NS1 >= NSMIN && NS2 - 1 >= NSMIN && NS3 - 1 >= NSMIN && NS0 <= NSMAX && NS1 + 1 <= NSMAX && NS2 <= NSMAX && NS3 <= NSMAX, "range");
constexpr int SK = 4096;
constexpr float C2 = 0.08838834764831845f * 1.4426950408889634f;
constexpr u64 GOLD = 0x9E3779B97F4A7C15ull;

constexpr unsigned WS_EP = 0;
constexpr unsigned WS_QX = 4096;
constexpr unsigned WS_QA = WS_QX + 2 * 32 * 256 * 8;
constexpr unsigned WS_AML = WS_QA + 2 * 32 * 512 * 2;
constexpr unsigned WS_CX = WS_AML + 32 * 2 * 16 * 128;
constexpr unsigned WS_GZ = WS_CX + 32 * 2 * 2 * 128 * 8;
constexpr unsigned WS_T0 = WS_GZ + 256 * 128;
constexpr unsigned WS_GB = WS_T0 + 64 * 128;
constexpr unsigned WS_GA = WS_GB + 2 * 128 * 256 * 8;
constexpr unsigned WS_APO = 2u << 20;
static_assert(WS_GA + 64 * 2 * 256 * 8 <= WS_APO, "layout");
constexpr unsigned WS_END = WS_APO + 32 * 2 * 64 * 256 * 2;
#define STAMP(i) do {} while (0)
#define STAMP_DECL do {} while (0)
#define STAMP_FLUSH(grp) do {} while (0)

#define TR() do {} while (0)
#define TR_FLUSH() do {} while (0)
DI float bflo(unsigned u) { return __uint_as_float(u << 16); }
DI float bfhi(unsigned u) { return __uint_as_float(u & 0xffff0000u); }
DI unsigned pk_bf(float a, float b) { const f32x2 v = {a, b}; return __builtin_bit_cast(unsigned, __builtin_convertvector(v, bf16x2_t)); }
DI float dot2(unsigned k, unsigned q, float acc) {
  return __builtin_amdgcn_fdot2_f32_bf16(__builtin_bit_cast(bf16x2_t, k), __builtin_bit_cast(bf16x2_t, q), acc, false);
}
DI unsigned plo(unsigned w0, unsigned w1) { return __builtin_amdgcn_perm(w1, w0, 0x05040100u); }
DI unsigned phi(unsigned w0, unsigned w1) { return __builtin_amdgcn_perm(w1, w0, 0x07060302u); }
template <int CTRL> DI float dpp(float x) {
  return __builtin_bit_cast(float, __builtin_amdgcn_mov_dpp(__builtin_bit_cast(int, x), CTRL, 0xf, 0xf, true));
}
DI float row16_sum(float x) { x += dpp<0xB1>(x); x += dpp<0x4E>(x); x += dpp<0x141>(x); x += dpp<0x128>(x); return x; }
DI float row16_max(float x) { x = fmaxf(x, dpp<0xB1>(x)); x = fmaxf(x, dpp<0x4E>(x)); x = fmaxf(x, dpp<0x141>(x)); x = fmaxf(x, dpp<0x128>(x)); return x; }
DI float xhalf_max(float x) { auto t = __builtin_amdgcn_permlane32_swap(__float_as_uint(x), __float_as_uint(x), false, false); return fmaxf(__uint_as_float(t[0]), __uint_as_float(t[1])); }
DI float xhalf_sum(float x) { auto t = __builtin_amdgcn_permlane32_swap(__float_as_uint(x), __float_as_uint(x), false, false); return __uint_as_float(t[0]) + __uint_as_float(t[1]); }
DI u32x4 ld16(const void* p) { return *reinterpret_cast<const u32x4*>(p); }
DI u32x4 ld16nt(const void* p) { return __builtin_nontemporal_load(reinterpret_cast<const u32x4*>(p)); }
DI float ex2(float x) { return __builtin_amdgcn_exp2f(x); }
#define SCHED() __builtin_amdgcn_sched_barrier(0)

typedef __amdgpu_buffer_rsrc_t rsrc_t;
DI u32x4 ld16c(rsrc_t rs, unsigned off) { return __builtin_amdgcn_raw_buffer_load_b128(rs, (int)off, 0, 16); }
DI u32x2 ld8c(rsrc_t rs, unsigned off) { return __builtin_amdgcn_raw_buffer_load_b64(rs, (int)off, 0, 16); }
DI void st16c(rsrc_t rs, unsigned off, u32x4 v) { __builtin_amdgcn_raw_buffer_store_b128(v, rs, (int)off, 0, 16); }
DI void st8c(rsrc_t rs, unsigned off, u32x2 v) { __builtin_amdgcn_raw_buffer_store_b64(v, rs, (int)off, 0, 16); }
template <int O1> DI void sld2(const char* p, u32x2& a, u32x2& b) {
  asm volatile("s_load_dwordx2 %0, %2, 0x0 glc\n\ts_load_dwordx2 %1, %2, %3 glc\n\ts_waitcnt lgkmcnt(0)" : "=&s"(a), "=&s"(b) : "s"(p), "n"(O1) : "memory");
}
typedef __attribute__((ext_vector_type(8))) unsigned u32x8;
DI void sld4x32(const char* p, u32x8 (&v)[4]) {
  asm volatile("s_load_dwordx8 %0, %4, 0x0 glc\n\ts_load_dwordx8 %1, %4, 0x80 glc\n\ts_load_dwordx8 %2, %4, 0x100 glc\n\ts_load_dwordx8 %3, %4, 0x180 glc\n\ts_waitcnt lgkmcnt(0)"
               : "=&s"(v[0]), "=&s"(v[1]), "=&s"(v[2]), "=&s"(v[3]) : "s"(p) : "memory");
}
constexpr int SPIN_MAX = 1 << 20;
constexpr unsigned OOB = 0x7ffffff0u;
DI void rawbar() { __builtin_amdgcn_sched_barrier(0); __builtin_amdgcn_s_barrier(); __builtin_amdgcn_sched_barrier(0); }
#define COMPILER_FENCE() asm volatile("" ::: "memory")
template <int O1, int SL> DI void shint(const char* p, unsigned tag) {
#pragma unroll 1
  for (int spin = 0; spin < SPIN_MAX; ++spin) {
    u32x2 a, b;
    sld2<O1>(p, a, b);
    if (((a[1] ^ tag) | (b[1] ^ tag)) == 0u) break;
    __builtin_amdgcn_s_sleep(SL);
  }
}

template <int N> struct IC { static constexpr int v = N; };
template <int I, int N, class F> DI void sfor(F&& f) { if constexpr (I < N) { f(IC<I>{}); sfor<I + 1, N>(f); } }
typedef __attribute__((address_space(3))) unsigned lu32;
DI void lds_wait(unsigned* cnt, unsigned target) {
#pragma unroll 1
  for (int spin = 0; spin < SPIN_MAX; ++spin) {
    if (__builtin_amdgcn_readfirstlane(__hip_atomic_load((lu32*)cnt, __ATOMIC_RELAXED, __HIP_MEMORY_SCOPE_WORKGROUP)) >= target) break;
    __builtin_amdgcn_s_sleep(1);
  }
  COMPILER_FENCE();
}
DI void lds_wait_slow(unsigned* cnt, unsigned target) {
#pragma unroll 1
  for (int spin = 0; spin < SPIN_MAX; ++spin) {
    if (__builtin_amdgcn_readfirstlane(__hip_atomic_load((lu32*)cnt, __ATOMIC_RELAXED, __HIP_MEMORY_SCOPE_WORKGROUP)) >= target) break;
    __builtin_amdgcn_s_sleep(3);
  }
  COMPILER_FENCE();
}
template <int SL = 1> DI void until(unsigned t) {
#pragma unroll 1
  for (int spin = 0; spin < 1024; ++spin) { if ((int)((unsigned)wall_clock64() - t) >= 0) break; __builtin_amdgcn_s_sleep(SL); }
}
DI void lds_hold(unsigned* flag) {
#pragma unroll 1
  for (int spin = 0; spin < SPIN_MAX; ++spin) {
    if (__builtin_amdgcn_readfirstlane(__hip_atomic_load((lu32*)flag, __ATOMIC_RELAXED, __HIP_MEMORY_SCOPE_WORKGROUP)) == 0u) break;
    __builtin_amdgcn_s_sleep(2);
  }
}
DI unsigned lds_arrive_nw(unsigned addr) {
  unsigned old, one = 1u;
  asm volatile("s_mov_b64 exec, 1\n\tds_add_rtn_u32 %0, %1, %2\n\ts_mov_b64 exec, -1" : "=v"(old) : "v"(addr), "v"(one) : "memory");
  return old;
}
DI unsigned lds_done(unsigned old) { asm volatile("s_waitcnt lgkmcnt(0)" : "+v"(old) :: "memory"); return __builtin_amdgcn_readfirstlane(old); }
DI unsigned lds_arrive(unsigned* cnt, int) { return lds_done(lds_arrive_nw((unsigned)(size_t)(lu32*)cnt)); }
DI void lds_add(unsigned* cnt, unsigned v, int) {
  asm volatile("s_mov_b64 exec, 1\n\tds_add_u32 %0, %1\n\ts_mov_b64 exec, -1" :: "v"((unsigned)(size_t)(lu32*)cnt), "v"(v) : "memory");
}
DI void sbar4(unsigned* cnt, int lane) { if (lds_arrive(cnt, lane) < 3u) lds_wait(cnt, 4u); }
DI float rs32(float a, float b) { auto t = __builtin_amdgcn_permlane32_swap(__float_as_uint(a), __float_as_uint(b), false, false); return __uint_as_float(t[0]) + __uint_as_float(t[1]); }
DI float rs16(float a, float b) { auto t = __builtin_amdgcn_permlane16_swap(__float_as_uint(a), __float_as_uint(b), false, false); return __uint_as_float(t[0]) + __uint_as_float(t[1]); }
template <int N> DI unsigned rowbcast(unsigned x) { return (unsigned)__builtin_amdgcn_mov_dpp((int)x, 0x150 + N, 0xf, 0xf, true); }
DI void pin2(u32x4& a, u32x4& b) { asm volatile("" : "+v"(a), "+v"(b)); }
DI void use2(const u32x4& a, const u32x4& b) { asm volatile("" :: "v"(a), "v"(b)); }
DI bool nz8(const u32x4& v) { return ((v[0] | v[1]) != 0u) & ((v[2] | v[3]) != 0u); }

DI u32x4 qsum(const u32x4& g) {
  const auto ta = __builtin_amdgcn_permlane32_swap(g[0], g[0], false, false), tc = __builtin_amdgcn_permlane32_swap(g[2], g[2], false, false);
  const unsigned a = pk_bf(bflo(ta[0]) + bflo(ta[1]), bfhi(ta[0]) + bfhi(ta[1])), c = pk_bf(bflo(tc[0]) + bflo(tc[1]), bfhi(tc[0]) + bfhi(tc[1]));
  const auto ra = __builtin_amdgcn_permlane16_swap(a, a, false, false), rc = __builtin_amdgcn_permlane16_swap(c, c, false, false);
  return u32x4{ra[0], rc[0], ra[1], rc[1]};
}
DI unsigned toff(unsigned row, unsigned ch) { return 256u * row + 16u * (ch ^ (((row & 3) << 2) | ((row >> 2) & 3))); }
constexpr int SMEM = 160 * 1024;
constexpr int L_SCR = 131072;
constexpr int L_MRG = 65536;
constexpr int L_CNT = L_SCR + 32 * 1008;

struct KV { u32x4 r[8]; };
constexpr int CODE_BYTES = 43008;

template <class T> DI T* gptr(unsigned lo, unsigned hi) { return (T*)(__attribute__((address_space(1))) T*)(((u64)hi << 32) | lo); }

__global__ __launch_bounds__(512, 2) void kmain(const u16*, const u16*, const u16*, const u16*, const u16*, const u16*, const u16*, const u16*, const u16*,
                                                const u16*, const u16*, char*, u16*) {
  u32x2 kpa, kpx, kc2; u32x4 kb4; u32x8 kb8; u64 t64;
  int tid = threadIdx.x, blk = blockIdx.x;
  const auto kp = __builtin_amdgcn_kernarg_segment_ptr();
  const int o1 = blk & 24, o2 = 32 + ((blk >> 1) & 8);
  asm volatile("s_load_dwordx2 %0, %8, %9\n\ts_load_dwordx2 %1, %8, %10\n\ts_load_dwordx4 %2, %8, 0x30\n\ts_load_dwordx8 %3, %8, 0x40\n\ts_load_dwordx2 %4, %8, 0x60\n\ts_memrealtime %5"
               : "=&s"(kpa), "=&s"(kpx), "=&s"(kb4), "=&s"(kb8), "=&s"(kc2), "=&s"(t64), "+v"(tid), "+s"(blk) : "s"(kp), "s"(o1), "s"(o2));
  __builtin_assume(tid >= 0 && tid < 512 && blk >= 0 && blk < 256);
  const unsigned cpf = __builtin_amdgcn_raw_buffer_load_b32(__builtin_amdgcn_make_buffer_rsrc((void*)(__builtin_amdgcn_s_getpc() & ~255ull), 0, CODE_BYTES, 0x00020000),
                                                            (int)((tid & 0x10f) == 0x100 ? (blk >> 3) * 2048 + (tid & 0xf0) * 8 : OOB), 0, 0);
  COMPILER_FENCE();
  SCHED();
  __shared__ __attribute__((aligned(16))) char smem[SMEM];
  const int lane = tid & 63;
  const int wave = __builtin_amdgcn_readfirstlane(tid >> 6), w4 = wave & 3;
  const int cls = (blk >> 3) & 3;
  int idx = 8 * (blk & 7) + (blk >> 5);
  idx = (idx & ~7) | ((idx + ((wave >= 4 ? 0 : 0x2400) >> (4 * cls))) & 7);
  const int h = idx >> 1, b = idx & 1;
  const int r4 = lane >> 4, c16 = lane & 15;
  unsigned* cnt = reinterpret_cast<unsigned*>(smem + L_CNT);
  STAMP_DECL;
  STAMP(0);
  const unsigned c0 = cls ? 0u : ~0u;
  unsigned sa = cls ? 8192u : 4096u;
  unsigned ao = ((((b * 256 + w4 * 64) * 8192 + h * 256) & ~c0) | (((idx * 128 + w4) * 1024) & c0)) + (r4 << (17u - (c0 & 9u))) + c16 * 16, xo = (b * 256 + w4 * 64 + 16 * r4) * 2;
  asm volatile("" : "+v"(ao), "+v"(xo), "+s"(sa));
  SCHED();
  asm volatile("s_waitcnt lgkmcnt(0)" : "+s"(kpa), "+s"(kpx), "+s"(kb4), "+s"(kb8), "+s"(kc2), "+s"(t64) : "s"(kp), "s"(o1), "s"(o2));
  const unsigned tstart = (unsigned)t64;
  const char* __restrict__ pa = gptr<const char>(kpa[0], kpa[1]); const char* __restrict__ xp = gptr<const char>(kpx[0], kpx[1]);
  const u16* __restrict__ w_uv = gptr<const u16>(kb4[0], kb4[1]); const u16* __restrict__ w_o = gptr<const u16>(kb4[2], kb4[3]);
  const u16* __restrict__ kc = gptr<const u16>(kb8[0], kb8[1]); const u16* __restrict__ vc = gptr<const u16>(kb8[2], kb8[3]);
  const u16* __restrict__ wo = gptr<const u16>(kb8[4], kb8[5]); char* __restrict__ ws = gptr<char>(kb8[6], kb8[7]);
  u16* __restrict__ out = gptr<u16>(kc2[0], kc2[1]);
  const rsrc_t rs = __builtin_amdgcn_make_buffer_rsrc(ws, 0, (int)WS_END, 0x00020000);
  unsigned tag, tlow;
  u32x2 epw;
  auto take_epoch = [&]() {
    asm volatile("" : "+v"(epw) : "v"(cpf));
    const u64 t64 = (((u64)(unsigned)__builtin_amdgcn_readfirstlane(epw[1]) << 32) | (unsigned)__builtin_amdgcn_readfirstlane(epw[0])) + GOLD;
    tag = (unsigned)(t64 >> 32); tlow = (unsigned)t64;
    asm volatile("s_waitcnt lgkmcnt(0)" : "+s"(tag), "+s"(tlow) :: "memory");
  };
  if (wave >= 4) {
    if (tid - 256 < 16) cnt[tid - 256] = 0u;
    asm volatile("s_waitcnt lgkmcnt(0)" ::: "memory");
    const unsigned pTL = PAR(0, TL), pTS = PAR(1 + cls, cls == 0 ? TS0 : (cls == 1 ? TS1 : (cls == 2 ? TS2 : TS3))), pSTG = PAR(9, STG), pBSG = PAR(10, BSG);
    const unsigned pRA2 = PAR(11, RA2), pRA3 = PAR(12, RA3), pTP = PAR(15 + cls, cls == 0 ? TP0 : TP2), pPA = PAR(19 + cls, (cls & 1) ? 0u : PA);
    rawbar();
    const int e2 = (pRA2 >> (idx & 7)) & 1, e3 = (pRA3 >> (idx & 7)) & 1;
    const int ns = cls == 0 ? NS0 : (cls == 1 ? NS1 + e2 + e3 : (cls == 2 ? NS2 - e2 : NS3 - e3));
    const unsigned pTA = __builtin_amdgcn_readfirstlane((unsigned)((float)pTS * __builtin_amdgcn_rcpf((float)(ns - 5)) + 0.5f));
    const size_t kvo = ((size_t)(b * 32 + h) * SK + w4 * 16) * 256;
    const char* kb = reinterpret_cast<const char*>(kc) + kvo;
    const char* vb = reinterpret_cast<const char*>(vc) + kvo;
    const unsigned lo = lane * 16u;
    u64 tlo = cls == 0 ? TL0 : (cls == 1 ? TL1 : (cls == 2 ? TL2 : TL3));
    u64 thi = cls == 0 ? TH0 : (cls == 1 ? TH1 | ((e2 ? LA2 : LA3) << (6 * (NS1 - 10))) : (cls == 2 ? TH2 : TH3));
    const u64 tlo0 = tlo, thi0 = thi;
    int hn = 0;
    const char* hb = kb;
    const char* pc[2];
    auto next2 = [&]() {
#pragma unroll
      for (int i = 0; i < 2; ++i) {
        if (hn == ns) { tlo = tlo0; thi = thi0; hb = vb; }
        ++hn;
        pc[i] = hb + ((((unsigned)tlo + (unsigned)idx) & 63u) << 14);
        tlo = (tlo >> 6) | ((thi & 63u) << 54); thi >>= 6;
      }
    };
    KV A, B;
    u32x4 W[8];
    u32x4 T[4][8];
    auto ldp = [&](KV& S, auto P) {
      constexpr int p = P.v;
      const char* q = pc[p >> 1] + lo + (p & 1) * 2048;
      S.r[2 * p] = ld16nt(q); S.r[2 * p + 1] = ld16nt(q + 1024);
    };
    auto landed = [&](KV& S, auto P) { pin2(S.r[2 * P.v], S.r[2 * P.v + 1]); };
    KV X;
    const unsigned qo = WS_QX + ((32 + h) * 2 + b) * 1024 + lane * 16;
    u32x4 g0;
    {
      unsigned tk = 0u;
      if (w4 == 0) {
        const int si = (idx & ~7) | ((idx + (0x2400 >> (4 * cls))) & 7);
        const char* p = kb + cls * 128;
        if (lane == 1) p = cls == 2 ? reinterpret_cast<const char*>(w_uv) + (si & 1) * 2097152 + (si >> 1) * 256 : reinterpret_cast<const char*>(cls == 1 ? w_o : wo) + si * 65536;
        if (lane == 2) p = vb + cls * 128;
        if (lane == 3) p = reinterpret_cast<const char*>(wo) + h * 131072;
        if (lane < 4) tk = __builtin_nontemporal_load(reinterpret_cast<const unsigned*>(p));
      }
      lds_wait_slow(cnt + 14, 4u);
      tag = __builtin_amdgcn_readfirstlane(cnt[16]);
      asm volatile("" :: "v"(tk), "v"(cpf));
    }
    STAMP(10);
    bool noorg = w4 == 3;
    auto origin = [&]() {
#pragma unroll 1
      for (int spin = 0; spin < 2 && noorg; ++spin) {
        u32x2 t[8];
        asm volatile("s_load_dwordx2 %0, %8, 0x0 glc\n\ts_load_dwordx2 %1, %8, 0x400 glc\n\ts_load_dwordx2 %2, %8, 0x800 glc\n\ts_load_dwordx2 %3, %8, 0xc00 glc\n\t"
                     "s_load_dwordx2 %4, %8, 0x1000 glc\n\ts_load_dwordx2 %5, %8, 0x1400 glc\n\ts_load_dwordx2 %6, %8, 0x1800 glc\n\ts_load_dwordx2 %7, %8, 0x1c00 glc\n\ts_waitcnt lgkmcnt(0)"
                     : "=&s"(t[0]), "=&s"(t[1]), "=&s"(t[2]), "=&s"(t[3]), "=&s"(t[4]), "=&s"(t[5]), "=&s"(t[6]), "=&s"(t[7]) : "s"(ws + WS_T0 + ((blk >> 3) & 7) * 128) : "memory");
        unsigned e = 0u; int sum = 0;
#pragma unroll
        for (int k = 0; k < 8; ++k) { e |= t[k][1] ^ tag; sum += (int)(t[k][0] - tstart); }
        if (e == 0u) { lds_add(cnt + 13, (unsigned)min(sum >> 3, 100), lane); noorg = false; }
      }
    };
    sfor<0, 12>([&](auto I) {
      constexpr int i = I.v, j = i - 4;
      if constexpr ((i & 3) == 0) next2();
      if constexpr (i == 4) origin();
      if constexpr (j >= 0) { if constexpr (j < 4) landed(X, IC<j & 3>{}); else landed(B, IC<j & 3>{}); }
      if constexpr (j >= 0 && (j & 1)) TR();
      if constexpr (i == QPE) g0 = ld16c(rs, qo);
      if constexpr (i < 4) ldp(X, IC<i & 3>{}); else if constexpr (i < 8) ldp(B, IC<i & 3>{}); else ldp(A, IC<i & 3>{});
      SCHED();
    });
    origin();
    const char* wp = reinterpret_cast<const char*>(wo) + ((size_t)h * 128 + r4 * 8) * 1024 + w4 * 256 + c16 * 16;
    u32x4 qpk;
    {
      if (!__all((g0[1] == tag) & (g0[3] == tag))) {
#pragma unroll 1
        for (int spin = 0; spin < SPIN_MAX; ++spin) {
          COMPILER_FENCE();
          g0 = ld16c(rs, qo);
          if (__all((g0[1] == tag) & (g0[3] == tag))) break;
        }
      }
      qpk = qsum(g0);
    }
    STAMP(7);
    const bool holds = cls == 0;
    float m = 0.f, l = 0.f, acc[8];
    unsigned sc[5];
#pragma unroll
    for (int k = 0; k < 8; ++k) acc[k] = 0.f;
#pragma unroll
    for (int k = 0; k < 5; ++k) sc[k] = 0xff800000u;
    int vn = 0;
    unsigned torg, due = pTL - (unsigned)(ns - 5) * pTA - (3 - w4) * pSTG - ((idx * 5 + cls * 3) & 15) * pBSG;
    unsigned rwo;
    const unsigned zo = WS_GZ + (b * 32 + h) * 512;
    u32x2 zs[4];
    float wr_;
    auto zreq = [&]() {
      asm volatile("s_load_dwordx2 %0, %4, 0x0 glc\n\ts_load_dwordx2 %1, %4, 0x80 glc\n\ts_load_dwordx2 %2, %4, 0x100 glc\n\ts_load_dwordx2 %3, %4, 0x180 glc"
                   : "=&s"(zs[0]), "=&s"(zs[1]), "=&s"(zs[2]), "=&s"(zs[3]) : "s"(ws + zo) : "memory");
    };
    auto zget = [&]() {
#pragma unroll 1
      for (int spin = 0; spin < SPIN_MAX; ++spin) {
        asm volatile("s_waitcnt lgkmcnt(0)" : "+s"(zs[0]), "+s"(zs[1]), "+s"(zs[2]), "+s"(zs[3]) :: "memory");
        if (((zs[0][1] ^ tag) | (zs[1][1] ^ tag) | (zs[2][1] ^ tag) | (zs[3][1] ^ tag)) == 0u) break;
        __builtin_amdgcn_s_sleep(2);
        zreq();
      }
      asm volatile("s_waitcnt lgkmcnt(0)" : "+s"(zs[0]), "+s"(zs[1]), "+s"(zs[2]), "+s"(zs[3]) :: "memory");
      const float z0 = __uint_as_float(zs[0][0]), z1 = __uint_as_float(zs[1][0]), z2 = __uint_as_float(zs[2][0]), z3 = __uint_as_float(zs[3][0]);
      const float Z = fmaxf(fmaxf(z0, z1), fmaxf(z2, z3));
      wr_ = ex2(fmaf(m, C2, -Z)) * __builtin_amdgcn_rcpf((ex2(z0 - Z) + ex2(z1 - Z)) + (ex2(z2 - Z) + ex2(z3 - Z)));
      asm volatile("" : "+v"(wr_));
    };
    auto qk = [&](const u32x4* K, auto I) {
      float x[4];
#pragma unroll
      for (int t = 0; t < 4; ++t) {
        x[t] = 0.f;
#pragma unroll
        for (int k = 0; k < 4; ++k) x[t] = dot2(K[t][k], qpk[k], x[t]);
      }
      const float u0 = (c16 & 1 ? x[1] : x[0]) + dpp<0xB1>(c16 & 1 ? x[0] : x[1]), u1 = (c16 & 1 ? x[3] : x[2]) + dpp<0xB1>(c16 & 1 ? x[2] : x[3]);
      float v = (c16 & 2 ? u1 : u0) + dpp<0x4E>(c16 & 2 ? u0 : u1);
      v += dpp<0x124>(v); v += dpp<0x128>(v);
      sc[I.v >> 2] = __builtin_amdgcn_update_dpp(sc[I.v >> 2], __float_as_uint(v), 0xE4, 0xf, 1 << (I.v & 3), false);
      asm volatile("" : "+v"(sc[I.v >> 2]));
    };
    auto soft = [&]() {
      m = row16_max(fmaxf(fmaxf(fmaxf(__uint_as_float(sc[0]), __uint_as_float(sc[1])), fmaxf(__uint_as_float(sc[2]), __uint_as_float(sc[3]))), __uint_as_float(sc[4])));
      const float mc = m * C2;
      l = 0.f;
#pragma unroll
      for (int k = 0; k < 5; ++k) {
        const float p = ex2(fmaf(__uint_as_float(sc[k]), C2, -mc));
        sc[k] = pk_bf(p, dpp<0xB1>(p));
        l = dot2(sc[k], 0x3f003f00u, l);
      }
      l = row16_sum(l);
    };
    auto pv = [&](const u32x4* V) {
      const unsigned p01 = rowbcast<0>(sc[0]), p23 = rowbcast<2>(sc[0]);
#pragma unroll
      for (int k = 0; k < 4; ++k) { acc[2 * k] = dot2(plo(V[0][k], V[1][k]), p01, acc[2 * k]); acc[2 * k + 1] = dot2(phi(V[0][k], V[1][k]), p01, acc[2 * k + 1]); }
#pragma unroll
      for (int k = 0; k < 4; ++k) { acc[2 * k] = dot2(plo(V[2][k], V[3][k]), p23, acc[2 * k]); acc[2 * k + 1] = dot2(phi(V[2][k], V[3][k]), p23, acc[2 * k + 1]); }
      sc[0] = (unsigned)__builtin_amdgcn_mov_dpp((int)sc[0], 0x12C, 0xf, 0xf, true);
      if ((++vn & 3) == 0) { sc[0] = sc[1]; sc[1] = sc[2]; sc[2] = sc[3]; sc[3] = sc[4]; }
      asm volatile("" : "+v"(acc[0]), "+v"(acc[1]), "+v"(acc[2]), "+v"(acc[3]), "+v"(acc[4]), "+v"(acc[5]), "+v"(acc[6]), "+v"(acc[7]));
    };
    auto step = [&](KV& S, KV& N, auto SI, auto REFILL, auto WR) {
      constexpr bool refill = REFILL.v == 1;
      constexpr int wr = WR.v;
      if constexpr (refill) next2();
      if constexpr (refill && SI.v >= 3) { until((int)(due - torg) < (int)pTP ? due - pPA : due); due += pTA; }
      constexpr int ah = wr < 0 ? AHL : 0;
      auto point = [&](auto P) {
        if constexpr (refill) {
          if constexpr (P.v == 0 && ah > 0) { if constexpr (wr >= 0) { if (holds) lds_hold(cnt + 8); } sfor<0, ah>([&](auto Q) { ldp(N, Q); }); SCHED(); }
          landed(S, P);
          if constexpr (P.v & 1) TR();
          if constexpr (P.v + ah < 4) { if constexpr ((P.v & 1) == 0 && wr >= 0) { if (holds) lds_hold(cnt + 8); } ldp(N, IC<(P.v + ah) & 3>{}); }
          if constexpr (wr >= 0 && wr < 8) W[(4 * wr + P.v) & 7] = ld16(wp + (((4 * wr + P.v) >> 3) * 32 + ((4 * wr + P.v) & 7)) * 1024);
          SCHED();
        }
      };
      sfor<0, 2>([&](auto H) {
        constexpr int i = 2 * SI.v + H.v;
        if constexpr (REFILL.v == 2 && H.v == 0) { zreq(); SCHED(); }
        point(IC<2 * H.v>{});
        point(IC<2 * H.v + 1>{});
        if constexpr (i < NSMIN) qk(S.r + 4 * H.v, IC<i>{});
        else if constexpr (i > NSMAX) pv(S.r + 4 * H.v);
        else {
          if (i < ns) { if constexpr (i < NSMAX) qk(S.r + 4 * H.v, IC<i>{}); }
          else { if (i == ns) soft(); pv(S.r + 4 * H.v); }
        }
        SCHED();
      });
      if constexpr (REFILL.v == 2) {
        zget();
        STAMP(1);
        *reinterpret_cast<u32x4*>(smem + rwo) = u32x4{pk_bf(acc[0] * wr_, acc[1] * wr_), pk_bf(acc[2] * wr_, acc[3] * wr_), pk_bf(acc[4] * wr_, acc[5] * wr_), pk_bf(acc[6] * wr_, acc[7] * wr_)};
        COMPILER_FENCE();
      }
      if constexpr (wr >= 1) {
        constexpr int t = (wr - 1) >> 1, j0 = ((wr - 1) & 1) * 2;
#pragma unroll
        for (int jj = 0; jj < 2; ++jj)
#pragma unroll
          for (int e = 0; e < 4; ++e) {
            const unsigned a_ = W[(4 * wr - 4 + 2 * jj) & 7][e], b_ = W[(4 * wr - 3 + 2 * jj) & 7][e];
            T[t][2 * e][j0 + jj] = plo(a_, b_); T[t][2 * e + 1][j0 + jj] = phi(a_, b_);
          }
#pragma unroll
        for (int e = 0; e < 8; e += 2) pin2(T[t][e], T[t][e + 1]);
      }
      SCHED();
    };
    step(B, B, IC<1>{}, IC<0>{}, IC<-1>{});
    step(A, B, IC<2>{}, IC<1>{}, IC<0>{});
    step(X, X, IC<0>{}, IC<0>{}, IC<-1>{});
    torg = tstart + __builtin_amdgcn_readfirstlane(cnt[13]); due += torg;
    sfor<0, 4>([&](auto I) {
      step(B, A, IC<2 * I.v + 3>{}, IC<1>{}, IC<2 * I.v + 1>{});
      if constexpr (I.v == 3) {
        float* ml = reinterpret_cast<float*>(cnt + 32);
        ml[(c16 & 1) * 16 + w4 * 4 + r4] = (c16 & 1) ? l : m;
        COMPILER_FENCE();
        const unsigned got = lds_arrive_nw(L_CNT + 44);
        const float mr = ml[c16], lr = ml[16 + c16];
        SCHED();
        if (lds_done(got) == 3u) {
          const float M = row16_max(mr);
          const unsigned z = __float_as_uint(fmaf(M, C2, __builtin_amdgcn_logf(row16_sum(ex2((mr - M) * C2) * lr))));
          asm volatile("buffer_store_dwordx4 %0, %1, %2, 0 offen sc1\n\ts_nop 2" :: "v"(u32x4{z, tag, z, tag}), "v"(lane < 8 ? zo + cls * 128 + lane * 16 : OOB), "s"(rs) : "memory");
        }
        SCHED();
      }
      step(A, B, IC<2 * I.v + 4>{}, IC<1>{}, IC<2 * I.v + 2>{});
    });
    unsigned yoff = WS_GB + ((b * 128 + h * 4 + cls) * 256 + 64 * w4 + lane) * 8, rro = L_MRG + c16 * 272 + r4 * 16;
    rwo = L_MRG + (w4 * 4 + r4) * 272 + c16 * 16;
    asm volatile("" : "+v"(yoff), "+v"(rwo), "+v"(rro));
    if (cls == 0) lds_wait(cnt + 1, 4u);
    const int nit = (ns - 12) >> 1;
#pragma unroll 1
    for (int i = 0; i < nit; ++i) {
      step(B, A, IC<11>{}, IC<1>{}, IC<-1>{});
      step(A, B, IC<11>{}, IC<1>{}, IC<-1>{});
    }
    if (ns & 1) { step(B, A, IC<11>{}, IC<1>{}, IC<-1>{}); step(A, A, IC<11>{}, IC<2>{}, IC<-1>{}); }
    else step(B, B, IC<11>{}, IC<2>{}, IC<-1>{});
    {
      u32x4 af[4];
      unsigned got = lds_arrive_nw(L_CNT), need = 3u;
#pragma unroll 1
      for (int spin = 0; spin < SPIN_MAX; ++spin) {
#pragma unroll
        for (int t = 0; t < 4; ++t) af[t] = *reinterpret_cast<const u32x4*>(smem + rro + t * 64);
        SCHED();
        if (lds_done(got) >= need) break;
        need = 4u;
        __builtin_amdgcn_s_sleep(1);
        asm volatile("ds_read_b32 %0, %1" : "=v"(got) : "v"((unsigned)L_CNT) : "memory");
      }
      SCHED();
      f32x4 d[8];
      float a[8], v4[4];
      sfor<0, 5>([&](auto I) {
        constexpr int i = I.v;
        sfor<0, 8>([&](auto J) {
          constexpr int e = i + 4 * (J.v & 1), t = J.v >> 1;
          if constexpr (i < 4) { d[e] = __builtin_amdgcn_mfma_f32_16x16x32_bf16(__builtin_bit_cast(bf16x8_t, af[t]), __builtin_bit_cast(bf16x8_t, T[t][e]), t ? d[e] : f32x4{0.f, 0.f, 0.f, 0.f}, 0, 0, 0); SCHED(); }
          if constexpr (i >= 1 && (J.v == 4 || J.v == 5)) { const f32x2 u = f32x2{d[e - 1][0], d[e - 1][1]} + f32x2{d[e - 1][2], d[e - 1][3]}; a[e - 1] = u[0] + u[1]; asm volatile("" : "+v"(a[e - 1])); }
          if constexpr (i >= 1 && J.v == 6) v4[i - 1] = rs32(a[i - 1], a[i + 3]);
          if constexpr (i < 4) SCHED();
        });
      });
      st8c(rs, yoff, u32x2{pk_bf(rs16(v4[0], v4[2]), rs16(v4[1], v4[3])), tag});
    }
    STAMP(2);
    STAMP_FLUSH(1);
    TR_FLUSH();
    return;
  }
  const rsrc_t ro = __builtin_amdgcn_make_buffer_rsrc(out, 0, 4096, 0x00020000);
  auto fin = [&](auto NL, unsigned base, unsigned col, bool bump) {
    constexpr int nl = NL.v;
    unsigned doff = base + (((idx >> 5) * 4 + w4) * 16 * nl + (lane >> 2)) * 2048 + (idx & 31) * 64 + (lane & 3) * 16;
    unsigned fwo = 32768 + (c16 < 4 ? (w4 * 16 + 4 * c16 + r4) * 4 : 256 + lane * 4);
    unsigned fro = 32768 + (lane & 7) * 8, foo = lane < 8 ? ((idx >> 5) * 1024 + col + (nl == 2 ? 16 * (idx & 24) + 64 * (idx & 1) + (idx & 6) + 8 * lane : 16 * (idx & 31) + 2 * lane)) * 2 : OOB;
    asm volatile("" : "+v"(doff), "+v"(fwo), "+v"(fro), "+v"(foo));
    lds_wait(cnt + 0, 4u);
    until(tstart + __builtin_amdgcn_readfirstlane(cnt[13]) + PAR(0, TL) + PAR(4 + nl, TF));
    STAMP(4);
    u32x4 d[nl];
    unsigned eh = 1u;
#pragma unroll 1
    for (int spin = SPIN_MAX; spin > 0; --spin) {
      COMPILER_FENCE();
      if (eh != 0u) {
#pragma unroll
        for (int j = 0; j < nl; ++j) d[j] = ld16c(rs, doff + j * 32768);
        eh = 0u;
#pragma unroll
        for (int j = 0; j < nl; ++j) { eh |= (d[j][1] ^ tag) | (d[j][3] ^ tag); asm volatile("" : "+v"(eh)); }
      }
      if (__builtin_amdgcn_ballot_w64(eh != 0u) == 0ull) break;
    }
    STAMP(11);
    float y[4] = {bflo(d[0][0]), bfhi(d[0][0]), bflo(d[0][2]), bfhi(d[0][2])};
    if constexpr (nl == 2) { y[0] += bflo(d[1][0]); y[1] += bfhi(d[1][0]); y[2] += bflo(d[1][2]); y[3] += bfhi(d[1][2]); }
    float t = rs16(rs32(y[0], y[2]), rs32(y[1], y[3]));
    t += dpp<0x124>(t); t += dpp<0x128>(t);
    *reinterpret_cast<float*>(smem + fwo) = t;
    COMPILER_FENCE();
    const unsigned got = lds_arrive_nw(L_CNT + 24);
    f32x2 t2[4];
#pragma unroll
    for (int w = 0; w < 4; ++w) t2[w] = *reinterpret_cast<const f32x2*>(smem + fro + w * 64);
    SCHED();
    if (lds_done(got) == 3u) {
      const f32x2 r = (t2[0] + t2[1]) + (t2[2] + t2[3]);
      __builtin_amdgcn_raw_buffer_store_b32(pk_bf(r[0], r[1]), ro, (int)foo, 0, 0);
      if (bump) st8c(rs, lane == 0 ? WS_EP : OOB, u32x2{tlow, tag});
    }
  };
  u32x4 Wa[16], Wb[16], xv[4];
  auto row = [&](u32x4& d) { d = ld16nt(pa + ao); SCHED(); ao += sa; asm volatile("" : "+v"(ao)); };
  sfor<0, 2>([&](auto I) { row(Wa[I.v]); });
  sfor<0, 4>([&](auto J) { xv[J.v] = ld16(xp + xo + (J.v >> 1) * 1024 + 16 * (J.v & 1)); });
  SCHED();
  sfor<2, 8>([&](auto I) { row(Wa[I.v]); });
  epw = ld8c(rs, WS_EP);
  const unsigned qa_off = WS_QA + (((c16 >> 1) & 1) * 32 + h) * 1024 + (b * 256 + w4 * 64 + 16 * r4 + 8 * (c16 & 1)) * 2;
  st16c(rs, cls == 1 && c16 < 4 ? qa_off : OOB, u32x4{0u, 0u, 0u, 0u});
  SCHED();
  auto sync = [&]() {
    rawbar();
    take_epoch();
    cnt[16] = tag;
    COMPILER_FENCE();
  };
  if (cls == 0) {
    sfor<8, 16>([&](auto I) { row(Wa[I.v]); });
    sync();
    asm volatile("" :: "v"(xv[0]), "v"(xv[1]), "v"(xv[2]), "v"(xv[3]));
    STAMP(13);
    char* scr = smem + L_SCR;
    const int ab = idx >> 5, r = lane & 31, hh = lane >> 5;
    const unsigned fo = WS_QX + (w4 * 8 + (c16 >> 1)) * 2048 + (c16 & 1) * 512 + r4 * 8;
    u32x2 fl;
    u32x4 g[9];
    const unsigned o8 = WS_QA + (ab * 32 + w4 * 8) * 1024 + lane * 16, ol = WS_QA + (ab * 32 + r) * 1024 + 992 + 16 * hh;
    auto put = [&](int i, const u32x4& v) { *reinterpret_cast<u32x4*>(smem + ((i >> 3) * 4 + r4) * 8192 + toff(4 * (i & 7) + w4, c16)) = v; };
    sfor<0, 16>([&](auto I) { put(I.v, Wa[I.v]); if constexpr ((I.v & 3) == 3) TR(); SCHED(); });
    until(tstart + PAR(26, T0B));
    sfor<0, 16>([&](auto I) { row(Wb[I.v]); });
    lds_add(cnt + 14, 1u, lane);
    SCHED();
    sfor<0, 16>([&](auto I) { put(16 + I.v, Wb[I.v]); if constexpr (I.v == 12) fl = ld8c(rs, fo); if constexpr ((I.v & 3) == 3) TR(); SCHED(); });
    STAMP(8);
    u32x4 qlast;
    {
      if (!__all(fl[1] == tag)) {
#pragma unroll 1
        for (int spin = 0; spin < SPIN_MAX; ++spin) {
          COMPILER_FENCE();
          if (__all(ld8c(rs, fo)[1] == tag)) break;
          __builtin_amdgcn_s_sleep(8);
        }
      }
      until(tstart + PAR(27, T0Q));
      STAMP(5);
      sbar4(cnt + 7, lane);
      STAMP(12);
      unsigned pend = 0x1ffu;
#pragma unroll 1
      for (int spin = 0; spin < SPIN_MAX && pend; ++spin) {
        COMPILER_FENCE();
        u32x4 t[9];
#pragma unroll
        for (int i = 0; i < 9; ++i) t[i] = ld16c(rs, (pend >> i & 1) ? (i < 8 ? o8 + i * 1024 : ol) : OOB);
#pragma unroll
        for (int i = 0; i < 9; ++i) {
          if (pend >> i & 1) g[i] = t[i];
          if (__all(nz8(t[i]))) pend &= ~(1u << i);
        }
      }
      STAMP(9);
      qlast = g[8];
#pragma unroll
      for (int i = 0; i < 8; ++i)
        if (lane < 62) *reinterpret_cast<u32x4*>(scr + (w4 * 8 + i) * 1008 + 16 * lane) = g[i];
    }
    sbar4(cnt + 3, lane);
    STAMP(10);
    f32x16 sacc;
#pragma unroll
    for (int i = 0; i < 16; ++i) sacc[i] = 0.f;
#pragma unroll
    for (int jj = 0; jj < 32; ++jj) {
      const u32x4 af = *reinterpret_cast<const u32x4*>(smem + (w4 * 4 + (jj >> 3)) * 8192 + toff(r, 2 * (jj & 7) + hh));
      const u32x4 bf = jj < 31 ? *reinterpret_cast<const u32x4*>(scr + r * 1008 + 16 * (2 * (jj < 31 ? jj : 0) + hh)) : qlast;
      sacc = __builtin_amdgcn_mfma_f32_32x32x16_bf16(__builtin_bit_cast(bf16x8_t, af), __builtin_bit_cast(bf16x8_t, bf), sacc, 0, 0, 0);
    }
    float m = sacc[0];
#pragma unroll
    for (int i = 1; i < 16; ++i) m = fmaxf(m, sacc[i]);
    m = xhalf_max(m);
    if (hh == 0) *reinterpret_cast<float*>(scr + r * 1008 + 992 + 4 * w4) = m;
    sbar4(cnt + 4, lane);
    STAMP(11);
    {
      const f32x4 m4 = *reinterpret_cast<const f32x4*>(scr + r * 1008 + 992);
      m = fmaxf(fmaxf(m4[0], m4[1]), fmaxf(m4[2], m4[3]));
    }
    const float mc = m * C2;
    float p[16], ls = 0.f;
#pragma unroll
    for (int i = 0; i < 16; ++i) { p[i] = ex2(fmaf(sacc[i], C2, -mc)); ls += p[i]; }
    ls = xhalf_sum(ls);
    sbar4(cnt + 5, lane);
    u32x4* pf = reinterpret_cast<u32x4*>(scr);
    float* wl = reinterpret_cast<float*>(scr + 8192);
    if (hh == 0) wl[r * 4 + w4] = ls;
    pf[(2 * w4) * 64 + lane] = u32x4{pk_bf(p[0], p[1]), pk_bf(p[2], p[3]), pk_bf(p[4], p[5]), pk_bf(p[6], p[7])};
    pf[(2 * w4 + 1) * 64 + lane] = u32x4{pk_bf(p[8], p[9]), pk_bf(p[10], p[11]), pk_bf(p[12], p[13]), pk_bf(p[14], p[15])};
    sbar4(cnt + 6, lane);
    u32x4 pb8[8];
#pragma unroll
    for (int ks = 0; ks < 8; ++ks) pb8[ks] = pf[ks * 64 + lane];
    const f32x4 l4 = *reinterpret_cast<const f32x4*>(wl + r * 4);
    const float lsum = (l4[0] + l4[1]) + (l4[2] + l4[3]), rl = __builtin_amdgcn_rcpf(lsum);
    const int qq = (lane & 15) >> 2, pp = lane & 3, blk2 = (lane >> 4) & 1;
    const unsigned tw = tstart + __builtin_amdgcn_readfirstlane(cnt[13]) + PAR(13, TW), th = tw - PAR(14, HL);
    u32x4 vv[2][4];
    bool held = false;
    const int hx = (lane >> 3) ^ (idx & 7);
    char* ot = scr + 12288 + w4 * 4096;
#pragma unroll
    for (int c2 = 0; c2 < 2; ++c2) {
      f32x16 oacc[2];
#pragma unroll
      for (int ct = 0; ct < 2; ++ct)
#pragma unroll
        for (int i = 0; i < 16; ++i) oacc[ct][i] = 0.f;
#pragma unroll
      for (int ks = 0; ks < 8; ++ks)
#pragma unroll
        for (int ct = 0; ct < 2; ++ct) {
          typedef __attribute__((address_space(3))) s16x4* lp;
          const char* tile = smem + ((ks >> 1) * 4 + w4) * 8192;
          const int cc = 2 * c2 + ct;
          const s16x4 lo = __builtin_amdgcn_ds_read_tr16_b64_v4i16((lp)(tile + toff(16 * (ks & 1) + 4 * hh + qq, 4 * cc + 2 * blk2 + (pp >> 1)) + 8 * (pp & 1)));
          const s16x4 hi = __builtin_amdgcn_ds_read_tr16_b64_v4i16((lp)(tile + toff(16 * (ks & 1) + 8 + 4 * hh + qq, 4 * cc + 2 * blk2 + (pp >> 1)) + 8 * (pp & 1)));
          const s16x8 afr = __builtin_shufflevector(lo, hi, 0, 1, 2, 3, 4, 5, 6, 7);
          oacc[ct] = __builtin_amdgcn_mfma_f32_32x32x16_bf16(__builtin_bit_cast(bf16x8_t, afr), __builtin_bit_cast(bf16x8_t, pb8[ks]), oacc[ct], 0, 0, 0);
        }
#pragma unroll
      for (int ct = 0; ct < 2; ++ct)
#pragma unroll
        for (int g = 0; g < 4; ++g)
          *reinterpret_cast<u32x2*>(ot + r * 128 + 16 * ((4 * ct + g) ^ (r & 7)) + 8 * hh) = u32x2{pk_bf(oacc[ct][4 * g] * rl, oacc[ct][4 * g + 1] * rl), pk_bf(oacc[ct][4 * g + 2] * rl, oacc[ct][4 * g + 3] * rl)};
      if (c2 == 0) { held = (int)((unsigned)wall_clock64() + 30u - th) >= 0; if (held) lds_add(cnt + 8, 1u, lane); }
      __builtin_amdgcn_wave_barrier();
#pragma unroll
      for (int i = 0; i < 4; ++i) { vv[c2][i] = *reinterpret_cast<const u32x4*>(ot + (8 * i + hx) * 128 + 16 * ((lane & 7) ^ hx)); asm volatile("" : "+v"(vv[c2][i])); }
      __builtin_amdgcn_wave_barrier();
    }
    lds_arrive(cnt + 1, lane);
    if (!held) { until(th); lds_add(cnt + 8, 1u, lane); }
    until(tw);
#pragma unroll
    for (int i = 0; i < 8; ++i) st16c(rs, WS_APO + idx * 32768 + w4 * 8192 + i * 1024 + lane * 16, vv[i & 1][i >> 1]);
    STAMP(1);
    asm volatile("s_waitcnt vmcnt(0)" ::: "memory");
    if (lds_arrive(cnt + 9 + (w4 >> 1), lane) == 1u && hh == 0)
      st8c(rs, WS_AML + ((r * 2 + (w4 >> 1)) * 16 + (idx >> 2)) * 128 + (idx & 3) * 8, u32x2{__float_as_uint(mc + __builtin_amdgcn_logf(lsum)), tag});
    lds_add(cnt + 8, ~0u, lane);
    STAMP(2);
    STAMP_FLUSH(0); TR_FLUSH();
    return;
  }
  sfor<8, 16>([&](auto I) { row(Wa[I.v]); });
  const u16* pb = cls == 2 ? w_uv + ((size_t)(b * 256 + w4 * 64 + 16 * r4) * 32 + h) * 128 + 8 * c16 : (cls == 1 ? w_o : wo) + ((size_t)h * 128 + b * 64 + w4 * 2) * 512 + 8 * lane;
  sync();
  if (blk < 16 && w4 == 0) asm volatile("buffer_store_dwordx4 %0, %1, %2, 0 offen sc1\n\ts_nop 2" :: "v"(u32x4{tstart, tag, tstart, tag}), "v"(WS_T0 + (blk & 7) * 1024 + lane * 16), "s"(rs) : "memory");
  STAMP(13);
  float* red = reinterpret_cast<float*>(smem);
  if (cls & 1) {
    float acc[2][8];
#pragma unroll
    for (int bb = 0; bb < 2; ++bb)
#pragma unroll
      for (int k = 0; k < 8; ++k) acc[bb][k] = 0.f;
    sfor<0, 8>([&](auto IP) {
      constexpr int ip = IP.v, j = ip - (8 - WBW);
      use2(Wa[2 * ip], Wa[2 * ip + 1]);
      if constexpr (ip & 1) TR();
      if constexpr (j >= 0) { Wb[2 * j] = ld16(pb + j * 4096); Wb[2 * j + 1] = ld16(pb + j * 4096 + 512); }
      SCHED();
      const unsigned xp0 = xv[ip >> 2][ip & 3], xp1 = xv[2 + (ip >> 2)][ip & 3];
#pragma unroll
      for (int k = 0; k < 4; ++k) {
        const unsigned lo = plo(Wa[2 * ip][k], Wa[2 * ip + 1][k]), hi = phi(Wa[2 * ip][k], Wa[2 * ip + 1][k]);
        acc[0][2 * k] = dot2(lo, xp0, acc[0][2 * k]); acc[0][2 * k + 1] = dot2(hi, xp0, acc[0][2 * k + 1]);
        acc[1][2 * k] = dot2(lo, xp1, acc[1][2 * k]); acc[1][2 * k + 1] = dot2(hi, xp1, acc[1][2 * k + 1]);
      }
      SCHED();
    });
    {
      float v8[8], v4[4];
#pragma unroll
      for (int k = 0; k < 8; ++k) v8[k] = rs32(acc[0][k], acc[1][k]);
#pragma unroll
      for (int k = 0; k < 4; ++k) v4[k] = rs16(v8[k], v8[k + 4]);
      *reinterpret_cast<f32x4*>(red + (w4 * 2 + (lane >> 5)) * 128 + 8 * c16 + 4 * (r4 & 1)) = f32x4{v4[0], v4[1], v4[2], v4[3]};
    }
    COMPILER_FENCE();
    const unsigned got = lds_arrive_nw(L_CNT + 8);
    f32x4 t4[4];
#pragma unroll
    for (int w = 0; w < 4; ++w) t4[w] = *reinterpret_cast<const f32x4*>(red + (w * 2 + (lane >> 5)) * 128 + 8 * c16 + 4 * (r4 & 1));
    SCHED();
    if (lds_done(got) == 3u) {
      const f32x4 t = (t4[0] + t4[1]) + (t4[2] + t4[3]);
      st16c(rs, WS_QX + (((cls >> 1) * 32 + h) * 2 + (lane >> 5)) * 1024 + b * 512 + (lane & 31) * 16, u32x4{pk_bf(t[0], t[1]), tag, pk_bf(t[2], t[3]), tag});
      STAMP(15);
      SCHED();
      lds_add(cnt + 2, 1u, lane);
    } else lds_wait(cnt + 2, 5u);
    sfor<WBW, 8>([&](auto J) {
      constexpr int j = J.v;
      use2(Wb[2 * (j - WBW)], Wb[2 * (j - WBW) + 1]);
      if constexpr ((j - WBW) & 1) TR();
      Wb[2 * j] = ld16(pb + j * 4096); Wb[2 * j + 1] = ld16(pb + j * 4096 + 512);
      if constexpr (j == 7) lds_add(cnt + 14, 1u, lane);
      SCHED();
    });
    {
      const unsigned tw = __builtin_nontemporal_load(reinterpret_cast<const unsigned*>(out + (idx >> 5) * 1024 + (cls >> 1) * 512 + 16 * (idx & 31)));
      sfor<8 - WBW, 8>([&](auto J) { use2(Wb[2 * J.v], Wb[2 * J.v + 1]); if constexpr (J.v & 1) TR(); });
      asm volatile("" :: "v"(tw));
    }
    STAMP(8);
  } else {
    sfor<0, 16 - WBW>([&](auto IP) {
      constexpr int ip = IP.v, j = ip - (8 - WBW);
      if constexpr (ip < 8) use2(Wa[2 * ip], Wa[2 * ip + 1]); else use2(Wb[2 * ip - 16], Wb[2 * ip - 15]);
      if constexpr (ip & 1) TR();
      if constexpr (j >= 0) { Wb[2 * j] = ld16nt(pb + j * 8192); Wb[2 * j + 1] = ld16nt(pb + j * 8192 + 4096); }
      SCHED();
    });
    u32x4 g0, g1;
    const unsigned qo = WS_QX + h * 2048 + lane * 16;
    shint<512, 1>(ws + WS_QX + h * 2048, tag);
    STAMP(5);
#pragma unroll 1
    for (int spin = 0; spin < SPIN_MAX; ++spin) {
      COMPILER_FENCE();
      g0 = ld16c(rs, qo); g1 = ld16c(rs, qo + 1024);
      SCHED();
      lds_add(cnt + 14, spin == 0, lane);
      SCHED();
      if (__all((g0[1] == tag) & (g0[3] == tag) & (g1[1] == tag) & (g1[3] == tag))) break;
      __builtin_amdgcn_s_sleep(1);
    }
    sfor<8 - WBW, 8>([&](auto J) { if constexpr (J.v & 1) TR(); });
    const u32x4 q0 = qsum(g0), q1 = qsum(g1);
    STAMP(9);
    float v[8];
    {
      float u[16];
#pragma unroll
      for (int i = 0; i < 16; ++i) {
        float a = 0.f, c = 0.f;
#pragma unroll
        for (int k = 0; k < 4; ++k) { a = dot2(Wa[i][k], q0[k], a); c = dot2(Wa[i][k], q1[k], c); }
        u[i] = (c16 & 2 ? c : a) + dpp<0x4E>(c16 & 2 ? a : c);
        asm volatile("" : "+v"(u[i]));
      }
#pragma unroll
      for (int i = 0; i < 8; ++i) {
        float t = (c16 & 1 ? u[i + 8] : u[i]) + dpp<0xB1>(c16 & 1 ? u[i] : u[i + 8]);
        asm volatile("" : "+v"(t));
        t += dpp<0x124>(t);
        asm volatile("" : "+v"(t));
        t += dpp<0x128>(t);
        asm volatile("" : "+v"(t));
        v[i] = t;
      }
    }
    u32x4 o = {pk_bf(v[0], v[1]), pk_bf(v[2], v[3]), pk_bf(v[4], v[5]), pk_bf(v[6], v[7])};
    if ((o[0] | o[1]) == 0u) o[0] = 0x8000u;
    if ((o[2] | o[3]) == 0u) o[2] = 0x8000u;
    if (c16 < 4) st16c(rs, qa_off, o);
    STAMP(10);
  }
  asm volatile("" :: "v"(xv[0]), "v"(xv[1]), "v"(xv[2]), "v"(xv[3]));
  SCHED();
  if (cls == 3) {
    fin(IC<2>{}, WS_GB, 512, false);
    STAMP(6);
    STAMP_FLUSH(0); TR_FLUSH();
    return;
  }
#pragma unroll
  for (int ip = 0; ip < 8; ++ip) {
#pragma unroll
    for (int k = 0; k < 4; ++k) { const unsigned lo = plo(Wb[2 * ip][k], Wb[2 * ip + 1][k]), hi = phi(Wb[2 * ip][k], Wb[2 * ip + 1][k]); Wb[2 * ip][k] = lo; Wb[2 * ip + 1][k] = hi; }
    pin2(Wb[2 * ip], Wb[2 * ip + 1]);
  }
  if (cls == 2) {
    float* red2 = red + 4 * 256;
    float* wml = red + 3072;
    u32x8 f[4];
    until<6>(tstart + PAR(13, TW) + PAR(28, 100));
#pragma unroll 1
    for (int spin = 0; spin < SPIN_MAX; ++spin) {
      sld4x32(ws + WS_AML + ((h * 2 + b) * 16 + w4 * 4) * 128, f);
      unsigned e = 0u;
#pragma unroll
      for (int i = 0; i < 16; ++i) e |= f[i >> 2][2 * (i & 3) + 1] ^ tag;
      if (e == 0u) break;
      __builtin_amdgcn_s_sleep(2);
    }
    STAMP(3);
    u32x4 pp[8];
#pragma unroll
    for (int i = 0; i < 8; ++i) pp[i] = ld16c(rs, WS_APO + (w4 * 16 + 2 * i + (lane >> 5)) * 32768 + (2 * b + (r4 & 1)) * 8192 + (2 * (h >> 3) + ((lane >> 3) & 1)) * 1024 + ((h ^ (2 * i + (lane >> 5))) & 7) * 128 + (lane & 7) * 16);
    SCHED();
    float wt[8];
    {
      float M = -INFINITY, L = 0.f;
#pragma unroll
      for (int i = 0; i < 8; ++i) {
        unsigned z0 = f[i >> 1][4 * (i & 1)], z1 = f[i >> 1][4 * (i & 1) + 2];
        asm("" : "+s"(z0), "+s"(z1));
        wt[i] = __uint_as_float(lane < 32 ? z0 : z1); M = fmaxf(M, wt[i]);
      }
      M = xhalf_max(M);
#pragma unroll
      for (int i = 0; i < 8; ++i) { wt[i] = ex2(wt[i] - M); L += wt[i]; }
      L = xhalf_sum(L);
      if (lane == 0) *reinterpret_cast<f32x2*>(wml + 2 * w4) = f32x2{M, L};
    }
    SCHED();
    float a8[8];
#pragma unroll
    for (int k = 0; k < 8; ++k) a8[k] = 0.f;
#pragma unroll
    for (int i = 0; i < 8; ++i) {
#pragma unroll
      for (int k = 0; k < 4; ++k) { a8[2 * k] = fmaf(wt[i], bflo(pp[i][k]), a8[2 * k]); a8[2 * k + 1] = fmaf(wt[i], bfhi(pp[i][k]), a8[2 * k + 1]); }
      asm volatile("" : "+v"(a8[0]), "+v"(a8[1]), "+v"(a8[2]), "+v"(a8[3]), "+v"(a8[4]), "+v"(a8[5]), "+v"(a8[6]), "+v"(a8[7]));
    }
    STAMP(6);
    *reinterpret_cast<f32x4*>(red + w4 * 256 + 8 * (lane & 31) + 4 * (lane >> 5)) = f32x4{rs32(a8[0], a8[4]), rs32(a8[1], a8[5]), rs32(a8[2], a8[6]), rs32(a8[3], a8[7])};
    sbar4(cnt + 3, lane);
    STAMP(4);
    float acc[2][8];
#pragma unroll
    for (int bb = 0; bb < 2; ++bb) {
      u32x4 cv[2];
      const f32x4 ml = *reinterpret_cast<const f32x4*>(wml + 4 * bb);
      const float M = fmaxf(ml[0], ml[2]), e0 = ex2(ml[0] - M), e1 = ex2(ml[2] - M), rl = 1.f / fmaf(e0, ml[1], e1 * ml[3]), s0 = e0 * rl, s1 = e1 * rl;
#pragma unroll
      for (int e = 0; e < 2; ++e) {
        const float* src = red + bb * 512 + w4 * 64 + 16 * r4 + 8 * e;
        const f32x4 t0 = s0 * *reinterpret_cast<const f32x4*>(src) + s1 * *reinterpret_cast<const f32x4*>(src + 256), t1 = s0 * *reinterpret_cast<const f32x4*>(src + 4) + s1 * *reinterpret_cast<const f32x4*>(src + 260);
        cv[e] = u32x4{pk_bf(t0[0], t0[1]), pk_bf(t0[2], t0[3]), pk_bf(t1[0], t1[1]), pk_bf(t1[2], t1[3])};
      }
#pragma unroll
      for (int k = 0; k < 8; ++k) acc[bb][k] = 0.f;
#pragma unroll
      for (int ip = 0; ip < 8; ++ip)
#pragma unroll
        for (int k = 0; k < 4; ++k) { acc[bb][2 * k] = dot2(Wb[2 * ip][k], cv[ip >> 2][ip & 3], acc[bb][2 * k]); acc[bb][2 * k + 1] = dot2(Wb[2 * ip + 1][k], cv[ip >> 2][ip & 3], acc[bb][2 * k + 1]); }
    }
    {
      float v8[8], v4[4];
#pragma unroll
      for (int k = 0; k < 8; ++k) v8[k] = rs32(acc[0][k], acc[1][k]);
#pragma unroll
      for (int k = 0; k < 4; ++k) v4[k] = rs16(v8[k], v8[k + 4]);
      *reinterpret_cast<f32x4*>(red2 + (w4 * 2 + (lane >> 5)) * 128 + 8 * c16 + 4 * (r4 & 1)) = f32x4{v4[0], v4[1], v4[2], v4[3]};
    }
    {
      COMPILER_FENCE();
      const unsigned got = lds_arrive_nw(L_CNT + 16);
      f32x4 t4[4];
#pragma unroll
      for (int w = 0; w < 4; ++w) t4[w] = *reinterpret_cast<const f32x4*>(red2 + w * 256 + 4 * lane);
      SCHED();
      if (lds_done(got) == 3u) {
        const f32x4 t = (t4[0] + t4[1]) + (t4[2] + t4[3]);
        const unsigned co = WS_CX + ((h * 2 + b) * 256 + 64 * r4 + 32 * (c16 & 1) + 2 * (c16 >> 1)) * 8;
        st16c(rs, co, u32x4{__float_as_uint(t[0]), tag, __float_as_uint(t[1]), tag});
        st16c(rs, co + 128, u32x4{__float_as_uint(t[2]), tag, __float_as_uint(t[3]), tag});
      }
    }
    STAMP(12);
    STAMP_FLUSH(0); TR_FLUSH();
    return;
  }
  {
    unsigned op;
    {
      u32x4 c;
      const unsigned co = WS_CX + ((h * 2 + ((lane >> 3) & 1)) * 256 + (lane >> 5) * 128 + b * 64 + w4 * 16 + 2 * (lane & 7)) * 8;
      if (idx == 0 && w4 == 1) {
#pragma unroll 1
        for (int spin = 0; spin < SPIN_MAX; ++spin) {
          COMPILER_FENCE();
          if (__all(ld8c(rs, WS_QX + 65536 + (lane >> 1) * 2048 + (lane & 1) * 512)[1] == tag)) break;
          __builtin_amdgcn_s_sleep(8);
        }
      }
      until<6>(tstart + PAR(13, TW) + PAR(29, 370));
      if (w4 == 0) { shint<2048, 0>(ws + WS_CX + (h * 512 + b * 64) * 8, tag); lds_add(cnt + 7, 1u, lane); }
      else lds_wait(cnt + 7, 1u);
      STAMP(3);
#pragma unroll 1
      for (int spin = 0; spin < SPIN_MAX; ++spin) {
        COMPILER_FENCE();
        c = ld16c(rs, co);
        if (__all((c[1] == tag) & (c[3] == tag))) break;
        __builtin_amdgcn_s_sleep(1);
      }
      float o0 = __uint_as_float(c[0]), o1 = __uint_as_float(c[2]);
      o0 += dpp<0x128>(o0); o1 += dpp<0x128>(o1);
      op = pk_bf(o0, o1);
    }
    STAMP(14);
    float* red3 = red + 4096;
    float a[2][8];
#pragma unroll
    for (int bb = 0; bb < 2; ++bb)
#pragma unroll
      for (int k = 0; k < 8; ++k) a[bb][k] = 0.f;
#pragma unroll
    for (int ip = 0; ip < 8; ++ip) {
      const unsigned v0 = __builtin_amdgcn_readlane(op, ip), v1 = __builtin_amdgcn_readlane(op, 32 + ip);
#pragma unroll
      for (int k = 0; k < 4; ++k) {
        a[0][2 * k] = dot2(Wb[2 * ip][k], v0, a[0][2 * k]); a[0][2 * k + 1] = dot2(Wb[2 * ip + 1][k], v0, a[0][2 * k + 1]);
        a[1][2 * k] = dot2(Wb[2 * ip][k], v1, a[1][2 * k]); a[1][2 * k + 1] = dot2(Wb[2 * ip + 1][k], v1, a[1][2 * k + 1]);
      }
    }
#pragma unroll
    for (int bb = 0; bb < 2; ++bb) {
      *reinterpret_cast<f32x4*>(red3 + (w4 * 2 + bb) * 512 + 8 * lane) = f32x4{a[bb][0], a[bb][1], a[bb][2], a[bb][3]};
      *reinterpret_cast<f32x4*>(red3 + (w4 * 2 + bb) * 512 + 8 * lane + 4) = f32x4{a[bb][4], a[bb][5], a[bb][6], a[bb][7]};
    }
    sbar4(cnt + 5, lane);
    f32x4 t = {0.f, 0.f, 0.f, 0.f};
#pragma unroll
    for (int w = 0; w < 4; ++w) t += *reinterpret_cast<const f32x4*>(red3 + w * 1024 + 4 * tid);
    st16c(rs, WS_GA + ((tid >> 7) * 64 + idx) * 2048 + (tid & 127) * 16, u32x4{pk_bf(t[0], t[1]), tag, pk_bf(t[2], t[3]), tag});
    STAMP(5);
  }
  fin(IC<1>{}, WS_GA, 0, idx == 0);
  STAMP(6);
  STAMP_FLUSH(0); TR_FLUSH();
}

extern "C" void kernel_launch(void* const* d_in, const int* in_sizes, int n_in, void* d_out, int out_size, void* d_ws, size_t ws_size, hipStream_t stream) {
  const u16* xA = (const u16*)d_in[0];
  const u16* ckv = (const u16*)d_in[1];
  const u16* w_q = (const u16*)d_in[2];
  const u16* w_uk = (const u16*)d_in[3];
  const u16* w_uv = (const u16*)d_in[4];
  const u16* w_o = (const u16*)d_in[5];
  const u16* xB = (const u16*)d_in[6];
  const u16* kc = (const u16*)d_in[7];
  const u16* vc = (const u16*)d_in[8];
  const u16* wq = (const u16*)d_in[9];
  const u16* wo = (const u16*)d_in[10];
  kmain<<<256, 512, 0, stream>>>(ckv, w_q, w_uk, wq, xA, xB, w_uv, w_o, kc, vc, wo, (char*)d_ws, (u16*)d_out);
}
```

```cpp
#include <hip/hip_runtime.h>
#include <hip/hip_bf16.h>
#include <stdint.h>

#define DI __device__ __forceinline__
typedef __attribute__((ext_vector_type(4))) unsigned u32x4;
typedef __attribute__((ext_vector_type(2))) unsigned u32x2;
typedef __attribute__((ext_vector_type(4))) float f32x4;
typedef __attribute__((ext_vector_type(2))) float f32x2;
typedef __attribute__((ext_vector_type(16))) float f32x16;
typedef __attribute__((ext_vector_type(8))) __bf16 bf16x8_t;
typedef __attribute__((ext_vector_type(2))) __bf16 bf16x2_t;
typedef __attribute__((ext_vector_type(4))) short s16x4;
typedef __attribute__((ext_vector_type(8))) short s16x8;
typedef unsigned short u16;
typedef unsigned long long u64;

constexpr int NS0 = 13, NS1 = 17, NS2 = 16, NS3 = 18;
constexpr u64 TL0 = 0xcaeaa685e6d54caull, TH0 = 0x3deb6ull, TL1 = 0x81c65444d2c7100ull, TH1 = 0x3ee33c2ca24ull;
constexpr u64 TL2 = 0x8e26163ce2460c1ull, TH2 = 0xf37d31ae9ull, TL3 = 0x7dd697490308142ull, TH3 = 0xffbe75bed9e5ull;
constexpr int RA2 = 0xC0, RA3 = 3;
constexpr int AHL = 2;
constexpr int QPE = 7;
constexpr int WBW = 5;
#define TL 2360
#define TW 1650
constexpr unsigned TS0 = 1160, TS1 = 1420, TS2 = 1320, TS3 = 1420, STG = 12, BSG = 6, HL = 40, TP0 = TW + 170, TP2 = TW + 380, PA = 150, TF = 180, T0B = 410, T0Q = 880;
#define PAR(i, v) (v)
constexpr u64 LA2 = TH2 >> (6 * (NS2 - 11)), LA3 = TH3 >> (6 * (NS3 - 11));
constexpr u64 chunk_mask(u64 lo, u64 hi, int n) { u64 m = 0; for (int i = 0; i < n; ++i) m |= 1ull << ((i < 10 ? lo >> (6 * i) : hi >> (6 * (i - 10))) & 63); return m; }
static_assert((chunk_mask(TL0, TH0, NS0) | chunk_mask(TL1, TH1, NS1) | chunk_mask(TL2, TH2, NS2) | chunk_mask(TL3, TH3, NS3)) == ~0ull && (RA2 & RA3) == 0, "every chunk once");
static_assert(NS0 + NS1 + NS2 + NS3 == 64, "split sizes");
constexpr int NSMIN = 13, NSMAX = 18;
static_assert(NS0 >= NSMIN && NS1 >= NSMIN && NS2 - 1 >= NSMIN && NS3 - 1 >= NSMIN && NS0 <= NSMAX && NS1 + 1 <= NSMAX && NS2 <= NSMAX && NS3 <= NSMAX, "range");
constexpr int SK = 4096;
constexpr float C2 = 0.08838834764831845f * 1.4426950408889634f;
constexpr u64 GOLD = 0x9E3779B97F4A7C15ull;

constexpr unsigned WS_EP = 0;
constexpr unsigned WS_QX = 4096;
constexpr unsigned WS_QA = WS_QX + 2 * 32 * 256 * 8;
constexpr unsigned WS_AML = WS_QA + 2 * 32 * 512 * 2;
constexpr unsigned WS_CX = WS_AML + 32 * 2 * 16 * 128;
constexpr unsigned WS_GZ = WS_CX + 32 * 2 * 2 * 128 * 8;
constexpr unsigned WS_T0 = WS_GZ + 256 * 128;
constexpr unsigned WS_GB = WS_T0 + 64 * 128;
constexpr unsigned WS_GA = WS_GB + 2 * 128 * 256 * 8;
constexpr unsigned WS_APO = 2u << 20;
static_assert(WS_GA + 64 * 2 * 256 * 8 <= WS_APO, "layout");
constexpr unsigned WS_END = WS_APO + 32 * 2 * 64 * 256 * 2;
#define STAMP(i) do {} while (0)
#define STAMP_DECL do {} while (0)
#define STAMP_FLUSH(grp) do {} while (0)

#define TR() do {} while (0)
#define TR_FLUSH() do {} while (0)
DI float bflo(unsigned u) { return __uint_as_float(u << 16); }
DI float bfhi(unsigned u) { return __uint_as_float(u & 0xffff0000u); }
DI unsigned pk_bf(float a, float b) { const f32x2 v = {a, b}; return __builtin_bit_cast(unsigned, __builtin_convertvector(v, bf16x2_t)); }
DI float dot2(unsigned k, unsigned q, float acc) {
  return __builtin_amdgcn_fdot2_f32_bf16(__builtin_bit_cast(bf16x2_t, k), __builtin_bit_cast(bf16x2_t, q), acc, false);
}
DI unsigned plo(unsigned w0, unsigned w1) { return __builtin_amdgcn_perm(w1, w0, 0x05040100u); }
DI unsigned phi(unsigned w0, unsigned w1) { return __builtin_amdgcn_perm(w1, w0, 0x07060302u); }
template <int CTRL> DI float dpp(float x) {
  return __builtin_bit_cast(float, __builtin_amdgcn_mov_dpp(__builtin_bit_cast(int, x), CTRL, 0xf, 0xf, true));
}
DI float row16_sum(float x) { x += dpp<0xB1>(x); x += dpp<0x4E>(x); x += dpp<0x141>(x); x += dpp<0x128>(x); return x; }
DI float row16_max(float x) { x = fmaxf(x, dpp<0xB1>(x)); x = fmaxf(x, dpp<0x4E>(x)); x = fmaxf(x, dpp<0x141>(x)); x = fmaxf(x, dpp<0x128>(x)); return x; }
DI float xhalf_max(float x) { auto t = __builtin_amdgcn_permlane32_swap(__float_as_uint(x), __float_as_uint(x), false, false); return fmaxf(__uint_as_float(t[0]), __uint_as_float(t[1])); }
DI float xhalf_sum(float x) { auto t = __builtin_amdgcn_permlane32_swap(__float_as_uint(x), __float_as_uint(x), false, false); return __uint_as_float(t[0]) + __uint_as_float(t[1]); }
DI u32x4 ld16(const void* p) { return *reinterpret_cast<const u32x4*>(p); }
DI u32x4 ld16nt(const void* p) { return __builtin_nontemporal_load(reinterpret_cast<const u32x4*>(p)); }
DI float ex2(float x) { return __builtin_amdgcn_exp2f(x); }
#define SCHED() __builtin_amdgcn_sched_barrier(0)

typedef __amdgpu_buffer_rsrc_t rsrc_t;
DI u32x4 ld16c(rsrc_t rs, unsigned off) { return __builtin_amdgcn_raw_buffer_load_b128(rs, (int)off, 0, 16); }
DI u32x2 ld8c(rsrc_t rs, unsigned off) { return __builtin_amdgcn_raw_buffer_load_b64(rs, (int)off, 0, 16); }
DI void st16c(rsrc_t rs, unsigned off, u32x4 v) { __builtin_amdgcn_raw_buffer_store_b128(v, rs, (int)off, 0, 16); }
DI void st8c(rsrc_t rs, unsigned off, u32x2 v) { __builtin_amdgcn_raw_buffer_store_b64(v, rs, (int)off, 0, 16); }
template <int O1> DI void sld2(const char* p, u32x2& a, u32x2& b) {
  asm volatile("s_load_dwordx2 %0, %2, 0x0 glc\n\ts_load_dwordx2 %1, %2, %3 glc\n\ts_waitcnt lgkmcnt(0)" : "=&s"(a), "=&s"(b) : "s"(p), "n"(O1) : "memory");
}
typedef __attribute__((ext_vector_type(8))) unsigned u32x8;
DI void sld4x32(const char* p, u32x8 (&v)[4]) {
  asm volatile("s_load_dwordx8 %0, %4, 0x0 glc\n\ts_load_dwordx8 %1, %4, 0x80 glc\n\ts_load_dwordx8 %2, %4, 0x100 glc\n\ts_load_dwordx8 %3, %4, 0x180 glc\n\ts_waitcnt lgkmcnt(0)"
               : "=&s"(v[0]), "=&s"(v[1]), "=&s"(v[2]), "=&s"(v[3]) : "s"(p) : "memory");
}
constexpr int SPIN_MAX = 1 << 20;
constexpr unsigned OOB = 0x7ffffff0u;
DI void rawbar() { __builtin_amdgcn_sched_barrier(0); __builtin_amdgcn_s_barrier(); __builtin_amdgcn_sched_barrier(0); }
#define COMPILER_FENCE() asm volatile("" ::: "memory")
template <int O1, int SL> DI void shint(const char* p, unsigned tag) {
#pragma unroll 1
  for (int spin = 0; spin < SPIN_MAX; ++spin) {
    u32x2 a, b;
    sld2<O1>(p, a, b);
    if (((a[1] ^ tag) | (b[1] ^ tag)) == 0u) break;
    __builtin_amdgcn_s_sleep(SL);
  }
}

template <int N> struct IC { static constexpr int v = N; };
template <int I, int N, class F> DI void sfor(F&& f) { if constexpr (I < N) { f(IC<I>{}); sfor<I + 1, N>(f); } }
typedef __attribute__((address_space(3))) unsigned lu32;
DI void lds_wait(unsigned* cnt, unsigned target) {
#pragma unroll 1
  for (int spin = 0; spin < SPIN_MAX; ++spin) {
    if (__builtin_amdgcn_readfirstlane(__hip_atomic_load((lu32*)cnt, __ATOMIC_RELAXED, __HIP_MEMORY_SCOPE_WORKGROUP)) >= target) break;
    __builtin_amdgcn_s_sleep(1);
  }
  COMPILER_FENCE();
}
DI void lds_wait_slow(unsigned* cnt, unsigned target) {
#pragma unroll 1
  for (int spin = 0; spin < SPIN_MAX; ++spin) {
    if (__builtin_amdgcn_readfirstlane(__hip_atomic_load((lu32*)cnt, __ATOMIC_RELAXED, __HIP_MEMORY_SCOPE_WORKGROUP)) >= target) break;
    __builtin_amdgcn_s_sleep(3);
  }
  COMPILER_FENCE();
}
template <int SL = 1> DI void until(unsigned t) {
#pragma unroll 1
  for (int spin = 0; spin < 1024; ++spin) { if ((int)((unsigned)wall_clock64() - t) >= 0) break; __builtin_amdgcn_s_sleep(SL); }
}
DI void lds_hold(unsigned* flag) {
#pragma unroll 1
  for (int spin = 0; spin < SPIN_MAX; ++spin) {
    if (__builtin_amdgcn_readfirstlane(__hip_atomic_load((lu32*)flag, __ATOMIC_RELAXED, __HIP_MEMORY_SCOPE_WORKGROUP)) == 0u) break;
    __builtin_amdgcn_s_sleep(2);
  }
}
DI unsigned lds_arrive_nw(unsigned addr) {
  unsigned old, one = 1u;
  asm volatile("s_mov_b64 exec, 1\n\tds_add_rtn_u32 %0, %1, %2\n\ts_mov_b64 exec, -1" : "=v"(old) : "v"(addr), "v"(one) : "memory");
  return old;
}
DI unsigned lds_done(unsigned old) { asm volatile("s_waitcnt lgkmcnt(0)" : "+v"(old) :: "memory"); return __builtin_amdgcn_readfirstlane(old); }
DI unsigned lds_arrive(unsigned* cnt, int) { return lds_done(lds_arrive_nw((unsigned)(size_t)(lu32*)cnt)); }
DI void lds_add(unsigned* cnt, unsigned v, int) {
  asm volatile("s_mov_b64 exec, 1\n\tds_add_u32 %0, %1\n\ts_mov_b64 exec, -1" :: "v"((unsigned)(size_t)(lu32*)cnt), "v"(v) : "memory");
}
DI void sbar4(unsigned* cnt, int lane) { if (lds_arrive(cnt, lane) < 3u) lds_wait(cnt, 4u); }
DI float rs32(float a, float b) { auto t = __builtin_amdgcn_permlane32_swap(__float_as_uint(a), __float_as_uint(b), false, false); return __uint_as_float(t[0]) + __uint_as_float(t[1]); }
DI float rs16(float a, float b) { auto t = __builtin_amdgcn_permlane16_swap(__float_as_uint(a), __float_as_uint(b), false, false); return __uint_as_float(t[0]) + __uint_as_float(t[1]); }
template <int N> DI unsigned rowbcast(unsigned x) { return (unsigned)__builtin_amdgcn_mov_dpp((int)x, 0x150 + N, 0xf, 0xf, true); }
DI void pin2(u32x4& a, u32x4& b) { asm volatile("" : "+v"(a), "+v"(b)); }
DI void use2(const u32x4& a, const u32x4& b) { asm volatile("" :: "v"(a), "v"(b)); }
DI bool nz8(const u32x4& v) { return ((v[0] | v[1]) != 0u) & ((v[2] | v[3]) != 0u); }

DI u32x4 qsum(const u32x4& g) {
  const auto ta = __builtin_amdgcn_permlane32_swap(g[0], g[0], false, false), tc = __builtin_amdgcn_permlane32_swap(g[2], g[2], false, false);
  const unsigned a = pk_bf(bflo(ta[0]) + bflo(ta[1]), bfhi(ta[0]) + bfhi(ta[1])), c = pk_bf(bflo(tc[0]) + bflo(tc[1]), bfhi(tc[0]) + bfhi(tc[1]));
  const auto ra = __builtin_amdgcn_permlane16_swap(a, a, false, false), rc = __builtin_amdgcn_permlane16_swap(c, c, false, false);
  return u32x4{ra[0], rc[0], ra[1], rc[1]};
}
DI unsigned toff(unsigned row, unsigned ch) { return 256u * row + 16u * (ch ^ (((row & 3) << 2) | ((row >> 2) & 3))); }
constexpr int SMEM = 160 * 1024;
constexpr int L_SCR = 131072;
constexpr int L_MRG = 65536;
constexpr int L_CNT = L_SCR + 32 * 1008;

struct KV { u32x4 r[8]; };
constexpr int CODE_BYTES = 43008;

template <class T> DI T* gptr(unsigned lo, unsigned hi) { return (T*)(__attribute__((address_space(1))) T*)(((u64)hi << 32) | lo); }

__global__ __launch_bounds__(512, 2) void kmain(const u16*, const u16*, const u16*, const u16*, const u16*, const u16*, const u16*, const u16*, const u16*,
                                                const u16*, const u16*, char*, u16*) {
  u32x2 kpa, kpx, kc2; u32x4 kb4; u32x8 kb8; u64 t64;
  int tid = threadIdx.x, blk = blockIdx.x;
  const auto kp = __builtin_amdgcn_kernarg_segment_ptr();
  const int o1 = blk & 24, o2 = 32 + ((blk >> 1) & 8);
  asm volatile("s_load_dwordx2 %0, %8, %9\n\ts_load_dwordx2 %1, %8, %10\n\ts_load_dwordx4 %2, %8, 0x30\n\ts_load_dwordx8 %3, %8, 0x40\n\ts_load_dwordx2 %4, %8, 0x60\n\ts_memrealtime %5"
               : "=&s"(kpa), "=&s"(kpx), "=&s"(kb4), "=&s"(kb8), "=&s"(kc2), "=&s"(t64), "+v"(tid), "+s"(blk) : "s"(kp), "s"(o1), "s"(o2));
  __builtin_assume(tid >= 0 && tid < 512 && blk >= 0 && blk < 256);
  const unsigned cpf = __builtin_amdgcn_raw_buffer_load_b32(__builtin_amdgcn_make_buffer_rsrc((void*)(__builtin_amdgcn_s_getpc() & ~255ull), 0, CODE_BYTES, 0x00020000),
                                                            (int)((tid & 0x10f) == 0x100 ? (blk >> 3) * 2048 + (tid & 0xf0) * 8 : OOB), 0, 0);
  COMPILER_FENCE();
  SCHED();
  __shared__ __attribute__((aligned(16))) char smem[SMEM];
  const int lane = tid & 63;
  const int wave = __builtin_amdgcn_readfirstlane(tid >> 6), w4 = wave & 3;
  const int cls = (blk >> 3) & 3;
  int idx = 8 * (blk & 7) + (blk >> 5);
  idx = (idx & ~7) | ((idx + ((wave >= 4 ? 0 : 0x2400) >> (4 * cls))) & 7);
  const int h = idx >> 1, b = idx & 1;
  const int r4 = lane >> 4, c16 = lane & 15;
  unsigned* cnt = reinterpret_cast<unsigned*>(smem + L_CNT);
  STAMP_DECL;
  STAMP(0);
  const unsigned c0 = cls ? 0u : ~0u;
  unsigned sa = cls ? 8192u : 4096u;
  unsigned ao = ((((b * 256 + w4 * 64) * 8192 + h * 256) & ~c0) | (((idx * 128 + w4) * 1024) & c0)) + (r4 << (17u - (c0 & 9u))) + c16 * 16, xo = (b * 256 + w4 * 64 + 16 * r4) * 2;
  asm volatile("" : "+v"(ao), "+v"(xo), "+s"(sa));
  SCHED();
  asm volatile("s_waitcnt lgkmcnt(0)" : "+s"(kpa), "+s"(kpx), "+s"(kb4), "+s"(kb8), "+s"(kc2), "+s"(t64) : "s"(kp), "s"(o1), "s"(o2));
  const unsigned tstart = (unsigned)t64;
  const char* __restrict__ pa = gptr<const char>(kpa[0], kpa[1]); const char* __restrict__ xp = gptr<const char>(kpx[0], kpx[1]);
  const u16* __restrict__ w_uv = gptr<const u16>(kb4[0], kb4[1]); const u16* __restrict__ w_o = gptr<const u16>(kb4[2], kb4[3]);
  const u16* __restrict__ kc = gptr<const u16>(kb8[0], kb8[1]); const u16* __restrict__ vc = gptr<const u16>(kb8[2], kb8[3]);
  const u16* __restrict__ wo = gptr<const u16>(kb8[4], kb8[5]); char* __restrict__ ws = gptr<char>(kb8[6], kb8[7]);
  u16* __restrict__ out = gptr<u16>(kc2[0], kc2[1]);
  const rsrc_t rs = __builtin_amdgcn_make_buffer_rsrc(ws, 0, (int)WS_END, 0x00020000);
  unsigned tag, tlow;
  u32x2 epw;
  auto take_epoch = [&]() {
    asm volatile("" : "+v"(epw) : "v"(cpf));
    const u64 t64 = (((u64)(unsigned)__builtin_amdgcn_readfirstlane(epw[1]) << 32) | (unsigned)__builtin_amdgcn_readfirstlane(epw[0])) + GOLD;
    tag = (unsigned)(t64 >> 32); tlow = (unsigned)t64;
    asm volatile("s_waitcnt lgkmcnt(0)" : "+s"(tag), "+s"(tlow) :: "memory");
  };
  if (wave >= 4) {
    if (tid - 256 < 16) cnt[tid - 256] = 0u;
    asm volatile("s_waitcnt lgkmcnt(0)" ::: "memory");
    const unsigned pTL = PAR(0, TL), pTS = PAR(1 + cls, cls == 0 ? TS0 : (cls == 1 ? TS1 : (cls == 2 ? TS2 : TS3))), pSTG = PAR(9, STG), pBSG = PAR(10, BSG);
    const unsigned pRA2 = PAR(11, RA2), pRA3 = PAR(12, RA3), pTP = PAR(15 + cls, cls == 0 ? TP0 : TP2), pPA = PAR(19 + cls, (cls & 1) ? 0u : PA);
    rawbar();
    const int e2 = (pRA2 >> (idx & 7)) & 1, e3 = (pRA3 >> (idx & 7)) & 1;
    const int ns = cls == 0 ? NS0 : (cls == 1 ? NS1 + e2 + e3 : (cls == 2 ? NS2 - e2 : NS3 - e3));
    const unsigned pTA = __builtin_amdgcn_readfirstlane((unsigned)((float)pTS * __builtin_amdgcn_rcpf((float)(ns - 5)) + 0.5f));
    const size_t kvo = ((size_t)(b * 32 + h) * SK + w4 * 16) * 256;
    const char* kb = reinterpret_cast<const char*>(kc) + kvo;
    const char* vb = reinterpret_cast<const char*>(vc) + kvo;
    const unsigned lo = lane * 16u;
    u64 tlo = cls == 0 ? TL0 : (cls == 1 ? TL1 : (cls == 2 ? TL2 : TL3));
    u64 thi = cls == 0 ? TH0 : (cls == 1 ? TH1 | ((e2 ? LA2 : LA3) << (6 * (NS1 - 10))) : (cls == 2 ? TH2 : TH3));
    const u64 tlo0 = tlo, thi0 = thi;
    int hn = 0;
    const char* hb = kb;
    const char* pc[2];
    auto next2 = [&]() {
#pragma unroll
      for (int i = 0; i < 2; ++i) {
        if (hn == ns) { tlo = tlo0; thi = thi0; hb = vb; }
        ++hn;
        pc[i] = hb + ((((unsigned)tlo + (unsigned)idx) & 63u) << 14);
        tlo = (tlo >> 6) | ((thi & 63u) << 54); thi >>= 6;
      }
    };
    KV A, B;
    u32x4 W[8];
    u32x4 T[4][8];
    auto ldp = [&](KV& S, auto P) {
      constexpr int p = P.v;
      const char* q = pc[p >> 1] + lo + (p & 1) * 2048;
      S.r[2 * p] = ld16nt(q); S.r[2 * p + 1] = ld16nt(q + 1024);
    };
    auto landed = [&](KV& S, auto P) { pin2(S.r[2 * P.v], S.r[2 * P.v + 1]); };
    KV X;
    const unsigned qo = WS_QX + ((32 + h) * 2 + b) * 1024 + lane * 16;
    u32x4 g0;
    {
      unsigned tk = 0u;
      if (w4 == 0) {
        const int si = (idx & ~7) | ((idx + (0x2400 >> (4 * cls))) & 7);
        const char* p = kb + cls * 128;
        if (lane == 1) p = cls == 2 ? reinterpret_cast<const char*>(w_uv) + (si & 1) * 2097152 + (si >> 1) * 256 : reinterpret_cast<const char*>(cls == 1 ? w_o : wo) + si * 65536;
        if (lane == 2) p = vb + cls * 128;
        if (lane == 3) p = reinterpret_cast<const char*>(wo) + h * 131072;
        if (lane < 4) tk = __builtin_nontemporal_load(reinterpret_cast<const unsigned*>(p));
      }
      lds_wait_slow(cnt + 14, 4u);
      tag = __builtin_amdgcn_readfirstlane(cnt[16]);
      asm volatile("" :: "v"(tk), "v"(cpf));
    }
    STAMP(10);
    bool noorg = w4 == 3;
    auto origin = [&]() {
#pragma unroll 1
      for (int spin = 0; spin < 2 && noorg; ++spin) {
        u32x2 t[8];
        asm volatile("s_load_dwordx2 %0, %8, 0x0 glc\n\ts_load_dwordx2 %1, %8, 0x400 glc\n\ts_load_dwordx2 %2, %8, 0x800 glc\n\ts_load_dwordx2 %3, %8, 0xc00 glc\n\t"
                     "s_load_dwordx2 %4, %8, 0x1000 glc\n\ts_load_dwordx2 %5, %8, 0x1400 glc\n\ts_load_dwordx2 %6, %8, 0x1800 glc\n\ts_load_dwordx2 %7, %8, 0x1c00 glc\n\ts_waitcnt lgkmcnt(0)"
                     : "=&s"(t[0]), "=&s"(t[1]), "=&s"(t[2]), "=&s"(t[3]), "=&s"(t[4]), "=&s"(t[5]), "=&s"(t[6]), "=&s"(t[7]) : "s"(ws + WS_T0 + ((blk >> 3) & 7) * 128) : "memory");
        unsigned e = 0u; int sum = 0;
#pragma unroll
        for (int k = 0; k < 8; ++k) { e |= t[k][1] ^ tag; sum += (int)(t[k][0] - tstart); }
        if (e == 0u) { lds_add(cnt + 13, (unsigned)min(sum >> 3, 100), lane); noorg = false; }
      }
    };
    sfor<0, 12>([&](auto I) {
      constexpr int i = I.v, j = i - 4;
      if constexpr ((i & 3) == 0) next2();
      if constexpr (i == 4) origin();
      if constexpr (j >= 0) { if constexpr (j < 4) landed(X, IC<j & 3>{}); else landed(B, IC<j & 3>{}); }
      if constexpr (j >= 0 && (j & 1)) TR();
      if constexpr (i == QPE) g0 = ld16c(rs, qo);
      if constexpr (i < 4) ldp(X, IC<i & 3>{}); else if constexpr (i < 8) ldp(B, IC<i & 3>{}); else ldp(A, IC<i & 3>{});
      SCHED();
    });
    origin();
    const char* wp = reinterpret_cast<const char*>(wo) + ((size_t)h * 128 + r4 * 8) * 1024 + w4 * 256 + c16 * 16;
    u32x4 qpk;
    {
      if (!__all((g0[1] == tag) & (g0[3] == tag))) {
#pragma unroll 1
        for (int spin = 0; spin < SPIN_MAX; ++spin) {
          COMPILER_FENCE();
          g0 = ld16c(rs, qo);
          if (__all((g0[1] == tag) & (g0[3] == tag))) break;
        }
      }
      qpk = qsum(g0);
    }
    STAMP(7);
    const bool holds = cls == 0;
    float m = 0.f, l = 0.f, acc[8];
    unsigned sc[5];
#pragma unroll
    for (int k = 0; k < 8; ++k) acc[k] = 0.f;
#pragma unroll
    for (int k = 0; k < 5; ++k) sc[k] = 0xff800000u;
    int vn = 0;
    unsigned torg, due = pTL - (unsigned)(ns - 5) * pTA - (3 - w4) * pSTG - ((idx * 5 + cls * 3) & 15) * pBSG;
    unsigned rwo;
    const unsigned zo = WS_GZ + (b * 32 + h) * 512;
    u32x2 zs[4];
    float wr_;
    auto zreq = [&]() {
      asm volatile("s_load_dwordx2 %0, %4, 0x0 glc\n\ts_load_dwordx2 %1, %4, 0x80 glc\n\ts_load_dwordx2 %2, %4, 0x100 glc\n\ts_load_dwordx2 %3, %4, 0x180 glc"
                   : "=&s"(zs[0]), "=&s"(zs[1]), "=&s"(zs[2]), "=&s"(zs[3]) : "s"(ws + zo) : "memory");
    };
    auto zget = [&]() {
#pragma unroll 1
      for (int spin = 0; spin < SPIN_MAX; ++spin) {
        asm volatile("s_waitcnt lgkmcnt(0)" : "+s"(zs[0]), "+s"(zs[1]), "+s"(zs[2]), "+s"(zs[3]) :: "memory");
        if (((zs[0][1] ^ tag) | (zs[1][1] ^ tag) | (zs[2][1] ^ tag) | (zs[3][1] ^ tag)) == 0u) break;
        __builtin_amdgcn_s_sleep(2);
        zreq();
      }
      asm volatile("s_waitcnt lgkmcnt(0)" : "+s"(zs[0]), "+s"(zs[1]), "+s"(zs[2]), "+s"(zs[3]) :: "memory");
      const float z0 = __uint_as_float(zs[0][0]), z1 = __uint_as_float(zs[1][0]), z2 = __uint_as_float(zs[2][0]), z3 = __uint_as_float(zs[3][0]);
      const float Z = fmaxf(fmaxf(z0, z1), fmaxf(z2, z3));
      wr_ = ex2(fmaf(m, C2, -Z)) * __builtin_amdgcn_rcpf((ex2(z0 - Z) + ex2(z1 - Z)) + (ex2(z2 - Z) + ex2(z3 - Z)));
      asm volatile("" : "+v"(wr_));
    };
    auto qk = [&](const u32x4* K, auto I) {
      float x[4];
#pragma unroll
      for (int t = 0; t < 4; ++t) {
        x[t] = 0.f;
#pragma unroll
        for (int k = 0; k < 4; ++k) x[t] = dot2(K[t][k], qpk[k], x[t]);
      }
      const float u0 = (c16 & 1 ? x[1] : x[0]) + dpp<0xB1>(c16 & 1 ? x[0] : x[1]), u1 = (c16 & 1 ? x[3] : x[2]) + dpp<0xB1>(c16 & 1 ? x[2] : x[3]);
      float v = (c16 & 2 ? u1 : u0) + dpp<0x4E>(c16 & 2 ? u0 : u1);
      v += dpp<0x124>(v); v += dpp<0x128>(v);
      sc[I.v >> 2] = __builtin_amdgcn_update_dpp(sc[I.v >> 2], __float_as_uint(v), 0xE4, 0xf, 1 << (I.v & 3), false);
      asm volatile("" : "+v"(sc[I.v >> 2]));
    };
    auto soft = [&]() {
      m = row16_max(fmaxf(fmaxf(fmaxf(__uint_as_float(sc[0]), __uint_as_float(sc[1])), fmaxf(__uint_as_float(sc[2]), __uint_as_float(sc[3]))), __uint_as_float(sc[4])));
      const float mc = m * C2;
      l = 0.f;
#pragma unroll
      for (int k = 0; k < 5; ++k) {
        const float p = ex2(fmaf(__uint_as_float(sc[k]), C2, -mc));
        sc[k] = pk_bf(p, dpp<0xB1>(p));
        l = dot2(sc[k], 0x3f003f00u, l);
      }
      l = row16_sum(l);
    };
    auto pv = [&](const u32x4* V) {
      const unsigned p01 = rowbcast<0>(sc[0]), p23 = rowbcast<2>(sc[0]);
#pragma unroll
      for (int k = 0; k < 4; ++k) { acc[2 * k] = dot2(plo(V[0][k], V[1][k]), p01, acc[2 * k]); acc[2 * k + 1] = dot2(phi(V[0][k], V[1][k]), p01, acc[2 * k + 1]); }
#pragma unroll
      for (int k = 0; k < 4; ++k) { acc[2 * k] = dot2(plo(V[2][k], V[3][k]), p23, acc[2 * k]); acc[2 * k + 1] = dot2(phi(V[2][k], V[3][k]), p23, acc[2 * k + 1]); }
      sc[0] = (unsigned)__builtin_amdgcn_mov_dpp((int)sc[0], 0x12C, 0xf, 0xf, true);
      if ((++vn & 3) == 0) { sc[0] = sc[1]; sc[1] = sc[2]; sc[2] = sc[3]; sc[3] = sc[4]; }
      asm volatile("" : "+v"(acc[0]), "+v"(acc[1]), "+v"(acc[2]), "+v"(acc[3]), "+v"(acc[4]), "+v"(acc[5]), "+v"(acc[6]), "+v"(acc[7]));
    };
    auto step = [&](KV& S, KV& N, auto SI, auto REFILL, auto WR) {
      constexpr bool refill = REFILL.v == 1;
      constexpr int wr = WR.v;
      if constexpr (refill) next2();
      if constexpr (refill && SI.v >= 3) { until((int)(due - torg) < (int)pTP ? due - pPA : due); due += pTA; }
      constexpr int ah = wr < 0 ? AHL : 0;
      auto point = [&](auto P) {
        if constexpr (refill) {
          if constexpr (P.v == 0 && ah > 0) { if constexpr (wr >= 0) { if (holds) lds_hold(cnt + 8); } sfor<0, ah>([&](auto Q) { ldp(N, Q); }); SCHED(); }
          landed(S, P);
          if constexpr (P.v & 1) TR();
          if constexpr (P.v + ah < 4) { if constexpr ((P.v & 1) == 0 && wr >= 0) { if (holds) lds_hold(cnt + 8); } ldp(N, IC<(P.v + ah) & 3>{}); }
          if constexpr (wr >= 0 && wr < 8) W[(4 * wr + P.v) & 7] = ld16(wp + (((4 * wr + P.v) >> 3) * 32 + ((4 * wr + P.v) & 7)) * 1024);
          SCHED();
        }
      };
      sfor<0, 2>([&](auto H) {
        constexpr int i = 2 * SI.v + H.v;
        if constexpr (REFILL.v == 2 && H.v == 0) { zreq(); SCHED(); }
        point(IC<2 * H.v>{});
        point(IC<2 * H.v + 1>{});
        if constexpr (i < NSMIN) qk(S.r + 4 * H.v, IC<i>{});
        else if constexpr (i > NSMAX) pv(S.r + 4 * H.v);
        else {
          if (i < ns) { if constexpr (i < NSMAX) qk(S.r + 4 * H.v, IC<i>{}); }
          else { if (i == ns) soft(); pv(S.r + 4 * H.v); }
        }
        SCHED();
      });
      if constexpr (REFILL.v == 2) {
        zget();
        STAMP(1);
        *reinterpret_cast<u32x4*>(smem + rwo) = u32x4{pk_bf(acc[0] * wr_, acc[1] * wr_), pk_bf(acc[2] * wr_, acc[3] * wr_), pk_bf(acc[4] * wr_, acc[5] * wr_), pk_bf(acc[6] * wr_, acc[7] * wr_)};
        COMPILER_FENCE();
      }
      if constexpr (wr >= 1) {
        constexpr int t = (wr - 1) >> 1, j0 = ((wr - 1) & 1) * 2;
#pragma unroll
        for (int jj = 0; jj < 2; ++jj)
#pragma unroll
          for (int e = 0; e < 4; ++e) {
            const unsigned a_ = W[(4 * wr - 4 + 2 * jj) & 7][e], b_ = W[(4 * wr - 3 + 2 * jj) & 7][e];
            T[t][2 * e][j0 + jj] = plo(a_, b_); T[t][2 * e + 1][j0 + jj] = phi(a_, b_);
          }
#pragma unroll
        for (int e = 0; e < 8; e += 2) pin2(T[t][e], T[t][e + 1]);
      }
      SCHED();
    };
    step(B, B, IC<1>{}, IC<0>{}, IC<-1>{});
    step(A, B, IC<2>{}, IC<1>{}, IC<0>{});
    step(X, X, IC<0>{}, IC<0>{}, IC<-1>{});
    torg = tstart + __builtin_amdgcn_readfirstlane(cnt[13]); due += torg;
    sfor<0, 4>([&](auto I) {
      step(B, A, IC<2 * I.v + 3>{}, IC<1>{}, IC<2 * I.v + 1>{});
      if constexpr (I.v == 3) {
        float* ml = reinterpret_cast<float*>(cnt + 32);
        ml[(c16 & 1) * 16 + w4 * 4 + r4] = (c16 & 1) ? l : m;
        COMPILER_FENCE();
        const unsigned got = lds_arrive_nw(L_CNT + 44);
        const float mr = ml[c16], lr = ml[16 + c16];
        SCHED();
        if (lds_done(got) == 3u) {
          const float M = row16_max(mr);
          const unsigned z = __float_as_uint(fmaf(M, C2, __builtin_amdgcn_logf(row16_sum(ex2((mr - M) * C2) * lr))));
          asm volatile("buffer_store_dwordx4 %0, %1, %2, 0 offen sc1\n\ts_nop 2" :: "v"(u32x4{z, tag, z, tag}), "v"(lane < 8 ? zo + cls * 128 + lane * 16 : OOB), "s"(rs) : "memory");
        }
        SCHED();
      }
      step(A, B, IC<2 * I.v + 4>{}, IC<1>{}, IC<2 * I.v + 2>{});
    });
    unsigned yoff = WS_GB + ((b * 128 + h * 4 + cls) * 256 + 64 * w4 + lane) * 8, rro = L_MRG + c16 * 272 + r4 * 16;
    rwo = L_MRG + (w4 * 4 + r4) * 272 + c16 * 16;
    asm volatile("" : "+v"(yoff), "+v"(rwo), "+v"(rro));
    if (cls == 0) lds_wait(cnt + 1, 4u);
    const int nit = (ns - 12) >> 1;
#pragma unroll 1
    for (int i = 0; i < nit; ++i) {
      step(B, A, IC<11>{}, IC<1>{}, IC<-1>{});
      step(A, B, IC<11>{}, IC<1>{}, IC<-1>{});
    }
    if (ns & 1) { step(B, A, IC<11>{}, IC<1>{}, IC<-1>{}); step(A, A, IC<11>{}, IC<2>{}, IC<-1>{}); }
    else step(B, B, IC<11>{}, IC<2>{}, IC<-1>{});
    {
      u32x4 af[4];
      unsigned got = lds_arrive_nw(L_CNT), need = 3u;
#pragma unroll 1
      for (int spin = 0; spin < SPIN_MAX; ++spin) {
#pragma unroll
        for (int t = 0; t < 4; ++t) af[t] = *reinterpret_cast<const u32x4*>(smem + rro + t * 64);
        SCHED();
        if (lds_done(got) >= need) break;
        need = 4u;
        __builtin_amdgcn_s_sleep(1);
        asm volatile("ds_read_b32 %0, %1" : "=v"(got) : "v"((unsigned)L_CNT) : "memory");
      }
      SCHED();
      f32x4 d[8];
      float a[8], v4[4];
      sfor<0, 5>([&](auto I) {
        constexpr int i = I.v;
        sfor<0, 8>([&](auto J) {
          constexpr int e = i + 4 * (J.v & 1), t = J.v >> 1;
          if constexpr (i < 4) { d[e] = __builtin_amdgcn_mfma_f32_16x16x32_bf16(__builtin_bit_cast(bf16x8_t, af[t]), __builtin_bit_cast(bf16x8_t, T[t][e]), t ? d[e] : f32x4{0.f, 0.f, 0.f, 0.f}, 0, 0, 0); SCHED(); }
          if constexpr (i >= 1 && (J.v == 4 || J.v == 5)) { const f32x2 u = f32x2{d[e - 1][0], d[e - 1][1]} + f32x2{d[e - 1][2], d[e - 1][3]}; a[e - 1] = u[0] + u[1]; asm volatile("" : "+v"(a[e - 1])); }
          if constexpr (i >= 1 && J.v == 6) v4[i - 1] = rs32(a[i - 1], a[i + 3]);
          if constexpr (i < 4) SCHED();
        });
      });
      st8c(rs, yoff, u32x2{pk_bf(rs16(v4[0], v4[2]), rs16(v4[1], v4[3])), tag});
    }
    STAMP(2);
    STAMP_FLUSH(1);
    TR_FLUSH();
    return;
  }
  const rsrc_t ro = __builtin_amdgcn_make_buffer_rsrc(out, 0, 4096, 0x00020000);
  auto fin = [&](auto NL, unsigned base, unsigned col, bool bump) {
    constexpr int nl = NL.v;
    unsigned doff = base + (((idx >> 5) * 4 + w4) * 16 * nl + (lane >> 2)) * 2048 + (idx & 31) * 64 + (lane & 3) * 16;
    unsigned fwo = 32768 + (c16 < 4 ? (w4 * 16 + 4 * c16 + r4) * 4 : 256 + lane * 4);
    unsigned fro = 32768 + (lane & 7) * 8, foo = lane < 8 ? ((idx >> 5) * 1024 + col + (nl == 2 ? 16 * (idx & 24) + 64 * (idx & 1) + (idx & 6) + 8 * lane : 16 * (idx & 31) + 2 * lane)) * 2 : OOB;
    asm volatile("" : "+v"(doff), "+v"(fwo), "+v"(fro), "+v"(foo));
    lds_wait(cnt + 0, 4u);
    until(tstart + __builtin_amdgcn_readfirstlane(cnt[13]) + PAR(0, TL) + PAR(4 + nl, TF));
    STAMP(4);
    u32x4 d[nl];
    unsigned eh = 1u;
#pragma unroll 1
    for (int spin = SPIN_MAX; spin > 0; --spin) {
      COMPILER_FENCE();
      if (eh != 0u) {
#pragma unroll
        for (int j = 0; j < nl; ++j) d[j] = ld16c(rs, doff + j * 32768);
        eh = 0u;
#pragma unroll
        for (int j = 0; j < nl; ++j) { eh |= (d[j][1] ^ tag) | (d[j][3] ^ tag); asm volatile("" : "+v"(eh)); }
      }
      if (__builtin_amdgcn_ballot_w64(eh != 0u) == 0ull) break;
    }
    STAMP(11);
    float y[4] = {bflo(d[0][0]), bfhi(d[0][0]), bflo(d[0][2]), bfhi(d[0][2])};
    if constexpr (nl == 2) { y[0] += bflo(d[1][0]); y[1] += bfhi(d[1][0]); y[2] += bflo(d[1][2]); y[3] += bfhi(d[1][2]); }
    float t = rs16(rs32(y[0], y[2]), rs32(y[1], y[3]));
    t += dpp<0x124>(t); t += dpp<0x128>(t);
    *reinterpret_cast<float*>(smem + fwo) = t;
    COMPILER_FENCE();
    const unsigned got = lds_arrive_nw(L_CNT + 24);
    f32x2 t2[4];
#pragma unroll
    for (int w = 0; w < 4; ++w) t2[w] = *reinterpret_cast<const f32x2*>(smem + fro + w * 64);
    SCHED();
    if (lds_done(got) == 3u) {
      const f32x2 r = (t2[0] + t2[1]) + (t2[2] + t2[3]);
      __builtin_amdgcn_raw_buffer_store_b32(pk_bf(r[0], r[1]), ro, (int)foo, 0, 0);
      if (bump) st8c(rs, lane == 0 ? WS_EP : OOB, u32x2{tlow, tag});
    }
  };
  u32x4 Wa[16], Wb[16], xv[4];
  auto row = [&](u32x4& d) { d = ld16nt(pa + ao); SCHED(); ao += sa; asm volatile("" : "+v"(ao)); };
  sfor<0, 2>([&](auto I) { row(Wa[I.v]); });
  sfor<0, 4>([&](auto J) { xv[J.v] = ld16(xp + xo + (J.v >> 1) * 1024 + 16 * (J.v & 1)); });
  SCHED();
  sfor<2, 8>([&](auto I) { row(Wa[I.v]); });
  epw = ld8c(rs, WS_EP);
  st16c(rs, cls == 1 && w4 == 0 ? WS_QA + (b * 32 + h) * 1024 + lane * 16 : OOB, u32x4{0u, 0u, 0u, 0u});
  SCHED();
  auto sync = [&]() {
    rawbar();
    take_epoch();
    cnt[16] = tag;
    COMPILER_FENCE();
  };
  if (cls == 0) {
    sfor<8, 16>([&](auto I) { row(Wa[I.v]); });
    sync();
    asm volatile("" :: "v"(xv[0]), "v"(xv[1]), "v"(xv[2]), "v"(xv[3]));
    STAMP(13);
    char* scr = smem + L_SCR;
    const int ab = idx >> 5, r = lane & 31, hh = lane >> 5;
    const unsigned fo = WS_QX + (w4 * 8 + (c16 >> 1)) * 2048 + (c16 & 1) * 512 + r4 * 8;
    u32x2 fl;
    u32x4 g[9];
    const unsigned o8 = WS_QA + (ab * 32 + w4 * 8) * 1024 + lane * 16, ol = WS_QA + (ab * 32 + r) * 1024 + 992 + 16 * hh;
    auto put = [&](int i, const u32x4& v) { *reinterpret_cast<u32x4*>(smem + ((i >> 3) * 4 + r4) * 8192 + toff(4 * (i & 7) + w4, c16)) = v; };
    sfor<0, 16>([&](auto I) { put(I.v, Wa[I.v]); if constexpr ((I.v & 3) == 3) TR(); SCHED(); });
    until(tstart + PAR(26, T0B));
    sfor<0, 16>([&](auto I) { row(Wb[I.v]); });
    lds_add(cnt + 14, 1u, lane);
    SCHED();
    sfor<0, 16>([&](auto I) { put(16 + I.v, Wb[I.v]); if constexpr (I.v == 12) fl = ld8c(rs, fo); if constexpr ((I.v & 3) == 3) TR(); SCHED(); });
    STAMP(8);
    u32x4 qlast;
    {
      if (!__all(fl[1] == tag)) {
#pragma unroll 1
        for (int spin = 0; spin < SPIN_MAX; ++spin) {
          COMPILER_FENCE();
          if (__all(ld8c(rs, fo)[1] == tag)) break;
          __builtin_amdgcn_s_sleep(8);
        }
      }
      until(tstart + PAR(27, T0Q));
      STAMP(5);
      sbar4(cnt + 7, lane);
      STAMP(12);
      unsigned pend = 0x1ffu;
#pragma unroll 1
      for (int spin = 0; spin < SPIN_MAX && pend; ++spin) {
        COMPILER_FENCE();
        u32x4 t[9];
#pragma unroll
        for (int i = 0; i < 9; ++i) t[i] = ld16c(rs, (pend >> i & 1) ? (i < 8 ? o8 + i * 1024 : ol) : OOB);
#pragma unroll
        for (int i = 0; i < 9; ++i) {
          if (pend >> i & 1) g[i] = t[i];
          if (__all(nz8(t[i]))) pend &= ~(1u << i);
        }
      }
      STAMP(9);
      qlast = g[8];
#pragma unroll
      for (int i = 0; i < 8; ++i)
        if (lane < 62) *reinterpret_cast<u32x4*>(scr + (w4 * 8 + i) * 1008 + 16 * lane) = g[i];
    }
    sbar4(cnt + 3, lane);
    STAMP(10);
    f32x16 sacc;
#pragma unroll
    for (int i = 0; i < 16; ++i) sacc[i] = 0.f;
#pragma unroll
    for (int jj = 0; jj < 32; ++jj) {
      const u32x4 af = *reinterpret_cast<const u32x4*>(smem + (w4 * 4 + (jj >> 3)) * 8192 + toff(r, 2 * (jj & 7) + hh));
      const u32x4 bf = jj < 31 ? *reinterpret_cast<const u32x4*>(scr + r * 1008 + 16 * (2 * (jj < 31 ? jj : 0) + hh)) : qlast;
      sacc = __builtin_amdgcn_mfma_f32_32x32x16_bf16(__builtin_bit_cast(bf16x8_t, af), __builtin_bit_cast(bf16x8_t, bf), sacc, 0, 0, 0);
    }
    float m = sacc[0];
#pragma unroll
    for (int i = 1; i < 16; ++i) m = fmaxf(m, sacc[i]);
    m = xhalf_max(m);
    if (hh == 0) *reinterpret_cast<float*>(scr + r * 1008 + 992 + 4 * w4) = m;
    sbar4(cnt + 4, lane);
    STAMP(11);
    {
      const f32x4 m4 = *reinterpret_cast<const f32x4*>(scr + r * 1008 + 992);
      m = fmaxf(fmaxf(m4[0], m4[1]), fmaxf(m4[2], m4[3]));
    }
    const float mc = m * C2;
    float p[16], ls = 0.f;
#pragma unroll
    for (int i = 0; i < 16; ++i) { p[i] = ex2(fmaf(sacc[i], C2, -mc)); ls += p[i]; }
    ls = xhalf_sum(ls);
    sbar4(cnt + 5, lane);
    u32x4* pf = reinterpret_cast<u32x4*>(scr);
    float* wl = reinterpret_cast<float*>(scr + 8192);
    if (hh == 0) wl[r * 4 + w4] = ls;
    pf[(2 * w4) * 64 + lane] = u32x4{pk_bf(p[0], p[1]), pk_bf(p[2], p[3]), pk_bf(p[4], p[5]), pk_bf(p[6], p[7])};
    pf[(2 * w4 + 1) * 64 + lane] = u32x4{pk_bf(p[8], p[9]), pk_bf(p[10], p[11]), pk_bf(p[12], p[13]), pk_bf(p[14], p[15])};
    sbar4(cnt + 6, lane);
    u32x4 pb8[8];
#pragma unroll
    for (int ks = 0; ks < 8; ++ks) pb8[ks] = pf[ks * 64 + lane];
    const f32x4 l4 = *reinterpret_cast<const f32x4*>(wl + r * 4);
    const float lsum = (l4[0] + l4[1]) + (l4[2] + l4[3]), rl = __builtin_amdgcn_rcpf(lsum);
    const int qq = (lane & 15) >> 2, pp = lane & 3, blk2 = (lane >> 4) & 1;
    const unsigned tw = tstart + __builtin_amdgcn_readfirstlane(cnt[13]) + PAR(13, TW), th = tw - PAR(14, HL);
    u32x4 vv[2][4];
    bool held = false;
    const int hx = (lane >> 3) ^ (idx & 7);
    char* ot = scr + 12288 + w4 * 4096;
#pragma unroll
    for (int c2 = 0; c2 < 2; ++c2) {
      f32x16 oacc[2];
#pragma unroll
      for (int ct = 0; ct < 2; ++ct)
#pragma unroll
        for (int i = 0; i < 16; ++i) oacc[ct][i] = 0.f;
#pragma unroll
      for (int ks = 0; ks < 8; ++ks)
#pragma unroll
        for (int ct = 0; ct < 2; ++ct) {
          typedef __attribute__((address_space(3))) s16x4* lp;
          const char* tile = smem + ((ks >> 1) * 4 + w4) * 8192;
          const int cc = 2 * c2 + ct;
          const s16x4 lo = __builtin_amdgcn_ds_read_tr16_b64_v4i16((lp)(tile + toff(16 * (ks & 1) + 4 * hh + qq, 4 * cc + 2 * blk2 + (pp >> 1)) + 8 * (pp & 1)));
          const s16x4 hi = __builtin_amdgcn_ds_read_tr16_b64_v4i16((lp)(tile + toff(16 * (ks & 1) + 8 + 4 * hh + qq, 4 * cc + 2 * blk2 + (pp >> 1)) + 8 * (pp & 1)));
          const s16x8 afr = __builtin_shufflevector(lo, hi, 0, 1, 2, 3, 4, 5, 6, 7);
          oacc[ct] = __builtin_amdgcn_mfma_f32_32x32x16_bf16(__builtin_bit_cast(bf16x8_t, afr), __builtin_bit_cast(bf16x8_t, pb8[ks]), oacc[ct], 0, 0, 0);
        }
#pragma unroll
      for (int ct = 0; ct < 2; ++ct)
#pragma unroll
        for (int g = 0; g < 4; ++g)
          *reinterpret_cast<u32x2*>(ot + r * 128 + 16 * ((4 * ct + g) ^ (r & 7)) + 8 * hh) = u32x2{pk_bf(oacc[ct][4 * g] * rl, oacc[ct][4 * g + 1] * rl), pk_bf(oacc[ct][4 * g + 2] * rl, oacc[ct][4 * g + 3] * rl)};
      if (c2 == 0) { held = (int)((unsigned)wall_clock64() + 30u - th) >= 0; if (held) lds_add(cnt + 8, 1u, lane); }
      __builtin_amdgcn_wave_barrier();
#pragma unroll
      for (int i = 0; i < 4; ++i) { vv[c2][i] = *reinterpret_cast<const u32x4*>(ot + (8 * i + hx) * 128 + 16 * ((lane & 7) ^ hx)); asm volatile("" : "+v"(vv[c2][i])); }
      __builtin_amdgcn_wave_barrier();
    }
    lds_arrive(cnt + 1, lane);
    if (!held) { until(th); lds_add(cnt + 8, 1u, lane); }
    until(tw);
#pragma unroll
    for (int i = 0; i < 8; ++i) st16c(rs, WS_APO + idx * 32768 + w4 * 8192 + i * 1024 + lane * 16, vv[i & 1][i >> 1]);
    STAMP(1);
    asm volatile("s_waitcnt vmcnt(0)" ::: "memory");
    if (lds_arrive(cnt + 9 + (w4 >> 1), lane) == 1u && hh == 0)
      st8c(rs, WS_AML + ((r * 2 + (w4 >> 1)) * 16 + (idx >> 2)) * 128 + (idx & 3) * 8, u32x2{__float_as_uint(mc + __builtin_amdgcn_logf(lsum)), tag});
    lds_add(cnt + 8, ~0u, lane);
    STAMP(2);
    STAMP_FLUSH(0); TR_FLUSH();
    return;
  }
  sfor<8, 16>([&](auto I) { row(Wa[I.v]); });
  const u16* pb = cls == 2 ? w_uv + ((size_t)(b * 256 + w4 * 64 + 16 * r4) * 32 + h) * 128 + 8 * c16 : (cls == 1 ? w_o : wo) + ((size_t)h * 128 + b * 64 + w4 * 2) * 512 + 8 * lane;
  sync();
  if (blk < 16 && w4 == 0) asm volatile("buffer_store_dwordx4 %0, %1, %2, 0 offen sc1\n\ts_nop 2" :: "v"(u32x4{tstart, tag, tstart, tag}), "v"(WS_T0 + (blk & 7) * 1024 + lane * 16), "s"(rs) : "memory");
  STAMP(13);
  float* red = reinterpret_cast<float*>(smem);
  if (cls & 1) {
    float acc[2][8];
#pragma unroll
    for (int bb = 0; bb < 2; ++bb)
#pragma unroll
      for (int k = 0; k < 8; ++k) acc[bb][k] = 0.f;
    sfor<0, 8>([&](auto IP) {
      constexpr int ip = IP.v, j = ip - (8 - WBW);
      use2(Wa[2 * ip], Wa[2 * ip + 1]);
      if constexpr (ip & 1) TR();
      if constexpr (j >= 0) { Wb[2 * j] = ld16(pb + j * 4096); Wb[2 * j + 1] = ld16(pb + j * 4096 + 512); }
      SCHED();
      const unsigned xp0 = xv[ip >> 2][ip & 3], xp1 = xv[2 + (ip >> 2)][ip & 3];
#pragma unroll
      for (int k = 0; k < 4; ++k) {
        const unsigned lo = plo(Wa[2 * ip][k], Wa[2 * ip + 1][k]), hi = phi(Wa[2 * ip][k], Wa[2 * ip + 1][k]);
        acc[0][2 * k] = dot2(lo, xp0, acc[0][2 * k]); acc[0][2 * k + 1] = dot2(hi, xp0, acc[0][2 * k + 1]);
        acc[1][2 * k] = dot2(lo, xp1, acc[1][2 * k]); acc[1][2 * k + 1] = dot2(hi, xp1, acc[1][2 * k + 1]);
      }
      SCHED();
    });
    {
      float v8[8], v4[4];
#pragma unroll
      for (int k = 0; k < 8; ++k) v8[k] = rs32(acc[0][k], acc[1][k]);
#pragma unroll
      for (int k = 0; k < 4; ++k) v4[k] = rs16(v8[k], v8[k + 4]);
      *reinterpret_cast<f32x4*>(red + (w4 * 2 + (lane >> 5)) * 128 + 8 * c16 + 4 * (r4 & 1)) = f32x4{v4[0], v4[1], v4[2], v4[3]};
    }
    COMPILER_FENCE();
    const unsigned got = lds_arrive_nw(L_CNT + 8);
    f32x4 t4[4];
#pragma unroll
    for (int w = 0; w < 4; ++w) t4[w] = *reinterpret_cast<const f32x4*>(red + (w * 2 + (lane >> 5)) * 128 + 8 * c16 + 4 * (r4 & 1));
    SCHED();
    if (lds_done(got) == 3u) {
      const f32x4 t = (t4[0] + t4[1]) + (t4[2] + t4[3]);
      st16c(rs, WS_QX + (((cls >> 1) * 32 + h) * 2 + (lane >> 5)) * 1024 + b * 512 + (lane & 31) * 16, u32x4{pk_bf(t[0], t[1]), tag, pk_bf(t[2], t[3]), tag});
      STAMP(15);
      SCHED();
      lds_add(cnt + 2, 1u, lane);
    } else lds_wait(cnt + 2, 5u);
    sfor<WBW, 8>([&](auto J) {
      constexpr int j = J.v;
      use2(Wb[2 * (j - WBW)], Wb[2 * (j - WBW) + 1]);
      if constexpr ((j - WBW) & 1) TR();
      Wb[2 * j] = ld16(pb + j * 4096); Wb[2 * j + 1] = ld16(pb + j * 4096 + 512);
      if constexpr (j == 7) lds_add(cnt + 14, 1u, lane);
      SCHED();
    });
    {
      const unsigned tw = __builtin_nontemporal_load(reinterpret_cast<const unsigned*>(out + (idx >> 5) * 1024 + (cls >> 1) * 512 + 16 * (idx & 31)));
      sfor<8 - WBW, 8>([&](auto J) { use2(Wb[2 * J.v], Wb[2 * J.v + 1]); if constexpr (J.v & 1) TR(); });
      asm volatile("" :: "v"(tw));
    }
    STAMP(8);
  } else {
    sfor<0, 16 - WBW>([&](auto IP) {
      constexpr int ip = IP.v, j = ip - (8 - WBW);
      if constexpr (ip < 8) use2(Wa[2 * ip], Wa[2 * ip + 1]); else use2(Wb[2 * ip - 16], Wb[2 * ip - 15]);
      if constexpr (ip & 1) TR();
      if constexpr (j >= 0) { Wb[2 * j] = ld16nt(pb + j * 8192); Wb[2 * j + 1] = ld16nt(pb + j * 8192 + 4096); }
      SCHED();
    });
    u32x4 g0, g1;
    const unsigned qo = WS_QX + h * 2048 + lane * 16;
    if (w4 == 0) { shint<512, 0>(ws + WS_QX + h * 2048, tag); lds_add(cnt + 2, 1u, lane); } else lds_wait(cnt + 2, 1u);
    STAMP(5);
#pragma unroll 1
    for (int spin = 0; spin < SPIN_MAX; ++spin) {
      COMPILER_FENCE();
      g0 = ld16c(rs, qo); g1 = ld16c(rs, qo + 1024);
      SCHED();
      lds_add(cnt + 14, spin == 0, lane);
      SCHED();
      if (__all((g0[1] == tag) & (g0[3] == tag) & (g1[1] == tag) & (g1[3] == tag))) break;
      __builtin_amdgcn_s_sleep(1);
    }
    sfor<8 - WBW, 8>([&](auto J) { if constexpr (J.v & 1) TR(); });
    const u32x4 q0 = qsum(g0), q1 = qsum(g1);
    STAMP(9);
    float v[8];
    {
      float u[16];
#pragma unroll
      for (int i = 0; i < 16; ++i) {
        float a = 0.f, c = 0.f;
#pragma unroll
        for (int k = 0; k < 4; ++k) { a = dot2(Wa[i][k], q0[k], a); c = dot2(Wa[i][k], q1[k], c); }
        u[i] = (c16 & 2 ? c : a) + dpp<0x4E>(c16 & 2 ? a : c);
        asm volatile("" : "+v"(u[i]));
      }
#pragma unroll
      for (int i = 0; i < 8; ++i) {
        float t = (c16 & 1 ? u[i + 8] : u[i]) + dpp<0xB1>(c16 & 1 ? u[i] : u[i + 8]);
        asm volatile("" : "+v"(t));
        t += dpp<0x124>(t);
        asm volatile("" : "+v"(t));
        t += dpp<0x128>(t);
        asm volatile("" : "+v"(t));
        v[i] = t;
      }
    }
    u32x4 o = {pk_bf(v[0], v[1]), pk_bf(v[2], v[3]), pk_bf(v[4], v[5]), pk_bf(v[6], v[7])};
    if ((o[0] | o[1]) == 0u) o[0] = 0x8000u;
    if ((o[2] | o[3]) == 0u) o[2] = 0x8000u;
    if (c16 < 4) st16c(rs, WS_QA + (((c16 >> 1) & 1) * 32 + h) * 1024 + (b * 256 + w4 * 64 + 16 * r4 + 8 * (c16 & 1)) * 2, o);
    STAMP(10);
  }
  asm volatile("" :: "v"(xv[0]), "v"(xv[1]), "v"(xv[2]), "v"(xv[3]));
  SCHED();
  if (cls == 3) {
    fin(IC<2>{}, WS_GB, 512, false);
    STAMP(6);
    STAMP_FLUSH(0); TR_FLUSH();
    return;
  }
#pragma unroll
  for (int ip = 0; ip < 8; ++ip) {
#pragma unroll
    for (int k = 0; k < 4; ++k) { const unsigned lo = plo(Wb[2 * ip][k], Wb[2 * ip + 1][k]), hi = phi(Wb[2 * ip][k], Wb[2 * ip + 1][k]); Wb[2 * ip][k] = lo; Wb[2 * ip + 1][k] = hi; }
    pin2(Wb[2 * ip], Wb[2 * ip + 1]);
  }
  if (cls == 2) {
    float* red2 = red + 4 * 256;
    float* wml = red + 3072;
    u32x8 f[4];
    until<6>(tstart + PAR(13, TW) + PAR(28, 100));
#pragma unroll 1
    for (int spin = 0; spin < SPIN_MAX; ++spin) {
      sld4x32(ws + WS_AML + ((h * 2 + b) * 16 + w4 * 4) * 128, f);
      unsigned e = 0u;
#pragma unroll
      for (int i = 0; i < 16; ++i) e |= f[i >> 2][2 * (i & 3) + 1] ^ tag;
      if (e == 0u) break;
      __builtin_amdgcn_s_sleep(2);
    }
    STAMP(3);
    u32x4 pp[8];
#pragma unroll
    for (int i = 0; i < 8; ++i) pp[i] = ld16c(rs, WS_APO + (w4 * 16 + 2 * i + (lane >> 5)) * 32768 + (2 * b + (r4 & 1)) * 8192 + (2 * (h >> 3) + ((lane >> 3) & 1)) * 1024 + ((h ^ (2 * i + (lane >> 5))) & 7) * 128 + (lane & 7) * 16);
    SCHED();
    float wt[8];
    {
      float M = -INFINITY, L = 0.f;
#pragma unroll
      for (int i = 0; i < 8; ++i) {
        unsigned z0 = f[i >> 1][4 * (i & 1)], z1 = f[i >> 1][4 * (i & 1) + 2];
        asm("" : "+s"(z0), "+s"(z1));
        wt[i] = __uint_as_float(lane < 32 ? z0 : z1); M = fmaxf(M, wt[i]);
      }
      M = xhalf_max(M);
#pragma unroll
      for (int i = 0; i < 8; ++i) { wt[i] = ex2(wt[i] - M); L += wt[i]; }
      L = xhalf_sum(L);
      if (lane == 0) *reinterpret_cast<f32x2*>(wml + 2 * w4) = f32x2{M, L};
    }
    SCHED();
    float a8[8];
#pragma unroll
    for (int k = 0; k < 8; ++k) a8[k] = 0.f;
#pragma unroll
    for (int i = 0; i < 8; ++i) {
#pragma unroll
      for (int k = 0; k < 4; ++k) { a8[2 * k] = fmaf(wt[i], bflo(pp[i][k]), a8[2 * k]); a8[2 * k + 1] = fmaf(wt[i], bfhi(pp[i][k]), a8[2 * k + 1]); }
      asm volatile("" : "+v"(a8[0]), "+v"(a8[1]), "+v"(a8[2]), "+v"(a8[3]), "+v"(a8[4]), "+v"(a8[5]), "+v"(a8[6]), "+v"(a8[7]));
    }
    STAMP(6);
    *reinterpret_cast<f32x4*>(red + w4 * 256 + 8 * (lane & 31) + 4 * (lane >> 5)) = f32x4{rs32(a8[0], a8[4]), rs32(a8[1], a8[5]), rs32(a8[2], a8[6]), rs32(a8[3], a8[7])};
    sbar4(cnt + 3, lane);
    STAMP(4);
    float acc[2][8];
#pragma unroll
    for (int bb = 0; bb < 2; ++bb) {
      u32x4 cv[2];
      const f32x4 ml = *reinterpret_cast<const f32x4*>(wml + 4 * bb);
      const float M = fmaxf(ml[0], ml[2]), e0 = ex2(ml[0] - M), e1 = ex2(ml[2] - M), rl = 1.f / fmaf(e0, ml[1], e1 * ml[3]), s0 = e0 * rl, s1 = e1 * rl;
#pragma unroll
      for (int e = 0; e < 2; ++e) {
        const float* src = red + bb * 512 + w4 * 64 + 16 * r4 + 8 * e;
        const f32x4 t0 = s0 * *reinterpret_cast<const f32x4*>(src) + s1 * *reinterpret_cast<const f32x4*>(src + 256), t1 = s0 * *reinterpret_cast<const f32x4*>(src + 4) + s1 * *reinterpret_cast<const f32x4*>(src + 260);
        cv[e] = u32x4{pk_bf(t0[0], t0[1]), pk_bf(t0[2], t0[3]), pk_bf(t1[0], t1[1]), pk_bf(t1[2], t1[3])};
      }
#pragma unroll
      for (int k = 0; k < 8; ++k) acc[bb][k] = 0.f;
#pragma unroll
      for (int ip = 0; ip < 8; ++ip)
#pragma unroll
        for (int k = 0; k < 4; ++k) { acc[bb][2 * k] = dot2(Wb[2 * ip][k], cv[ip >> 2][ip & 3], acc[bb][2 * k]); acc[bb][2 * k + 1] = dot2(Wb[2 * ip + 1][k], cv[ip >> 2][ip & 3], acc[bb][2 * k + 1]); }
    }
    {
      float v8[8], v4[4];
#pragma unroll
      for (int k = 0; k < 8; ++k) v8[k] = rs32(acc[0][k], acc[1][k]);
#pragma unroll
      for (int k = 0; k < 4; ++k) v4[k] = rs16(v8[k], v8[k + 4]);
      *reinterpret_cast<f32x4*>(red2 + (w4 * 2 + (lane >> 5)) * 128 + 8 * c16 + 4 * (r4 & 1)) = f32x4{v4[0], v4[1], v4[2], v4[3]};
    }
    {
      COMPILER_FENCE();
      const unsigned got = lds_arrive_nw(L_CNT + 16);
      f32x4 t4[4];
#pragma unroll
      for (int w = 0; w < 4; ++w) t4[w] = *reinterpret_cast<const f32x4*>(red2 + w * 256 + 4 * lane);
      SCHED();
      if (lds_done(got) == 3u) {
        const f32x4 t = (t4[0] + t4[1]) + (t4[2] + t4[3]);
        const unsigned co = WS_CX + ((h * 2 + b) * 256 + 64 * r4 + 32 * (c16 & 1) + 2 * (c16 >> 1)) * 8;
        st16c(rs, co, u32x4{__float_as_uint(t[0]), tag, __float_as_uint(t[1]), tag});
        st16c(rs, co + 128, u32x4{__float_as_uint(t[2]), tag, __float_as_uint(t[3]), tag});
      }
    }
    STAMP(12);
    STAMP_FLUSH(0); TR_FLUSH();
    return;
  }
  {
    unsigned op;
    {
      u32x4 c;
      const unsigned co = WS_CX + ((h * 2 + ((lane >> 3) & 1)) * 256 + (lane >> 5) * 128 + b * 64 + w4 * 16 + 2 * (lane & 7)) * 8;
      if (idx == 0 && w4 == 1) {
#pragma unroll 1
        for (int spin = 0; spin < SPIN_MAX; ++spin) {
          COMPILER_FENCE();
          if (__all(ld8c(rs, WS_QX + 65536 + (lane >> 1) * 2048 + (lane & 1) * 512)[1] == tag)) break;
          __builtin_amdgcn_s_sleep(8);
        }
      }
      until<6>(tstart + PAR(13, TW) + PAR(29, 370));
      if (w4 == 0) { shint<2048, 0>(ws + WS_CX + (h * 512 + b * 64) * 8, tag); lds_add(cnt + 7, 1u, lane); }
      else lds_wait(cnt + 7, 1u);
      STAMP(3);
#pragma unroll 1
      for (int spin = 0; spin < SPIN_MAX; ++spin) {
        COMPILER_FENCE();
        c = ld16c(rs, co);
        if (__all((c[1] == tag) & (c[3] == tag))) break;
        __builtin_amdgcn_s_sleep(1);
      }
      float o0 = __uint_as_float(c[0]), o1 = __uint_as_float(c[2]);
      o0 += dpp<0x128>(o0); o1 += dpp<0x128>(o1);
      op = pk_bf(o0, o1);
    }
    STAMP(14);
    float* red3 = red + 4096;
    float a[2][8];
#pragma unroll
    for (int bb = 0; bb < 2; ++bb)
#pragma unroll
      for (int k = 0; k < 8; ++k) a[bb][k] = 0.f;
#pragma unroll
    for (int ip = 0; ip < 8; ++ip) {
      const unsigned v0 = __builtin_amdgcn_readlane(op, ip), v1 = __builtin_amdgcn_readlane(op, 32 + ip);
#pragma unroll
      for (int k = 0; k < 4; ++k) {
        a[0][2 * k] = dot2(Wb[2 * ip][k], v0, a[0][2 * k]); a[0][2 * k + 1] = dot2(Wb[2 * ip + 1][k], v0, a[0][2 * k + 1]);
        a[1][2 * k] = dot2(Wb[2 * ip][k], v1, a[1][2 * k]); a[1][2 * k + 1] = dot2(Wb[2 * ip + 1][k], v1, a[1][2 * k + 1]);
      }
    }
#pragma unroll
    for (int bb = 0; bb < 2; ++bb) {
      *reinterpret_cast<f32x4*>(red3 + (w4 * 2 + bb) * 512 + 8 * lane) = f32x4{a[bb][0], a[bb][1], a[bb][2], a[bb][3]};
      *reinterpret_cast<f32x4*>(red3 + (w4 * 2 + bb) * 512 + 8 * lane + 4) = f32x4{a[bb][4], a[bb][5], a[bb][6], a[bb][7]};
    }
    sbar4(cnt + 5, lane);
    f32x4 t = {0.f, 0.f, 0.f, 0.f};
#pragma unroll
    for (int w = 0; w < 4; ++w) t += *reinterpret_cast<const f32x4*>(red3 + w * 1024 + 4 * tid);
    st16c(rs, WS_GA + ((tid >> 7) * 64 + idx) * 2048 + (tid & 127) * 16, u32x4{pk_bf(t[0], t[1]), tag, pk_bf(t[2], t[3]), tag});
    STAMP(5);
  }
  fin(IC<1>{}, WS_GA, 0, idx == 0);
  STAMP(6);
  STAMP_FLUSH(0); TR_FLUSH();
}

extern "C" void kernel_launch(void* const* d_in, const int* in_sizes, int n_in, void* d_out, int out_size, void* d_ws, size_t ws_size, hipStream_t stream) {
  const u16* xA = (const u16*)d_in[0];
  const u16* ckv = (const u16*)d_in[1];
  const u16* w_q = (const u16*)d_in[2];
  const u16* w_uk = (const u16*)d_in[3];
  const u16* w_uv = (const u16*)d_in[4];
  const u16* w_o = (const u16*)d_in[5];
  const u16* xB = (const u16*)d_in[6];
  const u16* kc = (const u16*)d_in[7];
  const u16* vc = (const u16*)d_in[8];
  const u16* wq = (const u16*)d_in[9];
  const u16* wo = (const u16*)d_in[10];
  kmain<<<256, 512, 0, stream>>>(ckv, w_q, w_uk, wq, xA, xB, w_uv, w_o, kc, vc, wo, (char*)d_ws, (u16*)d_out);
}
```

```cpp
#include <hip/hip_runtime.h>
#include <hip/hip_bf16.h>
#include <stdint.h>

#define DI __device__ __forceinline__
typedef __attribute__((ext_vector_type(4))) unsigned u32x4;
typedef __attribute__((ext_vector_type(2))) unsigned u32x2;
typedef __attribute__((ext_vector_type(4))) float f32x4;
typedef __attribute__((ext_vector_type(2))) float f32x2;
typedef __attribute__((ext_vector_type(16))) float f32x16;
typedef __attribute__((ext_vector_type(8))) __bf16 bf16x8_t;
typedef __attribute__((ext_vector_type(16))) __bf16 bf16x16_t;
typedef __attribute__((ext_vector_type(2))) __bf16 bf16x2_t;
typedef __attribute__((ext_vector_type(4))) short s16x4;
typedef __attribute__((ext_vector_type(8))) short s16x8;
typedef unsigned short u16;
typedef unsigned long long u64;

constexpr int NS0 = 13, NS1 = 17, NS2 = 16, NS3 = 18;
constexpr u64 TL0 = 0xcaeaa685e6d54caull, TH0 = 0x3deb6ull, TL1 = 0x81c65444d2c7100ull, TH1 = 0x3ee33c2ca24ull;
constexpr u64 TL2 = 0x8e26163ce2460c1ull, TH2 = 0xf37d31ae9ull, TL3 = 0x7dd697490308142ull, TH3 = 0xffbe75bed9e5ull;
constexpr int RA2 = 0xC0, RA3 = 3;
constexpr int AHL = 2;
constexpr int QPE = 7;
constexpr int WBW = 5;
#define TL 2325
#define TW 1620
constexpr unsigned TS0 = 1160, TS1 = 1420, TS2 = 1320, TS3 = 1420, STG = 12, BSG = 6, HL = 40, TP0 = TW + 170, TP2 = TW + 380, PA = 150, TF = 172, T0B = 380, T0Q = 850;
#define PAR(i, v) (v)
constexpr u64 LA2 = TH2 >> (6 * (NS2 - 11)), LA3 = TH3 >> (6 * (NS3 - 11));
constexpr u64 chunk_mask(u64 lo, u64 hi, int n) { u64 m = 0; for (int i = 0; i < n; ++i) m |= 1ull << ((i < 10 ? lo >> (6 * i) : hi >> (6 * (i - 10))) & 63); return m; }
static_assert((chunk_mask(TL0, TH0, NS0) | chunk_mask(TL1, TH1, NS1) | chunk_mask(TL2, TH2, NS2) | chunk_mask(TL3, TH3, NS3)) == ~0ull && (RA2 & RA3) == 0, "every chunk once");
static_assert(NS0 + NS1 + NS2 + NS3 == 64, "split sizes");
constexpr int NSMIN = 13, NSMAX = 18;
static_assert(NS0 >= NSMIN && NS1 >= NSMIN && NS2 - 1 >= NSMIN && NS3 - 1 >= NSMIN && NS0 <= NSMAX && NS1 + 1 <= NSMAX && NS2 <= NSMAX && NS3 <= NSMAX, "range");
constexpr int SK = 4096;
constexpr float C2 = 0.08838834764831845f * 1.4426950408889634f;
constexpr u64 GOLD = 0x9E3779B97F4A7C15ull;

constexpr unsigned WS_EP = 0;
constexpr unsigned WS_QX = 4096;
constexpr unsigned WS_QA = WS_QX + 2 * 32 * 256 * 8;
constexpr unsigned WS_AML = WS_QA + 2 * 32 * 512 * 2;
constexpr unsigned WS_CX = WS_AML + 32 * 2 * 16 * 128;
constexpr unsigned WS_GZ = WS_CX + 32 * 2 * 2 * 128 * 8;
constexpr unsigned WS_T0 = WS_GZ + 256 * 128;
constexpr unsigned WS_GB = WS_T0 + 64 * 128;
constexpr unsigned WS_GA = WS_GB + 2 * 128 * 256 * 8;
constexpr unsigned WS_APO = 2u << 20;
static_assert(WS_GA + 64 * 2 * 256 * 8 <= WS_APO, "layout");
constexpr unsigned WS_END = WS_APO + 32 * 2 * 64 * 256 * 2;
#define STAMP(i) do {} while (0)
#define STAMP_DECL do {} while (0)
#define STAMP_FLUSH(grp) do {} while (0)

#define TR() do {} while (0)
#define TR_FLUSH() do {} while (0)
DI float bflo(unsigned u) { return __uint_as_float(u << 16); }
DI float bfhi(unsigned u) { return __uint_as_float(u & 0xffff0000u); }
DI unsigned pk_bf(float a, float b) { const f32x2 v = {a, b}; return __builtin_bit_cast(unsigned, __builtin_convertvector(v, bf16x2_t)); }
DI float dot2(unsigned k, unsigned q, float acc) {
  return __builtin_amdgcn_fdot2_f32_bf16(__builtin_bit_cast(bf16x2_t, k), __builtin_bit_cast(bf16x2_t, q), acc, false);
}
DI unsigned plo(unsigned w0, unsigned w1) { return __builtin_amdgcn_perm(w1, w0, 0x05040100u); }
DI unsigned phi(unsigned w0, unsigned w1) { return __builtin_amdgcn_perm(w1, w0, 0x07060302u); }
template <int CTRL> DI float dpp(float x) {
  return __builtin_bit_cast(float, __builtin_amdgcn_mov_dpp(__builtin_bit_cast(int, x), CTRL, 0xf, 0xf, true));
}
DI float row16_sum(float x) { x += dpp<0xB1>(x); x += dpp<0x4E>(x); x += dpp<0x141>(x); x += dpp<0x128>(x); return x; }
DI float row16_max(float x) { x = fmaxf(x, dpp<0xB1>(x)); x = fmaxf(x, dpp<0x4E>(x)); x = fmaxf(x, dpp<0x141>(x)); x = fmaxf(x, dpp<0x128>(x)); return x; }
DI float xhalf_max(float x) { auto t = __builtin_amdgcn_permlane32_swap(__float_as_uint(x), __float_as_uint(x), false, false); return fmaxf(__uint_as_float(t[0]), __uint_as_float(t[1])); }
DI float xhalf_sum(float x) { auto t = __builtin_amdgcn_permlane32_swap(__float_as_uint(x), __float_as_uint(x), false, false); return __uint_as_float(t[0]) + __uint_as_float(t[1]); }
DI u32x4 ld16(const void* p) { return *reinterpret_cast<const u32x4*>(p); }
DI u32x4 ld16nt(const void* p) { return __builtin_nontemporal_load(reinterpret_cast<const u32x4*>(p)); }
DI float ex2(float x) { return __builtin_amdgcn_exp2f(x); }
#define SCHED() __builtin_amdgcn_sched_barrier(0)

typedef __amdgpu_buffer_rsrc_t rsrc_t;
DI u32x4 ld16c(rsrc_t rs, unsigned off) { return __builtin_amdgcn_raw_buffer_load_b128(rs, (int)off, 0, 16); }
DI u32x2 ld8c(rsrc_t rs, unsigned off) { return __builtin_amdgcn_raw_buffer_load_b64(rs, (int)off, 0, 16); }
DI void st16c(rsrc_t rs, unsigned off, u32x4 v) { __builtin_amdgcn_raw_buffer_store_b128(v, rs, (int)off, 0, 16); }
DI void st8c(rsrc_t rs, unsigned off, u32x2 v) { __builtin_amdgcn_raw_buffer_store_b64(v, rs, (int)off, 0, 16); }
template <int O1> DI void sld2(const char* p, u32x2& a, u32x2& b) {
  asm volatile("s_load_dwordx2 %0, %2, 0x0 glc\n\ts_load_dwordx2 %1, %2, %3 glc\n\ts_waitcnt lgkmcnt(0)" : "=&s"(a), "=&s"(b) : "s"(p), "n"(O1) : "memory");
}
typedef __attribute__((ext_vector_type(8))) unsigned u32x8;
DI void sld4x32(const char* p, u32x8 (&v)[4]) {
  asm volatile("s_load_dwordx8 %0, %4, 0x0 glc\n\ts_load_dwordx8 %1, %4, 0x80 glc\n\ts_load_dwordx8 %2, %4, 0x100 glc\n\ts_load_dwordx8 %3, %4, 0x180 glc\n\ts_waitcnt lgkmcnt(0)"
               : "=&s"(v[0]), "=&s"(v[1]), "=&s"(v[2]), "=&s"(v[3]) : "s"(p) : "memory");
}
constexpr int SPIN_MAX = 1 << 20;
constexpr unsigned OOB = 0x7ffffff0u;
DI void rawbar() { __builtin_amdgcn_sched_barrier(0); __builtin_amdgcn_s_barrier(); __builtin_amdgcn_sched_barrier(0); }
#define COMPILER_FENCE() asm volatile("" ::: "memory")
template <int O1, int SL> DI void shint(const char* p, unsigned tag) {
#pragma unroll 1
  for (int spin = 0; spin < SPIN_MAX; ++spin) {
    u32x2 a, b;
    sld2<O1>(p, a, b);
    if (((a[1] ^ tag) | (b[1] ^ tag)) == 0u) break;
    __builtin_amdgcn_s_sleep(SL);
  }
}

template <int N> struct IC { static constexpr int v = N; };
template <int I, int N, class F> DI void sfor(F&& f) { if constexpr (I < N) { f(IC<I>{}); sfor<I + 1, N>(f); } }
typedef __attribute__((address_space(3))) unsigned lu32;
DI void lds_wait(unsigned* cnt, unsigned target) {
#pragma unroll 1
  for (int spin = 0; spin < SPIN_MAX; ++spin) {
    if (__builtin_amdgcn_readfirstlane(__hip_atomic_load((lu32*)cnt, __ATOMIC_RELAXED, __HIP_MEMORY_SCOPE_WORKGROUP)) >= target) break;
    __builtin_amdgcn_s_sleep(1);
  }
  COMPILER_FENCE();
}
DI void lds_wait_slow(unsigned* cnt, unsigned target) {
#pragma unroll 1
  for (int spin = 0; spin < SPIN_MAX; ++spin) {
    if (__builtin_amdgcn_readfirstlane(__hip_atomic_load((lu32*)cnt, __ATOMIC_RELAXED, __HIP_MEMORY_SCOPE_WORKGROUP)) >= target) break;
    __builtin_amdgcn_s_sleep(3);
  }
  COMPILER_FENCE();
}
template <int SL = 1> DI void until(unsigned t) {
#pragma unroll 1
  for (int spin = 0; spin < 1024; ++spin) { if ((int)((unsigned)wall_clock64() - t) >= 0) break; __builtin_amdgcn_s_sleep(SL); }
}
DI void lds_hold(unsigned* flag) {
#pragma unroll 1
  for (int spin = 0; spin < SPIN_MAX; ++spin) {
    if (__builtin_amdgcn_readfirstlane(__hip_atomic_load((lu32*)flag, __ATOMIC_RELAXED, __HIP_MEMORY_SCOPE_WORKGROUP)) == 0u) break;
    __builtin_amdgcn_s_sleep(2);
  }
}
DI unsigned lds_arrive_nw(unsigned addr) {
  unsigned old, one = 1u;
  asm volatile("s_mov_b64 exec, 1\n\tds_add_rtn_u32 %0, %1, %2\n\ts_mov_b64 exec, -1" : "=v"(old) : "v"(addr), "v"(one) : "memory");
  return old;
}
DI unsigned lds_done(unsigned old) { asm volatile("s_waitcnt lgkmcnt(0)" : "+v"(old) :: "memory"); return __builtin_amdgcn_readfirstlane(old); }
DI unsigned lds_arrive(unsigned* cnt, int) { return lds_done(lds_arrive_nw((unsigned)(size_t)(lu32*)cnt)); }
DI void lds_add(unsigned* cnt, unsigned v, int) {
  asm volatile("s_mov_b64 exec, 1\n\tds_add_u32 %0, %1\n\ts_mov_b64 exec, -1" :: "v"((unsigned)(size_t)(lu32*)cnt), "v"(v) : "memory");
}
DI void sbar4(unsigned* cnt, int lane) { if (lds_arrive(cnt, lane) < 3u) lds_wait(cnt, 4u); }
DI float rs32(float a, float b) { auto t = __builtin_amdgcn_permlane32_swap(__float_as_uint(a), __float_as_uint(b), false, false); return __uint_as_float(t[0]) + __uint_as_float(t[1]); }
DI float rs16(float a, float b) { auto t = __builtin_amdgcn_permlane16_swap(__float_as_uint(a), __float_as_uint(b), false, false); return __uint_as_float(t[0]) + __uint_as_float(t[1]); }
template <int N> DI unsigned rowbcast(unsigned x) { return (unsigned)__builtin_amdgcn_mov_dpp((int)x, 0x150 + N, 0xf, 0xf, true); }
DI void pin2(u32x4& a, u32x4& b) { asm volatile("" : "+v"(a), "+v"(b)); }
DI void use2(const u32x4& a, const u32x4& b) { asm volatile("" :: "v"(a), "v"(b)); }
DI bool nz8(const u32x4& v) { return ((v[0] | v[1]) != 0u) & ((v[2] | v[3]) != 0u); }

DI u32x4 qsum(const u32x4& g) {
  const auto ta = __builtin_amdgcn_permlane32_swap(g[0], g[0], false, false), tc = __builtin_amdgcn_permlane32_swap(g[2], g[2], false, false);
  const unsigned a = pk_bf(bflo(ta[0]) + bflo(ta[1]), bfhi(ta[0]) + bfhi(ta[1])), c = pk_bf(bflo(tc[0]) + bflo(tc[1]), bfhi(tc[0]) + bfhi(tc[1]));
  const auto ra = __builtin_amdgcn_permlane16_swap(a, a, false, false), rc = __builtin_amdgcn_permlane16_swap(c, c, false, false);
  return u32x4{ra[0], rc[0], ra[1], rc[1]};
}
DI unsigned toff(unsigned row, unsigned ch) { return 256u * row + 16u * (ch ^ (((row & 3) << 2) | ((row >> 2) & 3))); }
constexpr int SMEM = 160 * 1024;
constexpr int L_SCR = 131072;
constexpr int L_MRG = 65536;
constexpr int L_CNT = L_SCR + 32 * 1008;

struct KV { u32x4 r[8]; };
constexpr int CODE_BYTES = 43392;

template <class T> DI T* gptr(unsigned lo, unsigned hi) { return (T*)(__attribute__((address_space(1))) T*)(((u64)hi << 32) | lo); }

__global__ __launch_bounds__(512, 2) void kmain(const u16*, const u16*, const u16*, const u16*, const u16*, const u16*, const u16*, const u16*, const u16*,
                                                const u16*, const u16*, char*, u16*) {
  u32x2 kpa, kpx, kc2, t64; u32x4 kb4; u32x8 kb8;
  int tid = threadIdx.x, blk = blockIdx.x;
  const auto kp = __builtin_amdgcn_kernarg_segment_ptr();
  const int o1 = blk & 24, o2 = 32 + ((blk >> 1) & 8);
  asm volatile("s_load_dwordx2 %0, %7, %8\n\ts_load_dwordx2 %1, %7, %9\n\ts_load_dwordx4 %2, %7, 0x30\n\ts_load_dwordx8 %3, %7, 0x40\n\ts_load_dwordx2 %4, %7, 0x60"
               : "=&s"(kpa), "=&s"(kpx), "=&s"(kb4), "=&s"(kb8), "=&s"(kc2), "+v"(tid), "+s"(blk) : "s"(kp), "s"(o1), "s"(o2));
  __builtin_assume(tid >= 0 && tid < 512 && blk >= 0 && blk < 256);
  const unsigned cpf = __builtin_amdgcn_raw_buffer_load_b32(__builtin_amdgcn_make_buffer_rsrc((void*)(__builtin_amdgcn_s_getpc() & ~255ull), 0, CODE_BYTES, 0x00020000),
                                                            (int)((tid & 0x10f) == 0x100 ? (blk >> 3) * 2048 + (tid & 0xf0) * 8 : OOB), 0, 0);
  COMPILER_FENCE();
  SCHED();
  __shared__ __attribute__((aligned(16))) char smem[SMEM];
  const int lane = tid & 63;
  const int wave = __builtin_amdgcn_readfirstlane(tid >> 6), w4 = wave & 3;
  const int cls = (blk >> 3) & 3;
  int idx = 8 * (blk & 7) + (blk >> 5);
  idx = (idx & ~7) | ((idx + ((wave >= 4 ? 0 : 0x2400) >> (4 * cls))) & 7);
  const int h = idx >> 1, b = idx & 1;
  const int r4 = lane >> 4, c16 = lane & 15;
  unsigned* cnt = reinterpret_cast<unsigned*>(smem + L_CNT);
  STAMP_DECL;
  STAMP(0);
  const unsigned c0 = cls ? 0u : ~0u;
  unsigned sa = cls ? 8192u : 4096u;
  unsigned ao = ((((b * 256 + w4 * 64) * 8192 + h * 256) & ~c0) | (((idx * 128 + w4) * 1024) & c0)) + (r4 << (17u - (c0 & 9u))) + c16 * 16, xo = (b * 256 + w4 * 64 + 16 * r4) * 2;
  asm volatile("" : "+v"(ao), "+v"(xo), "+s"(sa));
  SCHED();
  asm volatile("s_waitcnt lgkmcnt(0)\n\ts_memrealtime %5" : "+s"(kpa), "+s"(kpx), "+s"(kb4), "+s"(kb8), "+s"(kc2), "=&s"(t64) : "s"(kp), "s"(o1), "s"(o2));
  unsigned tstart;
  const char* __restrict__ pa = gptr<const char>(kpa[0], kpa[1]); const char* __restrict__ xp = gptr<const char>(kpx[0], kpx[1]);
  const u16* __restrict__ w_uv = gptr<const u16>(kb4[0], kb4[1]); const u16* __restrict__ w_o = gptr<const u16>(kb4[2], kb4[3]);
  const u16* __restrict__ kc = gptr<const u16>(kb8[0], kb8[1]); const u16* __restrict__ vc = gptr<const u16>(kb8[2], kb8[3]);
  const u16* __restrict__ wo = gptr<const u16>(kb8[4], kb8[5]); char* __restrict__ ws = gptr<char>(kb8[6], kb8[7]);
  u16* __restrict__ out = gptr<u16>(kc2[0], kc2[1]);
  const rsrc_t rs = __builtin_amdgcn_make_buffer_rsrc(ws, 0, (int)WS_END, 0x00020000);
  unsigned tag, tlow;
  u32x2 epw;
  auto take_epoch = [&]() {
    asm volatile("" : "+v"(epw) : "v"(cpf));
    const u64 e64 = (((u64)(unsigned)__builtin_amdgcn_readfirstlane(epw[1]) << 32) | (unsigned)__builtin_amdgcn_readfirstlane(epw[0])) + GOLD;
    tag = (unsigned)(e64 >> 32); tlow = (unsigned)e64;
    asm volatile("s_waitcnt lgkmcnt(0)" : "+s"(tag), "+s"(tlow) :: "memory");
  };
  if (wave >= 4) {
    if (tid - 256 < 16) cnt[tid - 256] = 0u;
    asm volatile("s_waitcnt lgkmcnt(0)\n\ts_mov_b32 %0, %1" : "=s"(tstart) : "s"(t64[0]), "s"(t64) : "memory");
    const unsigned pTL = PAR(0, TL), pTS = PAR(1 + cls, cls == 0 ? TS0 : (cls == 1 ? TS1 : (cls == 2 ? TS2 : TS3))), pSTG = PAR(9, STG), pBSG = PAR(10, BSG);
    const unsigned pRA2 = PAR(11, RA2), pRA3 = PAR(12, RA3), pTP = PAR(15 + cls, cls == 0 ? TP0 : TP2), pPA = PAR(19 + cls, (cls & 1) ? 0u : PA);
    rawbar();
    const int e2 = (pRA2 >> (idx & 7)) & 1, e3 = (pRA3 >> (idx & 7)) & 1;
    const int ns = cls == 0 ? NS0 : (cls == 1 ? NS1 + e2 + e3 : (cls == 2 ? NS2 - e2 : NS3 - e3));
    const unsigned pTA = __builtin_amdgcn_readfirstlane((unsigned)((float)pTS * __builtin_amdgcn_rcpf((float)(ns - 5)) + 0.5f));
    const size_t kvo = ((size_t)(b * 32 + h) * SK + w4 * 16) * 256;
    const char* kb = reinterpret_cast<const char*>(kc) + kvo;
    const char* vb = reinterpret_cast<const char*>(vc) + kvo;
    const unsigned lo = lane * 16u;
    u64 tlo = cls == 0 ? TL0 : (cls == 1 ? TL1 : (cls == 2 ? TL2 : TL3));
    u64 thi = cls == 0 ? TH0 : (cls == 1 ? TH1 | ((e2 ? LA2 : LA3) << (6 * (NS1 - 10))) : (cls == 2 ? TH2 : TH3));
    const u64 tlo0 = tlo, thi0 = thi;
    int hn = 0;
    const char* hb = kb;
    const char* pc[2];
    auto next2 = [&]() {
#pragma unroll
      for (int i = 0; i < 2; ++i) {
        if (hn == ns) { tlo = tlo0; thi = thi0; hb = vb; }
        ++hn;
        pc[i] = hb + ((((unsigned)tlo + (unsigned)idx) & 63u) << 14);
        tlo = (tlo >> 6) | ((thi & 63u) << 54); thi >>= 6;
      }
    };
    KV A, B;
    u32x4 W[8];
    u32x8 T[2][8];
    auto ldp = [&](KV& S, auto P) {
      constexpr int p = P.v;
      const char* q = pc[p >> 1] + lo + (p & 1) * 2048;
      S.r[2 * p] = ld16nt(q); S.r[2 * p + 1] = ld16nt(q + 1024);
    };
    auto landed = [&](KV& S, auto P) { pin2(S.r[2 * P.v], S.r[2 * P.v + 1]); };
    KV X;
    const unsigned qo = WS_QX + ((32 + h) * 2 + b) * 1024 + lane * 16;
    u32x4 g0;
    {
      unsigned tk = 0u;
      if (w4 == 0) {
        const int si = (idx & ~7) | ((idx + (0x2400 >> (4 * cls))) & 7);
        const char* p = kb + cls * 128;
        if (lane == 1) p = cls == 2 ? reinterpret_cast<const char*>(w_uv) + (si & 1) * 2097152 + (si >> 1) * 256 : reinterpret_cast<const char*>(cls == 1 ? w_o : wo) + si * 65536;
        if (lane == 2) p = vb + cls * 128;
        if (lane == 3) p = reinterpret_cast<const char*>(wo) + h * 131072;
        if (lane < 4) tk = __builtin_nontemporal_load(reinterpret_cast<const unsigned*>(p));
      }
      lds_wait_slow(cnt + 14, 4u);
      tag = __builtin_amdgcn_readfirstlane(cnt[16]);
      asm volatile("" :: "v"(tk), "v"(cpf));
    }
    STAMP(10);
    bool noorg = w4 == 3;
    auto origin = [&]() {
#pragma unroll 1
      for (int spin = 0; spin < 2 && noorg; ++spin) {
        u32x2 t[8];
        asm volatile("s_load_dwordx2 %0, %8, 0x0 glc\n\ts_load_dwordx2 %1, %8, 0x400 glc\n\ts_load_dwordx2 %2, %8, 0x800 glc\n\ts_load_dwordx2 %3, %8, 0xc00 glc\n\t"
                     "s_load_dwordx2 %4, %8, 0x1000 glc\n\ts_load_dwordx2 %5, %8, 0x1400 glc\n\ts_load_dwordx2 %6, %8, 0x1800 glc\n\ts_load_dwordx2 %7, %8, 0x1c00 glc\n\ts_waitcnt lgkmcnt(0)"
                     : "=&s"(t[0]), "=&s"(t[1]), "=&s"(t[2]), "=&s"(t[3]), "=&s"(t[4]), "=&s"(t[5]), "=&s"(t[6]), "=&s"(t[7]) : "s"(ws + WS_T0 + ((blk >> 3) & 7) * 128) : "memory");
        unsigned e = 0u; int sum = 0;
#pragma unroll
        for (int k = 0; k < 8; ++k) { e |= t[k][1] ^ tag; sum += (int)(t[k][0] - tstart); }
        if (e == 0u) { lds_add(cnt + 13, (unsigned)min(sum >> 3, 100), lane); noorg = false; }
      }
    };
    sfor<0, 12>([&](auto I) {
      constexpr int i = I.v, j = i - 4;
      if constexpr ((i & 3) == 0) next2();
      if constexpr (i == 4) origin();
      if constexpr (j >= 0) { if constexpr (j < 4) landed(X, IC<j & 3>{}); else landed(B, IC<j & 3>{}); }
      if constexpr (j >= 0 && (j & 1)) TR();
      if constexpr (i == QPE) g0 = ld16c(rs, qo);
      if constexpr (i < 4) ldp(X, IC<i & 3>{}); else if constexpr (i < 8) ldp(B, IC<i & 3>{}); else ldp(A, IC<i & 3>{});
      SCHED();
    });
    origin();
    const char* wp = reinterpret_cast<const char*>(wo) + ((size_t)h * 128 + r4 * 8) * 1024 + w4 * 256 + c16 * 16;
    u32x4 qpk;
    {
      if (!__all((g0[1] == tag) & (g0[3] == tag))) {
#pragma unroll 1
        for (int spin = 0; spin < SPIN_MAX; ++spin) {
          COMPILER_FENCE();
          g0 = ld16c(rs, qo);
          if (__all((g0[1] == tag) & (g0[3] == tag))) break;
        }
      }
      qpk = qsum(g0);
    }
    STAMP(7);
    const bool holds = cls == 0;
    float m = 0.f, l = 0.f, acc[8];
    unsigned sc[5];
#pragma unroll
    for (int k = 0; k < 8; ++k) acc[k] = 0.f;
#pragma unroll
    for (int k = 0; k < 5; ++k) sc[k] = 0xff800000u;
    int vn = 0;
    f32x4 d[8];
    unsigned torg, due = pTL - (unsigned)(ns - 5) * pTA - (3 - w4) * pSTG - ((idx * 5 + cls * 3) & 15) * pBSG;
    unsigned rwo;
    const unsigned zo = WS_GZ + (b * 32 + h) * 512;
    u32x2 zs[4];
    float wr_;
    auto zreq = [&]() {
      asm volatile("s_load_dwordx2 %0, %4, 0x0 glc\n\ts_load_dwordx2 %1, %4, 0x80 glc\n\ts_load_dwordx2 %2, %4, 0x100 glc\n\ts_load_dwordx2 %3, %4, 0x180 glc"
                   : "=&s"(zs[0]), "=&s"(zs[1]), "=&s"(zs[2]), "=&s"(zs[3]) : "s"(ws + zo) : "memory");
    };
    auto zget = [&]() {
#pragma unroll 1
      for (int spin = 0; spin < SPIN_MAX; ++spin) {
        asm volatile("s_waitcnt lgkmcnt(0)" : "+s"(zs[0]), "+s"(zs[1]), "+s"(zs[2]), "+s"(zs[3]) :: "memory");
        if (((zs[0][1] ^ tag) | (zs[1][1] ^ tag) | (zs[2][1] ^ tag) | (zs[3][1] ^ tag)) == 0u) break;
        __builtin_amdgcn_s_sleep(2);
        zreq();
      }
      asm volatile("s_waitcnt lgkmcnt(0)" : "+s"(zs[0]), "+s"(zs[1]), "+s"(zs[2]), "+s"(zs[3]) :: "memory");
      const float z0 = __uint_as_float(zs[0][0]), z1 = __uint_as_float(zs[1][0]), z2 = __uint_as_float(zs[2][0]), z3 = __uint_as_float(zs[3][0]);
      const float Z = fmaxf(fmaxf(z0, z1), fmaxf(z2, z3));
      wr_ = ex2(fmaf(m, C2, -Z)) * __builtin_amdgcn_rcpf((ex2(z0 - Z) + ex2(z1 - Z)) + (ex2(z2 - Z) + ex2(z3 - Z)));
      asm volatile("" : "+v"(wr_));
    };
    auto qk = [&](const u32x4* K, auto I) {
      float x[4];
#pragma unroll
      for (int t = 0; t < 4; ++t) {
        x[t] = 0.f;
#pragma unroll
        for (int k = 0; k < 4; ++k) x[t] = dot2(K[t][k], qpk[k], x[t]);
      }
      const float u0 = (c16 & 1 ? x[1] : x[0]) + dpp<0xB1>(c16 & 1 ? x[0] : x[1]), u1 = (c16 & 1 ? x[3] : x[2]) + dpp<0xB1>(c16 & 1 ? x[2] : x[3]);
      float v = (c16 & 2 ? u1 : u0) + dpp<0x4E>(c16 & 2 ? u0 : u1);
      v += dpp<0x124>(v); v += dpp<0x128>(v);
      sc[I.v >> 2] = __builtin_amdgcn_update_dpp(sc[I.v >> 2], __float_as_uint(v), 0xE4, 0xf, 1 << (I.v & 3), false);
      asm volatile("" : "+v"(sc[I.v >> 2]));
    };
    auto soft = [&]() {
      m = row16_max(fmaxf(fmaxf(fmaxf(__uint_as_float(sc[0]), __uint_as_float(sc[1])), fmaxf(__uint_as_float(sc[2]), __uint_as_float(sc[3]))), __uint_as_float(sc[4])));
      const float mc = m * C2;
      l = 0.f;
#pragma unroll
      for (int k = 0; k < 5; ++k) {
        const float p = ex2(fmaf(__uint_as_float(sc[k]), C2, -mc));
        sc[k] = pk_bf(p, dpp<0xB1>(p));
        l = dot2(sc[k], 0x3f003f00u, l);
      }
      l = row16_sum(l);
    };
    auto pv = [&](const u32x4* V) {
      const unsigned p01 = rowbcast<0>(sc[0]), p23 = rowbcast<2>(sc[0]);
#pragma unroll
      for (int k = 0; k < 4; ++k) { acc[2 * k] = dot2(plo(V[0][k], V[1][k]), p01, acc[2 * k]); acc[2 * k + 1] = dot2(phi(V[0][k], V[1][k]), p01, acc[2 * k + 1]); }
#pragma unroll
      for (int k = 0; k < 4; ++k) { acc[2 * k] = dot2(plo(V[2][k], V[3][k]), p23, acc[2 * k]); acc[2 * k + 1] = dot2(phi(V[2][k], V[3][k]), p23, acc[2 * k + 1]); }
      sc[0] = (unsigned)__builtin_amdgcn_mov_dpp((int)sc[0], 0x12C, 0xf, 0xf, true);
      if ((++vn & 3) == 0) { sc[0] = sc[1]; sc[1] = sc[2]; sc[2] = sc[3]; sc[3] = sc[4]; }
      asm volatile("" : "+v"(acc[0]), "+v"(acc[1]), "+v"(acc[2]), "+v"(acc[3]), "+v"(acc[4]), "+v"(acc[5]), "+v"(acc[6]), "+v"(acc[7]));
    };
    auto step = [&](KV& S, KV& N, auto SI, auto REFILL, auto WR) {
      constexpr bool refill = REFILL.v == 1;
      constexpr int wr = WR.v;
      if constexpr (refill) next2();
      if constexpr (refill && SI.v >= 3) { until((int)(due - torg) < (int)pTP ? due - pPA : due); due += pTA; }
      constexpr int ah = wr < 0 ? AHL : 0;
      auto point = [&](auto P) {
        if constexpr (refill) {
          if constexpr (P.v == 0 && ah > 0) { if constexpr (wr >= 0) { if (holds) lds_hold(cnt + 8); } sfor<0, ah>([&](auto Q) { ldp(N, Q); }); SCHED(); }
          landed(S, P);
          if constexpr (P.v & 1) TR();
          if constexpr (P.v + ah < 4) { if constexpr ((P.v & 1) == 0 && wr >= 0) { if (holds) lds_hold(cnt + 8); } ldp(N, IC<(P.v + ah) & 3>{}); }
          if constexpr (wr >= 0 && wr < 8) W[(4 * wr + P.v) & 7] = ld16(wp + (((4 * wr + P.v) >> 3) * 32 + ((4 * wr + P.v) & 7)) * 1024);
          SCHED();
        }
      };
      sfor<0, 2>([&](auto H) {
        constexpr int i = 2 * SI.v + H.v;
        if constexpr (REFILL.v == 2 && H.v == 0) {
          zreq();
#pragma unroll
          for (int e = 0; e < 8; ++e) { d[e] = f32x4{0.f, 0.f, 0.f, 0.f}; asm volatile("" : "+v"(d[e])); }
          SCHED();
        }
        point(IC<2 * H.v>{});
        point(IC<2 * H.v + 1>{});
        if constexpr (i < NSMIN) qk(S.r + 4 * H.v, IC<i>{});
        else if constexpr (i > NSMAX) pv(S.r + 4 * H.v);
        else {
          if (i < ns) { if constexpr (i < NSMAX) qk(S.r + 4 * H.v, IC<i>{}); }
          else { if (i == ns) soft(); pv(S.r + 4 * H.v); }
        }
        SCHED();
      });
      if constexpr (REFILL.v == 2) {
        zget();
        STAMP(1);
#pragma unroll
        for (int k = 0; k < 8; ++k) acc[k] *= wr_;
        *reinterpret_cast<u32x2*>(smem + rwo) = u32x2{pk_bf(rs32(acc[0], acc[2]), rs32(acc[1], acc[3])), pk_bf(rs32(acc[4], acc[6]), rs32(acc[5], acc[7]))};
        COMPILER_FENCE();
      }
      if constexpr (wr >= 1) {
        constexpr int t = (wr - 1) >> 1, j0 = ((wr - 1) & 1) * 2;
#pragma unroll
        for (int jj = 0; jj < 2; ++jj)
#pragma unroll
          for (int e = 0; e < 4; ++e) {
            const unsigned a_ = W[(4 * wr - 4 + 2 * jj) & 7][e], b_ = W[(4 * wr - 3 + 2 * jj) & 7][e];
            T[t >> 1][2 * e][4 * (t & 1) + j0 + jj] = plo(a_, b_); T[t >> 1][2 * e + 1][4 * (t & 1) + j0 + jj] = phi(a_, b_);
          }
#pragma unroll
        for (int e = 0; e < 8; e += 2) asm volatile("" : "+v"(T[t >> 1][e]), "+v"(T[t >> 1][e + 1]));
      }
      SCHED();
    };
    step(B, B, IC<1>{}, IC<0>{}, IC<-1>{});
    step(A, B, IC<2>{}, IC<1>{}, IC<0>{});
    step(X, X, IC<0>{}, IC<0>{}, IC<-1>{});
    torg = tstart + __builtin_amdgcn_readfirstlane(cnt[13]); due += torg;
    sfor<0, 4>([&](auto I) {
      step(B, A, IC<2 * I.v + 3>{}, IC<1>{}, IC<2 * I.v + 1>{});
      if constexpr (I.v == 3) {
        float* ml = reinterpret_cast<float*>(cnt + 32);
        ml[(c16 & 1) * 16 + w4 * 4 + r4] = (c16 & 1) ? l : m;
        COMPILER_FENCE();
        const unsigned got = lds_arrive_nw(L_CNT + 44);
        const float mr = ml[c16], lr = ml[16 + c16];
        SCHED();
        if (lds_done(got) == 3u) {
          const float M = row16_max(mr);
          const unsigned z = __float_as_uint(fmaf(M, C2, __builtin_amdgcn_logf(row16_sum(ex2((mr - M) * C2) * lr))));
          asm volatile("buffer_store_dwordx4 %0, %1, %2, 0 offen sc1\n\ts_nop 2" :: "v"(u32x4{z, tag, z, tag}), "v"(lane < 8 ? zo + cls * 128 + lane * 16 : OOB), "s"(rs) : "memory");
        }
        SCHED();
      }
      step(A, B, IC<2 * I.v + 4>{}, IC<1>{}, IC<2 * I.v + 2>{});
    });
    unsigned yoff = WS_GB + ((b * 128 + h * 4 + cls) * 256 + 64 * w4 + lane) * 8, rro = L_MRG + c16 * 144 + r4 * 16;
    int six = (lane & 1) ? 0xEEEE : 0x4444;
    rwo = L_MRG + (w4 * 4 + 2 * (r4 & 1) + (r4 >> 1)) * 144 + c16 * 8;
    asm volatile("" : "+v"(yoff), "+v"(rwo), "+v"(rro), "+v"(six));
    if (cls == 0) lds_wait(cnt + 1, 4u);
    const int nit = (ns - 12) >> 1;
#pragma unroll 1
    for (int i = 0; i < nit; ++i) {
      step(B, A, IC<11>{}, IC<1>{}, IC<-1>{});
      step(A, B, IC<11>{}, IC<1>{}, IC<-1>{});
    }
    if (ns & 1) { step(B, A, IC<11>{}, IC<1>{}, IC<-1>{}); step(A, A, IC<11>{}, IC<2>{}, IC<-1>{}); }
    else step(B, B, IC<11>{}, IC<2>{}, IC<-1>{});
    {
      u32x4 af[2];
      unsigned got = lds_arrive_nw(L_CNT), need = 3u;
#pragma unroll 1
      for (int spin = 0; spin < SPIN_MAX; ++spin) {
#pragma unroll
        for (int t = 0; t < 2; ++t) af[t] = *reinterpret_cast<const u32x4*>(smem + rro + t * 64);
        SCHED();
        if (lds_done(got) >= need) break;
        need = 4u;
        __builtin_amdgcn_s_sleep(1);
        asm volatile("ds_read_b32 %0, %1" : "=v"(got) : "v"((unsigned)L_CNT) : "memory");
      }
      SCHED();
      float a[8], v4[4];
      sfor<0, 3>([&](auto I) {
        constexpr int i = I.v;
        sfor<0, 8>([&](auto J) {
          constexpr int e = 2 * i + ((J.v >> 1) & 1) + 4 * (J.v & 1), t = J.v >> 2, f = e - 2;
          if constexpr (i < 2) { d[e] = __builtin_amdgcn_smfmac_f32_16x16x64_bf16(__builtin_bit_cast(bf16x8_t, af[t]), __builtin_bit_cast(bf16x16_t, T[t][e]), d[e], six, 0, 0); SCHED(); }
          if constexpr (i >= 1 && t == 1) { const f32x2 u = f32x2{d[f][0], d[f][1]} + f32x2{d[f][2], d[f][3]}; a[f] = u[0] + u[1]; asm volatile("" : "+v"(a[f])); }
          if constexpr (i >= 1 && t == 1 && (J.v & 1)) v4[f - 4] = rs32(a[f - 4], a[f]);
          if constexpr (i < 2) SCHED();
        });
      });
      st8c(rs, yoff, u32x2{pk_bf(rs16(v4[0], v4[2]), rs16(v4[1], v4[3])), tag});
    }
    STAMP(2);
    STAMP_FLUSH(1);
    TR_FLUSH();
    return;
  }
  const rsrc_t ro = __builtin_amdgcn_make_buffer_rsrc(out, 0, 4096, 0x00020000);
  auto fin = [&](auto NL, unsigned base, unsigned col, bool bump) {
    constexpr int nl = NL.v;
    unsigned doff = base + (((idx >> 5) * 4 + w4) * 16 * nl + (lane >> 2)) * 2048 + (idx & 31) * 64 + (lane & 3) * 16;
    unsigned fwo = 32768 + (c16 < 4 ? (w4 * 16 + 4 * c16 + r4) * 4 : 256 + lane * 4);
    unsigned fro = 32768 + (lane & 7) * 8, foo = lane < 8 ? ((idx >> 5) * 1024 + col + (nl == 2 ? 16 * (idx & 24) + 64 * (idx & 1) + (idx & 6) + 8 * lane : 16 * (idx & 31) + 2 * lane)) * 2 : OOB;
    asm volatile("" : "+v"(doff), "+v"(fwo), "+v"(fro), "+v"(foo));
    lds_wait(cnt + 0, 4u);
    until(tstart + __builtin_amdgcn_readfirstlane(cnt[13]) + PAR(0, TL) + PAR(4 + nl, TF));
    STAMP(4);
    u32x4 d[nl];
    unsigned eh = 1u;
#pragma unroll 1
    for (int spin = SPIN_MAX; spin > 0; --spin) {
      COMPILER_FENCE();
      if (eh != 0u) {
#pragma unroll
        for (int j = 0; j < nl; ++j) d[j] = ld16c(rs, doff + j * 32768);
        eh = 0u;
#pragma unroll
        for (int j = 0; j < nl; ++j) { eh |= (d[j][1] ^ tag) | (d[j][3] ^ tag); asm volatile("" : "+v"(eh)); }
      }
      if (__builtin_amdgcn_ballot_w64(eh != 0u) == 0ull) break;
    }
    STAMP(11);
    float y[4] = {bflo(d[0][0]), bfhi(d[0][0]), bflo(d[0][2]), bfhi(d[0][2])};
    if constexpr (nl == 2) { y[0] += bflo(d[1][0]); y[1] += bfhi(d[1][0]); y[2] += bflo(d[1][2]); y[3] += bfhi(d[1][2]); }
    float t = rs16(rs32(y[0], y[2]), rs32(y[1], y[3]));
    t += dpp<0x124>(t); t += dpp<0x128>(t);
    *reinterpret_cast<float*>(smem + fwo) = t;
    COMPILER_FENCE();
    const unsigned got = lds_arrive_nw(L_CNT + 24);
    f32x2 t2[4];
#pragma unroll
    for (int w = 0; w < 4; ++w) t2[w] = *reinterpret_cast<const f32x2*>(smem + fro + w * 64);
    SCHED();
    if (lds_done(got) == 3u) {
      const f32x2 r = (t2[0] + t2[1]) + (t2[2] + t2[3]);
      __builtin_amdgcn_raw_buffer_store_b32(pk_bf(r[0], r[1]), ro, (int)foo, 0, 0);
      if (bump) st8c(rs, lane == 0 ? WS_EP : OOB, u32x2{tlow, tag});
    }
  };
  u32x4 Wa[16], Wb[16], xv[4];
  auto row = [&](u32x4& d) { d = ld16nt(pa + ao); SCHED(); ao += sa; asm volatile("" : "+v"(ao)); };
  sfor<0, 2>([&](auto I) { row(Wa[I.v]); });
  sfor<0, 4>([&](auto J) { xv[J.v] = ld16(xp + xo + (J.v >> 1) * 1024 + 16 * (J.v & 1)); });
  SCHED();
  sfor<2, 8>([&](auto I) { row(Wa[I.v]); });
  epw = ld8c(rs, WS_EP);
  st16c(rs, cls == 1 && w4 == 0 ? WS_QA + (b * 32 + h) * 1024 + lane * 16 : OOB, u32x4{0u, 0u, 0u, 0u});
  SCHED();
  asm volatile("s_waitcnt lgkmcnt(0)\n\ts_mov_b32 %0, %1" : "=s"(tstart) : "s"(t64[0]), "s"(t64));
  auto sync = [&]() {
    rawbar();
    take_epoch();
    cnt[16] = tag;
    COMPILER_FENCE();
  };
  if (cls == 0) {
    sfor<8, 16>([&](auto I) { row(Wa[I.v]); });
    sync();
    asm volatile("" :: "v"(xv[0]), "v"(xv[1]), "v"(xv[2]), "v"(xv[3]));
    STAMP(13);
    char* scr = smem + L_SCR;
    const int ab = idx >> 5, r = lane & 31, hh = lane >> 5;
    const unsigned fo = WS_QX + (w4 * 8 + (c16 >> 1)) * 2048 + (c16 & 1) * 512 + r4 * 8;
    u32x2 fl;
    u32x4 g[9];
    const unsigned o8 = WS_QA + (ab * 32 + w4 * 8) * 1024 + lane * 16, ol = WS_QA + (ab * 32 + r) * 1024 + 992 + 16 * hh;
    auto put = [&](int i, const u32x4& v) { *reinterpret_cast<u32x4*>(smem + ((i >> 3) * 4 + r4) * 8192 + toff(4 * (i & 7) + w4, c16)) = v; };
    sfor<0, 16>([&](auto I) { put(I.v, Wa[I.v]); if constexpr ((I.v & 3) == 3) TR(); SCHED(); });
    until(tstart + PAR(26, T0B));
    sfor<0, 16>([&](auto I) { row(Wb[I.v]); });
    lds_add(cnt + 14, 1u, lane);
    SCHED();
    sfor<0, 16>([&](auto I) { put(16 + I.v, Wb[I.v]); if constexpr (I.v == 12) fl = ld8c(rs, fo); if constexpr ((I.v & 3) == 3) TR(); SCHED(); });
    STAMP(8);
    u32x4 qlast;
    {
      if (!__all(fl[1] == tag)) {
#pragma unroll 1
        for (int spin = 0; spin < SPIN_MAX; ++spin) {
          COMPILER_FENCE();
          if (__all(ld8c(rs, fo)[1] == tag)) break;
          __builtin_amdgcn_s_sleep(8);
        }
      }
      until(tstart + PAR(27, T0Q));
      STAMP(5);
      sbar4(cnt + 7, lane);
      STAMP(12);
      unsigned pend = 0x1ffu;
#pragma unroll 1
      for (int spin = 0; spin < SPIN_MAX && pend; ++spin) {
        COMPILER_FENCE();
        u32x4 t[9];
#pragma unroll
        for (int i = 0; i < 9; ++i) t[i] = ld16c(rs, (pend >> i & 1) ? (i < 8 ? o8 + i * 1024 : ol) : OOB);
#pragma unroll
        for (int i = 0; i < 9; ++i) {
          if (pend >> i & 1) g[i] = t[i];
          if (__all(nz8(t[i]))) pend &= ~(1u << i);
        }
      }
      STAMP(9);
      qlast = g[8];
#pragma unroll
      for (int i = 0; i < 8; ++i)
        if (lane < 62) *reinterpret_cast<u32x4*>(scr + (w4 * 8 + i) * 1008 + 16 * lane) = g[i];
    }
    sbar4(cnt + 3, lane);
    STAMP(10);
    f32x16 sacc;
#pragma unroll
    for (int i = 0; i < 16; ++i) sacc[i] = 0.f;
#pragma unroll
    for (int jj = 0; jj < 32; ++jj) {
      const u32x4 af = *reinterpret_cast<const u32x4*>(smem + (w4 * 4 + (jj >> 3)) * 8192 + toff(r, 2 * (jj & 7) + hh));
      const u32x4 bf = jj < 31 ? *reinterpret_cast<const u32x4*>(scr + r * 1008 + 16 * (2 * (jj < 31 ? jj : 0) + hh)) : qlast;
      sacc = __builtin_amdgcn_mfma_f32_32x32x16_bf16(__builtin_bit_cast(bf16x8_t, af), __builtin_bit_cast(bf16x8_t, bf), sacc, 0, 0, 0);
    }
    float m = sacc[0];
#pragma unroll
    for (int i = 1; i < 16; ++i) m = fmaxf(m, sacc[i]);
    m = xhalf_max(m);
    if (hh == 0) *reinterpret_cast<float*>(scr + r * 1008 + 992 + 4 * w4) = m;
    sbar4(cnt + 4, lane);
    STAMP(11);
    {
      const f32x4 m4 = *reinterpret_cast<const f32x4*>(scr + r * 1008 + 992);
      m = fmaxf(fmaxf(m4[0], m4[1]), fmaxf(m4[2], m4[3]));
    }
    const float mc = m * C2;
    float p[16], ls = 0.f;
#pragma unroll
    for (int i = 0; i < 16; ++i) { p[i] = ex2(fmaf(sacc[i], C2, -mc)); ls += p[i]; }
    ls = xhalf_sum(ls);
    sbar4(cnt + 5, lane);
    u32x4* pf = reinterpret_cast<u32x4*>(scr);
    float* wl = reinterpret_cast<float*>(scr + 8192);
    if (hh == 0) wl[r * 4 + w4] = ls;
    pf[(2 * w4) * 64 + lane] = u32x4{pk_bf(p[0], p[1]), pk_bf(p[2], p[3]), pk_bf(p[4], p[5]), pk_bf(p[6], p[7])};
    pf[(2 * w4 + 1) * 64 + lane] = u32x4{pk_bf(p[8], p[9]), pk_bf(p[10], p[11]), pk_bf(p[12], p[13]), pk_bf(p[14], p[15])};
    sbar4(cnt + 6, lane);
    u32x4 pb8[8];
#pragma unroll
    for (int ks = 0; ks < 8; ++ks) pb8[ks] = pf[ks * 64 + lane];
    const f32x4 l4 = *reinterpret_cast<const f32x4*>(wl + r * 4);
    const float lsum = (l4[0] + l4[1]) + (l4[2] + l4[3]), rl = __builtin_amdgcn_rcpf(lsum);
    const int qq = (lane & 15) >> 2, pp = lane & 3, blk2 = (lane >> 4) & 1;
    const unsigned tw = tstart + __builtin_amdgcn_readfirstlane(cnt[13]) + PAR(13, TW), th = tw - PAR(14, HL);
    u32x4 vv[2][4];
    bool held = false;
    const int hx = (lane >> 3) ^ (idx & 7);
    char* ot = scr + 12288 + w4 * 4096;
#pragma unroll
    for (int c2 = 0; c2 < 2; ++c2) {
      f32x16 oacc[2];
#pragma unroll
      for (int ct = 0; ct < 2; ++ct)
#pragma unroll
        for (int i = 0; i < 16; ++i) oacc[ct][i] = 0.f;
#pragma unroll
      for (int ks = 0; ks < 8; ++ks)
#pragma unroll
        for (int ct = 0; ct < 2; ++ct) {
          typedef __attribute__((address_space(3))) s16x4* lp;
          const char* tile = smem + ((ks >> 1) * 4 + w4) * 8192;
          const int cc = 2 * c2 + ct;
          const s16x4 lo = __builtin_amdgcn_ds_read_tr16_b64_v4i16((lp)(tile + toff(16 * (ks & 1) + 4 * hh + qq, 4 * cc + 2 * blk2 + (pp >> 1)) + 8 * (pp & 1)));
          const s16x4 hi = __builtin_amdgcn_ds_read_tr16_b64_v4i16((lp)(tile + toff(16 * (ks & 1) + 8 + 4 * hh + qq, 4 * cc + 2 * blk2 + (pp >> 1)) + 8 * (pp & 1)));
          const s16x8 afr = __builtin_shufflevector(lo, hi, 0, 1, 2, 3, 4, 5, 6, 7);
          oacc[ct] = __builtin_amdgcn_mfma_f32_32x32x16_bf16(__builtin_bit_cast(bf16x8_t, afr), __builtin_bit_cast(bf16x8_t, pb8[ks]), oacc[ct], 0, 0, 0);
        }
#pragma unroll
      for (int ct = 0; ct < 2; ++ct)
#pragma unroll
        for (int g = 0; g < 4; ++g)
          *reinterpret_cast<u32x2*>(ot + r * 128 + 16 * ((4 * ct + g) ^ (r & 7)) + 8 * hh) = u32x2{pk_bf(oacc[ct][4 * g] * rl, oacc[ct][4 * g + 1] * rl), pk_bf(oacc[ct][4 * g + 2] * rl, oacc[ct][4 * g + 3] * rl)};
      if (c2 == 0) { held = (int)((unsigned)wall_clock64() + 30u - th) >= 0; if (held) lds_add(cnt + 8, 1u, lane); }
      __builtin_amdgcn_wave_barrier();
#pragma unroll
      for (int i = 0; i < 4; ++i) { vv[c2][i] = *reinterpret_cast<const u32x4*>(ot + (8 * i + hx) * 128 + 16 * ((lane & 7) ^ hx)); asm volatile("" : "+v"(vv[c2][i])); }
      __builtin_amdgcn_wave_barrier();
    }
    lds_arrive(cnt + 1, lane);
    if (!held) { until(th); lds_add(cnt + 8, 1u, lane); }
    until(tw);
#pragma unroll
    for (int i = 0; i < 8; ++i) st16c(rs, WS_APO + idx * 32768 + w4 * 8192 + i * 1024 + lane * 16, vv[i & 1][i >> 1]);
    STAMP(1);
    asm volatile("s_waitcnt vmcnt(0)" ::: "memory");
    if (lds_arrive(cnt + 9 + (w4 >> 1), lane) == 1u && hh == 0)
      st8c(rs, WS_AML + ((r * 2 + (w4 >> 1)) * 16 + (idx >> 2)) * 128 + (idx & 3) * 8, u32x2{__float_as_uint(mc + __builtin_amdgcn_logf(lsum)), tag});
    lds_add(cnt + 8, ~0u, lane);
    STAMP(2);
    STAMP_FLUSH(0); TR_FLUSH();
    return;
  }
  sfor<8, 16>([&](auto I) { row(Wa[I.v]); });
  const u16* pb = cls == 2 ? w_uv + ((size_t)(b * 256 + w4 * 64 + 16 * r4) * 32 + h) * 128 + 8 * c16 : (cls == 1 ? w_o : wo) + ((size_t)h * 128 + b * 64 + w4 * 2) * 512 + 8 * lane;
  sync();
  if (blk < 16 && w4 == 0) asm volatile("buffer_store_dwordx4 %0, %1, %2, 0 offen sc1\n\ts_nop 2" :: "v"(u32x4{tstart, tag, tstart, tag}), "v"(WS_T0 + (blk & 7) * 1024 + lane * 16), "s"(rs) : "memory");
  STAMP(13);
  float* red = reinterpret_cast<float*>(smem);
  if (cls & 1) {
    float acc[2][8];
#pragma unroll
    for (int bb = 0; bb < 2; ++bb)
#pragma unroll
      for (int k = 0; k < 8; ++k) acc[bb][k] = 0.f;
    sfor<0, 8>([&](auto IP) {
      constexpr int ip = IP.v, j = ip - (8 - WBW);
      use2(Wa[2 * ip], Wa[2 * ip + 1]);
      if constexpr (ip & 1) TR();
      if constexpr (j >= 0) { Wb[2 * j] = ld16(pb + j * 4096); Wb[2 * j + 1] = ld16(pb + j * 4096 + 512); }
      SCHED();
      const unsigned xp0 = xv[ip >> 2][ip & 3], xp1 = xv[2 + (ip >> 2)][ip & 3];
#pragma unroll
      for (int k = 0; k < 4; ++k) {
        const unsigned lo = plo(Wa[2 * ip][k], Wa[2 * ip + 1][k]), hi = phi(Wa[2 * ip][k], Wa[2 * ip + 1][k]);
        acc[0][2 * k] = dot2(lo, xp0, acc[0][2 * k]); acc[0][2 * k + 1] = dot2(hi, xp0, acc[0][2 * k + 1]);
        acc[1][2 * k] = dot2(lo, xp1, acc[1][2 * k]); acc[1][2 * k + 1] = dot2(hi, xp1, acc[1][2 * k + 1]);
      }
      SCHED();
    });
    {
      float v8[8], v4[4];
#pragma unroll
      for (int k = 0; k < 8; ++k) v8[k] = rs32(acc[0][k], acc[1][k]);
#pragma unroll
      for (int k = 0; k < 4; ++k) v4[k] = rs16(v8[k], v8[k + 4]);
      *reinterpret_cast<f32x4*>(red + (w4 * 2 + (lane >> 5)) * 128 + 8 * c16 + 4 * (r4 & 1)) = f32x4{v4[0], v4[1], v4[2], v4[3]};
    }
    COMPILER_FENCE();
    const unsigned got = lds_arrive_nw(L_CNT + 8);
    f32x4 t4[4];
#pragma unroll
    for (int w = 0; w < 4; ++w) t4[w] = *reinterpret_cast<const f32x4*>(red + (w * 2 + (lane >> 5)) * 128 + 8 * c16 + 4 * (r4 & 1));
    SCHED();
    if (lds_done(got) == 3u) {
      const f32x4 t = (t4[0] + t4[1]) + (t4[2] + t4[3]);
      st16c(rs, WS_QX + (((cls >> 1) * 32 + h) * 2 + (lane >> 5)) * 1024 + b * 512 + (lane & 31) * 16, u32x4{pk_bf(t[0], t[1]), tag, pk_bf(t[2], t[3]), tag});
      STAMP(15);
      SCHED();
      lds_add(cnt + 2, 1u, lane);
    } else lds_wait(cnt + 2, 5u);
    sfor<WBW, 8>([&](auto J) {
      constexpr int j = J.v;
      use2(Wb[2 * (j - WBW)], Wb[2 * (j - WBW) + 1]);
      if constexpr ((j - WBW) & 1) TR();
      Wb[2 * j] = ld16(pb + j * 4096); Wb[2 * j + 1] = ld16(pb + j * 4096 + 512);
      if constexpr (j == 7) lds_add(cnt + 14, 1u, lane);
      SCHED();
    });
    {
      const unsigned tw = __builtin_nontemporal_load(reinterpret_cast<const unsigned*>(out + (idx >> 5) * 1024 + (cls >> 1) * 512 + 16 * (idx & 31)));
      sfor<8 - WBW, 8>([&](auto J) { use2(Wb[2 * J.v], Wb[2 * J.v + 1]); if constexpr (J.v & 1) TR(); });
      asm volatile("" :: "v"(tw));
    }
    STAMP(8);
  } else {
    sfor<0, 16 - WBW>([&](auto IP) {
      constexpr int ip = IP.v, j = ip - (8 - WBW);
      if constexpr (ip < 8) use2(Wa[2 * ip], Wa[2 * ip + 1]); else use2(Wb[2 * ip - 16], Wb[2 * ip - 15]);
      if constexpr (ip & 1) TR();
      if constexpr (j >= 0) { Wb[2 * j] = ld16nt(pb + j * 8192); Wb[2 * j + 1] = ld16nt(pb + j * 8192 + 4096); }
      SCHED();
    });
    u32x4 g0, g1;
    const unsigned qo = WS_QX + h * 2048 + lane * 16;
    if (w4 == 0) { shint<512, 0>(ws + WS_QX + h * 2048, tag); lds_add(cnt + 2, 1u, lane); } else lds_wait(cnt + 2, 1u);
    STAMP(5);
#pragma unroll 1
    for (int spin = 0; spin < SPIN_MAX; ++spin) {
      COMPILER_FENCE();
      g0 = ld16c(rs, qo); g1 = ld16c(rs, qo + 1024);
      SCHED();
      lds_add(cnt + 14, spin == 0, lane);
      SCHED();
      if (__all((g0[1] == tag) & (g0[3] == tag) & (g1[1] == tag) & (g1[3] == tag))) break;
      __builtin_amdgcn_s_sleep(1);
    }
    sfor<8 - WBW, 8>([&](auto J) { if constexpr (J.v & 1) TR(); });
    const u32x4 q0 = qsum(g0), q1 = qsum(g1);
    STAMP(9);
    float v[8];
    {
      float u[16];
#pragma unroll
      for (int i = 0; i < 16; ++i) {
        float a = 0.f, c = 0.f;
#pragma unroll
        for (int k = 0; k < 4; ++k) { a = dot2(Wa[i][k], q0[k], a); c = dot2(Wa[i][k], q1[k], c); }
        u[i] = (c16 & 2 ? c : a) + dpp<0x4E>(c16 & 2 ? a : c);
        asm volatile("" : "+v"(u[i]));
      }
#pragma unroll
      for (int i = 0; i < 8; ++i) {
        float t = (c16 & 1 ? u[i + 8] : u[i]) + dpp<0xB1>(c16 & 1 ? u[i] : u[i + 8]);
        asm volatile("" : "+v"(t));
        t += dpp<0x124>(t);
        asm volatile("" : "+v"(t));
        t += dpp<0x128>(t);
        asm volatile("" : "+v"(t));
        v[i] = t;
      }
    }
    u32x4 o = {pk_bf(v[0], v[1]), pk_bf(v[2], v[3]), pk_bf(v[4], v[5]), pk_bf(v[6], v[7])};
    if ((o[0] | o[1]) == 0u) o[0] = 0x8000u;
    if ((o[2] | o[3]) == 0u) o[2] = 0x8000u;
    if (c16 < 4) st16c(rs, WS_QA + (((c16 >> 1) & 1) * 32 + h) * 1024 + (b * 256 + w4 * 64 + 16 * r4 + 8 * (c16 & 1)) * 2, o);
    STAMP(10);
  }
  asm volatile("" :: "v"(xv[0]), "v"(xv[1]), "v"(xv[2]), "v"(xv[3]));
  SCHED();
  if (cls == 3) {
    fin(IC<2>{}, WS_GB, 512, false);
    STAMP(6);
    STAMP_FLUSH(0); TR_FLUSH();
    return;
  }
#pragma unroll
  for (int ip = 0; ip < 8; ++ip) {
#pragma unroll
    for (int k = 0; k < 4; ++k) { const unsigned lo = plo(Wb[2 * ip][k], Wb[2 * ip + 1][k]), hi = phi(Wb[2 * ip][k], Wb[2 * ip + 1][k]); Wb[2 * ip][k] = lo; Wb[2 * ip + 1][k] = hi; }
    pin2(Wb[2 * ip], Wb[2 * ip + 1]);
  }
  if (cls == 2) {
    float* red2 = red + 4 * 256;
    float* wml = red + 3072;
    u32x8 f[4];
    until<6>(tstart + PAR(13, TW) + PAR(28, 100));
#pragma unroll 1
    for (int spin = 0; spin < SPIN_MAX; ++spin) {
      sld4x32(ws + WS_AML + ((h * 2 + b) * 16 + w4 * 4) * 128, f);
      unsigned e = 0u;
#pragma unroll
      for (int i = 0; i < 16; ++i) e |= f[i >> 2][2 * (i & 3) + 1] ^ tag;
      if (e == 0u) break;
      __builtin_amdgcn_s_sleep(2);
    }
    STAMP(3);
    u32x4 pp[8];
#pragma unroll
    for (int i = 0; i < 8; ++i) pp[i] = ld16c(rs, WS_APO + (w4 * 16 + 2 * i + (lane >> 5)) * 32768 + (2 * b + (r4 & 1)) * 8192 + (2 * (h >> 3) + ((lane >> 3) & 1)) * 1024 + ((h ^ (2 * i + (lane >> 5))) & 7) * 128 + (lane & 7) * 16);
    SCHED();
    float wt[8];
    {
      float M = -INFINITY, L = 0.f;
#pragma unroll
      for (int i = 0; i < 8; ++i) {
        unsigned z0 = f[i >> 1][4 * (i & 1)], z1 = f[i >> 1][4 * (i & 1) + 2];
        asm("" : "+s"(z0), "+s"(z1));
        wt[i] = __uint_as_float(lane < 32 ? z0 : z1); M = fmaxf(M, wt[i]);
      }
      M = xhalf_max(M);
#pragma unroll
      for (int i = 0; i < 8; ++i) { wt[i] = ex2(wt[i] - M); L += wt[i]; }
      L = xhalf_sum(L);
      if (lane == 0) *reinterpret_cast<f32x2*>(wml + 2 * w4) = f32x2{M, L};
    }
    SCHED();
    float a8[8];
#pragma unroll
    for (int k = 0; k < 8; ++k) a8[k] = 0.f;
#pragma unroll
    for (int i = 0; i < 8; ++i) {
#pragma unroll
      for (int k = 0; k < 4; ++k) { a8[2 * k] = fmaf(wt[i], bflo(pp[i][k]), a8[2 * k]); a8[2 * k + 1] = fmaf(wt[i], bfhi(pp[i][k]), a8[2 * k + 1]); }
      asm volatile("" : "+v"(a8[0]), "+v"(a8[1]), "+v"(a8[2]), "+v"(a8[3]), "+v"(a8[4]), "+v"(a8[5]), "+v"(a8[6]), "+v"(a8[7]));
    }
    STAMP(6);
    *reinterpret_cast<f32x4*>(red + w4 * 256 + 8 * (lane & 31) + 4 * (lane >> 5)) = f32x4{rs32(a8[0], a8[4]), rs32(a8[1], a8[5]), rs32(a8[2], a8[6]), rs32(a8[3], a8[7])};
    sbar4(cnt + 3, lane);
    STAMP(4);
    float acc[2][8];
#pragma unroll
    for (int bb = 0; bb < 2; ++bb) {
      u32x4 cv[2];
      const f32x4 ml = *reinterpret_cast<const f32x4*>(wml + 4 * bb);
      const float M = fmaxf(ml[0], ml[2]), e0 = ex2(ml[0] - M), e1 = ex2(ml[2] - M), rl = 1.f / fmaf(e0, ml[1], e1 * ml[3]), s0 = e0 * rl, s1 = e1 * rl;
#pragma unroll
      for (int e = 0; e < 2; ++e) {
        const float* src = red + bb * 512 + w4 * 64 + 16 * r4 + 8 * e;
        const f32x4 t0 = s0 * *reinterpret_cast<const f32x4*>(src) + s1 * *reinterpret_cast<const f32x4*>(src + 256), t1 = s0 * *reinterpret_cast<const f32x4*>(src + 4) + s1 * *reinterpret_cast<const f32x4*>(src + 260);
        cv[e] = u32x4{pk_bf(t0[0], t0[1]), pk_bf(t0[2], t0[3]), pk_bf(t1[0], t1[1]), pk_bf(t1[2], t1[3])};
      }
#pragma unroll
      for (int k = 0; k < 8; ++k) acc[bb][k] = 0.f;
#pragma unroll
      for (int ip = 0; ip < 8; ++ip)
#pragma unroll
        for (int k = 0; k < 4; ++k) { acc[bb][2 * k] = dot2(Wb[2 * ip][k], cv[ip >> 2][ip & 3], acc[bb][2 * k]); acc[bb][2 * k + 1] = dot2(Wb[2 * ip + 1][k], cv[ip >> 2][ip & 3], acc[bb][2 * k + 1]); }
    }
    {
      float v8[8], v4[4];
#pragma unroll
      for (int k = 0; k < 8; ++k) v8[k] = rs32(acc[0][k], acc[1][k]);
#pragma unroll
      for (int k = 0; k < 4; ++k) v4[k] = rs16(v8[k], v8[k + 4]);
      *reinterpret_cast<f32x4*>(red2 + (w4 * 2 + (lane >> 5)) * 128 + 8 * c16 + 4 * (r4 & 1)) = f32x4{v4[0], v4[1], v4[2], v4[3]};
    }
    {
      COMPILER_FENCE();
      const unsigned got = lds_arrive_nw(L_CNT + 16);
      f32x4 t4[4];
#pragma unroll
      for (int w = 0; w < 4; ++w) t4[w] = *reinterpret_cast<const f32x4*>(red2 + w * 256 + 4 * lane);
      SCHED();
      if (lds_done(got) == 3u) {
        const f32x4 t = (t4[0] + t4[1]) + (t4[2] + t4[3]);
        const unsigned co = WS_CX + ((h * 2 + b) * 256 + 64 * r4 + 32 * (c16 & 1) + 2 * (c16 >> 1)) * 8;
        st16c(rs, co, u32x4{__float_as_uint(t[0]), tag, __float_as_uint(t[1]), tag});
        st16c(rs, co + 128, u32x4{__float_as_uint(t[2]), tag, __float_as_uint(t[3]), tag});
      }
    }
    STAMP(12);
    STAMP_FLUSH(0); TR_FLUSH();
    return;
  }
  {
    unsigned op;
    {
      u32x4 c;
      const unsigned co = WS_CX + ((h * 2 + ((lane >> 3) & 1)) * 256 + (lane >> 5) * 128 + b * 64 + w4 * 16 + 2 * (lane & 7)) * 8;
      if (idx == 0 && w4 == 1) {
#pragma unroll 1
        for (int spin = 0; spin < SPIN_MAX; ++spin) {
          COMPILER_FENCE();
          if (__all(ld8c(rs, WS_QX + 65536 + (lane >> 1) * 2048 + (lane & 1) * 512)[1] == tag)) break;
          __builtin_amdgcn_s_sleep(8);
        }
      }
      until<6>(tstart + PAR(13, TW) + PAR(29, 370));
      if (w4 == 0) { shint<2048, 0>(ws + WS_CX + (h * 512 + b * 64) * 8, tag); lds_add(cnt + 7, 1u, lane); }
      else lds_wait(cnt + 7, 1u);
      STAMP(3);
#pragma unroll 1
      for (int spin = 0; spin < SPIN_MAX; ++spin) {
        COMPILER_FENCE();
        c = ld16c(rs, co);
        if (__all((c[1] == tag) & (c[3] == tag))) break;
        __builtin_amdgcn_s_sleep(1);
      }
      float o0 = __uint_as_float(c[0]), o1 = __uint_as_float(c[2]);
      o0 += dpp<0x128>(o0); o1 += dpp<0x128>(o1);
      op = pk_bf(o0, o1);
    }
    STAMP(14);
    float* red3 = red + 4096;
    float a[2][8];
#pragma unroll
    for (int bb = 0; bb < 2; ++bb)
#pragma unroll
      for (int k = 0; k < 8; ++k) a[bb][k] = 0.f;
#pragma unroll
    for (int ip = 0; ip < 8; ++ip) {
      const unsigned v0 = __builtin_amdgcn_readlane(op, ip), v1 = __builtin_amdgcn_readlane(op, 32 + ip);
#pragma unroll
      for (int k = 0; k < 4; ++k) {
        a[0][2 * k] = dot2(Wb[2 * ip][k], v0, a[0][2 * k]); a[0][2 * k + 1] = dot2(Wb[2 * ip + 1][k], v0, a[0][2 * k + 1]);
        a[1][2 * k] = dot2(Wb[2 * ip][k], v1, a[1][2 * k]); a[1][2 * k + 1] = dot2(Wb[2 * ip + 1][k], v1, a[1][2 * k + 1]);
      }
    }
#pragma unroll
    for (int bb = 0; bb < 2; ++bb) {
      *reinterpret_cast<f32x4*>(red3 + (w4 * 2 + bb) * 512 + 8 * lane) = f32x4{a[bb][0], a[bb][1], a[bb][2], a[bb][3]};
      *reinterpret_cast<f32x4*>(red3 + (w4 * 2 + bb) * 512 + 8 * lane + 4) = f32x4{a[bb][4], a[bb][5], a[bb][6], a[bb][7]};
    }
    sbar4(cnt + 5, lane);
    f32x4 t = {0.f, 0.f, 0.f, 0.f};
#pragma unroll
    for (int w = 0; w < 4; ++w) t += *reinterpret_cast<const f32x4*>(red3 + w * 1024 + 4 * tid);
    st16c(rs, WS_GA + ((tid >> 7) * 64 + idx) * 2048 + (tid & 127) * 16, u32x4{pk_bf(t[0], t[1]), tag, pk_bf(t[2], t[3]), tag});
    STAMP(5);
  }
  fin(IC<1>{}, WS_GA, 0, idx == 0);
  STAMP(6);
  STAMP_FLUSH(0); TR_FLUSH();
}

extern "C" void kernel_launch(void* const* d_in, const int* in_sizes, int n_in, void* d_out, int out_size, void* d_ws, size_t ws_size, hipStream_t stream) {
  const u16* xA = (const u16*)d_in[0];
  const u16* ckv = (const u16*)d_in[1];
  const u16* w_q = (const u16*)d_in[2];
  const u16* w_uk = (const u16*)d_in[3];
  const u16* w_uv = (const u16*)d_in[4];
  const u16* w_o = (const u16*)d_in[5];
  const u16* xB = (const u16*)d_in[6];
  const u16* kc = (const u16*)d_in[7];
  const u16* vc = (const u16*)d_in[8];
  const u16* wq = (const u16*)d_in[9];
  const u16* wo = (const u16*)d_in[10];
  kmain<<<256, 512, 0, stream>>>(ckv, w_q, w_uk, wq, xA, xB, w_uv, w_o, kc, vc, wo, (char*)d_ws, (u16*)d_out);
}
```

```cpp
#include <hip/hip_runtime.h>
#include <hip/hip_bf16.h>
#include <stdint.h>

#define DI __device__ __forceinline__
typedef __attribute__((ext_vector_type(4))) unsigned u32x4;
typedef __attribute__((ext_vector_type(2))) unsigned u32x2;
typedef __attribute__((ext_vector_type(4))) float f32x4;
typedef __attribute__((ext_vector_type(2))) float f32x2;
typedef __attribute__((ext_vector_type(16))) float f32x16;
typedef __attribute__((ext_vector_type(8))) __bf16 bf16x8_t;
typedef __attribute__((ext_vector_type(16))) __bf16 bf16x16_t;
typedef __attribute__((ext_vector_type(2))) __bf16 bf16x2_t;
typedef __attribute__((ext_vector_type(4))) short s16x4;
typedef __attribute__((ext_vector_type(8))) short s16x8;
typedef unsigned short u16;
typedef unsigned long long u64;

constexpr int NS0 = 13, NS1 = 17, NS2 = 16, NS3 = 18;
constexpr u64 TL0 = 0xcaeaa685e6d54caull, TH0 = 0x3deb6ull, TL1 = 0x81c65444d2c7100ull, TH1 = 0x3ee33c2ca24ull;
constexpr u64 TL2 = 0x8e26163ce2460c1ull, TH2 = 0xf37d31ae9ull, TL3 = 0x7dd697490308142ull, TH3 = 0xffbe75bed9e5ull;
constexpr int RA2 = 0xC0, RA3 = 3;
constexpr int AHL = 2;
constexpr int QPE = 7;
constexpr int WBW = 5;
#define TL 2360
#define TW 1650
constexpr unsigned TS0 = 1160, TS1 = 1420, TS2 = 1320, TS3 = 1420, STG = 12, BSG = 6, HL = 40, TP0 = TW + 170, TP2 = TW + 380, PA = 150, TF = 172, T0B = 410, T0Q = 880;
#define PAR(i, v) (v)
constexpr u64 LA2 = TH2 >> (6 * (NS2 - 11)), LA3 = TH3 >> (6 * (NS3 - 11));
constexpr u64 chunk_mask(u64 lo, u64 hi, int n) { u64 m = 0; for (int i = 0; i < n; ++i) m |= 1ull << ((i < 10 ? lo >> (6 * i) : hi >> (6 * (i - 10))) & 63); return m; }
static_assert((chunk_mask(TL0, TH0, NS0) | chunk_mask(TL1, TH1, NS1) | chunk_mask(TL2, TH2, NS2) | chunk_mask(TL3, TH3, NS3)) == ~0ull && (RA2 & RA3) == 0, "every chunk once");
static_assert(NS0 + NS1 + NS2 + NS3 == 64, "split sizes");
constexpr int NSMIN = 13, NSMAX = 18;
static_assert(NS0 >= NSMIN && NS1 >= NSMIN && NS2 - 1 >= NSMIN && NS3 - 1 >= NSMIN && NS0 <= NSMAX && NS1 + 1 <= NSMAX && NS2 <= NSMAX && NS3 <= NSMAX, "range");
constexpr int SK = 4096;
constexpr float C2 = 0.08838834764831845f * 1.4426950408889634f;
constexpr u64 GOLD = 0x9E3779B97F4A7C15ull;

constexpr unsigned WS_EP = 0;
constexpr unsigned WS_QX = 4096;
constexpr unsigned WS_QA = WS_QX + 2 * 32 * 256 * 8;
constexpr unsigned WS_AML = WS_QA + 2 * 32 * 512 * 2;
constexpr unsigned WS_CX = WS_AML + 32 * 2 * 16 * 128;
constexpr unsigned WS_GZ = WS_CX + 32 * 2 * 2 * 128 * 8;
constexpr unsigned WS_T0 = WS_GZ + 256 * 128;
constexpr unsigned WS_GB = WS_T0 + 64 * 128;
constexpr unsigned WS_GA = WS_GB + 2 * 128 * 256 * 8;
constexpr unsigned WS_APO = 2u << 20;
static_assert(WS_GA + 64 * 2 * 256 * 8 <= WS_APO, "layout");
constexpr unsigned WS_END = WS_APO + 32 * 2 * 64 * 256 * 2;
#define STAMP(i) do {} while (0)
#define STAMP_DECL do {} while (0)
#define STAMP_FLUSH(grp) do {} while (0)

#define TR() do {} while (0)
#define TR_FLUSH() do {} while (0)
DI float bflo(unsigned u) { return __uint_as_float(u << 16); }
DI float bfhi(unsigned u) { return __uint_as_float(u & 0xffff0000u); }
DI unsigned pk_bf(float a, float b) { const f32x2 v = {a, b}; return __builtin_bit_cast(unsigned, __builtin_convertvector(v, bf16x2_t)); }
DI float dot2(unsigned k, unsigned q, float acc) {
  return __builtin_amdgcn_fdot2_f32_bf16(__builtin_bit_cast(bf16x2_t, k), __builtin_bit_cast(bf16x2_t, q), acc, false);
}
DI unsigned plo(unsigned w0, unsigned w1) { return __builtin_amdgcn_perm(w1, w0, 0x05040100u); }
DI unsigned phi(unsigned w0, unsigned w1) { return __builtin_amdgcn_perm(w1, w0, 0x07060302u); }
template <int CTRL> DI float dpp(float x) {
  return __builtin_bit_cast(float, __builtin_amdgcn_mov_dpp(__builtin_bit_cast(int, x), CTRL, 0xf, 0xf, true));
}
DI float row16_sum(float x) { x += dpp<0xB1>(x); x += dpp<0x4E>(x); x += dpp<0x141>(x); x += dpp<0x128>(x); return x; }
DI float row16_max(float x) { x = fmaxf(x, dpp<0xB1>(x)); x = fmaxf(x, dpp<0x4E>(x)); x = fmaxf(x, dpp<0x141>(x)); x = fmaxf(x, dpp<0x128>(x)); return x; }
DI float xhalf_max(float x) { auto t = __builtin_amdgcn_permlane32_swap(__float_as_uint(x), __float_as_uint(x), false, false); return fmaxf(__uint_as_float(t[0]), __uint_as_float(t[1])); }
DI float xhalf_sum(float x) { auto t = __builtin_amdgcn_permlane32_swap(__float_as_uint(x), __float_as_uint(x), false, false); return __uint_as_float(t[0]) + __uint_as_float(t[1]); }
DI u32x4 ld16(const void* p) { return *reinterpret_cast<const u32x4*>(p); }
DI u32x4 ld16nt(const void* p) { return __builtin_nontemporal_load(reinterpret_cast<const u32x4*>(p)); }
DI float ex2(float x) { return __builtin_amdgcn_exp2f(x); }
#define SCHED() __builtin_amdgcn_sched_barrier(0)

typedef __amdgpu_buffer_rsrc_t rsrc_t;
DI u32x4 ld16c(rsrc_t rs, unsigned off) { return __builtin_amdgcn_raw_buffer_load_b128(rs, (int)off, 0, 16); }
DI u32x2 ld8c(rsrc_t rs, unsigned off) { return __builtin_amdgcn_raw_buffer_load_b64(rs, (int)off, 0, 16); }
DI void st16c(rsrc_t rs, unsigned off, u32x4 v) { __builtin_amdgcn_raw_buffer_store_b128(v, rs, (int)off, 0, 16); }
DI void st8c(rsrc_t rs, unsigned off, u32x2 v) { __builtin_amdgcn_raw_buffer_store_b64(v, rs, (int)off, 0, 16); }
template <int O1> DI void sld2(const char* p, u32x2& a, u32x2& b) {
  asm volatile("s_load_dwordx2 %0, %2, 0x0 glc\n\ts_load_dwordx2 %1, %2, %3 glc\n\ts_waitcnt lgkmcnt(0)" : "=&s"(a), "=&s"(b) : "s"(p), "n"(O1) : "memory");
}
typedef __attribute__((ext_vector_type(8))) unsigned u32x8;
DI void sld4x32(const char* p, u32x8 (&v)[4]) {
  asm volatile("s_load_dwordx8 %0, %4, 0x0 glc\n\ts_load_dwordx8 %1, %4, 0x80 glc\n\ts_load_dwordx8 %2, %4, 0x100 glc\n\ts_load_dwordx8 %3, %4, 0x180 glc\n\ts_waitcnt lgkmcnt(0)"
               : "=&s"(v[0]), "=&s"(v[1]), "=&s"(v[2]), "=&s"(v[3]) : "s"(p) : "memory");
}
constexpr int SPIN_MAX = 1 << 20;
constexpr unsigned OOB = 0x7ffffff0u;
DI void rawbar() { __builtin_amdgcn_sched_barrier(0); __builtin_amdgcn_s_barrier(); __builtin_amdgcn_sched_barrier(0); }
#define COMPILER_FENCE() asm volatile("" ::: "memory")
template <int O1, int SL> DI void shint(const char* p, unsigned tag) {
#pragma unroll 1
  for (int spin = 0; spin < SPIN_MAX; ++spin) {
    u32x2 a, b;
    sld2<O1>(p, a, b);
    if (((a[1] ^ tag) | (b[1] ^ tag)) == 0u) break;
    __builtin_amdgcn_s_sleep(SL);
  }
}

template <int N> struct IC { static constexpr int v = N; };
template <int I, int N, class F> DI void sfor(F&& f) { if constexpr (I < N) { f(IC<I>{}); sfor<I + 1, N>(f); } }
typedef __attribute__((address_space(3))) unsigned lu32;
DI void lds_wait(unsigned* cnt, unsigned target) {
#pragma unroll 1
  for (int spin = 0; spin < SPIN_MAX; ++spin) {
    if (__builtin_amdgcn_readfirstlane(__hip_atomic_load((lu32*)cnt, __ATOMIC_RELAXED, __HIP_MEMORY_SCOPE_WORKGROUP)) >= target) break;
    __builtin_amdgcn_s_sleep(1);
  }
  COMPILER_FENCE();
}
DI void lds_wait_slow(unsigned* cnt, unsigned target) {
#pragma unroll 1
  for (int spin = 0; spin < SPIN_MAX; ++spin) {
    if (__builtin_amdgcn_readfirstlane(__hip_atomic_load((lu32*)cnt, __ATOMIC_RELAXED, __HIP_MEMORY_SCOPE_WORKGROUP)) >= target) break;
    __builtin_amdgcn_s_sleep(3);
  }
  COMPILER_FENCE();
}
template <int SL = 1> DI void until(unsigned t) {
#pragma unroll 1
  for (int spin = 0; spin < 1024; ++spin) { if ((int)((unsigned)wall_clock64() - t) >= 0) break; __builtin_amdgcn_s_sleep(SL); }
}
DI void lds_hold(unsigned* flag) {
#pragma unroll 1
  for (int spin = 0; spin < SPIN_MAX; ++spin) {
    if (__builtin_amdgcn_readfirstlane(__hip_atomic_load((lu32*)flag, __ATOMIC_RELAXED, __HIP_MEMORY_SCOPE_WORKGROUP)) == 0u) break;
    __builtin_amdgcn_s_sleep(2);
  }
}
DI unsigned lds_arrive_nw(unsigned addr) {
  unsigned old, one = 1u;
  asm volatile("s_mov_b64 exec, 1\n\tds_add_rtn_u32 %0, %1, %2\n\ts_mov_b64 exec, -1" : "=v"(old) : "v"(addr), "v"(one) : "memory");
  return old;
}
DI unsigned lds_done(unsigned old) { asm volatile("s_waitcnt lgkmcnt(0)" : "+v"(old) :: "memory"); return __builtin_amdgcn_readfirstlane(old); }
DI unsigned lds_arrive(unsigned* cnt, int) { return lds_done(lds_arrive_nw((unsigned)(size_t)(lu32*)cnt)); }
DI void lds_add(unsigned* cnt, unsigned v, int) {
  asm volatile("s_mov_b64 exec, 1\n\tds_add_u32 %0, %1\n\ts_mov_b64 exec, -1" :: "v"((unsigned)(size_t)(lu32*)cnt), "v"(v) : "memory");
}
DI void sbar4(unsigned* cnt, int lane) { if (lds_arrive(cnt, lane) < 3u) lds_wait(cnt, 4u); }
DI float rs32(float a, float b) { auto t = __builtin_amdgcn_permlane32_swap(__float_as_uint(a), __float_as_uint(b), false, false); return __uint_as_float(t[0]) + __uint_as_float(t[1]); }
DI float rs16(float a, float b) { auto t = __builtin_amdgcn_permlane16_swap(__float_as_uint(a), __float_as_uint(b), false, false); return __uint_as_float(t[0]) + __uint_as_float(t[1]); }
template <int N> DI unsigned rowbcast(unsigned x) { return (unsigned)__builtin_amdgcn_mov_dpp((int)x, 0x150 + N, 0xf, 0xf, true); }
DI void pin2(u32x4& a, u32x4& b) { asm volatile("" : "+v"(a), "+v"(b)); }
DI void use2(const u32x4& a, const u32x4& b) { asm volatile("" :: "v"(a), "v"(b)); }
DI bool nz8(const u32x4& v) { return ((v[0] | v[1]) != 0u) & ((v[2] | v[3]) != 0u); }

DI u32x4 qsum(const u32x4& g) {
  const auto ta = __builtin_amdgcn_permlane32_swap(g[0], g[0], false, false), tc = __builtin_amdgcn_permlane32_swap(g[2], g[2], false, false);
  const unsigned a = pk_bf(bflo(ta[0]) + bflo(ta[1]), bfhi(ta[0]) + bfhi(ta[1])), c = pk_bf(bflo(tc[0]) + bflo(tc[1]), bfhi(tc[0]) + bfhi(tc[1]));
  const auto ra = __builtin_amdgcn_permlane16_swap(a, a, false, false), rc = __builtin_amdgcn_permlane16_swap(c, c, false, false);
  return u32x4{ra[0], rc[0], ra[1], rc[1]};
}
DI unsigned toff(unsigned row, unsigned ch) { return 256u * row + 16u * (ch ^ (((row & 3) << 2) | ((row >> 2) & 3))); }
constexpr int SMEM = 160 * 1024;
constexpr int L_SCR = 131072;
constexpr int L_MRG = 65536;
constexpr int L_CNT = L_SCR + 32 * 1008;

struct KV { u32x4 r[8]; };
constexpr int CODE_BYTES = 43392;

template <class T> DI T* gptr(unsigned lo, unsigned hi) { return (T*)(__attribute__((address_space(1))) T*)(((u64)hi << 32) | lo); }

__global__ __launch_bounds__(512, 2) void kmain(const u16*, const u16*, const u16*, const u16*, const u16*, const u16*, const u16*, const u16*, const u16*,
                                                const u16*, const u16*, char*, u16*) {
  u32x2 kpa, kpx, kc2; u32x4 kb4; u32x8 kb8; u64 t64;
  int tid = threadIdx.x, blk = blockIdx.x;
  const auto kp = __builtin_amdgcn_kernarg_segment_ptr();
  const int o1 = blk & 24, o2 = 32 + ((blk >> 1) & 8);
  asm volatile("s_load_dwordx2 %0, %8, %9\n\ts_load_dwordx2 %1, %8, %10\n\ts_load_dwordx4 %2, %8, 0x30\n\ts_load_dwordx8 %3, %8, 0x40\n\ts_load_dwordx2 %4, %8, 0x60\n\ts_memrealtime %5"
               : "=&s"(kpa), "=&s"(kpx), "=&s"(kb4), "=&s"(kb8), "=&s"(kc2), "=&s"(t64), "+v"(tid), "+s"(blk) : "s"(kp), "s"(o1), "s"(o2));
  __builtin_assume(tid >= 0 && tid < 512 && blk >= 0 && blk < 256);
  const unsigned cpf = __builtin_amdgcn_raw_buffer_load_b32(__builtin_amdgcn_make_buffer_rsrc((void*)(__builtin_amdgcn_s_getpc() & ~255ull), 0, CODE_BYTES, 0x00020000),
                                                            (int)((tid & 0x10f) == 0x100 ? (blk >> 3) * 2048 + (tid & 0xf0) * 8 : OOB), 0, 0);
  COMPILER_FENCE();
  SCHED();
  __shared__ __attribute__((aligned(16))) char smem[SMEM];
  const int lane = tid & 63;
  const int wave = __builtin_amdgcn_readfirstlane(tid >> 6), w4 = wave & 3;
  const int cls = (blk >> 3) & 3;
  int idx = 8 * (blk & 7) + (blk >> 5);
  idx = (idx & ~7) | ((idx + ((wave >= 4 ? 0 : 0x2400) >> (4 * cls))) & 7);
  const int h = idx >> 1, b = idx & 1;
  const int r4 = lane >> 4, c16 = lane & 15;
  unsigned* cnt = reinterpret_cast<unsigned*>(smem + L_CNT);
  STAMP_DECL;
  STAMP(0);
  const unsigned c0 = cls ? 0u : ~0u;
  unsigned sa = cls ? 8192u : 4096u;
  unsigned ao = ((((b * 256 + w4 * 64) * 8192 + h * 256) & ~c0) | (((idx * 128 + w4) * 1024) & c0)) + (r4 << (17u - (c0 & 9u))) + c16 * 16, xo = (b * 256 + w4 * 64 + 16 * r4) * 2;
  asm volatile("" : "+v"(ao), "+v"(xo), "+s"(sa));
  SCHED();
  asm volatile("s_waitcnt lgkmcnt(0)" : "+s"(kpa), "+s"(kpx), "+s"(kb4), "+s"(kb8), "+s"(kc2), "+s"(t64) : "s"(kp), "s"(o1), "s"(o2));
  const unsigned tstart = (unsigned)t64;
  const char* __restrict__ pa = gptr<const char>(kpa[0], kpa[1]); const char* __restrict__ xp = gptr<const char>(kpx[0], kpx[1]);
  const u16* __restrict__ w_uv = gptr<const u16>(kb4[0], kb4[1]); const u16* __restrict__ w_o = gptr<const u16>(kb4[2], kb4[3]);
  const u16* __restrict__ kc = gptr<const u16>(kb8[0], kb8[1]); const u16* __restrict__ vc = gptr<const u16>(kb8[2], kb8[3]);
  const u16* __restrict__ wo = gptr<const u16>(kb8[4], kb8[5]); char* __restrict__ ws = gptr<char>(kb8[6], kb8[7]);
  u16* __restrict__ out = gptr<u16>(kc2[0], kc2[1]);
  const rsrc_t rs = __builtin_amdgcn_make_buffer_rsrc(ws, 0, (int)WS_END, 0x00020000);
  unsigned tag, tlow;
  u32x2 epw;
  auto take_epoch = [&]() {
    asm volatile("" : "+v"(epw) : "v"(cpf));
    const u64 t64 = (((u64)(unsigned)__builtin_amdgcn_readfirstlane(epw[1]) << 32) | (unsigned)__builtin_amdgcn_readfirstlane(epw[0])) + GOLD;
    tag = (unsigned)(t64 >> 32); tlow = (unsigned)t64;
    asm volatile("s_waitcnt lgkmcnt(0)" : "+s"(tag), "+s"(tlow) :: "memory");
  };
  if (wave >= 4) {
    if (tid - 256 < 16) cnt[tid - 256] = 0u;
    asm volatile("s_waitcnt lgkmcnt(0)" ::: "memory");
    const unsigned pTL = PAR(0, TL), pTS = PAR(1 + cls, cls == 0 ? TS0 : (cls == 1 ? TS1 : (cls == 2 ? TS2 : TS3))), pSTG = PAR(9, STG), pBSG = PAR(10, BSG);
    const unsigned pRA2 = PAR(11, RA2), pRA3 = PAR(12, RA3), pTP = PAR(15 + cls, cls == 0 ? TP0 : TP2), pPA = PAR(19 + cls, (cls & 1) ? 0u : PA);
    rawbar();
    const int e2 = (pRA2 >> (idx & 7)) & 1, e3 = (pRA3 >> (idx & 7)) & 1;
    const int ns = cls == 0 ? NS0 : (cls == 1 ? NS1 + e2 + e3 : (cls == 2 ? NS2 - e2 : NS3 - e3));
    const unsigned pTA = __builtin_amdgcn_readfirstlane((unsigned)((float)pTS * __builtin_amdgcn_rcpf((float)(ns - 5)) + 0.5f));
    const size_t kvo = ((size_t)(b * 32 + h) * SK + w4 * 16) * 256;
    const char* kb = reinterpret_cast<const char*>(kc) + kvo;
    const char* vb = reinterpret_cast<const char*>(vc) + kvo;
    const unsigned lo = lane * 16u;
    u64 tlo = cls == 0 ? TL0 : (cls == 1 ? TL1 : (cls == 2 ? TL2 : TL3));
    u64 thi = cls == 0 ? TH0 : (cls == 1 ? TH1 | ((e2 ? LA2 : LA3) << (6 * (NS1 - 10))) : (cls == 2 ? TH2 : TH3));
    const u64 tlo0 = tlo, thi0 = thi;
    int hn = 0;
    const char* hb = kb;
    const char* pc[2];
    auto next2 = [&]() {
#pragma unroll
      for (int i = 0; i < 2; ++i) {
        if (hn == ns) { tlo = tlo0; thi = thi0; hb = vb; }
        ++hn;
        pc[i] = hb + ((((unsigned)tlo + (unsigned)idx) & 63u) << 14);
        tlo = (tlo >> 6) | ((thi & 63u) << 54); thi >>= 6;
      }
    };
    KV A, B;
    u32x4 W[8];
    u32x8 T[2][8];
    auto ldp = [&](KV& S, auto P) {
      constexpr int p = P.v;
      const char* q = pc[p >> 1] + lo + (p & 1) * 2048;
      S.r[2 * p] = ld16nt(q); S.r[2 * p + 1] = ld16nt(q + 1024);
    };
    auto landed = [&](KV& S, auto P) { pin2(S.r[2 * P.v], S.r[2 * P.v + 1]); };
    KV X;
    const unsigned qo = WS_QX + ((32 + h) * 2 + b) * 1024 + lane * 16;
    u32x4 g0;
    {
      unsigned tk = 0u;
      if (w4 == 0) {
        const int si = (idx & ~7) | ((idx + (0x2400 >> (4 * cls))) & 7);
        const char* p = kb + cls * 128;
        if (lane == 1) p = cls == 2 ? reinterpret_cast<const char*>(w_uv) + (si & 1) * 2097152 + (si >> 1) * 256 : reinterpret_cast<const char*>(cls == 1 ? w_o : wo) + si * 65536;
        if (lane == 2) p = vb + cls * 128;
        if (lane == 3) p = reinterpret_cast<const char*>(wo) + h * 131072;
        if (lane < 4) tk = __builtin_nontemporal_load(reinterpret_cast<const unsigned*>(p));
      }
      lds_wait_slow(cnt + 14, 4u);
      tag = __builtin_amdgcn_readfirstlane(cnt[16]);
      asm volatile("" :: "v"(tk), "v"(cpf));
    }
    STAMP(10);
    bool noorg = w4 == 3;
    auto origin = [&]() {
#pragma unroll 1
      for (int spin = 0; spin < 2 && noorg; ++spin) {
        u32x2 t[8];
        asm volatile("s_load_dwordx2 %0, %8, 0x0 glc\n\ts_load_dwordx2 %1, %8, 0x400 glc\n\ts_load_dwordx2 %2, %8, 0x800 glc\n\ts_load_dwordx2 %3, %8, 0xc00 glc\n\t"
                     "s_load_dwordx2 %4, %8, 0x1000 glc\n\ts_load_dwordx2 %5, %8, 0x1400 glc\n\ts_load_dwordx2 %6, %8, 0x1800 glc\n\ts_load_dwordx2 %7, %8, 0x1c00 glc\n\ts_waitcnt lgkmcnt(0)"
                     : "=&s"(t[0]), "=&s"(t[1]), "=&s"(t[2]), "=&s"(t[3]), "=&s"(t[4]), "=&s"(t[5]), "=&s"(t[6]), "=&s"(t[7]) : "s"(ws + WS_T0 + ((blk >> 3) & 7) * 128) : "memory");
        unsigned e = 0u; int sum = 0;
#pragma unroll
        for (int k = 0; k < 8; ++k) { e |= t[k][1] ^ tag; sum += (int)(t[k][0] - tstart); }
        if (e == 0u) { lds_add(cnt + 13, (unsigned)min(sum >> 3, 100), lane); noorg = false; }
      }
    };
    sfor<0, 12>([&](auto I) {
      constexpr int i = I.v, j = i - 4;
      if constexpr ((i & 3) == 0) next2();
      if constexpr (i == 4) origin();
      if constexpr (j >= 0) { if constexpr (j < 4) landed(X, IC<j & 3>{}); else landed(B, IC<j & 3>{}); }
      if constexpr (j >= 0 && (j & 1)) TR();
      if constexpr (i == QPE) g0 = ld16c(rs, qo);
      if constexpr (i < 4) ldp(X, IC<i & 3>{}); else if constexpr (i < 8) ldp(B, IC<i & 3>{}); else ldp(A, IC<i & 3>{});
      SCHED();
    });
    origin();
    const char* wp = reinterpret_cast<const char*>(wo) + ((size_t)h * 128 + r4 * 8) * 1024 + w4 * 256 + c16 * 16;
    u32x4 qpk;
    {
      if (!__all((g0[1] == tag) & (g0[3] == tag))) {
#pragma unroll 1
        for (int spin = 0; spin < SPIN_MAX; ++spin) {
          COMPILER_FENCE();
          g0 = ld16c(rs, qo);
          if (__all((g0[1] == tag) & (g0[3] == tag))) break;
        }
      }
      qpk = qsum(g0);
    }
    STAMP(7);
    const bool holds = cls == 0;
    float m = 0.f, l = 0.f, acc[8];
    unsigned sc[5];
#pragma unroll
    for (int k = 0; k < 8; ++k) acc[k] = 0.f;
#pragma unroll
    for (int k = 0; k < 5; ++k) sc[k] = 0xff800000u;
    int vn = 0;
    f32x4 d[8];
    unsigned torg, due = pTL - (unsigned)(ns - 5) * pTA - (3 - w4) * pSTG - ((idx * 5 + cls * 3) & 15) * pBSG;
    unsigned rwo;
    const unsigned zo = WS_GZ + (b * 32 + h) * 512;
    u32x2 zs[4];
    float wr_;
    auto zreq = [&]() {
      asm volatile("s_load_dwordx2 %0, %4, 0x0 glc\n\ts_load_dwordx2 %1, %4, 0x80 glc\n\ts_load_dwordx2 %2, %4, 0x100 glc\n\ts_load_dwordx2 %3, %4, 0x180 glc"
                   : "=&s"(zs[0]), "=&s"(zs[1]), "=&s"(zs[2]), "=&s"(zs[3]) : "s"(ws + zo) : "memory");
    };
    auto zget = [&]() {
#pragma unroll 1
      for (int spin = 0; spin < SPIN_MAX; ++spin) {
        asm volatile("s_waitcnt lgkmcnt(0)" : "+s"(zs[0]), "+s"(zs[1]), "+s"(zs[2]), "+s"(zs[3]) :: "memory");
        if (((zs[0][1] ^ tag) | (zs[1][1] ^ tag) | (zs[2][1] ^ tag) | (zs[3][1] ^ tag)) == 0u) break;
        __builtin_amdgcn_s_sleep(2);
        zreq();
      }
      asm volatile("s_waitcnt lgkmcnt(0)" : "+s"(zs[0]), "+s"(zs[1]), "+s"(zs[2]), "+s"(zs[3]) :: "memory");
      const float z0 = __uint_as_float(zs[0][0]), z1 = __uint_as_float(zs[1][0]), z2 = __uint_as_float(zs[2][0]), z3 = __uint_as_float(zs[3][0]);
      const float Z = fmaxf(fmaxf(z0, z1), fmaxf(z2, z3));
      wr_ = ex2(fmaf(m, C2, -Z)) * __builtin_amdgcn_rcpf((ex2(z0 - Z) + ex2(z1 - Z)) + (ex2(z2 - Z) + ex2(z3 - Z)));
      asm volatile("" : "+v"(wr_));
    };
    auto qk = [&](const u32x4* K, auto I) {
      float x[4];
#pragma unroll
      for (int t = 0; t < 4; ++t) {
        x[t] = 0.f;
#pragma unroll
        for (int k = 0; k < 4; ++k) x[t] = dot2(K[t][k], qpk[k], x[t]);
      }
      const float u0 = (c16 & 1 ? x[1] : x[0]) + dpp<0xB1>(c16 & 1 ? x[0] : x[1]), u1 = (c16 & 1 ? x[3] : x[2]) + dpp<0xB1>(c16 & 1 ? x[2] : x[3]);
      float v = (c16 & 2 ? u1 : u0) + dpp<0x4E>(c16 & 2 ? u0 : u1);
      v += dpp<0x124>(v); v += dpp<0x128>(v);
      sc[I.v >> 2] = __builtin_amdgcn_update_dpp(sc[I.v >> 2], __float_as_uint(v), 0xE4, 0xf, 1 << (I.v & 3), false);
      asm volatile("" : "+v"(sc[I.v >> 2]));
    };
    auto soft = [&]() {
      m = row16_max(fmaxf(fmaxf(fmaxf(__uint_as_float(sc[0]), __uint_as_float(sc[1])), fmaxf(__uint_as_float(sc[2]), __uint_as_float(sc[3]))), __uint_as_float(sc[4])));
      const float mc = m * C2;
      l = 0.f;
#pragma unroll
      for (int k = 0; k < 5; ++k) {
        const float p = ex2(fmaf(__uint_as_float(sc[k]), C2, -mc));
        sc[k] = pk_bf(p, dpp<0xB1>(p));
        l = dot2(sc[k], 0x3f003f00u, l);
      }
      l = row16_sum(l);
    };
    auto pv = [&](const u32x4* V) {
      const unsigned p01 = rowbcast<0>(sc[0]), p23 = rowbcast<2>(sc[0]);
#pragma unroll
      for (int k = 0; k < 4; ++k) { acc[2 * k] = dot2(plo(V[0][k], V[1][k]), p01, acc[2 * k]); acc[2 * k + 1] = dot2(phi(V[0][k], V[1][k]), p01, acc[2 * k + 1]); }
#pragma unroll
      for (int k = 0; k < 4; ++k) { acc[2 * k] = dot2(plo(V[2][k], V[3][k]), p23, acc[2 * k]); acc[2 * k + 1] = dot2(phi(V[2][k], V[3][k]), p23, acc[2 * k + 1]); }
      sc[0] = (unsigned)__builtin_amdgcn_mov_dpp((int)sc[0], 0x12C, 0xf, 0xf, true);
      if ((++vn & 3) == 0) { sc[0] = sc[1]; sc[1] = sc[2]; sc[2] = sc[3]; sc[3] = sc[4]; }
      asm volatile("" : "+v"(acc[0]), "+v"(acc[1]), "+v"(acc[2]), "+v"(acc[3]), "+v"(acc[4]), "+v"(acc[5]), "+v"(acc[6]), "+v"(acc[7]));
    };
    auto step = [&](KV& S, KV& N, auto SI, auto REFILL, auto WR) {
      constexpr bool refill = REFILL.v == 1;
      constexpr int wr = WR.v;
      if constexpr (refill) next2();
      if constexpr (refill && SI.v >= 3) { until((int)(due - torg) < (int)pTP ? due - pPA : due); due += pTA; }
      constexpr int ah = wr < 0 ? AHL : 0;
      auto point = [&](auto P) {
        if constexpr (refill) {
          if constexpr (P.v == 0 && ah > 0) { if constexpr (wr >= 0) { if (holds) lds_hold(cnt + 8); } sfor<0, ah>([&](auto Q) { ldp(N, Q); }); SCHED(); }
          landed(S, P);
          if constexpr (P.v & 1) TR();
          if constexpr (P.v + ah < 4) { if constexpr ((P.v & 1) == 0 && wr >= 0) { if (holds) lds_hold(cnt + 8); } ldp(N, IC<(P.v + ah) & 3>{}); }
          if constexpr (wr >= 0 && wr < 8) W[(4 * wr + P.v) & 7] = ld16(wp + (((4 * wr + P.v) >> 3) * 32 + ((4 * wr + P.v) & 7)) * 1024);
          SCHED();
        }
      };
      sfor<0, 2>([&](auto H) {
        constexpr int i = 2 * SI.v + H.v;
        if constexpr (REFILL.v == 2 && H.v == 0) {
          zreq();
#pragma unroll
          for (int e = 0; e < 8; ++e) { d[e] = f32x4{0.f, 0.f, 0.f, 0.f}; asm volatile("" : "+v"(d[e])); }
          SCHED();
        }
        point(IC<2 * H.v>{});
        point(IC<2 * H.v + 1>{});
        if constexpr (i < NSMIN) qk(S.r + 4 * H.v, IC<i>{});
        else if constexpr (i > NSMAX) pv(S.r + 4 * H.v);
        else {
          if (i < ns) { if constexpr (i < NSMAX) qk(S.r + 4 * H.v, IC<i>{}); }
          else { if (i == ns) soft(); pv(S.r + 4 * H.v); }
        }
        SCHED();
      });
      if constexpr (REFILL.v == 2) {
        zget();
        STAMP(1);
#pragma unroll
        for (int k = 0; k < 8; ++k) acc[k] *= wr_;
        *reinterpret_cast<u32x2*>(smem + rwo) = u32x2{pk_bf(rs32(acc[0], acc[2]), rs32(acc[1], acc[3])), pk_bf(rs32(acc[4], acc[6]), rs32(acc[5], acc[7]))};
        COMPILER_FENCE();
      }
      if constexpr (wr >= 1) {
        constexpr int t = (wr - 1) >> 1, j0 = ((wr - 1) & 1) * 2;
#pragma unroll
        for (int jj = 0; jj < 2; ++jj)
#pragma unroll
          for (int e = 0; e < 4; ++e) {
            const unsigned a_ = W[(4 * wr - 4 + 2 * jj) & 7][e], b_ = W[(4 * wr - 3 + 2 * jj) & 7][e];
            T[t >> 1][2 * e][4 * (t & 1) + j0 + jj] = plo(a_, b_); T[t >> 1][2 * e + 1][4 * (t & 1) + j0 + jj] = phi(a_, b_);
          }
#pragma unroll
        for (int e = 0; e < 8; e += 2) asm volatile("" : "+v"(T[t >> 1][e]), "+v"(T[t >> 1][e + 1]));
      }
      SCHED();
    };
    step(B, B, IC<1>{}, IC<0>{}, IC<-1>{});
    step(A, B, IC<2>{}, IC<1>{}, IC<0>{});
    step(X, X, IC<0>{}, IC<0>{}, IC<-1>{});
    torg = tstart + __builtin_amdgcn_readfirstlane(cnt[13]); due += torg;
    sfor<0, 4>([&](auto I) {
      step(B, A, IC<2 * I.v + 3>{}, IC<1>{}, IC<2 * I.v + 1>{});
      if constexpr (I.v == 3) {
        float* ml = reinterpret_cast<float*>(cnt + 32);
        ml[(c16 & 1) * 16 + w4 * 4 + r4] = (c16 & 1) ? l : m;
        COMPILER_FENCE();
        const unsigned got = lds_arrive_nw(L_CNT + 44);
        const float mr = ml[c16], lr = ml[16 + c16];
        SCHED();
        if (lds_done(got) == 3u) {
          const float M = row16_max(mr);
          const unsigned z = __float_as_uint(fmaf(M, C2, __builtin_amdgcn_logf(row16_sum(ex2((mr - M) * C2) * lr))));
          asm volatile("buffer_store_dwordx4 %0, %1, %2, 0 offen sc1\n\ts_nop 2" :: "v"(u32x4{z, tag, z, tag}), "v"(lane < 8 ? zo + cls * 128 + lane * 16 : OOB), "s"(rs) : "memory");
        }
        SCHED();
      }
      step(A, B, IC<2 * I.v + 4>{}, IC<1>{}, IC<2 * I.v + 2>{});
    });
    unsigned yoff = WS_GB + ((b * 128 + h * 4 + cls) * 256 + 64 * w4 + lane) * 8, rro = L_MRG + c16 * 144 + r4 * 16;
    int six = (lane & 1) ? 0xEEEE : 0x4444;
    rwo = L_MRG + (w4 * 4 + 2 * (r4 & 1) + (r4 >> 1)) * 144 + c16 * 8;
    asm volatile("" : "+v"(yoff), "+v"(rwo), "+v"(rro), "+v"(six));
    if (cls == 0) lds_wait(cnt + 1, 4u);
    const int nit = (ns - 12) >> 1;
#pragma unroll 1
    for (int i = 0; i < nit; ++i) {
      step(B, A, IC<11>{}, IC<1>{}, IC<-1>{});
      step(A, B, IC<11>{}, IC<1>{}, IC<-1>{});
    }
    if (ns & 1) { step(B, A, IC<11>{}, IC<1>{}, IC<-1>{}); step(A, A, IC<11>{}, IC<2>{}, IC<-1>{}); }
    else step(B, B, IC<11>{}, IC<2>{}, IC<-1>{});
    {
      u32x4 af[2];
      unsigned got = lds_arrive_nw(L_CNT), need = 3u;
#pragma unroll 1
      for (int spin = 0; spin < SPIN_MAX; ++spin) {
#pragma unroll
        for (int t = 0; t < 2; ++t) af[t] = *reinterpret_cast<const u32x4*>(smem + rro + t * 64);
        SCHED();
        if (lds_done(got) >= need) break;
        need = 4u;
        __builtin_amdgcn_s_sleep(1);
        asm volatile("ds_read_b32 %0, %1" : "=v"(got) : "v"((unsigned)L_CNT) : "memory");
      }
      SCHED();
      float a[8], v4[4];
      sfor<0, 3>([&](auto I) {
        constexpr int i = I.v;
        sfor<0, 8>([&](auto J) {
          constexpr int e = 2 * i + ((J.v >> 1) & 1) + 4 * (J.v & 1), t = J.v >> 2, f = e - 2;
          if constexpr (i < 2) { d[e] = __builtin_amdgcn_smfmac_f32_16x16x64_bf16(__builtin_bit_cast(bf16x8_t, af[t]), __builtin_bit_cast(bf16x16_t, T[t][e]), d[e], six, 0, 0); SCHED(); }
          if constexpr (i >= 1 && t == 1) { const f32x2 u = f32x2{d[f][0], d[f][1]} + f32x2{d[f][2], d[f][3]}; a[f] = u[0] + u[1]; asm volatile("" : "+v"(a[f])); }
          if constexpr (i >= 1 && t == 1 && (J.v & 1)) v4[f - 4] = rs32(a[f - 4], a[f]);
          if constexpr (i < 2) SCHED();
        });
      });
      st8c(rs, yoff, u32x2{pk_bf(rs16(v4[0], v4[2]), rs16(v4[1], v4[3])), tag});
    }
    STAMP(2);
    STAMP_FLUSH(1);
    TR_FLUSH();
    return;
  }
  const rsrc_t ro = __builtin_amdgcn_make_buffer_rsrc(out, 0, 4096, 0x00020000);
  auto fin = [&](auto NL, unsigned base, unsigned col, bool bump) {
    constexpr int nl = NL.v;
    unsigned doff = base + (((idx >> 5) * 4 + w4) * 16 * nl + (lane >> 2)) * 2048 + (idx & 31) * 64 + (lane & 3) * 16;
    unsigned fwo = 32768 + (c16 < 4 ? (w4 * 16 + 4 * c16 + r4) * 4 : 256 + lane * 4);
    unsigned fro = 32768 + (lane & 7) * 8, foo = lane < 8 ? ((idx >> 5) * 1024 + col + (nl == 2 ? 16 * (idx & 24) + 64 * (idx & 1) + (idx & 6) + 8 * lane : 16 * (idx & 31) + 2 * lane)) * 2 : OOB;
    asm volatile("" : "+v"(doff), "+v"(fwo), "+v"(fro), "+v"(foo));
    lds_wait(cnt + 0, 4u);
    until(tstart + __builtin_amdgcn_readfirstlane(cnt[13]) + PAR(0, TL) + PAR(4 + nl, TF));
    STAMP(4);
    u32x4 d[nl];
    unsigned eh = 1u;
#pragma unroll 1
    for (int spin = SPIN_MAX; spin > 0; --spin) {
      COMPILER_FENCE();
      if (eh != 0u) {
#pragma unroll
        for (int j = 0; j < nl; ++j) d[j] = ld16c(rs, doff + j * 32768);
        eh = 0u;
#pragma unroll
        for (int j = 0; j < nl; ++j) { eh |= (d[j][1] ^ tag) | (d[j][3] ^ tag); asm volatile("" : "+v"(eh)); }
      }
      if (__builtin_amdgcn_ballot_w64(eh != 0u) == 0ull) break;
    }
    STAMP(11);
    float y[4] = {bflo(d[0][0]), bfhi(d[0][0]), bflo(d[0][2]), bfhi(d[0][2])};
    if constexpr (nl == 2) { y[0] += bflo(d[1][0]); y[1] += bfhi(d[1][0]); y[2] += bflo(d[1][2]); y[3] += bfhi(d[1][2]); }
    float t = rs16(rs32(y[0], y[2]), rs32(y[1], y[3]));
    t += dpp<0x124>(t); t += dpp<0x128>(t);
    *reinterpret_cast<float*>(smem + fwo) = t;
    COMPILER_FENCE();
    const unsigned got = lds_arrive_nw(L_CNT + 24);
    f32x2 t2[4];
#pragma unroll
    for (int w = 0; w < 4; ++w) t2[w] = *reinterpret_cast<const f32x2*>(smem + fro + w * 64);
    SCHED();
    if (lds_done(got) == 3u) {
      const f32x2 r = (t2[0] + t2[1]) + (t2[2] + t2[3]);
      __builtin_amdgcn_raw_buffer_store_b32(pk_bf(r[0], r[1]), ro, (int)foo, 0, 0);
      if (bump) st8c(rs, lane == 0 ? WS_EP : OOB, u32x2{tlow, tag});
    }
  };
  u32x4 Wa[16], Wb[16], xv[4];
  auto row = [&](u32x4& d) { d = ld16nt(pa + ao); SCHED(); ao += sa; asm volatile("" : "+v"(ao)); };
  sfor<0, 2>([&](auto I) { row(Wa[I.v]); });
  sfor<0, 4>([&](auto J) { xv[J.v] = ld16(xp + xo + (J.v >> 1) * 1024 + 16 * (J.v & 1)); });
  SCHED();
  sfor<2, 8>([&](auto I) { row(Wa[I.v]); });
  epw = ld8c(rs, WS_EP);
  st16c(rs, cls == 1 && w4 == 0 ? WS_QA + (b * 32 + h) * 1024 + lane * 16 : OOB, u32x4{0u, 0u, 0u, 0u});
  SCHED();
  auto sync = [&]() {
    rawbar();
    take_epoch();
    cnt[16] = tag;
    COMPILER_FENCE();
  };
  if (cls == 0) {
    sfor<8, 16>([&](auto I) { row(Wa[I.v]); });
    sync();
    asm volatile("" :: "v"(xv[0]), "v"(xv[1]), "v"(xv[2]), "v"(xv[3]));
    STAMP(13);
    char* scr = smem + L_SCR;
    const int ab = idx >> 5, r = lane & 31, hh = lane >> 5;
    const unsigned fo = WS_QX + (w4 * 8 + (c16 >> 1)) * 2048 + (c16 & 1) * 512 + r4 * 8;
    u32x2 fl;
    u32x4 g[9];
    const unsigned o8 = WS_QA + (ab * 32 + w4 * 8) * 1024 + lane * 16, ol = WS_QA + (ab * 32 + r) * 1024 + 992 + 16 * hh;
    auto put = [&](int i, const u32x4& v) { *reinterpret_cast<u32x4*>(smem + ((i >> 3) * 4 + r4) * 8192 + toff(4 * (i & 7) + w4, c16)) = v; };
    sfor<0, 16>([&](auto I) { put(I.v, Wa[I.v]); if constexpr ((I.v & 3) == 3) TR(); SCHED(); });
    until(tstart + PAR(26, T0B));
    sfor<0, 16>([&](auto I) { row(Wb[I.v]); });
    lds_add(cnt + 14, 1u, lane);
    SCHED();
    sfor<0, 16>([&](auto I) { put(16 + I.v, Wb[I.v]); if constexpr (I.v == 12) fl = ld8c(rs, fo); if constexpr ((I.v & 3) == 3) TR(); SCHED(); });
    STAMP(8);
    u32x4 qlast;
    {
      if (!__all(fl[1] == tag)) {
#pragma unroll 1
        for (int spin = 0; spin < SPIN_MAX; ++spin) {
          COMPILER_FENCE();
          if (__all(ld8c(rs, fo)[1] == tag)) break;
          __builtin_amdgcn_s_sleep(8);
        }
      }
      until(tstart + PAR(27, T0Q));
      STAMP(5);
      sbar4(cnt + 7, lane);
      STAMP(12);
      unsigned pend = 0x1ffu;
#pragma unroll 1
      for (int spin = 0; spin < SPIN_MAX && pend; ++spin) {
        COMPILER_FENCE();
        u32x4 t[9];
#pragma unroll
        for (int i = 0; i < 9; ++i) t[i] = ld16c(rs, (pend >> i & 1) ? (i < 8 ? o8 + i * 1024 : ol) : OOB);
#pragma unroll
        for (int i = 0; i < 9; ++i) {
          if (pend >> i & 1) g[i] = t[i];
          if (__all(nz8(t[i]))) pend &= ~(1u << i);
        }
      }
      STAMP(9);
      qlast = g[8];
#pragma unroll
      for (int i = 0; i < 8; ++i)
        if (lane < 62) *reinterpret_cast<u32x4*>(scr + (w4 * 8 + i) * 1008 + 16 * lane) = g[i];
    }
    sbar4(cnt + 3, lane);
    STAMP(10);
    f32x16 sacc;
#pragma unroll
    for (int i = 0; i < 16; ++i) sacc[i] = 0.f;
#pragma unroll
    for (int jj = 0; jj < 32; ++jj) {
      const u32x4 af = *reinterpret_cast<const u32x4*>(smem + (w4 * 4 + (jj >> 3)) * 8192 + toff(r, 2 * (jj & 7) + hh));
      const u32x4 bf = jj < 31 ? *reinterpret_cast<const u32x4*>(scr + r * 1008 + 16 * (2 * (jj < 31 ? jj : 0) + hh)) : qlast;
      sacc = __builtin_amdgcn_mfma_f32_32x32x16_bf16(__builtin_bit_cast(bf16x8_t, af), __builtin_bit_cast(bf16x8_t, bf), sacc, 0, 0, 0);
    }
    float m = sacc[0];
#pragma unroll
    for (int i = 1; i < 16; ++i) m = fmaxf(m, sacc[i]);
    m = xhalf_max(m);
    if (hh == 0) *reinterpret_cast<float*>(scr + r * 1008 + 992 + 4 * w4) = m;
    sbar4(cnt + 4, lane);
    STAMP(11);
    {
      const f32x4 m4 = *reinterpret_cast<const f32x4*>(scr + r * 1008 + 992);
      m = fmaxf(fmaxf(m4[0], m4[1]), fmaxf(m4[2], m4[3]));
    }
    const float mc = m * C2;
    float p[16], ls = 0.f;
#pragma unroll
    for (int i = 0; i < 16; ++i) { p[i] = ex2(fmaf(sacc[i], C2, -mc)); ls += p[i]; }
    ls = xhalf_sum(ls);
    sbar4(cnt + 5, lane);
    u32x4* pf = reinterpret_cast<u32x4*>(scr);
    float* wl = reinterpret_cast<float*>(scr + 8192);
    if (hh == 0) wl[r * 4 + w4] = ls;
    pf[(2 * w4) * 64 + lane] = u32x4{pk_bf(p[0], p[1]), pk_bf(p[2], p[3]), pk_bf(p[4], p[5]), pk_bf(p[6], p[7])};
    pf[(2 * w4 + 1) * 64 + lane] = u32x4{pk_bf(p[8], p[9]), pk_bf(p[10], p[11]), pk_bf(p[12], p[13]), pk_bf(p[14], p[15])};
    sbar4(cnt + 6, lane);
    u32x4 pb8[8];
#pragma unroll
    for (int ks = 0; ks < 8; ++ks) pb8[ks] = pf[ks * 64 + lane];
    const f32x4 l4 = *reinterpret_cast<const f32x4*>(wl + r * 4);
    const float lsum = (l4[0] + l4[1]) + (l4[2] + l4[3]), rl = __builtin_amdgcn_rcpf(lsum);
    const int qq = (lane & 15) >> 2, pp = lane & 3, blk2 = (lane >> 4) & 1;
    const unsigned tw = tstart + __builtin_amdgcn_readfirstlane(cnt[13]) + PAR(13, TW), th = tw - PAR(14, HL);
    u32x4 vv[2][4];
    bool held = false;
    const int hx = (lane >> 3) ^ (idx & 7);
    char* ot = scr + 12288 + w4 * 4096;
#pragma unroll
    for (int c2 = 0; c2 < 2; ++c2) {
      f32x16 oacc[2];
#pragma unroll
      for (int ct = 0; ct < 2; ++ct)
#pragma unroll
        for (int i = 0; i < 16; ++i) oacc[ct][i] = 0.f;
#pragma unroll
      for (int ks = 0; ks < 8; ++ks)
#pragma unroll
        for (int ct = 0; ct < 2; ++ct) {
          typedef __attribute__((address_space(3))) s16x4* lp;
          const char* tile = smem + ((ks >> 1) * 4 + w4) * 8192;
          const int cc = 2 * c2 + ct;
          const s16x4 lo = __builtin_amdgcn_ds_read_tr16_b64_v4i16((lp)(tile + toff(16 * (ks & 1) + 4 * hh + qq, 4 * cc + 2 * blk2 + (pp >> 1)) + 8 * (pp & 1)));
          const s16x4 hi = __builtin_amdgcn_ds_read_tr16_b64_v4i16((lp)(tile + toff(16 * (ks & 1) + 8 + 4 * hh + qq, 4 * cc + 2 * blk2 + (pp >> 1)) + 8 * (pp & 1)));
          const s16x8 afr = __builtin_shufflevector(lo, hi, 0, 1, 2, 3, 4, 5, 6, 7);
          oacc[ct] = __builtin_amdgcn_mfma_f32_32x32x16_bf16(__builtin_bit_cast(bf16x8_t, afr), __builtin_bit_cast(bf16x8_t, pb8[ks]), oacc[ct], 0, 0, 0);
        }
#pragma unroll
      for (int ct = 0; ct < 2; ++ct)
#pragma unroll
        for (int g = 0; g < 4; ++g)
          *reinterpret_cast<u32x2*>(ot + r * 128 + 16 * ((4 * ct + g) ^ (r & 7)) + 8 * hh) = u32x2{pk_bf(oacc[ct][4 * g] * rl, oacc[ct][4 * g + 1] * rl), pk_bf(oacc[ct][4 * g + 2] * rl, oacc[ct][4 * g + 3] * rl)};
      if (c2 == 0) { held = (int)((unsigned)wall_clock64() + 30u - th) >= 0; if (held) lds_add(cnt + 8, 1u, lane); }
      __builtin_amdgcn_wave_barrier();
#pragma unroll
      for (int i = 0; i < 4; ++i) { vv[c2][i] = *reinterpret_cast<const u32x4*>(ot + (8 * i + hx) * 128 + 16 * ((lane & 7) ^ hx)); asm volatile("" : "+v"(vv[c2][i])); }
      __builtin_amdgcn_wave_barrier();
    }
    lds_arrive(cnt + 1, lane);
    if (!held) { until(th); lds_add(cnt + 8, 1u, lane); }
    until(tw);
#pragma unroll
    for (int i = 0; i < 8; ++i) st16c(rs, WS_APO + idx * 32768 + w4 * 8192 + i * 1024 + lane * 16, vv[i & 1][i >> 1]);
    STAMP(1);
    asm volatile("s_waitcnt vmcnt(0)" ::: "memory");
    if (lds_arrive(cnt + 9 + (w4 >> 1), lane) == 1u && hh == 0)
      st8c(rs, WS_AML + ((r * 2 + (w4 >> 1)) * 16 + (idx >> 2)) * 128 + (idx & 3) * 8, u32x2{__float_as_uint(mc + __builtin_amdgcn_logf(lsum)), tag});
    lds_add(cnt + 8, ~0u, lane);
    STAMP(2);
    STAMP_FLUSH(0); TR_FLUSH();
    return;
  }
  sfor<8, 16>([&](auto I) { row(Wa[I.v]); });
  const u16* pb = cls == 2 ? w_uv + ((size_t)(b * 256 + w4 * 64 + 16 * r4) * 32 + h) * 128 + 8 * c16 : (cls == 1 ? w_o : wo) + ((size_t)h * 128 + b * 64 + w4 * 2) * 512 + 8 * lane;
  sync();
  if (blk < 16 && w4 == 0) asm volatile("buffer_store_dwordx4 %0, %1, %2, 0 offen sc1\n\ts_nop 2" :: "v"(u32x4{tstart, tag, tstart, tag}), "v"(WS_T0 + (blk & 7) * 1024 + lane * 16), "s"(rs) : "memory");
  STAMP(13);
  float* red = reinterpret_cast<float*>(smem);
  if (cls & 1) {
    float acc[2][8];
#pragma unroll
    for (int bb = 0; bb < 2; ++bb)
#pragma unroll
      for (int k = 0; k < 8; ++k) acc[bb][k] = 0.f;
    sfor<0, 8>([&](auto IP) {
      constexpr int ip = IP.v, j = ip - (8 - WBW);
      use2(Wa[2 * ip], Wa[2 * ip + 1]);
      if constexpr (ip & 1) TR();
      if constexpr (j >= 0) { Wb[2 * j] = ld16(pb + j * 4096); Wb[2 * j + 1] = ld16(pb + j * 4096 + 512); }
      SCHED();
      const unsigned xp0 = xv[ip >> 2][ip & 3], xp1 = xv[2 + (ip >> 2)][ip & 3];
#pragma unroll
      for (int k = 0; k < 4; ++k) {
        const unsigned lo = plo(Wa[2 * ip][k], Wa[2 * ip + 1][k]), hi = phi(Wa[2 * ip][k], Wa[2 * ip + 1][k]);
        acc[0][2 * k] = dot2(lo, xp0, acc[0][2 * k]); acc[0][2 * k + 1] = dot2(hi, xp0, acc[0][2 * k + 1]);
        acc[1][2 * k] = dot2(lo, xp1, acc[1][2 * k]); acc[1][2 * k + 1] = dot2(hi, xp1, acc[1][2 * k + 1]);
      }
      SCHED();
    });
    {
      float v8[8], v4[4];
#pragma unroll
      for (int k = 0; k < 8; ++k) v8[k] = rs32(acc[0][k], acc[1][k]);
#pragma unroll
      for (int k = 0; k < 4; ++k) v4[k] = rs16(v8[k], v8[k + 4]);
      *reinterpret_cast<f32x4*>(red + (w4 * 2 + (lane >> 5)) * 128 + 8 * c16 + 4 * (r4 & 1)) = f32x4{v4[0], v4[1], v4[2], v4[3]};
    }
    COMPILER_FENCE();
    const unsigned got = lds_arrive_nw(L_CNT + 8);
    f32x4 t4[4];
#pragma unroll
    for (int w = 0; w < 4; ++w) t4[w] = *reinterpret_cast<const f32x4*>(red + (w * 2 + (lane >> 5)) * 128 + 8 * c16 + 4 * (r4 & 1));
    SCHED();
    if (lds_done(got) == 3u) {
      const f32x4 t = (t4[0] + t4[1]) + (t4[2] + t4[3]);
      st16c(rs, WS_QX + (((cls >> 1) * 32 + h) * 2 + (lane >> 5)) * 1024 + b * 512 + (lane & 31) * 16, u32x4{pk_bf(t[0], t[1]), tag, pk_bf(t[2], t[3]), tag});
      STAMP(15);
      SCHED();
      lds_add(cnt + 2, 1u, lane);
    } else lds_wait(cnt + 2, 5u);
    sfor<WBW, 8>([&](auto J) {
      constexpr int j = J.v;
      use2(Wb[2 * (j - WBW)], Wb[2 * (j - WBW) + 1]);
      if constexpr ((j - WBW) & 1) TR();
      Wb[2 * j] = ld16(pb + j * 4096); Wb[2 * j + 1] = ld16(pb + j * 4096 + 512);
      if constexpr (j == 7) lds_add(cnt + 14, 1u, lane);
      SCHED();
    });
    {
      const unsigned tw = __builtin_nontemporal_load(reinterpret_cast<const unsigned*>(out + (idx >> 5) * 1024 + (cls >> 1) * 512 + 16 * (idx & 31)));
      sfor<8 - WBW, 8>([&](auto J) { use2(Wb[2 * J.v], Wb[2 * J.v + 1]); if constexpr (J.v & 1) TR(); });
      asm volatile("" :: "v"(tw));
    }
    STAMP(8);
  } else {
    sfor<0, 16 - WBW>([&](auto IP) {
      constexpr int ip = IP.v, j = ip - (8 - WBW);
      if constexpr (ip < 8) use2(Wa[2 * ip], Wa[2 * ip + 1]); else use2(Wb[2 * ip - 16], Wb[2 * ip - 15]);
      if constexpr (ip & 1) TR();
      if constexpr (j >= 0) { Wb[2 * j] = ld16nt(pb + j * 8192); Wb[2 * j + 1] = ld16nt(pb + j * 8192 + 4096); }
      SCHED();
    });
    u32x4 g0, g1;
    const unsigned qo = WS_QX + h * 2048 + lane * 16;
    if (w4 == 0) { shint<512, 0>(ws + WS_QX + h * 2048, tag); lds_add(cnt + 2, 1u, lane); } else lds_wait(cnt + 2, 1u);
    STAMP(5);
#pragma unroll 1
    for (int spin = 0; spin < SPIN_MAX; ++spin) {
      COMPILER_FENCE();
      g0 = ld16c(rs, qo); g1 = ld16c(rs, qo + 1024);
      SCHED();
      lds_add(cnt + 14, spin == 0, lane);
      SCHED();
      if (__all((g0[1] == tag) & (g0[3] == tag) & (g1[1] == tag) & (g1[3] == tag))) break;
      __builtin_amdgcn_s_sleep(1);
    }
    sfor<8 - WBW, 8>([&](auto J) { if constexpr (J.v & 1) TR(); });
    const u32x4 q0 = qsum(g0), q1 = qsum(g1);
    STAMP(9);
    float v[8];
    {
      float u[16];
#pragma unroll
      for (int i = 0; i < 16; ++i) {
        float a = 0.f, c = 0.f;
#pragma unroll
        for (int k = 0; k < 4; ++k) { a = dot2(Wa[i][k], q0[k], a); c = dot2(Wa[i][k], q1[k], c); }
        u[i] = (c16 & 2 ? c : a) + dpp<0x4E>(c16 & 2 ? a : c);
        asm volatile("" : "+v"(u[i]));
      }
#pragma unroll
      for (int i = 0; i < 8; ++i) {
        float t = (c16 & 1 ? u[i + 8] : u[i]) + dpp<0xB1>(c16 & 1 ? u[i] : u[i + 8]);
        asm volatile("" : "+v"(t));
        t += dpp<0x124>(t);
        asm volatile("" : "+v"(t));
        t += dpp<0x128>(t);
        asm volatile("" : "+v"(t));
        v[i] = t;
      }
    }
    u32x4 o = {pk_bf(v[0], v[1]), pk_bf(v[2], v[3]), pk_bf(v[4], v[5]), pk_bf(v[6], v[7])};
    if ((o[0] | o[1]) == 0u) o[0] = 0x8000u;
    if ((o[2] | o[3]) == 0u) o[2] = 0x8000u;
    if (c16 < 4) st16c(rs, WS_QA + (((c16 >> 1) & 1) * 32 + h) * 1024 + (b * 256 + w4 * 64 + 16 * r4 + 8 * (c16 & 1)) * 2, o);
    STAMP(10);
  }
  asm volatile("" :: "v"(xv[0]), "v"(xv[1]), "v"(xv[2]), "v"(xv[3]));
  SCHED();
  if (cls == 3) {
    fin(IC<2>{}, WS_GB, 512, false);
    STAMP(6);
    STAMP_FLUSH(0); TR_FLUSH();
    return;
  }
#pragma unroll
  for (int ip = 0; ip < 8; ++ip) {
#pragma unroll
    for (int k = 0; k < 4; ++k) { const unsigned lo = plo(Wb[2 * ip][k], Wb[2 * ip + 1][k]), hi = phi(Wb[2 * ip][k], Wb[2 * ip + 1][k]); Wb[2 * ip][k] = lo; Wb[2 * ip + 1][k] = hi; }
    pin2(Wb[2 * ip], Wb[2 * ip + 1]);
  }
  if (cls == 2) {
    float* red2 = red + 4 * 256;
    float* wml = red + 3072;
    u32x8 f[4];
    until<6>(tstart + PAR(13, TW) + PAR(28, 100));
#pragma unroll 1
    for (int spin = 0; spin < SPIN_MAX; ++spin) {
      sld4x32(ws + WS_AML + ((h * 2 + b) * 16 + w4 * 4) * 128, f);
      unsigned e = 0u;
#pragma unroll
      for (int i = 0; i < 16; ++i) e |= f[i >> 2][2 * (i & 3) + 1] ^ tag;
      if (e == 0u) break;
      __builtin_amdgcn_s_sleep(2);
    }
    STAMP(3);
    u32x4 pp[8];
#pragma unroll
    for (int i = 0; i < 8; ++i) pp[i] = ld16c(rs, WS_APO + (w4 * 16 + 2 * i + (lane >> 5)) * 32768 + (2 * b + (r4 & 1)) * 8192 + (2 * (h >> 3) + ((lane >> 3) & 1)) * 1024 + ((h ^ (2 * i + (lane >> 5))) & 7) * 128 + (lane & 7) * 16);
    SCHED();
    float wt[8];
    {
      float M = -INFINITY, L = 0.f;
#pragma unroll
      for (int i = 0; i < 8; ++i) {
        unsigned z0 = f[i >> 1][4 * (i & 1)], z1 = f[i >> 1][4 * (i & 1) + 2];
        asm("" : "+s"(z0), "+s"(z1));
        wt[i] = __uint_as_float(lane < 32 ? z0 : z1); M = fmaxf(M, wt[i]);
      }
      M = xhalf_max(M);
#pragma unroll
      for (int i = 0; i < 8; ++i) { wt[i] = ex2(wt[i] - M); L += wt[i]; }
      L = xhalf_sum(L);
      if (lane == 0) *reinterpret_cast<f32x2*>(wml + 2 * w4) = f32x2{M, L};
    }
    SCHED();
    float a8[8];
#pragma unroll
    for (int k = 0; k < 8; ++k) a8[k] = 0.f;
#pragma unroll
    for (int i = 0; i < 8; ++i) {
#pragma unroll
      for (int k = 0; k < 4; ++k) { a8[2 * k] = fmaf(wt[i], bflo(pp[i][k]), a8[2 * k]); a8[2 * k + 1] = fmaf(wt[i], bfhi(pp[i][k]), a8[2 * k + 1]); }
      asm volatile("" : "+v"(a8[0]), "+v"(a8[1]), "+v"(a8[2]), "+v"(a8[3]), "+v"(a8[4]), "+v"(a8[5]), "+v"(a8[6]), "+v"(a8[7]));
    }
    STAMP(6);
    *reinterpret_cast<f32x4*>(red + w4 * 256 + 8 * (lane & 31) + 4 * (lane >> 5)) = f32x4{rs32(a8[0], a8[4]), rs32(a8[1], a8[5]), rs32(a8[2], a8[6]), rs32(a8[3], a8[7])};
    sbar4(cnt + 3, lane);
    STAMP(4);
    float acc[2][8];
#pragma unroll
    for (int bb = 0; bb < 2; ++bb) {
      u32x4 cv[2];
      const f32x4 ml = *reinterpret_cast<const f32x4*>(wml + 4 * bb);
      const float M = fmaxf(ml[0], ml[2]), e0 = ex2(ml[0] - M), e1 = ex2(ml[2] - M), rl = 1.f / fmaf(e0, ml[1], e1 * ml[3]), s0 = e0 * rl, s1 = e1 * rl;
#pragma unroll
      for (int e = 0; e < 2; ++e) {
        const float* src = red + bb * 512 + w4 * 64 + 16 * r4 + 8 * e;
        const f32x4 t0 = s0 * *reinterpret_cast<const f32x4*>(src) + s1 * *reinterpret_cast<const f32x4*>(src + 256), t1 = s0 * *reinterpret_cast<const f32x4*>(src + 4) + s1 * *reinterpret_cast<const f32x4*>(src + 260);
        cv[e] = u32x4{pk_bf(t0[0], t0[1]), pk_bf(t0[2], t0[3]), pk_bf(t1[0], t1[1]), pk_bf(t1[2], t1[3])};
      }
#pragma unroll
      for (int k = 0; k < 8; ++k) acc[bb][k] = 0.f;
#pragma unroll
      for (int ip = 0; ip < 8; ++ip)
#pragma unroll
        for (int k = 0; k < 4; ++k) { acc[bb][2 * k] = dot2(Wb[2 * ip][k], cv[ip >> 2][ip & 3], acc[bb][2 * k]); acc[bb][2 * k + 1] = dot2(Wb[2 * ip + 1][k], cv[ip >> 2][ip & 3], acc[bb][2 * k + 1]); }
    }
    {
      float v8[8], v4[4];
#pragma unroll
      for (int k = 0; k < 8; ++k) v8[k] = rs32(acc[0][k], acc[1][k]);
#pragma unroll
      for (int k = 0; k < 4; ++k) v4[k] = rs16(v8[k], v8[k + 4]);
      *reinterpret_cast<f32x4*>(red2 + (w4 * 2 + (lane >> 5)) * 128 + 8 * c16 + 4 * (r4 & 1)) = f32x4{v4[0], v4[1], v4[2], v4[3]};
    }
    {
      COMPILER_FENCE();
      const unsigned got = lds_arrive_nw(L_CNT + 16);
      f32x4 t4[4];
#pragma unroll
      for (int w = 0; w < 4; ++w) t4[w] = *reinterpret_cast<const f32x4*>(red2 + w * 256 + 4 * lane);
      SCHED();
      if (lds_done(got) == 3u) {
        const f32x4 t = (t4[0] + t4[1]) + (t4[2] + t4[3]);
        const unsigned co = WS_CX + ((h * 2 + b) * 256 + 64 * r4 + 32 * (c16 & 1) + 2 * (c16 >> 1)) * 8;
        st16c(rs, co, u32x4{__float_as_uint(t[0]), tag, __float_as_uint(t[1]), tag});
        st16c(rs, co + 128, u32x4{__float_as_uint(t[2]), tag, __float_as_uint(t[3]), tag});
      }
    }
    STAMP(12);
    STAMP_FLUSH(0); TR_FLUSH();
    return;
  }
  {
    unsigned op;
    {
      u32x4 c;
      const unsigned co = WS_CX + ((h * 2 + ((lane >> 3) & 1)) * 256 + (lane >> 5) * 128 + b * 64 + w4 * 16 + 2 * (lane & 7)) * 8;
      if (idx == 0 && w4 == 1) {
#pragma unroll 1
        for (int spin = 0; spin < SPIN_MAX; ++spin) {
          COMPILER_FENCE();
          if (__all(ld8c(rs, WS_QX + 65536 + (lane >> 1) * 2048 + (lane & 1) * 512)[1] == tag)) break;
          __builtin_amdgcn_s_sleep(8);
        }
      }
      until<6>(tstart + PAR(13, TW) + PAR(29, 370));
      if (w4 == 0) { shint<2048, 0>(ws + WS_CX + (h * 512 + b * 64) * 8, tag); lds_add(cnt + 7, 1u, lane); }
      else lds_wait(cnt + 7, 1u);
      STAMP(3);
#pragma unroll 1
      for (int spin = 0; spin < SPIN_MAX; ++spin) {
        COMPILER_FENCE();
        c = ld16c(rs, co);
        if (__all((c[1] == tag) & (c[3] == tag))) break;
        __builtin_amdgcn_s_sleep(1);
      }
      float o0 = __uint_as_float(c[0]), o1 = __uint_as_float(c[2]);
      o0 += dpp<0x128>(o0); o1 += dpp<0x128>(o1);
      op = pk_bf(o0, o1);
    }
    STAMP(14);
    float* red3 = red + 4096;
    float a[2][8];
#pragma unroll
    for (int bb = 0; bb < 2; ++bb)
#pragma unroll
      for (int k = 0; k < 8; ++k) a[bb][k] = 0.f;
#pragma unroll
    for (int ip = 0; ip < 8; ++ip) {
      const unsigned v0 = __builtin_amdgcn_readlane(op, ip), v1 = __builtin_amdgcn_readlane(op, 32 + ip);
#pragma unroll
      for (int k = 0; k < 4; ++k) {
        a[0][2 * k] = dot2(Wb[2 * ip][k], v0, a[0][2 * k]); a[0][2 * k + 1] = dot2(Wb[2 * ip + 1][k], v0, a[0][2 * k + 1]);
        a[1][2 * k] = dot2(Wb[2 * ip][k], v1, a[1][2 * k]); a[1][2 * k + 1] = dot2(Wb[2 * ip + 1][k], v1, a[1][2 * k + 1]);
      }
    }
#pragma unroll
    for (int bb = 0; bb < 2; ++bb) {
      *reinterpret_cast<f32x4*>(red3 + (w4 * 2 + bb) * 512 + 8 * lane) = f32x4{a[bb][0], a[bb][1], a[bb][2], a[bb][3]};
      *reinterpret_cast<f32x4*>(red3 + (w4 * 2 + bb) * 512 + 8 * lane + 4) = f32x4{a[bb][4], a[bb][5], a[bb][6], a[bb][7]};
    }
    sbar4(cnt + 5, lane);
    f32x4 t = {0.f, 0.f, 0.f, 0.f};
#pragma unroll
    for (int w = 0; w < 4; ++w) t += *reinterpret_cast<const f32x4*>(red3 + w * 1024 + 4 * tid);
    st16c(rs, WS_GA + ((tid >> 7) * 64 + idx) * 2048 + (tid & 127) * 16, u32x4{pk_bf(t[0], t[1]), tag, pk_bf(t[2], t[3]), tag});
    STAMP(5);
  }
  fin(IC<1>{}, WS_GA, 0, idx == 0);
  STAMP(6);
  STAMP_FLUSH(0); TR_FLUSH();
}

extern "C" void kernel_launch(void* const* d_in, const int* in_sizes, int n_in, void* d_out, int out_size, void* d_ws, size_t ws_size, hipStream_t stream) {
  const u16* xA = (const u16*)d_in[0];
  const u16* ckv = (const u16*)d_in[1];
  const u16* w_q = (const u16*)d_in[2];
  const u16* w_uk = (const u16*)d_in[3];
  const u16* w_uv = (const u16*)d_in[4];
  const u16* w_o = (const u16*)d_in[5];
  const u16* xB = (const u16*)d_in[6];
  const u16* kc = (const u16*)d_in[7];
  const u16* vc = (const u16*)d_in[8];
  const u16* wq = (const u16*)d_in[9];
  const u16* wo = (const u16*)d_in[10];
  kmain<<<256, 512, 0, stream>>>(ckv, w_q, w_uk, wq, xA, xB, w_uv, w_o, kc, vc, wo, (char*)d_ws, (u16*)d_out);
}
```
